```python
import torch

def _softmax(x, axis=-1):
    return torch.softmax(x, dim=axis)

def _gelu(x):
    return torch.nn.functional.gelu(x, approximate='tanh')

def _silu(x):
    return torch.nn.functional.silu(x)

def _sigmoid(x):
    return torch.sigmoid(x)

def _layernorm(x, axis=-1, eps=1e-05):
    m = x.mean(dim=axis, keepdim=True)
    v = x.var(dim=axis, keepdim=True, unbiased=False)
    return (x - m) / torch.sqrt(v + eps)

def _scatter_add(tgt, idx, src, axis=0):
    return tgt.clone().index_add_(axis, idx, src)

def _segment_sum(src, seg, n):
    out = torch.zeros((n,) + src.shape[1:], dtype=src.dtype)
    return out.index_add_(0, seg, src)

def _pad(x, pw):
    flat = tuple((v for pair in reversed(pw) for v in pair))
    return torch.nn.functional.pad(x, flat)
kernel_name = 'adaLN_cond__sliding_block_cached__dense_activation_gelu__decode'

def setup_inputs():

    _w = lambda a: (lambda t: t.to(torch.bfloat16) if t.is_floating_point() else t)(torch.as_tensor(a))

    def _A_setup():
        B, Sq, D = (4, 1, 640)
        Hq, Hkv, Sk, Dh = (32, 32, 8192, 128)
        assert Hq % Hkv == 0, (Hq, Hkv)
        return {'x': _w((lambda *s: torch.randn(*s, dtype=torch.bfloat16) * 0.1)(B, Sq, D)), 'k_cache': _w((lambda *s: torch.randn(*s, dtype=torch.bfloat16) * 0.8)(B, Hkv, Sk, Dh)), 'v_cache': _w((lambda *s: torch.randn(*s, dtype=torch.bfloat16) * 3.2)(B, Hkv, Sk, Dh)), 'wq': _w((lambda *s: torch.randn(*s, dtype=torch.bfloat16) * 0.8)(D, Hq * Dh) * D ** (-0.5)), 'wo': _w((lambda *s: torch.randn(*s, dtype=torch.bfloat16) * 3.2)(Hq * Dh, D) * (Hq * Dh) ** (-0.5))}
    _A_in = _A_setup()

    def _B_setup():
        B, S, D, F = (4, 1, 640, 1728)
        return {'x': _w((lambda *s: torch.randn(*s, dtype=torch.bfloat16) * 0.1)(B, S, D)), 'w1': _w((lambda *s: torch.randn(*s, dtype=torch.bfloat16) * 3.2)(D, F) * D ** (-0.5)), 'b1': _w((lambda *s: torch.randn(*s, dtype=torch.bfloat16) * 3.2)(F) * 0.01), 'w2': _w((lambda *s: torch.randn(*s, dtype=torch.bfloat16) * 3.2)(F, D) * F ** (-0.5)), 'b2': _w((lambda *s: torch.randn(*s, dtype=torch.bfloat16) * 3.2)(D) * 0.01)}
    _B_in = _B_setup()
    return {'A_x': _A_in['x'], 'A_k_cache': _A_in['k_cache'], 'A_v_cache': _A_in['v_cache'], 'A_wq': _A_in['wq'], 'A_wo': _A_in['wo'], 'B_x': _B_in['x'], 'B_w1': _B_in['w1'], 'B_b1': _B_in['b1'], 'B_w2': _B_in['w2'], 'B_b2': _B_in['b2'], 'c': _w((lambda *s: torch.randn(*s, dtype=torch.bfloat16) * 0.1)(4, 640) * 8.0), 'w_mod': _w((lambda *s: torch.randn(*s, dtype=torch.bfloat16) * 0.1)(640, 6 * 640) * 640 ** (-0.5) * 8.0)}

def reference(A_x, A_k_cache, A_v_cache, A_wq, A_wo, B_x, B_w1, B_b1, B_w2, B_b2, c, w_mod):

    def _A_ref(x, k_cache, v_cache, wq, wo):
        B, Sq, D = x.shape
        Hq, Dh = (32, 128)
        Hkv, Sk = (k_cache.shape[1], k_cache.shape[2])
        W = min(4096, max(256, Sk // 4))
        rep = Hq // Hkv
        q = (lambda x, *d: x.permute(*d))((x @ wq).reshape(B, Sq, Hq, Dh), 0, 2, 1, 3)
        k = (lambda x, n, axis: torch.repeat_interleave(x, n, dim=axis))(k_cache[:, :, Sk - W:, :], rep, axis=1)
        v = (lambda x, n, axis: torch.repeat_interleave(x, n, dim=axis))(v_cache[:, :, Sk - W:, :], rep, axis=1)
        scores = torch.einsum('bhqd,bhkd->bhqk', q, k) * Dh ** (-0.5)
        p = _softmax(scores, axis=-1)
        o = torch.einsum('bhqk,bhkd->bhqd', p, v)
        return (lambda x, *d: x.permute(*d))(o, 0, 2, 1, 3).reshape(B, Sq, Hq * Dh) @ wo

    def _B_ref(x, w1, b1, w2, b2):
        h = torch.einsum('bsd,df->bsf', x, w1) + b1
        h = _gelu(h)
        return torch.einsum('bsf,fd->bsd', h, w2) + b2
    x = A_x
    mod = (_silu(c) @ w_mod).reshape(c.shape[0], 6, 1, -1)
    sa, ca, ga, sb, cb, gb = (mod[:, 0].clone(), mod[:, 1].clone(), mod[:, 2].clone(), mod[:, 3].clone(), mod[:, 4].clone(), mod[:, 5].clone())
    h = _layernorm(x, axis=-1) * (1 + ca) + sa
    x = x + ga * _A_ref(h, A_k_cache, A_v_cache, A_wq, A_wo)
    h = _layernorm(x, axis=-1) * (1 + cb) + sb
    x = x + gb * _B_ref(h, B_w1, B_b1, B_w2, B_b2)
    return x
```

```cpp
#include <hip/hip_runtime.h>
#include <hip/hip_bf16.h>
#include <stdint.h>

using bf16 = __hip_bfloat16;
using u32 = unsigned;
using u64 = unsigned long long;
using u32x2 = __attribute__((ext_vector_type(2))) unsigned;
using u32x4 = __attribute__((ext_vector_type(4))) unsigned;
using f32x4 = __attribute__((ext_vector_type(4))) float;
using bf16x2 = __attribute__((ext_vector_type(2))) __bf16;
typedef __attribute__((ext_vector_type(2))) float f32x2;
typedef short s16x8 __attribute__((ext_vector_type(8)));
typedef short s16x4 __attribute__((ext_vector_type(4)));
typedef __attribute__((address_space(3))) s16x4* lds_s4p;
typedef __attribute__((ext_vector_type(8))) __bf16 bf16x8v;

constexpr int NB = 4, DM = 640, NH = 32, DH = 128, SK = 8192, WIN = 2048, FF = 1728, QD = 4096, MODN = 3840;
constexpr int NT = 512, NWG = 256;

constexpr u32 WS_EPOCH = 0;
constexpr u32 G_MOD = 256;
constexpr u32 G_Q = G_MOD + 16 * 960 * 16;
constexpr u32 G_MF = G_Q + 16 * 1024 * 16;
constexpr u32 G_ML = G_MF + 16 * 160 * 16;
constexpr u32 G_ATT = G_ML + 256 * 64;
constexpr u32 G_WO = G_ATT + 256 * 32 * 16;
constexpr u32 WO_REP = 32 * 160 * 16;
constexpr u32 G_OUT = G_WO + 4 * WO_REP;
constexpr u32 WS_END = G_OUT + 160 * 54 * 4 * 16;
constexpr u32 OOB = 0xF0000000u;

#define WSTAMP(k) do {} while (0)
#define STAMPT(i, tt) do {} while (0)
#define STAMP_DECL
#define STAMP_FLUSH do {} while (0)
#define BAR() asm volatile("s_waitcnt lgkmcnt(0)\n\ts_barrier" ::: "memory")
#define VM0() asm volatile("s_waitcnt vmcnt(0)" ::: "memory")

__device__ __forceinline__ float bfr(float x) { return __bfloat162float(__float2bfloat16(x)); }
__device__ __forceinline__ float bf2f(bf16 x) { return __bfloat162float(x); }
__device__ __forceinline__ float lo16(unsigned u) { return __builtin_bit_cast(float, u << 16); }
__device__ __forceinline__ float hi16(unsigned u) { return __builtin_bit_cast(float, u & 0xffff0000u); }
__device__ __forceinline__ u32 f2u(float f) { return __builtin_bit_cast(u32, f); }
__device__ __forceinline__ float u2f(u32 f) { return __builtin_bit_cast(float, f); }
__device__ __forceinline__ u32 cvt2(float a, float b) {
  const f32x2 v = {a, b};
  return __builtin_bit_cast(u32, __builtin_convertvector(v, bf16x2));
}
__device__ __forceinline__ float frcp(float x) { return __builtin_amdgcn_rcpf(x); }
__device__ __forceinline__ float fsqrt(float x) { return __builtin_amdgcn_sqrtf(x); }
__device__ __forceinline__ float fexp(float x) { return __builtin_amdgcn_exp2f(x * 1.4426950408889634f); }
__device__ __forceinline__ float dot2(unsigned k, unsigned q, float acc) {
  return __builtin_amdgcn_fdot2_f32_bf16(__builtin_bit_cast(bf16x2, k), __builtin_bit_cast(bf16x2, q), acc, false);
}
__device__ __forceinline__ float addlo(unsigned w, float acc) { u32 k = 0x00003f80u; asm("" : "+s"(k)); return dot2(w, k, acc); }
__device__ __forceinline__ float addhi(unsigned w, float acc) { return dot2(w, 0x3f800000u, acc); }
template <int CTRL> __device__ __forceinline__ float dpp(float x) {
  return __builtin_bit_cast(float, __builtin_amdgcn_mov_dpp(__builtin_bit_cast(int, x), CTRL, 0xf, 0xf, true));
}
template <int CTRL> __device__ __forceinline__ u32 dppu(u32 x) { return (u32)__builtin_amdgcn_mov_dpp((int)x, CTRL, 0xf, 0xf, true); }
constexpr int XOR1 = 0xB1, XOR2 = 0x4E, XOR7 = 0x141, XOR8 = 0x128, ROR4 = 0x124, ROR2 = 0x122, ROR1 = 0x121, SHL4 = 0x104;
__device__ __forceinline__ float swap32_add(float a, float b) {
  auto s = __builtin_amdgcn_permlane32_swap(f2u(a), f2u(b), false, false);
  return u2f(s[0]) + u2f(s[1]);
}
__device__ __forceinline__ float swap16_add(float a, float b) {
  auto s = __builtin_amdgcn_permlane16_swap(f2u(a), f2u(b), false, false);
  return u2f(s[0]) + u2f(s[1]);
}
__device__ __forceinline__ float cross_rows_sum(float x) { x = swap16_add(x, x); return swap32_add(x, x); }
__device__ __forceinline__ float cross_rows_max(float x) {
  auto s = __builtin_amdgcn_permlane16_swap(f2u(x), f2u(x), false, false);
  x = fmaxf(u2f(s[0]), u2f(s[1]));
  auto s2 = __builtin_amdgcn_permlane32_swap(f2u(x), f2u(x), false, false);
  return fmaxf(u2f(s2[0]), u2f(s2[1]));
}
__device__ __forceinline__ float wave_sum(float x) {
  x += dpp<XOR8>(x); x += dpp<ROR4>(x); x += dpp<ROR2>(x); x += dpp<ROR1>(x);
  return cross_rows_sum(x);
}
__device__ __forceinline__ float wave_max(float x) {
  x = fmaxf(x, dpp<XOR8>(x)); x = fmaxf(x, dpp<ROR4>(x)); x = fmaxf(x, dpp<ROR2>(x)); x = fmaxf(x, dpp<ROR1>(x));
  return cross_rows_max(x);
}

__device__ __forceinline__ void st_gran(__amdgpu_buffer_rsrc_t rs, u32 off, u32 a, u32 b, u32 tg) {
  u32x4 v = {a, tg, b, tg};
  __builtin_amdgcn_raw_buffer_store_b128(v, rs, off, 0, 16);
}
__device__ __forceinline__ u32x4 ld_gran(__amdgpu_buffer_rsrc_t rs, u32 off) {
  return __builtin_amdgcn_raw_buffer_load_b128(rs, off, 0, 16);
}
constexpr int NREP = 4;
constexpr unsigned SPIN_CAP = 1u << 18;
template <int N>
__device__ __forceinline__ void gather(__amdgpu_buffer_rsrc_t rs, const u32 (&offs)[N], u32x4 (&g)[N], u32 tg) {
  for (unsigned spins = 0; spins < SPIN_CAP; ++spins) {
    bool ok = true;
#pragma unroll
    for (int i = 0; i < N; ++i) g[i] = ld_gran(rs, offs[i]);
#pragma unroll
    for (int i = 0; i < N; ++i) ok &= (offs[i] == OOB) | ((g[i][1] == tg) & (g[i][3] == tg));
    if (__all(ok)) break;
    __builtin_amdgcn_s_sleep(1);
  }
}
template <int N>
__device__ __forceinline__ void gather_m(__amdgpu_buffer_rsrc_t rs, u32 (&offs)[N], u32 (&va)[N], u32 (&vb)[N], u32 tg) {
#pragma unroll
  for (int i = 0; i < N; ++i) { va[i] = 0; vb[i] = 0; }
  for (unsigned spins = 0; spins < SPIN_CAP; ++spins) {
    u32x4 g[N];
#pragma unroll
    for (int i = 0; i < N; ++i) g[i] = ld_gran(rs, offs[i]);
    bool ok = true;
#pragma unroll
    for (int i = 0; i < N; ++i) {
      const bool hit = (offs[i] != OOB) & (g[i][1] == tg) & (g[i][3] == tg);
      va[i] = hit ? g[i][0] : va[i]; vb[i] = hit ? g[i][2] : vb[i];
      offs[i] = hit ? OOB : offs[i];
      ok &= (offs[i] == OOB);
    }
    if (__all(ok)) break;
    __builtin_amdgcn_s_sleep(1);
  }
}

__device__ __forceinline__ void st_g22(__amdgpu_buffer_rsrc_t rs, u32 off, f32x4 v, u32 tg) {
  const u32 a0 = f2u(v[0]) + 0x200u, a1 = f2u(v[1]) + 0x200u, a2 = f2u(v[2]) + 0x200u, a3 = f2u(v[3]) + 0x200u;
  const u32x4 w = {(a0 >> 10) | ((a1 << 12) & 0xffc00000u), (a1 >> 20) | (tg << 12), (a2 >> 10) | ((a3 << 12) & 0xffc00000u), (a3 >> 20) | (tg << 12)};
  __builtin_amdgcn_raw_buffer_store_b128(w, rs, off, 0, 16);
}
__device__ __forceinline__ f32x4 un_g22(u32x4 w) {
  return f32x4{u2f(w[0] << 10), u2f(__builtin_amdgcn_alignbit(w[1], w[0], 22) << 10), u2f(w[2] << 10), u2f(__builtin_amdgcn_alignbit(w[3], w[2], 22) << 10)};
}
template <int N, int SLEEP>
__device__ __forceinline__ void gather22(__amdgpu_buffer_rsrc_t rs, u32 (&offs)[N], u32x4 (&v)[N], u32 tg) {
#pragma unroll
  for (int i = 0; i < N; ++i) v[i] = u32x4{0u, 0u, 0u, 0u};
  for (unsigned spins = 0; spins < SPIN_CAP; ++spins) {
    u32x4 g[N];
#pragma unroll
    for (int i = 0; i < N; ++i) g[i] = ld_gran(rs, offs[i]);
    bool ok = true;
#pragma unroll
    for (int i = 0; i < N; ++i) {
      const bool hit = (offs[i] != OOB) & (((g[i][1] ^ (tg << 12)) | (g[i][3] ^ (tg << 12))) < 4096u);
#pragma unroll
      for (int k = 0; k < 4; ++k) v[i][k] = hit ? g[i][k] : v[i][k];
      offs[i] = hit ? OOB : offs[i];
      ok &= (offs[i] == OOB);
    }
    if (__all(ok)) break;
    __builtin_amdgcn_s_sleep(SLEEP);
  }
}

__device__ __forceinline__ u32 next_tag(u32 ep) { const u32 t = ep + 1u; return (t & 0xfffffu) ? t : t + 1u; }

__device__ __forceinline__ void lds_arrive(u32 addr, int lane) {
  asm volatile("s_waitcnt lgkmcnt(0)" ::: "memory");
  if (lane == 0) asm volatile("ds_add_u32 %0, %1" :: "v"(addr), "v"(1u) : "memory");
}
__device__ __forceinline__ u32 lds_ticket(u32 addr, int lane) {
  u32 r = 0;
  asm volatile("s_waitcnt lgkmcnt(0)" ::: "memory");
  if (lane == 0) asm volatile("ds_add_rtn_u32 %0, %1, %2\n\ts_waitcnt lgkmcnt(0)" : "=v"(r) : "v"(addr), "v"(1u) : "memory");
  return __builtin_amdgcn_readfirstlane(r);
}
template <int SLEEP>
__device__ __forceinline__ void lds_wait(u32 addr, u32 target) {
  for (unsigned spins = 0; spins < (1u << 22); ++spins) {
    u32 v;
    asm volatile("ds_read_b32 %0, %1\n\ts_waitcnt lgkmcnt(0)" : "=v"(v) : "v"(addr) : "memory");
    if (v >= target) break;
    __builtin_amdgcn_s_sleep(SLEEP);
  }
}
__device__ __forceinline__ void dma16(const void* gsrc, u32 lds_dst) {
  u32 keep;
  asm volatile("s_mov_b32 %0, m0\n\ts_mov_b32 m0, %2\n\ts_nop 0\n\tglobal_load_lds_dwordx4 %1, off\n\ts_mov_b32 m0, %0"
               : "=&s"(keep) : "v"(gsrc), "s"(lds_dst) : "memory");
}
__device__ __forceinline__ void dma16nt(const void* gsrc, u32 lds_dst) {
  u32 keep;
  asm volatile("s_mov_b32 %0, m0\n\ts_mov_b32 m0, %2\n\ts_nop 0\n\tglobal_load_lds_dwordx4 %1, off nt\n\ts_mov_b32 m0, %0"
               : "=&s"(keep) : "v"(gsrc), "s"(lds_dst) : "memory");
}
__device__ __forceinline__ u32x4 ldasm(const void* p) {
  u32x4 v;
  asm volatile("global_load_dwordx4 %0, %1, off" : "=v"(v) : "v"(p) : "memory");
  return v;
}
__device__ __forceinline__ u32 ldasm_sc1(const void* p) {
  u32 v;
  asm volatile("global_load_dword %0, %1, off sc1" : "=v"(v) : "v"(p) : "memory");
  return v;
}
__device__ __forceinline__ u32x4 ldw(const bf16* p) { return *reinterpret_cast<const u32x4*>(p); }
template <int D2>
__device__ __forceinline__ s16x8 wfrag(u32 addr) {
  const s16x4 lo = __builtin_amdgcn_ds_read_tr16_b64_v4i16((lds_s4p)(uintptr_t)addr);
  const s16x4 hi = __builtin_amdgcn_ds_read_tr16_b64_v4i16((lds_s4p)(uintptr_t)(addr + D2));
  return s16x8{lo[0], lo[1], lo[2], lo[3], hi[0], hi[1], hi[2], hi[3]};
}
#define MFMA(w_, x_, c_) __builtin_amdgcn_mfma_f32_16x16x32_bf16(__builtin_bit_cast(bf16x8v, w_), __builtin_bit_cast(bf16x8v, x_), c_, 0, 0, 0)

template <int N> struct IC { static constexpr int value = N; };
struct Args {
  const bf16 *x, *kc, *vc, *wq, *wo, *w1, *b1, *w2, *b2, *c, *wmod;
  bf16* out;
  unsigned char* ws;
};

constexpr int L_W = 0;
constexpr int L_WM2 = 40960;
constexpr int L_XA = 81920;
constexpr int XS = 1312;
constexpr int L_RED = L_XA + 16384;
constexpr int L_MOD = L_RED + 8192;
constexpr int L_XR = L_MOD + 5120;
constexpr int L_PT = L_XR + 5120;
constexpr int L_ACC = L_PT + 2048;
constexpr int L_QS = L_ACC + 4096;
constexpr int L_FI = L_QS + 256;
constexpr int L_ST = L_FI + 256;
constexpr int L_CNT = L_ST + 256;
constexpr int L_END = L_CNT + 64;
static_assert(L_END <= 160 * 1024, "LDS");
constexpr u32 C_Q = L_CNT, C_SC = L_CNT + 4, C_ISS = L_CNT + 8, C_P1 = L_CNT + 20, C_P2 = L_CNT + 24, C_GO = L_CNT + 28, C_W = L_CNT + 12, C_FIN = L_CNT + 32;

constexpr int NL = 32;
constexpr int NV0 = 8;
constexpr int PVLAG = 14;
constexpr int NPRE = 18;
static_assert(NV0 + 6 <= PVLAG && (PVLAG & 1) == 0 && (NV0 & 1) == 0, "row groups");

__global__ void __launch_bounds__(NT, 2) mega_kernel(Args a) {
  __shared__ __attribute__((aligned(1024))) unsigned char lds[L_END];
  float* red = reinterpret_cast<float*>(lds + L_RED);
  u32* xraw = reinterpret_cast<u32*>(lds + L_XR);
  u32* pT = reinterpret_cast<u32*>(lds + L_PT);
  float* accs = reinterpret_cast<float*>(lds + L_ACC);
  u32* qs = reinterpret_cast<u32*>(lds + L_QS);
  float* st = reinterpret_cast<float*>(lds + L_ST);
  u32* cnt = reinterpret_cast<u32*>(lds + L_CNT);

  const int t = threadIdx.x, lane = t & 63;
  const int wave = __builtin_amdgcn_readfirstlane(t >> 6);
  const int u = blockIdx.x;
  const int bh = u >> 1, half = u & 1, b_att = bh >> 5, h_att = bh & 31;
  const int r = lane >> 4, c = lane & 15;
  const __amdgpu_buffer_rsrc_t rs = __builtin_amdgcn_make_buffer_rsrc(a.ws, 0, WS_END, 0x00020000);
  STAMP_DECL
  STAMPT(0, 0);
  if (t < 16) cnt[t] = 0;
  u32 tg;
  const u32 tr128 = (8 * r + (c >> 2)) * 128 + ((((c >> 3) & 1) | ((r & 1) << 1)) * 32) + (c & 3) * 8;
  const int s128e = (((((lane & 7) >> 1) ^ ((lane >> 4) & 1)) << 1) | (lane & 1)) * 8;
  const int g = u >> 1, ksp = g & 3, cbp = g >> 2;
  if (wave < 4) {
    __builtin_amdgcn_s_setprio(3);
    if (half) {
      const bf16* pm = a.wmod + (size_t)(ksp * 160 + (lane >> 3)) * MODN + cbp * 64;
      if (wave >= 2) {
#pragma unroll
        for (int j = 0; j < 10; ++j) dma16nt(pm + (size_t)(8 * (2 * j + wave - 2)) * MODN + (s128e ^ ((wave & 1) * 32)), L_W + (2 * j + wave - 2) * 1024);
        BAR();
        VM0();
        STAMPT(5, 128);
      } else {
        u32 ep = ldasm_sc1(a.ws + WS_EPOCH);
        const int sc = t < 80 ? t : 0;
        u32x4 cv = ldasm(a.c + (sc / 20) * DM + ksp * 160 + (sc % 20) * 8);
        pm += (g < 112) ? 32 * 64 : 0;
#pragma unroll
        for (int j = 0; j < 10; ++j) dma16nt(pm + (size_t)(8 * (2 * j + wave)) * MODN + (s128e ^ (wave * 32)), L_W + 20480 + (2 * j + wave) * 1024);
        BAR();
        asm volatile("s_waitcnt vmcnt(10)" : "+v"(ep), "+v"(cv) :: "memory");
        u32x4 sv;
#pragma unroll
        for (int k = 0; k < 4; ++k) {
          const float c0 = lo16(cv[k]), c1 = hi16(cv[k]);
          sv[k] = cvt2(c0 * frcp(1.f + fexp(-c0)), c1 * frcp(1.f + fexp(-c1)));
        }
        if (t < 80) *reinterpret_cast<u32x4*>(lds + L_XA + t * 16) = sv;
        if (t == 0) cnt[4] = next_tag(ep);
        STAMPT(2, 0);
      }
      lds_arrive(C_P1, lane);
      lds_wait<1>(C_P1, 4);
      STAMPT(20, 0);
      tg = __builtin_amdgcn_readfirstlane(cnt[4]);
      u32x4 av[5];
#pragma unroll
      for (int s = 0; s < 5; ++s) av[s] = *reinterpret_cast<const u32x4*>(lds + L_XA + (c & 3) * 320 + s * 64 + r * 16);
      {
        f32x4 acc = {0.f, 0.f, 0.f, 0.f};
#pragma unroll
        for (int s = 0; s < 5; ++s) acc = MFMA(wfrag<512>(L_W + (tr128 ^ (wave * 32)) + s * 4096), av[s], acc);
        if (c < 4) st_g22(rs, G_MOD + ((ksp * 4 + c) * 960 + cbp * 16 + wave * 4 + r) * 16, acc, tg);
      }
      STAMPT(7, 0);
      if (wave < 2) { VM0(); lds_arrive(C_P2, lane); }
      lds_wait<1>(C_P2, 2);
      {
        f32x4 acc = {0.f, 0.f, 0.f, 0.f};
#pragma unroll
        for (int s = 0; s < 5; ++s) acc = MFMA(wfrag<512>(L_W + 20480 + (tr128 ^ (wave * 32)) + s * 4096), av[s], acc);
        if (c < 4 && g < 112) st_g22(rs, G_MOD + ((ksp * 4 + c) * 960 + (cbp + 32) * 16 + wave * 4 + r) * 16, acc, tg);
      }
      STAMPT(9, 0);
    } else {
      u32 ep;
      u32x4 xv[2][2];
      if (wave < 2) {
        ep = ldasm_sc1(a.ws + WS_EPOCH);
#pragma unroll
        for (int i = 0; i < 2; ++i) {
          xv[i][0] = ldasm(a.x + (wave + 2 * i) * DM + lane * 8);
          xv[i][1] = ldasm(a.x + (wave + 2 * i) * DM + 512 + c * 8);
        }
      }
      {
        const bf16* pq = a.wq + (size_t)(ksp * 160 + r) * QD + cbp * 128 + (c & 1) * 8;
        const int sq0 = (((c >> 1) ^ r) * 16) ^ ((wave >> 1) * 64);
#pragma unroll
        for (int j = 0; j < 10; ++j) dma16nt(pq + (size_t)(4 * (4 * j + wave)) * QD + sq0, L_W + (4 * j + wave) * 1024);
      }
      BAR();
      if (wave < 2) {
        asm volatile("s_waitcnt vmcnt(10)" : "+v"(ep), "+v"(xv[0][0]), "+v"(xv[0][1]), "+v"(xv[1][0]), "+v"(xv[1][1]) :: "memory");
#pragma unroll
        for (int i = 0; i < 2; ++i) {
          const int b = wave + 2 * i;
          float xf[16];
#pragma unroll
          for (int k = 0; k < 4; ++k) { xf[2 * k] = lo16(xv[i][0][k]); xf[2 * k + 1] = hi16(xv[i][0][k]); xf[8 + 2 * k] = lo16(xv[i][1][k]); xf[8 + 2 * k + 1] = hi16(xv[i][1][k]); }
          float s = 0.f, s2 = 0.f;
#pragma unroll
          for (int k = 0; k < 8; ++k) { s += xf[k]; s2 += xf[8 + k]; }
          if (lane < 16) s += s2;
          const float mean = wave_sum(s) * (1.f / DM);
          float v = 0.f, v2 = 0.f;
#pragma unroll
          for (int k = 0; k < 8; ++k) { const float d = xf[k] - mean; v += d * d; const float d2 = xf[8 + k] - mean; v2 += d2 * d2; }
          if (lane < 16) v += v2;
          v = wave_sum(v);
          if (lane == 0) { st[24 + 2 * b] = bfr(mean); st[24 + 2 * b + 1] = frcp(bfr(fsqrt(bfr(bfr(v * (1.f / DM)) + 1e-5f)))); }
          *reinterpret_cast<u32x4*>(xraw + b * 320 + lane * 4) = xv[i][0];
          if (lane < 16) *reinterpret_cast<u32x4*>(xraw + b * 320 + 256 + lane * 4) = xv[i][1];
        }
        if (t == 0) cnt[4] = next_tag(ep);
        lds_arrive(C_P1, lane);
      }
      lds_wait<1>(C_P1, 2);
      STAMPT(20, 0);
      tg = __builtin_amdgcn_readfirstlane(cnt[4]);
      if (wave < 3) {
        const int b = t / 40, j = t % 40;
        u32 offs[8];
        u32x4 gm[8];
#pragma unroll
        for (int sel = 0; sel < 2; ++sel)
#pragma unroll
          for (int ks = 0; ks < 4; ++ks) offs[sel * 4 + ks] = t < 160 ? G_MOD + ((ks * 4 + b) * 960 + sel * 160 + ksp * 40 + j) * 16 : OOB;
        __builtin_amdgcn_s_sleep(16);
        gather22<8, 4>(rs, offs, gm, tg);
        STAMPT(8, 0);
        const f32x4 sa = (un_g22(gm[0]) + un_g22(gm[1])) + (un_g22(gm[2]) + un_g22(gm[3]));
        const f32x4 ca = (un_g22(gm[4]) + un_g22(gm[5])) + (un_g22(gm[6]) + un_g22(gm[7]));
        const int bb = b & 3;
        const float mean = st[24 + 2 * bb], sd = st[24 + 2 * bb + 1];
        const u32x2 xw = *reinterpret_cast<const u32x2*>(xraw + bb * 320 + ksp * 80 + 2 * j);
        float hv[4];
#pragma unroll
        for (int k = 0; k < 4; ++k) {
          const float xf = (k & 1) ? hi16(xw[k >> 1]) : lo16(xw[k >> 1]);
          const float n = bfr(bfr(xf - mean) * sd);
          hv[k] = bfr(n * bfr(1.f + bfr(ca[k]))) + bfr(sa[k]);
        }
        if (t < 160) *reinterpret_cast<u32x2*>(lds + L_XA + t * 8) = u32x2{cvt2(hv[0], hv[1]), cvt2(hv[2], hv[3])};
      }
      VM0();
      lds_arrive(C_P2, lane);
      lds_wait<1>(C_P2, 4);
      u32x4 av[5];
#pragma unroll
      for (int s = 0; s < 5; ++s) av[s] = *reinterpret_cast<const u32x4*>(lds + L_XA + (c & 3) * 320 + s * 64 + r * 16);
#pragma unroll
      for (int e = 0; e < 2; ++e) {
        const int nt = 2 * wave + e;
        const u32 tr256 = (8 * r + (c >> 2)) * 256 + ((((c >> 2) | ((r & 1) << 2)) ^ nt) * 32) + (c & 3) * 8;
        f32x4 acc = {0.f, 0.f, 0.f, 0.f};
#pragma unroll
        for (int s = 0; s < 5; ++s) acc = MFMA(wfrag<1024>(L_W + tr256 + s * 8192), av[s], acc);
        if (c < 4) st_g22(rs, G_Q + ((ksp * 4 + c) * 1024 + cbp * 32 + nt * 4 + r) * 16, acc, tg);
      }
      lds_arrive(C_GO, lane);
      STAMPT(9, 0);
    }
    if (wave == 0) {
      u32 offs[2];
      u32x4 gq[2];
#pragma unroll
      for (int e = 0; e < 2; ++e) offs[e] = G_Q + (((2 * (lane >> 5) + e) * 4 + b_att) * 1024 + h_att * 32 + (lane & 31)) * 16;
      gather22<2, 4>(rs, offs, gq, tg);
      STAMPT(10, 0);
      f32x4 q4 = un_g22(gq[0]) + un_g22(gq[1]);
#pragma unroll
      for (int k = 0; k < 4; ++k) q4[k] = swap32_add(q4[k], q4[k]);
      if (lane < 32) *reinterpret_cast<u32x2*>(qs + 2 * lane) = u32x2{cvt2(q4[0], q4[1]), cvt2(q4[2], q4[3])};
      lds_arrive(C_Q, lane);
      if (half == 0 && g >= 80 && g < 120) {
        const int fb = g - 80;
        u32 of2[4];
        u32x4 gm[4];
#pragma unroll
        for (int ks = 0; ks < 4; ++ks) of2[ks] = G_MOD + ((ks * 4 + r) * 960 + 320 + fb * 16 + c) * 16;
        gather22<4, 4>(rs, of2, gm, tg);
        const f32x4 mv = (un_g22(gm[0]) + un_g22(gm[1])) + (un_g22(gm[2]) + un_g22(gm[3]));
        st_gran(rs, G_MF + (((fb / 10) * 4 + r) * 160 + (fb % 10) * 16 + c) * 16, cvt2(mv[0], mv[1]), cvt2(mv[2], mv[3]), tg);
      }
    }
    __builtin_amdgcn_s_setprio(0);
  }
  {
    u32x4 kv[NL], vv[NV0];
    const __amdgpu_buffer_rsrc_t rk = __builtin_amdgcn_make_buffer_rsrc((void*)a.kc, 0, 1u << 28, 0x00020000);
    const __amdgpu_buffer_rsrc_t rv = __builtin_amdgcn_make_buffer_rsrc((void*)a.vc, 0, 1u << 28, 0x00020000);
    const u32 kvoff = (u32)(bh * SK + (SK - WIN)) * 256u + (u32)(half * 8 + wave) * (NL * 1024u) + lane * 16;
#define LDKV(rsrc, i) __builtin_amdgcn_raw_buffer_load_b128(rsrc, kvoff + ((i) & 3) * 1024, ((((i) >> 2) + (u >> 3)) & 7) * 4096, 2)
#define VTH 80
    const bool vdef = bh >= VTH;
#define LDV(i) __builtin_amdgcn_raw_buffer_load_b128(rv, kvoff + ((i) & 3) * 1024, ((((i) >> 2) + (u >> 3)) & 7) * 4096, decltype(AUXV)::value)
    if (wave >= 4) {
      __builtin_amdgcn_s_setprio(2);
      BAR();
    }
#pragma unroll
    for (int i = 0; i < NPRE; ++i) kv[i] = LDKV(rk, i);
    if (half == 0) lds_wait<2>(C_GO, 4);
#pragma unroll
    for (int i = NPRE; i < NL; ++i) kv[i] = LDKV(rk, i);
    auto body = [&](auto AUXV) __attribute__((always_inline)) {
#pragma unroll
    for (int i = 0; i < NV0; ++i) vv[i] = LDV(i);
    STAMPT(1, 0); STAMPT(25, 448);
    lds_wait<2>(C_Q, 1);
    u32x4 qpk;
    asm volatile("ds_read_b128 %0, %2\n\tds_read_b32 %1, %3\n\ts_waitcnt lgkmcnt(0)" : "=&v"(qpk), "=&v"(tg) : "v"((u32)(L_QS + 16 * c)), "v"((u32)(L_CNT + 16)) : "memory");
    tg = __builtin_amdgcn_readfirstlane(tg);
    float x[16];
#pragma unroll
    for (int i = 0; i < 16; ++i) {
      float d0 = 0.f, d1 = 0.f;
#pragma unroll
      for (int k = 0; k < 4; ++k) { d0 = dot2(kv[i][k], qpk[k], d0); d1 = dot2(kv[i + 16][k], qpk[k], d1); }
      const float t1 = d0 + dpp<XOR8>(d0), t2 = d1 + dpp<XOR8>(d1);
      x[i] = (c & 8) ? t2 : t1;
    }
#pragma unroll
    for (int i = 0; i < 16; ++i) asm volatile("" : "+v"(x[i]));
    asm volatile("" ::: "memory");
#pragma unroll
    for (int i = NV0; i < NV0 + 6; ++i) kv[i - NV0] = LDV(i);
    asm volatile("" ::: "memory");
#define TR_STEP(x, HALF, CTRL, BIT) _Pragma("unroll") for (int hh = 0; hh < (HALF); ++hh) { \
    const float lo_ = x[hh], hi_ = x[hh + (HALF)]; const float t1_ = lo_ + dpp<CTRL>(lo_), t2_ = hi_ + dpp<CTRL>(hi_); \
    x[hh] = (c & (BIT)) ? t2_ : t1_; }
    TR_STEP(x, 8, XOR7, 4) TR_STEP(x, 4, XOR2, 2) TR_STEP(x, 2, XOR1, 1)
#undef TR_STEP
    const float s0 = bfr(bfr(x[0]) * 0.08838834764831845f), s1 = bfr(bfr(x[1]) * 0.08838834764831845f);
    const float m = wave_max(fmaxf(s0, s1));
    const float p0 = __expf(s0 - m), p1 = __expf(s1 - m);
    const float l = wave_sum(p0 + p1);
    if (lane == 0) { st[wave] = m; st[8 + wave] = l; }
    pT[(wave * 4 + r) * 16 + c] = cvt2(p0, p1);
    __builtin_amdgcn_s_setprio(0);
    STAMPT(4, 0); STAMPT(27, 448); WSTAMP(1);
    if (lds_ticket(C_SC, lane) == 7) {
      const float mw = st[lane & 7], lw = st[8 + (lane & 7)];
      float M = fmaxf(mw, dpp<XOR1>(mw)); M = fmaxf(M, dpp<XOR2>(M)); M = fmaxf(M, dpp<XOR7>(M));
      const float e = __expf(mw - M);
      float L = lw * e;
      L += dpp<XOR1>(L); L += dpp<XOR2>(L); L += dpp<XOR7>(L);
      if (lane == 0) st_gran(rs, G_ML + u * 64, f2u(M), f2u(L), tg);
      if (lane < 8) st[16 + lane] = e * frcp(L);
    }
    float acc[8];
#pragma unroll
    for (int k = 0; k < 8; ++k) acc[k] = 0.f;
    u32 pw[16];
#pragma unroll
    for (int i4 = 0; i4 < 4; ++i4) {
      const u32x4 pv = *reinterpret_cast<const u32x4*>(pT + (wave * 4 + r) * 16 + i4 * 4);
#pragma unroll
      for (int j = 0; j < 4; ++j) pw[i4 * 4 + j] = pv[j];
    }
#define VREG(i) ((i) < NV0 ? vv[(i) < NV0 ? (i) : 0] : kv[(i) < NV0 ? 0 : (i) - NV0])
#define PV2(i) _Pragma("unroll") for (int k = 0; k < 4; ++k) { \
      acc[2 * k] = dot2(__builtin_amdgcn_perm(VREG((i) + 1)[k], VREG(i)[k], 0x05040100u), pw[(i) / 2], acc[2 * k]); \
      acc[2 * k + 1] = dot2(__builtin_amdgcn_perm(VREG((i) + 1)[k], VREG(i)[k], 0x07060302u), pw[(i) / 2], acc[2 * k + 1]); }
#pragma unroll
    for (int i = NV0 + 6; i < NL; i += 2) {
      kv[i - NV0] = LDV(i);
      kv[i + 1 - NV0] = LDV(i + 1);
      asm volatile("" ::: "memory");
      if (i == NL - 2 && lane == 0) asm volatile("ds_add_u32 %0, %1" :: "v"(C_ISS), "v"(1u) : "memory");
      if (i >= PVLAG) {
        PV2(i >= PVLAG ? i - PVLAG : 0)
#pragma unroll
        for (int k = 0; k < 8; ++k) asm volatile("" : "+v"(acc[k]));
      }
    }
#pragma unroll
    for (int i = NL - PVLAG; i < NL; i += 2) { PV2(i) }
#undef PV2
#undef VREG
#undef LDKV
#undef LDV
#pragma unroll
    for (int i = 0; i < 4; ++i) acc[i] = swap32_add(acc[i], acc[i + 4]);
#pragma unroll
    for (int i = 0; i < 2; ++i) acc[i] = swap16_add(acc[i], acc[i + 2]);
    *reinterpret_cast<float2*>(accs + wave * 128 + c * 8 + 2 * r) = float2{acc[0], acc[1]};
    };
    if (vdef) body(IC<0>{}); else body(IC<2>{});
    STAMPT(6, 0); STAMPT(28, 448); WSTAMP(2);
  }
  const int role = half ? (g < 74 ? 0 : 1) : (g >= 122 ? 0 : g < 80 ? 2 : 3);
  const int ia = half ? g : g - 48;
  const int ksA = ia & 7, cbA = ia >> 3;
#define HEADA(hh) ((ksA & 3) + (ksA >> 2) * 16 + (hh) * 4)
  const int jB = g - 74;
  if (lds_ticket(C_FIN, lane) == 7) {
    if (lane < 32) {
      f32x4 o = {0.f, 0.f, 0.f, 0.f};
#pragma unroll
      for (int w = 0; w < 8; ++w) o += st[16 + w] * *reinterpret_cast<const f32x4*>(accs + w * 128 + 4 * lane);
      st_gran(rs, G_ATT + (u * 32 + lane) * 16, cvt2(o[0], o[1]), cvt2(o[2], o[3]), tg);
    }
  }
  if (role == 1 && wave < 4) {
    const u32x4 x0 = ldw(a.x + t * 8), x1 = ldw(a.x + 2048 + lane * 8);
    u32 offs[8];
    u32x4 gm[8];
#pragma unroll
    for (int i = 0; i < 8; ++i) { const int it = t + 256 * i; offs[i] = it < 1920 ? G_MF + it * 16 : OOB; }
    gather<8>(rs, offs, gm, tg);
#pragma unroll
    for (int i = 0; i < 8; ++i) {
      const int it = t + 256 * i, sel = it / 640, rem = it % 640, b = rem / 160, gi = rem % 160;
      u32 w0 = gm[i][0], w1 = gm[i][2];
      if (sel == 2) { w0 = cvt2(1.f + lo16(w0), 1.f + hi16(w0)); w1 = cvt2(1.f + lo16(w1), 1.f + hi16(w1)); }
      const int dst = sel == 0 ? L_MOD + b * 1280 + gi * 8 : L_XA + ((sel == 2 ? 4 : 8) + b) * XS + gi * 8;
      if (it < 1920) *reinterpret_cast<u32x2*>(lds + dst) = u32x2{w0, w1};
    }
    *reinterpret_cast<u32x4*>(lds + L_XR + t * 16) = x0;
    if (wave == 0) *reinterpret_cast<u32x4*>(lds + L_XR + 4096 + lane * 16) = x1;
  }
  if (role < 2 && wave < 4) {
    lds_wait<2>(C_ISS, 8);
    if (role == 0) {
      const bf16* wp = a.wo + (size_t)(HEADA(wave) * 128 + (lane >> 3)) * DM + cbA * 64;
#pragma unroll
      for (int i = 0; i < 16; ++i) dma16(wp + (size_t)(8 * i) * DM + (s128e ^ ((i & 1) * 32)), L_W + wave * 16384 + i * 1024);
    } else {
      const bf16* wp = a.w1 + (size_t)(wave * 160 + (lane >> 2)) * FF + jB * 32 + ((((((lane >> 1) & 1) ^ (lane >> 5)) << 1) | (lane & 1)) * 8);
#pragma unroll
      for (int i = 0; i < 10; ++i) dma16(wp + (size_t)(16 * i) * FF, L_W + (wave * 10 + i) * 1024);
      const bf16* wp2 = a.w2 + (size_t)(jB * 32 + wave * 8 + (lane >> 3)) * DM + (s128e ^ ((wave & 1) * 32));
#pragma unroll
      for (int k = 0; k < 10; ++k) dma16(wp2 + k * 64, L_WM2 + (k * 4 + wave) * 1024);
    }
  }
  if (role == 0) {
    const int head = HEADA(wave >> 1), kh = wave & 1;
    s16x8 wf[2][4];
    if (wave < 4) { VM0(); lds_arrive(C_W, lane); }
    lds_wait<1>(C_W, 4);
#pragma unroll
    for (int s = 0; s < 2; ++s)
#pragma unroll
      for (int nt = 0; nt < 4; ++nt) wf[s][nt] = wfrag<512>(L_W + wave * 8192 + (tr128 ^ (nt * 32)) + s * 4096);
    STAMPT(21, 0);
    u32 offs[3], va[3], vb[3];
#pragma unroll
    for (int e = 0; e < 2; ++e) offs[e] = G_ATT + ((((r * 32 + head) * 2 + e) * 32) + 16 * kh + c) * 16;
    offs[2] = lane < 8 ? G_ML + (u32)((((c & 3) * 32 + head) * 2 + (c >> 2)) * 64) : OOB;
    gather_m<3>(rs, offs, va, vb, tg);
    STAMPT(12, 0); STAMPT(22, 448);
    unsigned char* xa = lds + L_XA + wave * 1280;
#pragma unroll
    for (int e = 0; e < 2; ++e) *reinterpret_cast<u32x2*>(xa + (4 * e + r) * 160 + c * 8) = u32x2{va[e], vb[e]};
    float wsc;
    {
      const float M = u2f(va[2]), L = u2f(vb[2]), Mo = dpp<XOR7>(dpp<0x1B>(M)), Lo = dpp<XOR7>(dpp<0x1B>(L));
      const float Mx = fmaxf(M, Mo), le = L * __expf(M - Mx);
      wsc = le * frcp(fmaf(Lo, __expf(Mo - Mx), le));
      wsc = u2f(__builtin_amdgcn_ds_bpermute((c & 7) * 4, f2u(wsc)));
    }
    f32x4 acc[4];
#pragma unroll
    for (int nt = 0; nt < 4; ++nt) acc[nt] = f32x4{0.f, 0.f, 0.f, 0.f};
#pragma unroll
    for (int s = 0; s < 2; ++s) {
      const u32x4 av = *reinterpret_cast<const u32x4*>(xa + (c & 7) * 160 + s * 64 + r * 16);
#pragma unroll
      for (int nt = 0; nt < 4; ++nt) acc[nt] = MFMA(wf[s][nt], av, acc[nt]);
    }
#pragma unroll
    for (int nt = 0; nt < 4; ++nt) {
#pragma unroll
      for (int k = 0; k < 4; ++k) { const float v = acc[nt][k] * wsc; acc[nt][k] = v + dpp<SHL4>(v); }
      if (c < 4) *reinterpret_cast<f32x4*>(red + ((wave * 4 + c) * 16 + nt * 4 + r) * 4) = acc[nt];
    }
    BAR();
    if (wave < NREP) {
      f32x4 s = {0.f, 0.f, 0.f, 0.f};
#pragma unroll
      for (int w = 0; w < 8; ++w) s += *reinterpret_cast<const f32x4*>(red + (w * 64 + lane) * 4);
      st_gran(rs, G_WO + wave * WO_REP + ((ksA * 4 + r) * 160 + cbA * 16 + c) * 16, cvt2(s[0], s[1]), cvt2(s[2], s[3]), tg);
    }
    STAMPT(13, 0);

  } else if (role == 1) {
    const int b1 = wave >> 1, ch = wave & 1;
    const float b1v = bf2f(a.b1[jB * 32 + (t & 31)]);
    VM0();
    BAR();
    const int kq = wave >> 1, nt = wave & 1;
    s16x8 wf[5], wg[5];
    const u32 tr64 = (8 * r + (c >> 2)) * 64 + ((nt ^ (r & 1)) * 32) + (c & 3) * 8;
#pragma unroll
    for (int s = 0; s < 5; ++s) wf[s] = wfrag<256>(L_W + kq * 10240 + tr64 + s * 2048);
#pragma unroll
    for (int k = 0; k < 5; ++k) { const int n2 = wave * 5 + k; wg[k] = wfrag<512>(L_WM2 + (n2 >> 2) * 4096 + (tr128 ^ ((n2 & 3) * 32))); }
    {
      f32x4 acc = {0.f, 0.f, 0.f, 0.f};
#pragma unroll
      for (int s = 0; s < 5; ++s) {
        const u32x4 av = *reinterpret_cast<const u32x4*>(lds + L_XA + (4 + (c & 7)) * XS + kq * 320 + s * 64 + r * 16);
        acc = MFMA(wf[s], av, acc);
      }
      if (c < 8) *reinterpret_cast<f32x4*>(red + ((wave * 8 + c) * 4 + r) * 4) = acc;
    }
    float xf[5], gaf[5], c1f[5];
#pragma unroll
    for (int i = 0; i < 5; ++i) {
      const int col = (ch * 80 + 16 * i + c) * 4 + r;
      xf[i] = u2f((u32)*reinterpret_cast<const unsigned short*>(lds + L_XR + b1 * 1280 + col * 2) << 16);
      gaf[i] = u2f((u32)*reinterpret_cast<const unsigned short*>(lds + L_MOD + b1 * 1280 + col * 2) << 16);
      c1f[i] = u2f((u32)*reinterpret_cast<const unsigned short*>(lds + L_XA + (4 + b1) * XS + col * 2) << 16);
    }
    BAR();
    const int fb = (t >> 5) & 3, fc = t & 31;
    float Tc = 0.f, Ts = 0.f;
#pragma unroll
    for (int k = 0; k < 4; ++k) {
      Tc += red[((2 * k + (fc >> 4)) * 8 + fb) * 16 + (fc & 15)];
      Ts += red[((2 * k + (fc >> 4)) * 8 + 4 + fb) * 16 + (fc & 15)];
    }
#pragma unroll
    for (int i = 0; i < 5; ++i) asm volatile("" : "+v"(xf[i]), "+v"(gaf[i]), "+v"(c1f[i]));
    asm volatile("" : "+v"(Tc), "+v"(Ts));
    u32x4 sel;
#pragma unroll
    for (int d = 0; d < 4; ++d) sel[d] = ((c & 3) == 0 && (d & 1) == (c >> 3)) ? ((c & 4) ? 0x3f800000u : 0x00003f80u) : 0u;
    u32 a_raw = L_W + (((wave >> 1) * 4 + r) * 160 + c) * 16 + (wave & 1) * 8;
    u32 a_sum = L_W + ((r * 4 + b1) * 160 + ch * 80 + c) * 16;
    u32 a_u = L_XA + b1 * XS + ((ch * 80 + c) * 4 + r) * 2;
    u32 a_av = L_XA + (c & 3) * XS + kq * 320 + r * 16;
    u32 a_red = L_RED + ((wave * 4 + c) * 4 + r) * 16;
    u32 a_fin = L_RED + (((fc >> 4) * 4 + fb) * 16 + (fc & 15)) * 4;
    u32 a_out = c < 4 ? G_OUT + (((wave * 20 + r) * 54 + jB) * 4 + c) * 16 : OOB;
    u32 a_fi = L_FI + (c & 3) * 64 + r * 16;
    asm volatile("" : "+v"(sel), "+v"(a_raw), "+v"(a_sum), "+v"(a_u), "+v"(a_av), "+v"(a_red), "+v"(a_fin), "+v"(a_out), "+v"(a_fi));
    STAMPT(18, 0);
    u32x4 pa[10];
    {
      const u32 go = G_WO + (jB & (NREP - 1)) * WO_REP + ((wave * 4 + r) * 160 + c) * 16;
      u32 pend = 0x3ffu;
      for (unsigned spins = 0; spins < SPIN_CAP; ++spins) {
        const u32x4 sg = ld_gran(rs, lane == 0 ? go + (jB % 10) * 256 : OOB);
        if (__builtin_amdgcn_readfirstlane(sg[1]) == tg) break;
        __builtin_amdgcn_s_sleep(2);
      }
      for (unsigned spins = 0; spins < SPIN_CAP; ++spins) {
        asm volatile("" ::: "memory");
#pragma unroll
        for (int j = 0; j < 10; ++j) if (pend & (1u << j)) pa[j] = ld_gran(rs, go + j * 256);
#pragma unroll
        for (int j = 0; j < 10; ++j) if (pend & (1u << j)) {
          asm volatile("ds_write2_b32 %0, %1, %2 offset0:%3 offset1:%4" :: "v"(a_raw + (j >> 2) * 1024), "v"(pa[j][0]), "v"(pa[j][2]), "i"((j & 3) * 64), "i"((j & 3) * 64 + 1) : "memory");
          if (__all((pa[j][1] == tg) & (pa[j][3] == tg))) pend &= ~(1u << j);
        }
        if (!pend) break;
      }
    }
    STAMPT(14, 0); WSTAMP(3);
    BAR();
    STAMPT(29, 0);
    float ps = 0.f, pq = 0.f;
    {
      u32x4 v[5];
#pragma unroll
      for (int i = 0; i < 5; ++i) v[i] = *reinterpret_cast<const u32x4*>(lds + a_sum + i * 256);
      asm volatile("" : "+v"(v[0]), "+v"(v[1]), "+v"(v[2]), "+v"(v[3]), "+v"(v[4]));
      f32x4 sm[5];
#pragma unroll
      for (int i = 0; i < 5; ++i) { const f32x4 z = {0.f, 0.f, 0.f, 0.f}; sm[i] = MFMA(sel, v[i], z); }
#pragma unroll
      for (int i = 0; i < 5; ++i) {
        const float x1 = bfr(fmaf(gaf[i], sm[i][0], xf[i]));
        ps += x1; pq = fmaf(x1, x1, pq);
        const u32 uw = cvt2(x1 * c1f[i], 0.f);
        *reinterpret_cast<unsigned short*>(lds + a_u + i * 128) = (unsigned short)uw;
      }
    }
    BAR();
    STAMPT(19, 0);
    {
      u32x4 av[5];
#pragma unroll
      for (int s = 0; s < 5; ++s) av[s] = *reinterpret_cast<const u32x4*>(lds + a_av + s * 64);
      const float t1 = ps + dpp<XOR8>(ps), t2 = pq + dpp<XOR8>(pq);
      float z = (c & 8) ? t2 : t1;
      asm volatile("" : "+v"(av[0]), "+v"(av[1]), "+v"(av[2]), "+v"(av[3]), "+v"(av[4]), "+v"(z));
      f32x4 acc = {0.f, 0.f, 0.f, 0.f};
#pragma unroll
      for (int s = 0; s < 5; ++s) acc = MFMA(wf[s], av[s], acc);
      z += dpp<XOR7>(z); z += dpp<XOR2>(z); z += dpp<XOR1>(z);
      z = cross_rows_sum(z);
      if ((lane & 55) == 0) st[24 + 2 * wave + (lane >> 3)] = z;
      if (c < 4) *reinterpret_cast<f32x4*>(lds + a_red) = acc;
    }
    BAR();
    STAMPT(24, 0);
    if (t < 128) {
      const f32x4 sv = *reinterpret_cast<const f32x4*>(st + 24 + 4 * fb);
      float s = 0.f;
#pragma unroll
      for (int k = 0; k < 4; ++k) s += *reinterpret_cast<const float*>(lds + a_fin + k * 512);
      const float mean = (sv[0] + sv[2]) * (1.f / DM);
      const float var = fmaxf((sv[1] + sv[3]) * (1.f / DM) - mean * mean, 0.f);
      const float rstd = __builtin_amdgcn_rsqf(var + 1e-5f);
      const float hh = bfr(bfr(fmaf(rstd, fmaf(-mean, Tc, s), Ts)) + b1v);
      const float e2 = __builtin_amdgcn_exp2f(hh * fmaf(hh * hh, 0.1029432397f, 2.302208198f));
      const float f = fmaf(-hh, frcp(e2 + 1.f), hh);
      *reinterpret_cast<unsigned short*>(lds + L_FI + t * 2) = (unsigned short)cvt2(f, 0.f);
    }
    BAR();
    {
      const u32x4 av = *reinterpret_cast<const u32x4*>(lds + a_fi);
      f32x4 acc[5];
#pragma unroll
      for (int k = 0; k < 5; ++k) { const f32x4 z = {0.f, 0.f, 0.f, 0.f}; acc[k] = MFMA(wg[k], av, z); }
#pragma unroll
      for (int k = 0; k < 5; ++k) st_gran(rs, a_out + k * (4 * 54 * 64), cvt2(acc[k][0], acc[k][1]), cvt2(acc[k][2], acc[k][3]), tg);
    }
    STAMPT(15, 0);

  } else if (role == 2 && wave < 2) {
    const int cg = g * 2 + wave, b = lane & 3, jj = lane >> 2, col = cg * 4 + r;
    const float xw = bf2f(a.x[b * DM + col]), b2v = bf2f(a.b2[col]);
    float x1v, gbv;
    {
      u32 offs[1], va[1], vb[1];
      offs[0] = jj < 8 ? G_WO + (g & (NREP - 1)) * WO_REP + ((jj * 4 + b) * 160 + cg) * 16 : jj < 10 ? G_MF + (((jj - 8) * 12 + b) * 160 + cg) * 16 : OOB;
      gather_m<1>(rs, offs, va, vb, tg);
      const u32 gaw0 = __builtin_amdgcn_ds_bpermute((32 + b) * 4, va[0]), gaw1 = __builtin_amdgcn_ds_bpermute((32 + b) * 4, vb[0]);
      const u32 gbw0 = __builtin_amdgcn_ds_bpermute((36 + b) * 4, va[0]), gbw1 = __builtin_amdgcn_ds_bpermute((36 + b) * 4, vb[0]);
      if (jj >= 8) { va[0] = 0; vb[0] = 0; }
      float s = swap16_add(swap32_add(lo16(va[0]), lo16(vb[0])), swap32_add(hi16(va[0]), hi16(vb[0])));
      s += dpp<ROR4>(s); s += dpp<XOR8>(s);
      const u32 gaw = (r & 2) ? gaw1 : gaw0, gbw = (r & 2) ? gbw1 : gbw0;
      const float ga = (r & 1) ? hi16(gaw) : lo16(gaw);
      gbv = (r & 1) ? hi16(gbw) : lo16(gbw);
      x1v = bfr(xw + bfr(ga * bfr(s)));
    }
    STAMPT(23, 0);
    u32 offs[4], va[4], vb[4];
#pragma unroll
    for (int i = 0; i < 4; ++i) offs[i] = (jj + 16 * i < 54) ? G_OUT + ((cg * 54 + jj + 16 * i) * 4 + b) * 16 : OOB;
    gather_m<4>(rs, offs, va, vb, tg);
    STAMPT(16, 0);
    float s0 = lo16(va[0]), s1 = hi16(va[0]), s2 = lo16(vb[0]), s3 = hi16(vb[0]);
#pragma unroll
    for (int i = 1; i < 4; ++i) { s0 = addlo(va[i], s0); s1 = addhi(va[i], s1); s2 = addlo(vb[i], s2); s3 = addhi(vb[i], s3); }
    float s = swap16_add(swap32_add(s0, s2), swap32_add(s1, s3));
    s += dpp<ROR4>(s); s += dpp<XOR8>(s);
    const float y = bfr(bfr(s) + b2v);
    if ((lane & 12) == 0) a.out[b * DM + col] = __float2bfloat16(x1v + bfr(gbv * y));
    STAMPT(17, 0);

    if (u == 0 && t == 0)
      __hip_atomic_store(reinterpret_cast<u32*>(a.ws + WS_EPOCH), tg, __ATOMIC_RELAXED, __HIP_MEMORY_SCOPE_AGENT);
  }
  STAMP_FLUSH;
}

extern "C" void kernel_launch(void* const* d_in, const int* in_sizes, int n_in, void* d_out, int out_size, void* d_ws,
                              size_t ws_size, hipStream_t stream) {
  Args a;
  a.x = (const bf16*)d_in[0];
  a.kc = (const bf16*)d_in[1];
  a.vc = (const bf16*)d_in[2];
  a.wq = (const bf16*)d_in[3];
  a.wo = (const bf16*)d_in[4];
  a.w1 = (const bf16*)d_in[6];
  a.b1 = (const bf16*)d_in[7];
  a.w2 = (const bf16*)d_in[8];
  a.b2 = (const bf16*)d_in[9];
  a.c = (const bf16*)d_in[10];
  a.wmod = (const bf16*)d_in[11];
  a.out = (bf16*)d_out;
  a.ws = (unsigned char*)d_ws;
  mega_kernel<<<NWG, NT, 0, stream>>>(a);
}
```

```cpp
#define SPINLOOP _Pragma("nounroll")
#define P_LAG 14
#define P_NPRE 32
#define P_NVE 6
#define P_NVA 6
#define QODD 4
#define QEVEN 4
#define PT 0
#define PTAUX 16
#define P_GATE 8
#include <hip/hip_runtime.h>
#include <hip/hip_bf16.h>
#include <stdint.h>

using bf16 = __hip_bfloat16;
using u32 = unsigned;
using u64 = unsigned long long;
using u32x2 = __attribute__((ext_vector_type(2))) unsigned;
using u32x4 = __attribute__((ext_vector_type(4))) unsigned;
using f32x4 = __attribute__((ext_vector_type(4))) float;
using bf16x2 = __attribute__((ext_vector_type(2))) __bf16;
typedef __attribute__((ext_vector_type(2))) float f32x2;
typedef short s16x8 __attribute__((ext_vector_type(8)));
typedef short s16x4 __attribute__((ext_vector_type(4)));
typedef __attribute__((address_space(3))) s16x4* lds_s4p;
typedef __attribute__((ext_vector_type(8))) __bf16 bf16x8v;

constexpr int NB = 4, DM = 640, NH = 32, DH = 128, SK = 8192, WIN = 2048, FF = 1728, QD = 4096, MODN = 3840;
constexpr int NT = 512, NWG = 256;

constexpr u32 WS_EPOCH = 0;
constexpr u32 G_MOD = 256;
constexpr u32 G_Q = G_MOD + 16 * 960 * 16;
constexpr u32 G_MF = G_Q + 16 * 1024 * 16;
constexpr u32 G_ML = G_MF + 16 * 160 * 16;
constexpr u32 G_ATT = G_ML + 256 * 64;
constexpr u32 G_WO = G_ATT + 256 * 32 * 16;
constexpr u32 WO_REP = 32 * 160 * 16;
constexpr u32 G_OUT = G_WO + 4 * WO_REP;
constexpr u32 WS_END = G_OUT + 160 * 54 * 4 * 16;
constexpr u32 OOB = 0xF0000000u;

#define WSTAMP(k) do {} while (0)
#define STAMPW(i) do {} while (0)
#define STAMPT(i, tt) do {} while (0)
#define STAMP_DECL
#define STAMP_FLUSH do {} while (0)
#define BAR() asm volatile("s_waitcnt lgkmcnt(0)\n\ts_barrier" ::: "memory")
#define VM0() asm volatile("s_waitcnt vmcnt(0)" ::: "memory")

__device__ __forceinline__ float bfr(float x) { return __bfloat162float(__float2bfloat16(x)); }
__device__ __forceinline__ float bf2f(bf16 x) { return __bfloat162float(x); }
__device__ __forceinline__ float lo16(unsigned u) { return __builtin_bit_cast(float, u << 16); }
__device__ __forceinline__ float hi16(unsigned u) { return __builtin_bit_cast(float, u & 0xffff0000u); }
__device__ __forceinline__ u32 f2u(float f) { return __builtin_bit_cast(u32, f); }
__device__ __forceinline__ float u2f(u32 f) { return __builtin_bit_cast(float, f); }
__device__ __forceinline__ u32 cvt2(float a, float b) {
  const f32x2 v = {a, b};
  return __builtin_bit_cast(u32, __builtin_convertvector(v, bf16x2));
}
__device__ __forceinline__ float frcp(float x) { return __builtin_amdgcn_rcpf(x); }
__device__ __forceinline__ float fsqrt(float x) { return __builtin_amdgcn_sqrtf(x); }
__device__ __forceinline__ float fexp(float x) { return __builtin_amdgcn_exp2f(x * 1.4426950408889634f); }
__device__ __forceinline__ float dot2(unsigned k, unsigned q, float acc) {
  return __builtin_amdgcn_fdot2_f32_bf16(__builtin_bit_cast(bf16x2, k), __builtin_bit_cast(bf16x2, q), acc, false);
}
__device__ __forceinline__ float addlo(unsigned w, float acc) { u32 k = 0x00003f80u; asm("" : "+s"(k)); return dot2(w, k, acc); }
__device__ __forceinline__ float addhi(unsigned w, float acc) { return dot2(w, 0x3f800000u, acc); }
template <int CTRL> __device__ __forceinline__ float dpp(float x) {
  return __builtin_bit_cast(float, __builtin_amdgcn_mov_dpp(__builtin_bit_cast(int, x), CTRL, 0xf, 0xf, true));
}
template <int CTRL> __device__ __forceinline__ u32 dppu(u32 x) { return (u32)__builtin_amdgcn_mov_dpp((int)x, CTRL, 0xf, 0xf, true); }
constexpr int XOR1 = 0xB1, XOR2 = 0x4E, XOR7 = 0x141, XOR8 = 0x128, ROR4 = 0x124, ROR2 = 0x122, ROR1 = 0x121, SHL4 = 0x104;
__device__ __forceinline__ float swap32_add(float a, float b) {
  auto s = __builtin_amdgcn_permlane32_swap(f2u(a), f2u(b), false, false);
  return u2f(s[0]) + u2f(s[1]);
}
__device__ __forceinline__ float swap16_add(float a, float b) {
  auto s = __builtin_amdgcn_permlane16_swap(f2u(a), f2u(b), false, false);
  return u2f(s[0]) + u2f(s[1]);
}
__device__ __forceinline__ float cross_rows_sum(float x) { x = swap16_add(x, x); return swap32_add(x, x); }
__device__ __forceinline__ float cross_rows_max(float x) {
  auto s = __builtin_amdgcn_permlane16_swap(f2u(x), f2u(x), false, false);
  x = fmaxf(u2f(s[0]), u2f(s[1]));
  auto s2 = __builtin_amdgcn_permlane32_swap(f2u(x), f2u(x), false, false);
  return fmaxf(u2f(s2[0]), u2f(s2[1]));
}
__device__ __forceinline__ float wave_sum(float x) {
  x += dpp<XOR8>(x); x += dpp<ROR4>(x); x += dpp<ROR2>(x); x += dpp<ROR1>(x);
  return cross_rows_sum(x);
}
__device__ __forceinline__ float wave_max(float x) {
  x = fmaxf(x, dpp<XOR8>(x)); x = fmaxf(x, dpp<ROR4>(x)); x = fmaxf(x, dpp<ROR2>(x)); x = fmaxf(x, dpp<ROR1>(x));
  return cross_rows_max(x);
}

__device__ __forceinline__ void st_gran(__amdgpu_buffer_rsrc_t rs, u32 off, u32 a, u32 b, u32 tg) {
  u32x4 v = {a, tg, b, tg};
  __builtin_amdgcn_raw_buffer_store_b128(v, rs, off, 0, 16);
}
__device__ __forceinline__ u32x4 ld_gran(__amdgpu_buffer_rsrc_t rs, u32 off) {
  return __builtin_amdgcn_raw_buffer_load_b128(rs, off, 0, 16);
}
constexpr int NREP = 4;
constexpr unsigned SPIN_CAP = 1u << 18;
template <int N>
__device__ __forceinline__ void gather(__amdgpu_buffer_rsrc_t rs, const u32 (&offs)[N], u32x4 (&g)[N], u32 tg) {
  SPINLOOP for (unsigned spins = 0; spins < SPIN_CAP; ++spins) {
    bool ok = true;
#pragma unroll
    for (int i = 0; i < N; ++i) g[i] = ld_gran(rs, offs[i]);
#pragma unroll
    for (int i = 0; i < N; ++i) ok &= (offs[i] == OOB) | ((g[i][1] == tg) & (g[i][3] == tg));
    if (__all(ok)) break;
    __builtin_amdgcn_s_sleep(1);
  }
}
template <int N>
__device__ __forceinline__ void gather_m(__amdgpu_buffer_rsrc_t rs, u32 (&offs)[N], u32 (&va)[N], u32 (&vb)[N], u32 tg) {
#pragma unroll
  for (int i = 0; i < N; ++i) { va[i] = 0; vb[i] = 0; }
  SPINLOOP for (unsigned spins = 0; spins < SPIN_CAP; ++spins) {
    u32x4 g[N];
#pragma unroll
    for (int i = 0; i < N; ++i) g[i] = ld_gran(rs, offs[i]);
    bool ok = true;
#pragma unroll
    for (int i = 0; i < N; ++i) {
      const bool hit = (offs[i] != OOB) & (g[i][1] == tg) & (g[i][3] == tg);
      va[i] = hit ? g[i][0] : va[i]; vb[i] = hit ? g[i][2] : vb[i];
      offs[i] = hit ? OOB : offs[i];
      ok &= (offs[i] == OOB);
    }
    if (__all(ok)) break;
    __builtin_amdgcn_s_sleep(1);
  }
}

__device__ __forceinline__ void st_g22(__amdgpu_buffer_rsrc_t rs, u32 off, f32x4 v, u32 tg) {
  const u32 a0 = f2u(v[0]) + 0x200u, a1 = f2u(v[1]) + 0x200u, a2 = f2u(v[2]) + 0x200u, a3 = f2u(v[3]) + 0x200u;
  const u32x4 w = {(a0 >> 10) | ((a1 << 12) & 0xffc00000u), (a1 >> 20) | (tg << 12), (a2 >> 10) | ((a3 << 12) & 0xffc00000u), (a3 >> 20) | (tg << 12)};
  __builtin_amdgcn_raw_buffer_store_b128(w, rs, off, 0, 16);
}
__device__ __forceinline__ f32x4 un_g22(u32x4 w) {
  return f32x4{u2f(w[0] << 10), u2f(__builtin_amdgcn_alignbit(w[1], w[0], 22) << 10), u2f(w[2] << 10), u2f(__builtin_amdgcn_alignbit(w[3], w[2], 22) << 10)};
}
template <int N, int SLEEP>
__device__ __forceinline__ void gather22(__amdgpu_buffer_rsrc_t rs, u32 (&offs)[N], u32x4 (&v)[N], u32 tg) {
#pragma unroll
  for (int i = 0; i < N; ++i) v[i] = u32x4{0u, 0u, 0u, 0u};
  SPINLOOP for (unsigned spins = 0; spins < SPIN_CAP; ++spins) {
    u32x4 g[N];
#pragma unroll
    for (int i = 0; i < N; ++i) g[i] = ld_gran(rs, offs[i]);
    bool ok = true;
#pragma unroll
    for (int i = 0; i < N; ++i) {
      const bool hit = (offs[i] != OOB) & (((g[i][1] ^ (tg << 12)) | (g[i][3] ^ (tg << 12))) < 4096u);
#pragma unroll
      for (int k = 0; k < 4; ++k) v[i][k] = hit ? g[i][k] : v[i][k];
      offs[i] = hit ? OOB : offs[i];
      ok &= (offs[i] == OOB);
    }
    if (__all(ok)) break;
    __builtin_amdgcn_s_sleep(SLEEP);
  }
}

__device__ __forceinline__ u32 next_tag(u32 ep) { const u32 t = ep + 1u; return (t & 0xfffffu) ? t : t + 1u; }

__device__ __forceinline__ void lds_arrive(u32 addr, int lane) {
  asm volatile("s_waitcnt lgkmcnt(0)" ::: "memory");
  if (lane == 0) asm volatile("ds_add_u32 %0, %1" :: "v"(addr), "v"(1u) : "memory");
}
__device__ __forceinline__ u32 lds_ticket(u32 addr, int lane) {
  u32 r = 0;
  asm volatile("s_waitcnt lgkmcnt(0)" ::: "memory");
  if (lane == 0) asm volatile("ds_add_rtn_u32 %0, %1, %2\n\ts_waitcnt lgkmcnt(0)" : "=v"(r) : "v"(addr), "v"(1u) : "memory");
  return __builtin_amdgcn_readfirstlane(r);
}
template <int SLEEP>
__device__ __forceinline__ void lds_wait(u32 addr, u32 target) {
  SPINLOOP for (unsigned spins = 0; spins < (1u << 22); ++spins) {
    u32 v;
    asm volatile("ds_read_b32 %0, %1\n\ts_waitcnt lgkmcnt(0)" : "=v"(v) : "v"(addr) : "memory");
    if (v >= target) break;
    __builtin_amdgcn_s_sleep(SLEEP);
  }
}
__device__ __forceinline__ void dma16(const void* gsrc, u32 lds_dst) {
  u32 keep;
  asm volatile("s_mov_b32 %0, m0\n\ts_mov_b32 m0, %2\n\ts_nop 0\n\tglobal_load_lds_dwordx4 %1, off\n\ts_mov_b32 m0, %0"
               : "=&s"(keep) : "v"(gsrc), "s"(lds_dst) : "memory");
}
__device__ __forceinline__ void dma16nt(const void* gsrc, u32 lds_dst) {
  u32 keep;
  asm volatile("s_mov_b32 %0, m0\n\ts_mov_b32 m0, %2\n\ts_nop 0\n\tglobal_load_lds_dwordx4 %1, off nt\n\ts_mov_b32 m0, %0"
               : "=&s"(keep) : "v"(gsrc), "s"(lds_dst) : "memory");
}
__device__ __forceinline__ u32x4 ldasm(const void* p) {
  u32x4 v;
  asm volatile("global_load_dwordx4 %0, %1, off" : "=v"(v) : "v"(p) : "memory");
  return v;
}
__device__ __forceinline__ u32 ldasm_sc1(const void* p) {
  u32 v;
  asm volatile("global_load_dword %0, %1, off sc1" : "=v"(v) : "v"(p) : "memory");
  return v;
}
__device__ __forceinline__ u32x4 ldw(const bf16* p) { return *reinterpret_cast<const u32x4*>(p); }
template <int D2>
__device__ __forceinline__ s16x8 wfrag(u32 addr) {
  const s16x4 lo = __builtin_amdgcn_ds_read_tr16_b64_v4i16((lds_s4p)(uintptr_t)addr);
  const s16x4 hi = __builtin_amdgcn_ds_read_tr16_b64_v4i16((lds_s4p)(uintptr_t)(addr + D2));
  return s16x8{lo[0], lo[1], lo[2], lo[3], hi[0], hi[1], hi[2], hi[3]};
}
#define MFMA(w_, x_, c_) __builtin_amdgcn_mfma_f32_16x16x32_bf16(__builtin_bit_cast(bf16x8v, w_), __builtin_bit_cast(bf16x8v, x_), c_, 0, 0, 0)

struct Desc { const bf16* p; u32 bytes, flags; };
struct Args {
  Desc wq, x, wmod, c;
  unsigned char* ws;
  const bf16 *kc, *vc, *wo, *w1, *b1, *w2, *b2;
  bf16* out;
};

constexpr int L_W = 0;
constexpr int L_WM2 = 40960;
constexpr int L_XA = 81920;
constexpr int XS = 1312;
constexpr int L_RED = L_XA + 16384;
constexpr int L_MOD = L_RED + 8192;
constexpr int L_XR = L_MOD + 5120;
constexpr int L_PT = L_XR + 5120;
constexpr int L_ACC = L_PT + 2048;
constexpr int L_QS = L_ACC + 4096;
constexpr int L_FI = L_QS + 256;
constexpr int L_ST = L_FI + 256;
constexpr int L_CNT = L_ST + 256;
constexpr int L_END = L_CNT + 64;
static_assert(L_END <= 160 * 1024, "LDS");
constexpr u32 C_Q = L_CNT, C_SC = L_CNT + 4, C_ISS = L_CNT + 8, C_P1 = L_CNT + 20, C_P2 = L_CNT + 24, C_GO = L_CNT + 28, C_W = L_CNT + 12, C_FIN = L_CNT + 32,
              C_KI = L_CNT + 36, C_PP = L_CNT + 40;

constexpr int NL = 32;
constexpr int PVLAG = P_LAG;
constexpr int NPRE = P_NPRE;
constexpr int NVE = P_NVE;
static_assert(PVLAG <= 16 && (PVLAG & 1) == 0 && NVE <= PVLAG && (NVE & 1) == 0, "row groups");

__global__ void __launch_bounds__(NT, 2) mega_kernel(Args a) {
  __shared__ __attribute__((aligned(1024))) unsigned char lds[L_END];
  float* red = reinterpret_cast<float*>(lds + L_RED);
  u32* xraw = reinterpret_cast<u32*>(lds + L_XR);
  u32* pT = reinterpret_cast<u32*>(lds + L_PT);
  float* accs = reinterpret_cast<float*>(lds + L_ACC);
  u32* qs = reinterpret_cast<u32*>(lds + L_QS);
  float* st = reinterpret_cast<float*>(lds + L_ST);
  u32* cnt = reinterpret_cast<u32*>(lds + L_CNT);

  u32x4 rw, rx;
  unsigned char* wsq;
  asm volatile("s_load_dwordx4 %0, %3, %4\n\ts_load_dwordx4 %1, %3, %4 offset:16\n\ts_load_dwordx2 %2, %3, 64"
               : "=&s"(rw), "=&s"(rx), "=&s"(wsq) : "s"(__builtin_amdgcn_kernarg_segment_ptr()), "s"((blockIdx.x & 1) * 32));
  __builtin_amdgcn_sched_barrier(0);
  const int t = threadIdx.x, lane = t & 63;
  const int wave = __builtin_amdgcn_readfirstlane(t >> 6);
  const int u = blockIdx.x;
  const int bh = u >> 1, half = u & 1, b_att = bh >> 5, h_att = bh & 31;
  const int r = lane >> 4, c = lane & 15;
  STAMP_DECL
  STAMPT(0, 0);
  u32 tg;
  const int g = u >> 1, ksp = g & 3, cbp = g >> 2;
  const int s128e = (((((lane & 7) >> 1) ^ ((lane >> 4) & 1)) << 1) | (lane & 1)) * 8;
  const u32 tr128 = (8 * r + (c >> 2)) * 128 + ((((c >> 3) & 1) | ((r & 1) << 1)) * 32) + (c & 3) * 8;
  if (wave < 4) {
    __builtin_amdgcn_s_setprio(3);
    const u32 rb = 8192 - 512 * half;
    const u32 hm = 0u - (u32)half;
    const u32 vo = (u32)(lane >> (4 - half)) * rb + (((lane & (15 >> half)) ^ ((lane >> 3) & (6 - 4 * half))) ^ (half ? (wave & 1) * 4 : (wave >> 1) * 8)) * 16;
    u32 so = (u32)(ksp * 160 + (half ? 8 * (wave & 1) : 4 * wave)) * rb + (cbp << (8 - half)) + ((half && wave < 2 && g < 112) ? 4096 : 0);
    const u32 step = 16 * rb, dstep = 4096 >> half, dst = L_W + wave * 1024 + (half ? (wave < 2 ? 20480 : -2048) : 0);
    const u32 oe = wave * 1280 + lane * 16, oo = (r * 1280 + ksp * 320 + (c + 16 * wave) * 16) | ((u32)(19 - c - 16 * wave) & OOB);
    const u32 oa = (oo & hm) | (oe & ~hm), ob = (wave * 1280 + 1024 + c * 16) | (hm & OOB), oc = oe | (hm & OOB), zero = 0;
    u32 ep, keep;
    u32x4 xv[4];
#define DMA1 "buffer_load_dwordx4 %7, %8, %1 offen nt lds\n\t"
#define DMAN "s_add_u32 m0, m0, %11\n\ts_add_u32 %1, %1, %10\n\t" DMA1
    asm volatile("s_mov_b32 %0, m0\n\ts_mov_b32 m0, %9\n\t"
                 "s_cmp_gt_u32 %18, 1\n\ts_waitcnt lgkmcnt(0)\n\ts_cbranch_scc1 1f\n\t"
                 "global_load_dword %2, %12, %13 sc1\n\t"
                 "buffer_load_dwordx4 %3, %14, %17, 0 offen\n\tbuffer_load_dwordx4 %4, %15, %17, 0 offen\n\t"
                 "buffer_load_dwordx4 %5, %16, %17, 0 offen offset:2560\n\tbuffer_load_dwordx4 %6, %15, %17, 0 offen offset:2560\n"
                 "1:\n\t" DMA1 DMAN DMAN DMAN DMAN DMAN DMAN DMAN DMAN DMAN "s_mov_b32 m0, %0\n\t"
                 "s_barrier\n\ts_waitcnt vmcnt(10)"
                 : "=&s"(keep), "+s"(so), "=&v"(ep), "=&v"(xv[0]), "=&v"(xv[1]), "=&v"(xv[2]), "=&v"(xv[3])
                 : "v"(vo), "s"(rw), "s"(dst), "s"(step), "s"(dstep), "v"(zero), "s"(wsq), "v"(oa), "v"(ob), "v"(oc), "s"(rx), "s"(wave) : "memory", "scc");
#undef DMAN
#undef DMA1
    asm volatile("" : "+s"(wsq));
    const __amdgpu_buffer_rsrc_t rs = __builtin_amdgcn_make_buffer_rsrc(wsq, 0, WS_END, 0x00020000);
    if (half) {
      if (wave >= 2) {
        VM0();
        STAMPT(5, 128);
      } else {
        u32x4 sv;
#pragma unroll
        for (int k = 0; k < 4; ++k) {
          const float c0 = lo16(xv[0][k]), c1 = hi16(xv[0][k]);
          sv[k] = cvt2(c0 * frcp(1.f + fexp(-c0)), c1 * frcp(1.f + fexp(-c1)));
        }
        if (c + 16 * wave < 20) *reinterpret_cast<u32x4*>(lds + L_XA + (r * 20 + c + 16 * wave) * 16) = sv;
        if (t == 0) cnt[4] = next_tag(ep);
        STAMPT(2, 0);
      }
      lds_arrive(C_P1, lane);
      lds_wait<1>(C_P1, 4);
      STAMPT(20, 0);
      tg = __builtin_amdgcn_readfirstlane(cnt[4]);
      u32x4 av[5];
#pragma unroll
      for (int s = 0; s < 5; ++s) av[s] = *reinterpret_cast<const u32x4*>(lds + L_XA + (c & 3) * 320 + s * 64 + r * 16);
      {
        f32x4 acc = {0.f, 0.f, 0.f, 0.f};
#pragma unroll
        for (int s = 0; s < 5; ++s) acc = MFMA(wfrag<512>(L_W + (tr128 ^ (wave * 32)) + s * 4096), av[s], acc);
        if (c < 4) st_g22(rs, G_MOD + ((ksp * 4 + c) * 960 + cbp * 16 + wave * 4 + r) * 16, acc, tg);
      }
      STAMPT(7, 0);
      if (wave < 2) { VM0(); lds_arrive(C_P2, lane); }
      lds_wait<1>(C_P2, 2);
      {
        f32x4 acc = {0.f, 0.f, 0.f, 0.f};
#pragma unroll
        for (int s = 0; s < 5; ++s) acc = MFMA(wfrag<512>(L_W + 20480 + (tr128 ^ (wave * 32)) + s * 4096), av[s], acc);
        if (c < 4 && g < 112) st_g22(rs, G_MOD + ((ksp * 4 + c) * 960 + (cbp + 32) * 16 + wave * 4 + r) * 16, acc, tg);
      }
      STAMPT(9, 0);
    } else {
      if (wave < 2) {
#pragma unroll
        for (int i = 0; i < 2; ++i) {
          const int b = wave + 2 * i;
          float xf[16];
#pragma unroll
          for (int k = 0; k < 4; ++k) { xf[2 * k] = lo16(xv[2 * i][k]); xf[2 * k + 1] = hi16(xv[2 * i][k]); xf[8 + 2 * k] = lo16(xv[2 * i + 1][k]); xf[8 + 2 * k + 1] = hi16(xv[2 * i + 1][k]); }
          float s = 0.f, s2 = 0.f;
#pragma unroll
          for (int k = 0; k < 8; ++k) { s += xf[k]; s2 += xf[8 + k]; }
          if (lane < 16) s += s2;
          const float mean = wave_sum(s) * (1.f / DM);
          float v = 0.f, v2 = 0.f;
#pragma unroll
          for (int k = 0; k < 8; ++k) { const float d = xf[k] - mean; v += d * d; const float d2 = xf[8 + k] - mean; v2 += d2 * d2; }
          if (lane < 16) v += v2;
          v = wave_sum(v);
          if (lane == 0) { st[24 + 2 * b] = bfr(mean); st[24 + 2 * b + 1] = frcp(bfr(fsqrt(bfr(bfr(v * (1.f / DM)) + 1e-5f)))); }
          *reinterpret_cast<u32x4*>(xraw + b * 320 + lane * 4) = xv[2 * i];
          if (lane < 16) *reinterpret_cast<u32x4*>(xraw + b * 320 + 256 + lane * 4) = xv[2 * i + 1];
        }
        if (t == 0) cnt[4] = next_tag(ep);
        lds_arrive(C_P1, lane);
      }
      lds_wait<1>(C_P1, 2);
      STAMPT(20, 0);
      tg = __builtin_amdgcn_readfirstlane(cnt[4]);
      if (wave < 3) {
        const int b = t / 40, j = t % 40;
        u32 offs[8];
        u32x4 gm[8];
#pragma unroll
        for (int sel = 0; sel < 2; ++sel)
#pragma unroll
          for (int ks = 0; ks < 4; ++ks) offs[sel * 4 + ks] = t < 160 ? G_MOD + ((ks * 4 + b) * 960 + sel * 160 + ksp * 40 + j) * 16 : OOB;
        __builtin_amdgcn_s_sleep(16);
        gather22<8, 4>(rs, offs, gm, tg);
        STAMPT(8, 0);
        const f32x4 sa = (un_g22(gm[0]) + un_g22(gm[1])) + (un_g22(gm[2]) + un_g22(gm[3]));
        const f32x4 ca = (un_g22(gm[4]) + un_g22(gm[5])) + (un_g22(gm[6]) + un_g22(gm[7]));
        const int bb = b & 3;
        const float mean = st[24 + 2 * bb], sd = st[24 + 2 * bb + 1];
        const u32x2 xw = *reinterpret_cast<const u32x2*>(xraw + bb * 320 + ksp * 80 + 2 * j);
        float hv[4];
#pragma unroll
        for (int k = 0; k < 4; ++k) {
          const float xf = (k & 1) ? hi16(xw[k >> 1]) : lo16(xw[k >> 1]);
          const float n = bfr(bfr(xf - mean) * sd);
          hv[k] = bfr(n * bfr(1.f + bfr(ca[k]))) + bfr(sa[k]);
        }
        if (t < 160) *reinterpret_cast<u32x2*>(lds + L_XA + t * 8) = u32x2{cvt2(hv[0], hv[1]), cvt2(hv[2], hv[3])};
      }
      VM0();
      lds_arrive(C_P2, lane);
      lds_wait<1>(C_P2, 4);
      u32x4 av[5];
#pragma unroll
      for (int s = 0; s < 5; ++s) av[s] = *reinterpret_cast<const u32x4*>(lds + L_XA + (c & 3) * 320 + s * 64 + r * 16);
#pragma unroll
      for (int e = 0; e < 2; ++e) {
        const int nt = 2 * wave + e;
        const u32 tr256 = (8 * r + (c >> 2)) * 256 + ((((c >> 2) | ((r & 1) << 2)) ^ nt) * 32) + (c & 3) * 8;
        f32x4 acc = {0.f, 0.f, 0.f, 0.f};
#pragma unroll
        for (int s = 0; s < 5; ++s) acc = MFMA(wfrag<1024>(L_W + tr256 + s * 8192), av[s], acc);
        if (c < 4) st_g22(rs, G_Q + ((ksp * 4 + c) * 1024 + cbp * 32 + nt * 4 + r) * 16, acc, tg);
      }
      lds_arrive(C_GO, lane);
      STAMPT(9, 0);
    }
  } else {
    __builtin_amdgcn_s_setprio(2);
    if (t < 256 + 16) cnt[lane] = 0;
  }
  u32 kvoff = (u32)(bh * SK + (SK - WIN)) * 256u + (u32)(half * 8 + wave) * (NL * 1024u) + lane * 16, ph[8];
#pragma unroll
  for (int i = 0; i < 8; ++i) ph[i] = ((i + (u >> 3)) & 7) * 4096;
  asm volatile("" : "+v"(kvoff));
  asm volatile("s_waitcnt lgkmcnt(0)" : "+s"(wsq), "+s"(ph[0]), "+s"(ph[1]), "+s"(ph[2]), "+s"(ph[3]), "+s"(ph[4]), "+s"(ph[5]), "+s"(ph[6]), "+s"(ph[7])
               : "s"(rw), "s"(rx));
  const __amdgpu_buffer_rsrc_t rs = __builtin_amdgcn_make_buffer_rsrc(wsq, 0, WS_END, 0x00020000);
  {
    u32x4 kv[NL], kh[16];
    const __amdgpu_buffer_rsrc_t rk = __builtin_amdgcn_make_buffer_rsrc((void*)a.kc, 0, 1u << 28, 0x00020000);
    const __amdgpu_buffer_rsrc_t rv = __builtin_amdgcn_make_buffer_rsrc((void*)a.vc, 0, 1u << 28, 0x00020000);
#define LDKV(rsrc, off, i) __builtin_amdgcn_raw_buffer_load_b128(rsrc, (off) + ((i) & 3) * 1024, ph[(i) >> 2], 2)
#define LDS_INC(addr) do { asm volatile("" ::: "memory"); if (lane == 0) asm volatile("ds_add_u32 %0, %1" :: "v"(addr), "v"(1u) : "memory"); } while (0)
#define TR_STEP(x, HALF, CTRL, BIT) _Pragma("unroll") for (int hh = 0; hh < (HALF); ++hh) { \
    const float lo_ = x[hh], hi_ = x[hh + (HALF)]; const float t1_ = lo_ + dpp<CTRL>(lo_), t2_ = hi_ + dpp<CTRL>(hi_); \
    x[hh] = (c & (BIT)) ? t2_ : t1_; }
#define SCORE16(x, src, o) float x[16]; \
    _Pragma("unroll") for (int i = 0; i < 16; ++i) { float d = 0.f; _Pragma("unroll") for (int k = 0; k < 4; ++k) d = dot2(src[(o) + i][k], qpk[k], d); x[i] = d; } \
    TR_STEP(x, 8, XOR8, 8) TR_STEP(x, 4, XOR7, 4) TR_STEP(x, 2, XOR2, 2)
#define SCALED(v) bfr(bfr(v) * 0.08838834764831845f)
#define PV2(i) _Pragma("unroll") for (int k = 0; k < 4; ++k) { \
      acc[2 * k] = dot2(__builtin_amdgcn_perm(kv[(i) + 1][k], kv[i][k], 0x05040100u), pw[(i) / 2], acc[2 * k]); \
      acc[2 * k + 1] = dot2(__builtin_amdgcn_perm(kv[(i) + 1][k], kv[i][k], 0x07060302u), pw[(i) / 2], acc[2 * k + 1]); }
#define GET_Q() lds_wait<2>(C_Q, 1); u32x4 qpk; \
    asm volatile("ds_read_b128 %0, %2\n\tds_read_b32 %1, %3\n\ts_waitcnt lgkmcnt(0)" : "=&v"(qpk), "=&v"(tg) : "v"((u32)(L_QS + 16 * c)), "v"((u32)(L_CNT + 16)) : "memory"); \
    tg = __builtin_amdgcn_readfirstlane(tg);
#define GET_P() _Pragma("unroll") for (int i4 = 0; i4 < 4; ++i4) { \
      const u32x4 pv = *reinterpret_cast<const u32x4*>(pT + (wave * 4 + r) * 16 + i4 * 4); \
      _Pragma("unroll") for (int j = 0; j < 4; ++j) pw[i4 * 4 + j] = pv[j]; }
    auto gather_q = [&]() __attribute__((always_inline)) {
        u32 offs[2];
        u32x4 gq[2];
#pragma unroll
        for (int e = 0; e < 2; ++e) offs[e] = G_Q + (((2 * (lane >> 5) + e) * 4 + b_att) * 1024 + h_att * 32 + (lane & 31)) * 16;
        gather22<2, 4>(rs, offs, gq, tg);
        STAMPW(10);
        f32x4 q4 = un_g22(gq[0]) + un_g22(gq[1]);
#pragma unroll
        for (int k = 0; k < 4; ++k) q4[k] = swap32_add(q4[k], q4[k]);
        if (lane < 32) *reinterpret_cast<u32x2*>(qs + 2 * lane) = u32x2{cvt2(q4[0], q4[1]), cvt2(q4[2], q4[3])};
        lds_arrive(C_Q, lane);
    };
    const int qw = half ? QODD : QEVEN;
    float acc[8];
#pragma unroll
    for (int k = 0; k < 8; ++k) acc[k] = 0.f;
    u32 pw[16];
    if (__builtin_expect(wave >= 4, 1)) {
      STAMPT(30, 448);
      BAR();
      STAMPT(31, 448);
      const u32 kvoff2 = kvoff ^ 131072u;
#pragma unroll
      for (int i = 0; i < 48; ++i) {
        if (i == NPRE && half == 0) lds_wait<2>(C_GO, 4);
        if (i < NL) kv[i < NL ? i : 0] = LDKV(rk, kvoff, i & 31); else kh[i < NL ? 0 : i - NL] = LDKV(rk, kvoff2, i & 31);
      }
      LDS_INC(C_KI);
      STAMPT(25, 448);
      if ((QODD >= 4 || QEVEN >= 4) && wave == qw) {
        __builtin_amdgcn_s_setprio(3);
        lds_wait<2>(C_P1, half ? 4 : 2);
        tg = __builtin_amdgcn_readfirstlane(cnt[4]);
        gather_q();
        __builtin_amdgcn_s_setprio(2);
      }
      GET_Q()
      SCORE16(xa, kv, 0)
      asm volatile("" : "+v"(xa[0]), "+v"(xa[1]), "+v"(kvoff) :: "memory");
#pragma unroll
      for (int i = 0; i < P_NVA; ++i) kv[i] = LDKV(rv, kvoff, i);
      asm volatile("" ::: "memory");
      SCORE16(xb, kv, 16)
      xa[2] = xb[0]; xa[3] = xb[1];
      TR_STEP(xa, 2, XOR1, 1)
      float s0 = SCALED(xa[0]), s1 = SCALED(xa[1]);
      asm volatile("" : "+v"(s0), "+v"(s1), "+v"(kvoff) :: "memory");
#pragma unroll
      for (int i = P_NVA; i < NVE; ++i) kv[i] = LDKV(rv, kvoff, i);
      asm volatile("" ::: "memory");
      SCORE16(xh, kh, 0)
      const float s2 = SCALED(xh[0] + dpp<XOR1>(xh[0])), s3 = SCALED(xh[1] + dpp<XOR1>(xh[1]));
      const float m = wave_max(fmaxf(fmaxf(s0, s1), fmaxf(s2, s3)));
      const float p0 = __expf(s0 - m), p1 = __expf(s1 - m), p2 = __expf(s2 - m), p3 = __expf(s3 - m);
      const float l = wave_sum(fmaf(p2 + p3, 0.5f, p0 + p1));
      if (lane == 0) { st[wave] = m; st[8 + wave] = l; }
      pT[(wave * 4 + r) * 16 + (c & 1) * 8 + (c >> 1)] = cvt2(p0, p1);
      pT[((wave - 4) * 4 + r) * 16 + (c >> 1)] = cvt2(p2, p3);
      lds_arrive(C_PP + 4 * (wave - 4), lane);
      __builtin_amdgcn_s_setprio(0);
      STAMPT(27, 448); WSTAMP(1);
      GET_P()
#pragma unroll
      for (int i = NVE; i < NL; i += 2) {
        kv[i] = LDKV(rv, kvoff, i);
        kv[i + 1] = LDKV(rv, kvoff, i + 1);
        asm volatile("" ::: "memory");
        if (i == NL - 2) LDS_INC(C_ISS);
        if (i >= PVLAG) {
          PV2(i >= PVLAG ? i - PVLAG : 0)
#pragma unroll
          for (int k = 0; k < 8; ++k) asm volatile("" : "+v"(acc[k]));
        }
      }
    } else {
#pragma unroll
      for (int i = 0; i < 16; ++i) kh[i] = LDKV(rk, kvoff, 16 + i);
      LDS_INC(C_KI);
      if ((QODD == 0 || QEVEN == 0) && wave == qw) gather_q();
      if (wave == 0) {
        if (half == 0 && g >= 80 && g < 120) {
          const int fb = g - 80;
          u32 of2[4];
          u32x4 gm[4];
#pragma unroll
          for (int ks = 0; ks < 4; ++ks) of2[ks] = G_MOD + ((ks * 4 + r) * 960 + 320 + fb * 16 + c) * 16;
          gather22<4, 4>(rs, of2, gm, tg);
          const f32x4 mv = (un_g22(gm[0]) + un_g22(gm[1])) + (un_g22(gm[2]) + un_g22(gm[3]));
          st_gran(rs, G_MF + (((fb / 10) * 4 + r) * 160 + (fb % 10) * 16 + c) * 16, cvt2(mv[0], mv[1]), cvt2(mv[2], mv[3]), tg);
        }
      }
      __builtin_amdgcn_s_setprio(0);
      lds_wait<2>(C_KI, P_GATE);
      asm volatile("" : "+v"(kvoff));
#pragma unroll
      for (int i = 0; i < NL; ++i) kv[i] = LDKV(rv, kvoff, i);
      LDS_INC(C_ISS);
      STAMPT(1, 0);
      GET_Q()
      SCORE16(xh, kh, 0)
      const float s2 = SCALED(xh[0] + dpp<XOR1>(xh[0])), s3 = SCALED(xh[1] + dpp<XOR1>(xh[1]));
      float m = wave_max(fmaxf(s2, s3));
      lds_wait<2>(C_PP + 4 * wave, 1);
      const float me = st[wave + 4];
      m = fmaxf(m, me);
      const float p2 = __expf(s2 - m), p3 = __expf(s3 - m);
      const float l = wave_sum(p2 + p3) * 0.5f;
      if (lane == 0) { st[wave] = m; st[8 + wave] = l; }
      pT[(wave * 4 + r) * 16 + 8 + (c >> 1)] = cvt2(p2, p3);
      STAMPT(4, 0); WSTAMP(1);
      if (lds_ticket(C_SC, lane) == 3) {
        const float mw = st[lane & 7], lw = st[8 + (lane & 7)];
        float M = fmaxf(mw, dpp<XOR1>(mw)); M = fmaxf(M, dpp<XOR2>(M)); M = fmaxf(M, dpp<XOR7>(M));
        const float e = __expf(mw - M);
        float L = lw * e;
        L += dpp<XOR1>(L); L += dpp<XOR2>(L); L += dpp<XOR7>(L);
        if (lane == 0) st_gran(rs, G_ML + u * 64, f2u(M), f2u(L), tg);
        if (lane < 8) st[16 + lane] = e * frcp(L);
      }
      GET_P()
#pragma unroll
      for (int i = 0; i < 16; i += 2) { PV2(i) }
      const float f = __expf(me - m);
#pragma unroll
      for (int k = 0; k < 8; ++k) acc[k] *= f;
#pragma unroll
      for (int i = 16; i < NL - PVLAG; i += 2) { PV2(i) }
    }
#pragma unroll
    for (int i = NL - PVLAG; i < NL; i += 2) { PV2(i) }
#undef PV2
#undef TR_STEP
#undef SCORE16
#undef SCALED
#undef GET_Q
#undef GET_P
#undef LDKV
#pragma unroll
    for (int i = 0; i < 4; ++i) acc[i] = swap32_add(acc[i], acc[i + 4]);
#pragma unroll
    for (int i = 0; i < 2; ++i) acc[i] = swap16_add(acc[i], acc[i + 2]);
    *reinterpret_cast<float2*>(accs + wave * 128 + c * 8 + 2 * r) = float2{acc[0], acc[1]};
    STAMPT(6, 0); STAMPT(28, 448); WSTAMP(2);
  }
  const int role = half ? (g < 74 ? 0 : 1) : (g >= 122 ? 0 : g < 80 ? 2 : 3);
  const int ia = half ? g : g - 48;
  const int ksA = ia & 7, cbA = ia >> 3;
#define HEADA(hh) ((ksA & 3) + (ksA >> 2) * 16 + (hh) * 4)
  const int jB = g - 74;
  if (lds_ticket(C_FIN, lane) == 7) {
    if (lane < 32) {
      f32x4 o = {0.f, 0.f, 0.f, 0.f};
#pragma unroll
      for (int w = 0; w < 8; ++w) o += st[16 + w] * *reinterpret_cast<const f32x4*>(accs + w * 128 + 4 * lane);
      st_gran(rs, G_ATT + (u * 32 + lane) * 16, cvt2(o[0], o[1]), cvt2(o[2], o[3]), tg);
    }
    STAMPW(11);
  }
  if (role == 1 && wave < 4) {
    const u32x4 x0 = ldw(a.x.p + t * 8), x1 = ldw(a.x.p + 2048 + lane * 8);
    u32 offs[8];
    u32x4 gm[8];
#pragma unroll
    for (int i = 0; i < 8; ++i) { const int it = t + 256 * i; offs[i] = it < 1920 ? G_MF + it * 16 : OOB; }
    gather<8>(rs, offs, gm, tg);
#pragma unroll
    for (int i = 0; i < 8; ++i) {
      const int it = t + 256 * i, sel = it / 640, rem = it % 640, b = rem / 160, gi = rem % 160;
      u32 w0 = gm[i][0], w1 = gm[i][2];
      if (sel == 2) { w0 = cvt2(1.f + lo16(w0), 1.f + hi16(w0)); w1 = cvt2(1.f + lo16(w1), 1.f + hi16(w1)); }
      const int dst = sel == 0 ? L_MOD + b * 1280 + gi * 8 : L_XA + ((sel == 2 ? 4 : 8) + b) * XS + gi * 8;
      if (it < 1920) *reinterpret_cast<u32x2*>(lds + dst) = u32x2{w0, w1};
    }
    *reinterpret_cast<u32x4*>(lds + L_XR + t * 16) = x0;
    if (wave == 0) *reinterpret_cast<u32x4*>(lds + L_XR + 4096 + lane * 16) = x1;
  }
  if (role < 2 && wave < 4) {
    lds_wait<2>(C_ISS, 8);
    if (role == 0) {
      const bf16* wp = a.wo + (size_t)(HEADA(wave) * 128 + (lane >> 3)) * DM + cbA * 64;
#pragma unroll
      for (int i = 0; i < 16; ++i) dma16(wp + (size_t)(8 * i) * DM + (s128e ^ ((i & 1) * 32)), L_W + wave * 16384 + i * 1024);
    } else {
      const bf16* wp = a.w1 + (size_t)(wave * 160 + (lane >> 2)) * FF + jB * 32 + ((((((lane >> 1) & 1) ^ (lane >> 5)) << 1) | (lane & 1)) * 8);
#pragma unroll
      for (int i = 0; i < 10; ++i) dma16(wp + (size_t)(16 * i) * FF, L_W + (wave * 10 + i) * 1024);
      const bf16* wp2 = a.w2 + (size_t)(jB * 32 + wave * 8 + (lane >> 3)) * DM + (s128e ^ ((wave & 1) * 32));
#pragma unroll
      for (int k = 0; k < 10; ++k) dma16(wp2 + k * 64, L_WM2 + (k * 4 + wave) * 1024);
    }
  }
  if (role == 0) {
    const int head = HEADA(wave >> 1), kh = wave & 1;
    s16x8 wf[2][4];
    if (wave < 4) { VM0(); lds_arrive(C_W, lane); }
    lds_wait<1>(C_W, 4);
#pragma unroll
    for (int s = 0; s < 2; ++s)
#pragma unroll
      for (int nt = 0; nt < 4; ++nt) wf[s][nt] = wfrag<512>(L_W + wave * 8192 + (tr128 ^ (nt * 32)) + s * 4096);
    STAMPT(21, 0);
    u32 offs[3], va[3], vb[3];
#pragma unroll
    for (int e = 0; e < 2; ++e) offs[e] = G_ATT + ((((r * 32 + head) * 2 + e) * 32) + 16 * kh + c) * 16;
    offs[2] = lane < 8 ? G_ML + (u32)((((c & 3) * 32 + head) * 2 + (c >> 2)) * 64) : OOB;
    gather_m<3>(rs, offs, va, vb, tg);
    STAMPT(12, 0); STAMPT(22, 448);
    unsigned char* xa = lds + L_XA + wave * 1280;
#pragma unroll
    for (int e = 0; e < 2; ++e) *reinterpret_cast<u32x2*>(xa + (4 * e + r) * 160 + c * 8) = u32x2{va[e], vb[e]};
    float wsc;
    {
      const float M = u2f(va[2]), L = u2f(vb[2]), Mo = dpp<XOR7>(dpp<0x1B>(M)), Lo = dpp<XOR7>(dpp<0x1B>(L));
      const float Mx = fmaxf(M, Mo), le = L * __expf(M - Mx);
      wsc = le * frcp(fmaf(Lo, __expf(Mo - Mx), le));
      wsc = u2f(__builtin_amdgcn_ds_bpermute((c & 7) * 4, f2u(wsc)));
    }
    f32x4 acc[4];
#pragma unroll
    for (int nt = 0; nt < 4; ++nt) acc[nt] = f32x4{0.f, 0.f, 0.f, 0.f};
#pragma unroll
    for (int s = 0; s < 2; ++s) {
      const u32x4 av = *reinterpret_cast<const u32x4*>(xa + (c & 7) * 160 + s * 64 + r * 16);
#pragma unroll
      for (int nt = 0; nt < 4; ++nt) acc[nt] = MFMA(wf[s][nt], av, acc[nt]);
    }
#pragma unroll
    for (int nt = 0; nt < 4; ++nt) {
#pragma unroll
      for (int k = 0; k < 4; ++k) { const float v = acc[nt][k] * wsc; acc[nt][k] = v + dpp<SHL4>(v); }
      if (c < 4) *reinterpret_cast<f32x4*>(red + ((wave * 4 + c) * 16 + nt * 4 + r) * 4) = acc[nt];
    }
    BAR();
    if (wave < NREP) {
      f32x4 s = {0.f, 0.f, 0.f, 0.f};
#pragma unroll
      for (int w = 0; w < 8; ++w) s += *reinterpret_cast<const f32x4*>(red + (w * 64 + lane) * 4);
      st_gran(rs, G_WO + wave * WO_REP + ((ksA * 4 + r) * 160 + cbA * 16 + c) * 16, cvt2(s[0], s[1]), cvt2(s[2], s[3]), tg);
    }
    STAMPT(13, 0);

  } else if (role == 1) {
    const int b1 = wave >> 1, ch = wave & 1;
    const float b1v = bf2f(a.b1[jB * 32 + (t & 31)]);
    VM0();
    BAR();
    const int kq = wave >> 1, nt = wave & 1;
    s16x8 wf[5], wg[5];
    const u32 tr64 = (8 * r + (c >> 2)) * 64 + ((nt ^ (r & 1)) * 32) + (c & 3) * 8;
#pragma unroll
    for (int s = 0; s < 5; ++s) wf[s] = wfrag<256>(L_W + kq * 10240 + tr64 + s * 2048);
#pragma unroll
    for (int k = 0; k < 5; ++k) { const int n2 = wave * 5 + k; wg[k] = wfrag<512>(L_WM2 + (n2 >> 2) * 4096 + (tr128 ^ ((n2 & 3) * 32))); }
    {
      f32x4 acc = {0.f, 0.f, 0.f, 0.f};
#pragma unroll
      for (int s = 0; s < 5; ++s) {
        const u32x4 av = *reinterpret_cast<const u32x4*>(lds + L_XA + (4 + (c & 7)) * XS + kq * 320 + s * 64 + r * 16);
        acc = MFMA(wf[s], av, acc);
      }
      if (c < 8) *reinterpret_cast<f32x4*>(red + ((wave * 8 + c) * 4 + r) * 4) = acc;
    }
    float xf[5], gaf[5], c1f[5];
#pragma unroll
    for (int i = 0; i < 5; ++i) {
      const int col = (ch * 80 + 16 * i + c) * 4 + r;
      xf[i] = u2f((u32)*reinterpret_cast<const unsigned short*>(lds + L_XR + b1 * 1280 + col * 2) << 16);
      gaf[i] = u2f((u32)*reinterpret_cast<const unsigned short*>(lds + L_MOD + b1 * 1280 + col * 2) << 16);
      c1f[i] = u2f((u32)*reinterpret_cast<const unsigned short*>(lds + L_XA + (4 + b1) * XS + col * 2) << 16);
    }
    BAR();
    const int fb = (t >> 5) & 3, fc = t & 31;
    float Tc = 0.f, Ts = 0.f;
#pragma unroll
    for (int k = 0; k < 4; ++k) {
      Tc += red[((2 * k + (fc >> 4)) * 8 + fb) * 16 + (fc & 15)];
      Ts += red[((2 * k + (fc >> 4)) * 8 + 4 + fb) * 16 + (fc & 15)];
    }
#pragma unroll
    for (int i = 0; i < 5; ++i) asm volatile("" : "+v"(xf[i]), "+v"(gaf[i]), "+v"(c1f[i]));
    asm volatile("" : "+v"(Tc), "+v"(Ts));
    u32x4 sel;
#pragma unroll
    for (int d = 0; d < 4; ++d) sel[d] = ((c & 3) == 0 && (d & 1) == (c >> 3)) ? ((c & 4) ? 0x3f800000u : 0x00003f80u) : 0u;
    u32 a_raw = L_W + (((wave >> 1) * 4 + r) * 160 + c) * 16 + (wave & 1) * 8;
    u32 a_sum = L_W + ((r * 4 + b1) * 160 + ch * 80 + c) * 16;
    u32 a_u = L_XA + b1 * XS + ((ch * 80 + c) * 4 + r) * 2;
    u32 a_av = L_XA + (c & 3) * XS + kq * 320 + r * 16;
    u32 a_red = L_RED + ((wave * 4 + c) * 4 + r) * 16;
    u32 a_fin = L_RED + (((fc >> 4) * 4 + fb) * 16 + (fc & 15)) * 4;
    u32 a_out = c < 4 ? G_OUT + (((wave * 20 + r) * 54 + jB) * 4 + c) * 16 : OOB;
    u32 a_fi = L_FI + (c & 3) * 64 + r * 16;
    asm volatile("" : "+v"(sel), "+v"(a_raw), "+v"(a_sum), "+v"(a_u), "+v"(a_av), "+v"(a_red), "+v"(a_fin), "+v"(a_out), "+v"(a_fi));
    STAMPT(18, 0);
    u32x4 pa[10];
    {
      const u32 go = G_WO + (jB & (NREP - 1)) * WO_REP + ((wave * 4 + r) * 160 + c) * 16;
      u32 pend = 0x3ffu;
      SPINLOOP for (unsigned spins = 0; spins < SPIN_CAP; ++spins) {
        const u32x4 sg = ld_gran(rs, lane == 0 ? go + (jB % 10) * 256 : OOB);
        if (__builtin_amdgcn_readfirstlane(sg[1]) == tg) break;
        __builtin_amdgcn_s_sleep(2);
      }
      SPINLOOP for (unsigned spins = 0; spins < SPIN_CAP; ++spins) {
        asm volatile("" ::: "memory");
#pragma unroll
        for (int j = 0; j < 10; ++j) if (pend & (1u << j)) pa[j] = ld_gran(rs, go + j * 256);
#pragma unroll
        for (int j = 0; j < 10; ++j) if (pend & (1u << j)) {
          asm volatile("ds_write2_b32 %0, %1, %2 offset0:%3 offset1:%4" :: "v"(a_raw + (j >> 2) * 1024), "v"(pa[j][0]), "v"(pa[j][2]), "i"((j & 3) * 64), "i"((j & 3) * 64 + 1) : "memory");
          if (__all((pa[j][1] == tg) & (pa[j][3] == tg))) pend &= ~(1u << j);
        }
        if (!pend) break;
      }
    }
    STAMPT(14, 0); WSTAMP(3);
    BAR();
    STAMPT(29, 0);
    float ps = 0.f, pq = 0.f;
    {
      u32x4 v[5];
#pragma unroll
      for (int i = 0; i < 5; ++i) v[i] = *reinterpret_cast<const u32x4*>(lds + a_sum + i * 256);
      asm volatile("" : "+v"(v[0]), "+v"(v[1]), "+v"(v[2]), "+v"(v[3]), "+v"(v[4]));
      f32x4 sm[5];
#pragma unroll
      for (int i = 0; i < 5; ++i) { const f32x4 z = {0.f, 0.f, 0.f, 0.f}; sm[i] = MFMA(sel, v[i], z); }
#pragma unroll
      for (int i = 0; i < 5; ++i) {
        const float x1 = bfr(fmaf(gaf[i], sm[i][0], xf[i]));
        ps += x1; pq = fmaf(x1, x1, pq);
        const u32 uw = cvt2(x1 * c1f[i], 0.f);
        *reinterpret_cast<unsigned short*>(lds + a_u + i * 128) = (unsigned short)uw;
      }
    }
    BAR();
    STAMPT(19, 0);
    {
      u32x4 av[5];
#pragma unroll
      for (int s = 0; s < 5; ++s) av[s] = *reinterpret_cast<const u32x4*>(lds + a_av + s * 64);
      const float t1 = ps + dpp<XOR8>(ps), t2 = pq + dpp<XOR8>(pq);
      float z = (c & 8) ? t2 : t1;
      asm volatile("" : "+v"(av[0]), "+v"(av[1]), "+v"(av[2]), "+v"(av[3]), "+v"(av[4]), "+v"(z));
      f32x4 acc = {0.f, 0.f, 0.f, 0.f};
#pragma unroll
      for (int s = 0; s < 5; ++s) acc = MFMA(wf[s], av[s], acc);
      z += dpp<XOR7>(z); z += dpp<XOR2>(z); z += dpp<XOR1>(z);
      z = cross_rows_sum(z);
      if ((lane & 55) == 0) st[24 + 2 * wave + (lane >> 3)] = z;
      if (c < 4) *reinterpret_cast<f32x4*>(lds + a_red) = acc;
    }
    BAR();
    STAMPT(24, 0);
    if (t < 128) {
      const f32x4 sv = *reinterpret_cast<const f32x4*>(st + 24 + 4 * fb);
      float s = 0.f;
#pragma unroll
      for (int k = 0; k < 4; ++k) s += *reinterpret_cast<const float*>(lds + a_fin + k * 512);
      const float mean = (sv[0] + sv[2]) * (1.f / DM);
      const float var = fmaxf((sv[1] + sv[3]) * (1.f / DM) - mean * mean, 0.f);
      const float rstd = __builtin_amdgcn_rsqf(var + 1e-5f);
      const float hh = bfr(bfr(fmaf(rstd, fmaf(-mean, Tc, s), Ts)) + b1v);
      const float e2 = __builtin_amdgcn_exp2f(hh * fmaf(hh * hh, 0.1029432397f, 2.302208198f));
      const float f = fmaf(-hh, frcp(e2 + 1.f), hh);
      *reinterpret_cast<unsigned short*>(lds + L_FI + t * 2) = (unsigned short)cvt2(f, 0.f);
    }
    BAR();
    {
      const u32x4 av = *reinterpret_cast<const u32x4*>(lds + a_fi);
      f32x4 acc[5];
#pragma unroll
      for (int k = 0; k < 5; ++k) { const f32x4 z = {0.f, 0.f, 0.f, 0.f}; acc[k] = MFMA(wg[k], av, z); }
#pragma unroll
      for (int k = 0; k < 5; ++k) st_gran(rs, a_out + k * (4 * 54 * 64), cvt2(acc[k][0], acc[k][1]), cvt2(acc[k][2], acc[k][3]), tg);
    }
    STAMPT(15, 0);

  } else if (role == 2 && wave < 2) {
    const int cg = g * 2 + wave, b = lane & 3, jj = lane >> 2, col = cg * 4 + r;
    const float xw = bf2f(a.x.p[b * DM + col]), b2v = bf2f(a.b2[col]);
    float x1v, gbv;
    {
      u32 offs[1], va[1], vb[1];
      offs[0] = jj < 8 ? G_WO + (g & (NREP - 1)) * WO_REP + ((jj * 4 + b) * 160 + cg) * 16 : jj < 10 ? G_MF + (((jj - 8) * 12 + b) * 160 + cg) * 16 : OOB;
      gather_m<1>(rs, offs, va, vb, tg);
      const u32 gaw0 = __builtin_amdgcn_ds_bpermute((32 + b) * 4, va[0]), gaw1 = __builtin_amdgcn_ds_bpermute((32 + b) * 4, vb[0]);
      const u32 gbw0 = __builtin_amdgcn_ds_bpermute((36 + b) * 4, va[0]), gbw1 = __builtin_amdgcn_ds_bpermute((36 + b) * 4, vb[0]);
      if (jj >= 8) { va[0] = 0; vb[0] = 0; }
      float s = swap16_add(swap32_add(lo16(va[0]), lo16(vb[0])), swap32_add(hi16(va[0]), hi16(vb[0])));
      s += dpp<ROR4>(s); s += dpp<XOR8>(s);
      const u32 gaw = (r & 2) ? gaw1 : gaw0, gbw = (r & 2) ? gbw1 : gbw0;
      const float ga = (r & 1) ? hi16(gaw) : lo16(gaw);
      gbv = (r & 1) ? hi16(gbw) : lo16(gbw);
      x1v = bfr(xw + bfr(ga * bfr(s)));
    }
    STAMPT(23, 0);
    u32 offs[4], va[4], vb[4];
#pragma unroll
    for (int i = 0; i < 4; ++i) offs[i] = (jj + 16 * i < 54) ? G_OUT + ((cg * 54 + jj + 16 * i) * 4 + b) * 16 : OOB;
    gather_m<4>(rs, offs, va, vb, tg);
    STAMPT(16, 0);
    float s0 = lo16(va[0]), s1 = hi16(va[0]), s2 = lo16(vb[0]), s3 = hi16(vb[0]);
#pragma unroll
    for (int i = 1; i < 4; ++i) { s0 = addlo(va[i], s0); s1 = addhi(va[i], s1); s2 = addlo(vb[i], s2); s3 = addhi(vb[i], s3); }
    float s = swap16_add(swap32_add(s0, s2), swap32_add(s1, s3));
    s += dpp<ROR4>(s); s += dpp<XOR8>(s);
    const float y = bfr(bfr(s) + b2v);
    if ((lane & 12) == 0) a.out[b * DM + col] = __float2bfloat16(x1v + bfr(gbv * y));
    STAMPT(17, 0);

    if (u == 0 && t == 0)
      __hip_atomic_store(reinterpret_cast<u32*>(a.ws + WS_EPOCH), tg, __ATOMIC_RELAXED, __HIP_MEMORY_SCOPE_AGENT);
  }
  STAMP_FLUSH;
}

extern "C" void kernel_launch(void* const* d_in, const int* in_sizes, int n_in, void* d_out, int out_size, void* d_ws,
                              size_t ws_size, hipStream_t stream) {
  Args a;
  a.x = Desc{(const bf16*)d_in[0], NB * DM * 2, 0x00020000u};
  a.kc = (const bf16*)d_in[1];
  a.vc = (const bf16*)d_in[2];
  a.wq = Desc{(const bf16*)d_in[3], DM * QD * 2, 0x00020000u};
  a.wo = (const bf16*)d_in[4];
  a.w1 = (const bf16*)d_in[6];
  a.b1 = (const bf16*)d_in[7];
  a.w2 = (const bf16*)d_in[8];
  a.b2 = (const bf16*)d_in[9];
  a.c = Desc{(const bf16*)d_in[10], NB * DM * 2, 0x00020000u};
  a.wmod = Desc{(const bf16*)d_in[11], DM * MODN * 2, 0x00020000u};
  a.out = (bf16*)d_out;
  a.ws = (unsigned char*)d_ws;
  mega_kernel<<<NWG, NT, 0, stream>>>(a);
}
```

```cpp
#define SPINLOOP _Pragma("nounroll")
#define P_LAG 14
#define P_NPRE 32
#define P_NVE 6
#define P_NVA 6
#define QODD 4
#define QEVEN 4
#define MFPOS 1
#define EA 0
#define EB 0
#define EC 4
#define APOLL 0
#define N1E N1A
#define TAU_A 234
#define TAU_B TAU_A
#define TAU_X TAU_A
#define TAU_E 247
#define PK 0
#define PL 1
#define QPRE 1
#define QHOLD 0
#define PB 0
#define GOPOS 0
#define JMAP 1
#define N1A 6
#define N1B 0
#define PT 0
#define PTAUX 16
#define P_GATE 8
#include <hip/hip_runtime.h>
#include <hip/hip_bf16.h>
#include <stdint.h>

using bf16 = __hip_bfloat16;
using u32 = unsigned;
using u64 = unsigned long long;
using u32x2 = __attribute__((ext_vector_type(2))) unsigned;
using u32x4 = __attribute__((ext_vector_type(4))) unsigned;
using f32x4 = __attribute__((ext_vector_type(4))) float;
using bf16x2 = __attribute__((ext_vector_type(2))) __bf16;
typedef __attribute__((ext_vector_type(2))) float f32x2;
typedef short s16x8 __attribute__((ext_vector_type(8)));
typedef short s16x4 __attribute__((ext_vector_type(4)));
typedef __attribute__((address_space(3))) s16x4* lds_s4p;
typedef __attribute__((ext_vector_type(8))) __bf16 bf16x8v;

constexpr int NB = 4, DM = 640, NH = 32, DH = 128, SK = 8192, WIN = 2048, FF = 1728, QD = 4096, MODN = 3840;
constexpr int NT = 512, NWG = 256;

constexpr u32 WS_EPOCH = 0;
constexpr u32 G_MOD = 256;
constexpr u32 G_Q = G_MOD + 16 * 960 * 16;
constexpr u32 G_MF = G_Q + 16 * 1024 * 16;
constexpr u32 G_ML = G_MF + 16 * 160 * 16;
constexpr u32 G_ATT = G_ML + 256 * 64;
constexpr u32 G_WO = G_ATT + 256 * 32 * 16;
constexpr u32 WO_REP = 32 * 160 * 16;
constexpr u32 G_OUT = G_WO + 4 * WO_REP;
constexpr u32 WS_END = G_OUT + 160 * 54 * 4 * 16;
constexpr u32 OOB = 0xF0000000u;

#define WSTAMP(k) do {} while (0)
#define STAMPW(i) do {} while (0)
#define STAMPT(i, tt) do {} while (0)
#define STAMP_DECL
#define STAMP_FLUSH do {} while (0)
#define BAR() asm volatile("s_waitcnt lgkmcnt(0)\n\ts_barrier" ::: "memory")
#define VM0() asm volatile("s_waitcnt vmcnt(0)" ::: "memory")

__device__ __forceinline__ float bfr(float x) { return __bfloat162float(__float2bfloat16(x)); }
__device__ __forceinline__ float bf2f(bf16 x) { return __bfloat162float(x); }
__device__ __forceinline__ float lo16(unsigned u) { return __builtin_bit_cast(float, u << 16); }
__device__ __forceinline__ float hi16(unsigned u) { return __builtin_bit_cast(float, u & 0xffff0000u); }
__device__ __forceinline__ u32 f2u(float f) { return __builtin_bit_cast(u32, f); }
__device__ __forceinline__ float u2f(u32 f) { return __builtin_bit_cast(float, f); }
__device__ __forceinline__ u32 cvt2(float a, float b) {
  const f32x2 v = {a, b};
  return __builtin_bit_cast(u32, __builtin_convertvector(v, bf16x2));
}
__device__ __forceinline__ float frcp(float x) { return __builtin_amdgcn_rcpf(x); }
__device__ __forceinline__ float fsqrt(float x) { return __builtin_amdgcn_sqrtf(x); }
__device__ __forceinline__ float fexp(float x) { return __builtin_amdgcn_exp2f(x * 1.4426950408889634f); }
__device__ __forceinline__ float dot2(unsigned k, unsigned q, float acc) {
  return __builtin_amdgcn_fdot2_f32_bf16(__builtin_bit_cast(bf16x2, k), __builtin_bit_cast(bf16x2, q), acc, false);
}
__device__ __forceinline__ float addlo(unsigned w, float acc) { u32 k = 0x00003f80u; asm("" : "+s"(k)); return dot2(w, k, acc); }
__device__ __forceinline__ float addhi(unsigned w, float acc) { return dot2(w, 0x3f800000u, acc); }
template <int CTRL> __device__ __forceinline__ float dpp(float x) {
  return __builtin_bit_cast(float, __builtin_amdgcn_mov_dpp(__builtin_bit_cast(int, x), CTRL, 0xf, 0xf, true));
}
template <int CTRL> __device__ __forceinline__ u32 dppu(u32 x) { return (u32)__builtin_amdgcn_mov_dpp((int)x, CTRL, 0xf, 0xf, true); }
constexpr int XOR1 = 0xB1, XOR2 = 0x4E, XOR7 = 0x141, XOR8 = 0x128, ROR4 = 0x124, ROR2 = 0x122, ROR1 = 0x121, SHL4 = 0x104;
__device__ __forceinline__ float swap32_add(float a, float b) {
  auto s = __builtin_amdgcn_permlane32_swap(f2u(a), f2u(b), false, false);
  return u2f(s[0]) + u2f(s[1]);
}
__device__ __forceinline__ float swap16_add(float a, float b) {
  auto s = __builtin_amdgcn_permlane16_swap(f2u(a), f2u(b), false, false);
  return u2f(s[0]) + u2f(s[1]);
}
__device__ __forceinline__ float cross_rows_sum(float x) { x = swap16_add(x, x); return swap32_add(x, x); }
__device__ __forceinline__ float cross_rows_max(float x) {
  auto s = __builtin_amdgcn_permlane16_swap(f2u(x), f2u(x), false, false);
  x = fmaxf(u2f(s[0]), u2f(s[1]));
  auto s2 = __builtin_amdgcn_permlane32_swap(f2u(x), f2u(x), false, false);
  return fmaxf(u2f(s2[0]), u2f(s2[1]));
}
__device__ __forceinline__ float wave_sum(float x) {
  x += dpp<XOR8>(x); x += dpp<ROR4>(x); x += dpp<ROR2>(x); x += dpp<ROR1>(x);
  return cross_rows_sum(x);
}
__device__ __forceinline__ float wave_max(float x) {
  x = fmaxf(x, dpp<XOR8>(x)); x = fmaxf(x, dpp<ROR4>(x)); x = fmaxf(x, dpp<ROR2>(x)); x = fmaxf(x, dpp<ROR1>(x));
  return cross_rows_max(x);
}

__device__ __forceinline__ void st_gran(__amdgpu_buffer_rsrc_t rs, u32 off, u32 a, u32 b, u32 tg) {
  u32x4 v = {a, tg, b, tg};
  __builtin_amdgcn_raw_buffer_store_b128(v, rs, off, 0, 16);
}
__device__ __forceinline__ u32x4 ld_gran(__amdgpu_buffer_rsrc_t rs, u32 off) {
  return __builtin_amdgcn_raw_buffer_load_b128(rs, off, 0, 16);
}
constexpr int NREP = 4;
constexpr unsigned SPIN_CAP = 1u << 18;
template <int N>
__device__ __forceinline__ void gather(__amdgpu_buffer_rsrc_t rs, const u32 (&offs)[N], u32x4 (&g)[N], u32 tg) {
  SPINLOOP for (unsigned spins = 0; spins < SPIN_CAP; ++spins) {
    bool ok = true;
#pragma unroll
    for (int i = 0; i < N; ++i) g[i] = ld_gran(rs, offs[i]);
#pragma unroll
    for (int i = 0; i < N; ++i) ok &= (offs[i] == OOB) | ((g[i][1] == tg) & (g[i][3] == tg));
    if (__all(ok)) break;
    __builtin_amdgcn_s_sleep(1);
  }
}
template <int N>
__device__ __forceinline__ void gather_m(__amdgpu_buffer_rsrc_t rs, u32 (&offs)[N], u32 (&va)[N], u32 (&vb)[N], u32 tg) {
#pragma unroll
  for (int i = 0; i < N; ++i) { va[i] = 0; vb[i] = 0; }
  SPINLOOP for (unsigned spins = 0; spins < SPIN_CAP; ++spins) {
    u32x4 g[N];
#pragma unroll
    for (int i = 0; i < N; ++i) g[i] = ld_gran(rs, offs[i]);
    bool ok = true;
#pragma unroll
    for (int i = 0; i < N; ++i) {
      const bool hit = (offs[i] != OOB) & (g[i][1] == tg) & (g[i][3] == tg);
      va[i] = hit ? g[i][0] : va[i]; vb[i] = hit ? g[i][2] : vb[i];
      offs[i] = hit ? OOB : offs[i];
      ok &= (offs[i] == OOB);
    }
    if (__all(ok)) break;
    __builtin_amdgcn_s_sleep(1);
  }
}

__device__ __forceinline__ void st_g22(__amdgpu_buffer_rsrc_t rs, u32 off, f32x4 v, u32 tg) {
  const u32 a0 = f2u(v[0]) + 0x200u, a1 = f2u(v[1]) + 0x200u, a2 = f2u(v[2]) + 0x200u, a3 = f2u(v[3]) + 0x200u;
  const u32x4 w = {(a0 >> 10) | ((a1 << 12) & 0xffc00000u), (a1 >> 20) | (tg << 12), (a2 >> 10) | ((a3 << 12) & 0xffc00000u), (a3 >> 20) | (tg << 12)};
  __builtin_amdgcn_raw_buffer_store_b128(w, rs, off, 0, 16);
}
__device__ __forceinline__ f32x4 un_g22(u32x4 w) {
  return f32x4{u2f(w[0] << 10), u2f(__builtin_amdgcn_alignbit(w[1], w[0], 22) << 10), u2f(w[2] << 10), u2f(__builtin_amdgcn_alignbit(w[3], w[2], 22) << 10)};
}
template <int N, int SLEEP>
__device__ __forceinline__ void gather22(__amdgpu_buffer_rsrc_t rs, u32 (&offs)[N], u32x4 (&v)[N], u32 tg) {
#pragma unroll
  for (int i = 0; i < N; ++i) v[i] = u32x4{0u, 0u, 0u, 0u};
  SPINLOOP for (unsigned spins = 0; spins < SPIN_CAP; ++spins) {
    u32x4 g[N];
#pragma unroll
    for (int i = 0; i < N; ++i) g[i] = ld_gran(rs, offs[i]);
    bool ok = true;
#pragma unroll
    for (int i = 0; i < N; ++i) {
      const bool hit = (offs[i] != OOB) & (((g[i][1] ^ (tg << 12)) | (g[i][3] ^ (tg << 12))) < 4096u);
#pragma unroll
      for (int k = 0; k < 4; ++k) v[i][k] = hit ? g[i][k] : v[i][k];
      offs[i] = hit ? OOB : offs[i];
      ok &= (offs[i] == OOB);
    }
    if (__all(ok)) break;
    __builtin_amdgcn_s_sleep(SLEEP);
  }
}

__device__ __forceinline__ u32 next_tag(u32 ep) { const u32 t = ep + 1u; return (t & 0xfffffu) ? t : t + 1u; }

__device__ __forceinline__ void lds_arrive(u32 addr, int lane) {
  asm volatile("s_waitcnt lgkmcnt(0)" ::: "memory");
  if (lane == 0) asm volatile("ds_add_u32 %0, %1" :: "v"(addr), "v"(1u) : "memory");
}
__device__ __forceinline__ u32 lds_ticket(u32 addr, int lane) {
  u32 r = 0;
  asm volatile("s_waitcnt lgkmcnt(0)" ::: "memory");
  if (lane == 0) asm volatile("ds_add_rtn_u32 %0, %1, %2\n\ts_waitcnt lgkmcnt(0)" : "=v"(r) : "v"(addr), "v"(1u) : "memory");
  return __builtin_amdgcn_readfirstlane(r);
}
template <int SLEEP>
__device__ __forceinline__ void lds_wait(u32 addr, u32 target) {
  SPINLOOP for (unsigned spins = 0; spins < (1u << 22); ++spins) {
    u32 v;
    asm volatile("ds_read_b32 %0, %1\n\ts_waitcnt lgkmcnt(0)" : "=v"(v) : "v"(addr) : "memory");
    if (v >= target) break;
    __builtin_amdgcn_s_sleep(SLEEP);
  }
}
__device__ __forceinline__ void dma16(const void* gsrc, u32 lds_dst) {
  u32 keep;
  asm volatile("s_mov_b32 %0, m0\n\ts_mov_b32 m0, %2\n\ts_nop 0\n\tglobal_load_lds_dwordx4 %1, off\n\ts_mov_b32 m0, %0"
               : "=&s"(keep) : "v"(gsrc), "s"(lds_dst) : "memory");
}
__device__ __forceinline__ void dma16nt(const void* gsrc, u32 lds_dst) {
  u32 keep;
  asm volatile("s_mov_b32 %0, m0\n\ts_mov_b32 m0, %2\n\ts_nop 0\n\tglobal_load_lds_dwordx4 %1, off nt\n\ts_mov_b32 m0, %0"
               : "=&s"(keep) : "v"(gsrc), "s"(lds_dst) : "memory");
}
__device__ __forceinline__ u32x4 ldasm(const void* p) {
  u32x4 v;
  asm volatile("global_load_dwordx4 %0, %1, off" : "=v"(v) : "v"(p) : "memory");
  return v;
}
__device__ __forceinline__ u32 ldasm_sc1(const void* p) {
  u32 v;
  asm volatile("global_load_dword %0, %1, off sc1" : "=v"(v) : "v"(p) : "memory");
  return v;
}
__device__ __forceinline__ u32x4 ldw(const bf16* p) { return *reinterpret_cast<const u32x4*>(p); }
template <int D2>
__device__ __forceinline__ s16x8 wfrag(u32 addr) {
  const s16x4 lo = __builtin_amdgcn_ds_read_tr16_b64_v4i16((lds_s4p)(uintptr_t)addr);
  const s16x4 hi = __builtin_amdgcn_ds_read_tr16_b64_v4i16((lds_s4p)(uintptr_t)(addr + D2));
  return s16x8{lo[0], lo[1], lo[2], lo[3], hi[0], hi[1], hi[2], hi[3]};
}
#define MFMA(w_, x_, c_) __builtin_amdgcn_mfma_f32_16x16x32_bf16(__builtin_bit_cast(bf16x8v, w_), __builtin_bit_cast(bf16x8v, x_), c_, 0, 0, 0)

struct Desc { const bf16* p; u32 bytes, flags; };
struct Args {
  Desc wq, x, wmod, c;
  unsigned char* ws;
  const bf16 *kc, *vc, *wo, *w1, *b1, *w2, *b2;
  bf16* out;
};

constexpr int L_W = 0;
constexpr int L_WM2 = 40960;
constexpr int L_XA = 81920;
constexpr int XS = 1312;
constexpr int L_RED = L_XA + 16384;
constexpr int L_MOD = L_RED + 8192;
constexpr int L_XR = L_MOD + 5120;
constexpr int L_PT = L_XR + 5120;
constexpr int L_ACC = L_PT + 2048;
constexpr int L_QS = L_ACC + 4096;
constexpr int L_FI = L_QS + 256;
constexpr int L_ST = L_FI + 256;
constexpr int L_CNT = L_ST + 256;
constexpr int L_END = L_CNT + 64;
static_assert(L_END <= 160 * 1024, "LDS");
constexpr u32 C_Q = L_CNT, C_SC = L_CNT + 4, C_ISS = L_CNT + 8, C_P1 = L_CNT + 20, C_P2 = L_CNT + 24, C_GO = L_CNT + 28, C_W = L_CNT + 12, C_FIN = L_CNT + 32,
              C_KI = L_CNT + 36, C_PP = L_CNT + 40, C_PUB = L_CNT + 56;

constexpr int NL = 32;
constexpr int PVLAG = P_LAG;
constexpr int NPRE = P_NPRE;
constexpr int NVE = P_NVE;
static_assert(PVLAG <= 16 && (PVLAG & 1) == 0 && NVE <= PVLAG && (NVE & 1) == 0, "row groups");

__global__ void __launch_bounds__(NT, 2) mega_kernel(Args a) {
  __shared__ __attribute__((aligned(1024))) unsigned char lds[L_END];
  float* red = reinterpret_cast<float*>(lds + L_RED);
  u32* xraw = reinterpret_cast<u32*>(lds + L_XR);
  u32* pT = reinterpret_cast<u32*>(lds + L_PT);
  float* accs = reinterpret_cast<float*>(lds + L_ACC);
  u32* qs = reinterpret_cast<u32*>(lds + L_QS);
  float* st = reinterpret_cast<float*>(lds + L_ST);
  u32* cnt = reinterpret_cast<u32*>(lds + L_CNT);

  u32x4 rw, rx;
  unsigned char* wsq;
  asm volatile("s_load_dwordx4 %0, %3, %4\n\ts_load_dwordx4 %1, %3, %4 offset:16\n\ts_load_dwordx2 %2, %3, 64"
               : "=&s"(rw), "=&s"(rx), "=&s"(wsq) : "s"(__builtin_amdgcn_kernarg_segment_ptr()), "s"((blockIdx.x & 1) * 32));
  __builtin_amdgcn_sched_barrier(0);
  const int t = threadIdx.x, lane = t & 63;
  const int wave = __builtin_amdgcn_readfirstlane(t >> 6);
  const int u = blockIdx.x;
  const int bh = u >> 1, half = u & 1, b_att = bh >> 5, h_att = bh & 31;
  const int r = lane >> 4, c = lane & 15;
  STAMP_DECL
  STAMPT(0, 0);
  u32 tg;
  const int g = u >> 1, ksp = g & 3, cbp = g >> 2;
  const int s128e = (((((lane & 7) >> 1) ^ ((lane >> 4) & 1)) << 1) | (lane & 1)) * 8;
  const u32 tr128 = (8 * r + (c >> 2)) * 128 + ((((c >> 3) & 1) | ((r & 1) << 1)) * 32) + (c & 3) * 8;
  if (wave < 4) {
    __builtin_amdgcn_s_setprio(3);
    const u32 rb = 8192 - 512 * half;
    const u32 hm = 0u - (u32)half;
    const u32 vo = (u32)(lane >> (4 - half)) * rb + (((lane & (15 >> half)) ^ ((lane >> 3) & (6 - 4 * half))) ^ (half ? (wave & 1) * 4 : (wave >> 1) * 8)) * 16;
    u32 so = (u32)(ksp * 160 + (half ? 8 * (wave & 1) : 4 * wave)) * rb + (cbp << (8 - half)) + ((half && wave < 2 && g < 112) ? 4096 : 0);
    const u32 step = 16 * rb, dstep = 4096 >> half, dst = L_W + wave * 1024 + (half ? (wave < 2 ? 20480 : -2048) : 0);
    const u32 oe = wave * 1280 + lane * 16, oo = (r * 1280 + ksp * 320 + (c + 16 * wave) * 16) | ((u32)(19 - c - 16 * wave) & OOB);
    const u32 oa = (oo & hm) | (oe & ~hm), ob = (wave * 1280 + 1024 + c * 16) | (hm & OOB), oc = oe | (hm & OOB), zero = 0;
    u32 ep, keep;
    u32x4 xv[4];
#define DMA1 "buffer_load_dwordx4 %7, %8, %1 offen nt lds\n\t"
#define DMAN "s_add_u32 m0, m0, %11\n\ts_add_u32 %1, %1, %10\n\t" DMA1
    asm volatile("s_mov_b32 %0, m0\n\ts_mov_b32 m0, %9\n\t"
                 "s_cmp_gt_u32 %18, 1\n\ts_waitcnt lgkmcnt(0)\n\ts_cbranch_scc1 1f\n\t"
                 "global_load_dword %2, %12, %13 sc1\n\t"
                 "buffer_load_dwordx4 %3, %14, %17, 0 offen\n\tbuffer_load_dwordx4 %4, %15, %17, 0 offen\n\t"
                 "buffer_load_dwordx4 %5, %16, %17, 0 offen offset:2560\n\tbuffer_load_dwordx4 %6, %15, %17, 0 offen offset:2560\n"
                 "1:\n\t" DMA1 DMAN DMAN DMAN DMAN DMAN DMAN DMAN DMAN DMAN "s_mov_b32 m0, %0\n\t"
                 "s_barrier\n\ts_waitcnt vmcnt(10)"
                 : "=&s"(keep), "+s"(so), "=&v"(ep), "=&v"(xv[0]), "=&v"(xv[1]), "=&v"(xv[2]), "=&v"(xv[3])
                 : "v"(vo), "s"(rw), "s"(dst), "s"(step), "s"(dstep), "v"(zero), "s"(wsq), "v"(oa), "v"(ob), "v"(oc), "s"(rx), "s"(wave) : "memory", "scc");
#undef DMAN
#undef DMA1
    asm volatile("" : "+s"(wsq));
    const __amdgpu_buffer_rsrc_t rs = __builtin_amdgcn_make_buffer_rsrc(wsq, 0, WS_END, 0x00020000);
    if (half) {
      if (wave >= 2) {
        VM0();
        STAMPT(5, 128);
      } else {
        u32x4 sv;
#pragma unroll
        for (int k = 0; k < 4; ++k) {
          const float c0 = lo16(xv[0][k]), c1 = hi16(xv[0][k]);
          sv[k] = cvt2(c0 * frcp(1.f + fexp(-c0)), c1 * frcp(1.f + fexp(-c1)));
        }
        if (c + 16 * wave < 20) *reinterpret_cast<u32x4*>(lds + L_XA + (r * 20 + c + 16 * wave) * 16) = sv;
        if (t == 0) cnt[4] = next_tag(ep);
        STAMPT(2, 0);
      }
      lds_arrive(C_P1, lane);
      lds_wait<1>(C_P1, 4);
      STAMPT(20, 0);
      tg = __builtin_amdgcn_readfirstlane(cnt[4]);
      u32x4 av[5];
#pragma unroll
      for (int s = 0; s < 5; ++s) av[s] = *reinterpret_cast<const u32x4*>(lds + L_XA + (c & 3) * 320 + s * 64 + r * 16);
      {
        f32x4 acc = {0.f, 0.f, 0.f, 0.f};
#pragma unroll
        for (int s = 0; s < 5; ++s) acc = MFMA(wfrag<512>(L_W + (tr128 ^ (wave * 32)) + s * 4096), av[s], acc);
        if (c < 4) st_g22(rs, G_MOD + ((ksp * 4 + c) * 960 + cbp * 16 + wave * 4 + r) * 16, acc, tg);
      }
      STAMPT(7, 0);
      if (wave < 2) { VM0(); lds_arrive(C_P2, lane); }
      lds_wait<1>(C_P2, 2);
      {
        f32x4 acc = {0.f, 0.f, 0.f, 0.f};
#pragma unroll
        for (int s = 0; s < 5; ++s) acc = MFMA(wfrag<512>(L_W + 20480 + (tr128 ^ (wave * 32)) + s * 4096), av[s], acc);
        if (c < 4 && g < 112) st_g22(rs, G_MOD + ((ksp * 4 + c) * 960 + (cbp + 32) * 16 + wave * 4 + r) * 16, acc, tg);
      }
      STAMPT(9, 0);
    } else {
      if (wave < 2) {
#pragma unroll
        for (int i = 0; i < 2; ++i) {
          const int b = wave + 2 * i;
          float xf[16];
#pragma unroll
          for (int k = 0; k < 4; ++k) { xf[2 * k] = lo16(xv[2 * i][k]); xf[2 * k + 1] = hi16(xv[2 * i][k]); xf[8 + 2 * k] = lo16(xv[2 * i + 1][k]); xf[8 + 2 * k + 1] = hi16(xv[2 * i + 1][k]); }
          float s = 0.f, s2 = 0.f;
#pragma unroll
          for (int k = 0; k < 8; ++k) { s += xf[k]; s2 += xf[8 + k]; }
          if (lane < 16) s += s2;
          const float mean = wave_sum(s) * (1.f / DM);
          float v = 0.f, v2 = 0.f;
#pragma unroll
          for (int k = 0; k < 8; ++k) { const float d = xf[k] - mean; v += d * d; const float d2 = xf[8 + k] - mean; v2 += d2 * d2; }
          if (lane < 16) v += v2;
          v = wave_sum(v);
          if (lane == 0) { st[24 + 2 * b] = bfr(mean); st[24 + 2 * b + 1] = frcp(bfr(fsqrt(bfr(bfr(v * (1.f / DM)) + 1e-5f)))); }
          *reinterpret_cast<u32x4*>(xraw + b * 320 + lane * 4) = xv[2 * i];
          if (lane < 16) *reinterpret_cast<u32x4*>(xraw + b * 320 + 256 + lane * 4) = xv[2 * i + 1];
        }
        if (t == 0) cnt[4] = next_tag(ep);
        lds_arrive(C_P1, lane);
      }
      lds_wait<1>(C_P1, 2);
      STAMPT(20, 0);
      tg = __builtin_amdgcn_readfirstlane(cnt[4]);
      if (wave < 3) {
        const int b = t / 40, j = t % 40;
        u32 offs[8];
        u32x4 gm[8];
#pragma unroll
        for (int sel = 0; sel < 2; ++sel)
#pragma unroll
          for (int ks = 0; ks < 4; ++ks) offs[sel * 4 + ks] = t < 160 ? G_MOD + ((ks * 4 + b) * 960 + sel * 160 + ksp * 40 + j) * 16 : OOB;
        __builtin_amdgcn_s_sleep(16);
        gather22<8, 4>(rs, offs, gm, tg);
        STAMPT(8, 0);
        const f32x4 sa = (un_g22(gm[0]) + un_g22(gm[1])) + (un_g22(gm[2]) + un_g22(gm[3]));
        const f32x4 ca = (un_g22(gm[4]) + un_g22(gm[5])) + (un_g22(gm[6]) + un_g22(gm[7]));
        const int bb = b & 3;
        const float mean = st[24 + 2 * bb], sd = st[24 + 2 * bb + 1];
        const u32x2 xw = *reinterpret_cast<const u32x2*>(xraw + bb * 320 + ksp * 80 + 2 * j);
        float hv[4];
#pragma unroll
        for (int k = 0; k < 4; ++k) {
          const float xf = (k & 1) ? hi16(xw[k >> 1]) : lo16(xw[k >> 1]);
          const float n = bfr(bfr(xf - mean) * sd);
          hv[k] = bfr(n * bfr(1.f + bfr(ca[k]))) + bfr(sa[k]);
        }
        if (t < 160) *reinterpret_cast<u32x2*>(lds + L_XA + t * 8) = u32x2{cvt2(hv[0], hv[1]), cvt2(hv[2], hv[3])};
      }
      VM0();
      lds_arrive(C_P2, lane);
      lds_wait<1>(C_P2, 4);
      u32x4 av[5];
#pragma unroll
      for (int s = 0; s < 5; ++s) av[s] = *reinterpret_cast<const u32x4*>(lds + L_XA + (c & 3) * 320 + s * 64 + r * 16);
#pragma unroll
      for (int e = 0; e < 2; ++e) {
        const int nt = 2 * wave + e;
        const u32 tr256 = (8 * r + (c >> 2)) * 256 + ((((c >> 2) | ((r & 1) << 2)) ^ nt) * 32) + (c & 3) * 8;
        f32x4 acc = {0.f, 0.f, 0.f, 0.f};
#pragma unroll
        for (int s = 0; s < 5; ++s) acc = MFMA(wfrag<1024>(L_W + tr256 + s * 8192), av[s], acc);
        if (c < 4) st_g22(rs, G_Q + ((ksp * 4 + c) * 1024 + cbp * 32 + nt * 4 + r) * 16, acc, tg);
      }
      lds_arrive(C_GO, lane);
      STAMPT(9, 0);
    }
  } else {
    __builtin_amdgcn_s_setprio(2);
    if (t < 256 + 16) cnt[lane] = lane == 15 ? (u32)__builtin_amdgcn_s_memrealtime() : 0;
  }
  u32 kvoff = (u32)(bh * SK + (SK - WIN)) * 256u + (u32)(half * 8 + wave) * (NL * 1024u) + lane * 16, ph[8];
#pragma unroll
  for (int i = 0; i < 8; ++i) ph[i] = ((i + (u >> 3)) & 7) * 4096;
  asm volatile("" : "+v"(kvoff));
  asm volatile("s_waitcnt lgkmcnt(0)" : "+s"(wsq), "+s"(ph[0]), "+s"(ph[1]), "+s"(ph[2]), "+s"(ph[3]), "+s"(ph[4]), "+s"(ph[5]), "+s"(ph[6]), "+s"(ph[7])
               : "s"(rw), "s"(rx));
  const __amdgpu_buffer_rsrc_t rs = __builtin_amdgcn_make_buffer_rsrc(wsq, 0, WS_END, 0x00020000);
  {
    u32x4 kv[NL], kh[16];
    const __amdgpu_buffer_rsrc_t rk = __builtin_amdgcn_make_buffer_rsrc((void*)a.kc, 0, 1u << 28, 0x00020000);
    const __amdgpu_buffer_rsrc_t rv = __builtin_amdgcn_make_buffer_rsrc((void*)a.vc, 0, 1u << 28, 0x00020000);
#define LDKV(rsrc, off, i) __builtin_amdgcn_raw_buffer_load_b128(rsrc, (off) + ((i) & 3) * 1024, ph[(i) >> 2], 2)
#define LDS_INC(addr) do { asm volatile("" ::: "memory"); if (lane == 0) asm volatile("ds_add_u32 %0, %1" :: "v"(addr), "v"(1u) : "memory"); } while (0)
#define TR_STEP(x, HALF, CTRL, BIT) _Pragma("unroll") for (int hh = 0; hh < (HALF); ++hh) { \
    const float lo_ = x[hh], hi_ = x[hh + (HALF)]; const float t1_ = lo_ + dpp<CTRL>(lo_), t2_ = hi_ + dpp<CTRL>(hi_); \
    x[hh] = (c & (BIT)) ? t2_ : t1_; }
#define SCORE16(x, src, o) float x[16]; \
    _Pragma("unroll") for (int i = 0; i < 16; ++i) { float d = 0.f; _Pragma("unroll") for (int k = 0; k < 4; ++k) d = dot2(src[(o) + i][k], qpk[k], d); x[i] = d; } \
    TR_STEP(x, 8, XOR8, 8) TR_STEP(x, 4, XOR7, 4) TR_STEP(x, 2, XOR2, 2)
#define SCALED(v) bfr(bfr(v) * 0.08838834764831845f)
#define PV2(i) _Pragma("unroll") for (int k = 0; k < 4; ++k) { \
      acc[2 * k] = dot2(__builtin_amdgcn_perm(kv[(i) + 1][k], kv[i][k], 0x05040100u), pw[(i) / 2], acc[2 * k]); \
      acc[2 * k + 1] = dot2(__builtin_amdgcn_perm(kv[(i) + 1][k], kv[i][k], 0x07060302u), pw[(i) / 2], acc[2 * k + 1]); }
#define GET_Q() lds_wait<2>(C_Q, 1); u32x4 qpk; \
    asm volatile("ds_read_b128 %0, %2\n\tds_read_b32 %1, %3\n\ts_waitcnt lgkmcnt(0)" : "=&v"(qpk), "=&v"(tg) : "v"((u32)(L_QS + 16 * c)), "v"((u32)(L_CNT + 16)) : "memory"); \
    tg = __builtin_amdgcn_readfirstlane(tg);
#define GET_P() _Pragma("unroll") for (int i4 = 0; i4 < 4; ++i4) { \
      const u32x4 pv = *reinterpret_cast<const u32x4*>(pT + (wave * 4 + r) * 16 + i4 * 4); \
      _Pragma("unroll") for (int j = 0; j < 4; ++j) pw[i4 * 4 + j] = pv[j]; }
    auto gather_q = [&]() __attribute__((always_inline)) {
        u32 offs[2];
        u32x4 gq[2];
#pragma unroll
        for (int e = 0; e < 2; ++e) offs[e] = G_Q + (((2 * (lane >> 5) + e) * 4 + b_att) * 1024 + h_att * 32 + (lane & 31)) * 16;
        {
          gq[0] = ld_gran(rs, offs[0]); gq[1] = ld_gran(rs, offs[1]);
          LDS_INC(C_KI);
          const bool hit = (((gq[0][1] ^ (tg << 12)) | (gq[0][3] ^ (tg << 12)) | (gq[1][1] ^ (tg << 12)) | (gq[1][3] ^ (tg << 12))) < 4096u);
          if (!__all(hit)) gather22<2, 4>(rs, offs, gq, tg);
        }
        STAMPW(10);
        f32x4 q4 = un_g22(gq[0]) + un_g22(gq[1]);
#pragma unroll
        for (int k = 0; k < 4; ++k) q4[k] = swap32_add(q4[k], q4[k]);
        if (lane < 32) *reinterpret_cast<u32x2*>(qs + 2 * lane) = u32x2{cvt2(q4[0], q4[1]), cvt2(q4[2], q4[3])};
        lds_arrive(C_Q, lane);
    };
    const int qw = half ? QODD : QEVEN;
    const u32 tau = half ? (g < 74 ? TAU_A : TAU_B) : (g >= 122 ? TAU_X : TAU_E);
#define PACE(n) if ((n) > PB) { const u32 tt_ = t0 + (((n) - PB) * tau >> 4); \
      SPINLOOP while ((int)((u32)__builtin_amdgcn_s_memrealtime() - tt_) < 0) __builtin_amdgcn_s_sleep(1); asm volatile("" ::: "memory"); }
    float acc[8];
#pragma unroll
    for (int k = 0; k < 8; ++k) acc[k] = 0.f;
    u32 pw[16];
    if (__builtin_expect(wave >= 4, 1)) {
      STAMPT(30, 448);
      const u32 t0 = (u32)__builtin_amdgcn_s_memrealtime();
      BAR();
      STAMPT(31, 448);
      const u32 kvoff2 = kvoff ^ 131072u;
#pragma unroll
      for (int i = 0; i < 48; ++i) {
        if (i == NPRE && half == 0) lds_wait<2>(C_GO, GOPOS ? 3 : 4);
        if (PK && (i & 3) == 0) PACE(i)
        if (i < NL) kv[i < NL ? i : 0] = LDKV(rk, kvoff, i & 31); else kh[i < NL ? 0 : i - NL] = LDKV(rk, kvoff2, i & 31);
      }
      if (!QPRE || wave != qw) LDS_INC(C_KI);
      STAMPT(25, 448);
      if ((QODD >= 4 || QEVEN >= 4) && wave == qw) {
        __builtin_amdgcn_s_setprio(3);
        lds_wait<2>(C_P1, half ? 4 : 2);
        tg = __builtin_amdgcn_readfirstlane(cnt[4]);
        gather_q();
        __builtin_amdgcn_s_setprio(2);
      }
      GET_Q()
      SCORE16(xa, kv, 0)
      asm volatile("" : "+v"(xa[0]), "+v"(xa[1]), "+v"(kvoff) :: "memory");
#pragma unroll
      for (int i = 0; i < P_NVA; ++i) kv[i] = LDKV(rv, kvoff, i);
      asm volatile("" ::: "memory");
      SCORE16(xb, kv, 16)
      xa[2] = xb[0]; xa[3] = xb[1];
      TR_STEP(xa, 2, XOR1, 1)
      float s0 = SCALED(xa[0]), s1 = SCALED(xa[1]);
      asm volatile("" : "+v"(s0), "+v"(s1), "+v"(kvoff) :: "memory");
#pragma unroll
      for (int i = P_NVA; i < NVE; ++i) kv[i] = LDKV(rv, kvoff, i);
      asm volatile("" ::: "memory");
      SCORE16(xh, kh, 0)
      const float s2 = SCALED(xh[0] + dpp<XOR1>(xh[0])), s3 = SCALED(xh[1] + dpp<XOR1>(xh[1]));
      const float m = wave_max(fmaxf(fmaxf(s0, s1), fmaxf(s2, s3)));
      const float p0 = __expf(s0 - m), p1 = __expf(s1 - m), p2 = __expf(s2 - m), p3 = __expf(s3 - m);
      const float l = wave_sum(fmaf(p2 + p3, 0.5f, p0 + p1));
      if (lane == 0) { st[wave] = m; st[8 + wave] = l; }
      pT[(wave * 4 + r) * 16 + (c & 1) * 8 + (c >> 1)] = cvt2(p0, p1);
      pT[((wave - 4) * 4 + r) * 16 + (c >> 1)] = cvt2(p2, p3);
      lds_arrive(C_PP + 4 * (wave - 4), lane);
      __builtin_amdgcn_s_setprio(0);
      STAMPT(27, 448); WSTAMP(1);
      GET_P()
#pragma unroll
      for (int i = NVE; i < NL; i += 2) {
        PACE(96 + i)
        kv[i] = LDKV(rv, kvoff, i);
        kv[i + 1] = LDKV(rv, kvoff, i + 1);
        asm volatile("" ::: "memory");
        if (i == NL - 2) { LDS_INC(C_ISS); STAMPT(26, 448); }
        if (i >= PVLAG) {
          PV2(i >= PVLAG ? i - PVLAG : 0)
#pragma unroll
          for (int k = 0; k < 8; ++k) asm volatile("" : "+v"(acc[k]));
        }
      }
    } else {
      const int ex = g < 74 ? EA : g < 122 ? EB : EC;
      u32 kva = (!half && ex > 2) ? OOB : kvoff, kvb = (!half && ex > 0) ? OOB : kvoff;
      const u32 kxa = (half && ex > 2) ? kvoff - 8 * NL * 1024u : OOB, kxb = (half && ex > 0) ? kvoff - 8 * NL * 1024u : OOB;
      u32x4 kx[4];
      const u32 t0 = __builtin_amdgcn_readfirstlane(cnt[15]);
#pragma unroll
      for (int i = 0; i < 16; ++i) {
        if (PK && (i & 3) == 0) PACE(48 + i)
        kh[i] = LDKV(rk, i < 12 ? kvoff : i < 14 ? kva : kvb, 16 + i);
      }
#pragma unroll
      for (int i = 0; i < 4; ++i) kx[i] = LDKV(rk, i < 2 ? kxa : kxb, 28 + i);
      LDS_INC(C_KI);
      if (wave == 0) {
        if (half == 0 && g >= 80 && g < 120) {
          const int fb = g - 80;
          u32 of2[4];
          u32x4 gm[4];
#pragma unroll
          for (int ks = 0; ks < 4; ++ks) of2[ks] = G_MOD + ((ks * 4 + r) * 960 + 320 + fb * 16 + c) * 16;
          gather22<4, 4>(rs, of2, gm, tg);
          const f32x4 mv = (un_g22(gm[0]) + un_g22(gm[1])) + (un_g22(gm[2]) + un_g22(gm[3]));
          st_gran(rs, G_MF + (((fb / 10) * 4 + r) * 160 + (fb % 10) * 16 + c) * 16, cvt2(mv[0], mv[1]), cvt2(mv[2], mv[3]), tg);
        }
      }
      __builtin_amdgcn_s_setprio(0);
      lds_wait<2>(C_KI, P_GATE);
      if (QHOLD && wave == qw - 4) lds_wait<2>(C_Q, 1);
      asm volatile("" : "+v"(kvoff), "+v"(kva), "+v"(kvb));
#pragma unroll
      for (int i = 0; i < NL; ++i) {
        if (PL && (i & 3) == 0) PACE(64 + i)
        kv[i] = LDKV(rv, i < 28 ? kvoff : i < 30 ? kva : kvb, i);
      }
      STAMPT(1, 0);
      GET_Q()
      float xx[4];
#pragma unroll
      for (int i = 0; i < 4; ++i) { float d = 0.f;
#pragma unroll
        for (int k = 0; k < 4; ++k) d = dot2(kx[i][k], qpk[k], d);
        xx[i] = d; }
      asm volatile("" : "+v"(xx[0]), "+v"(xx[1]), "+v"(xx[2]), "+v"(xx[3]) :: "memory");
#pragma unroll
      for (int i = 0; i < 4; ++i) kx[i] = LDKV(rv, i < 2 ? kxa : kxb, 28 + i);
      LDS_INC(C_ISS);
      TR_STEP(xx, 2, XOR8, 8)
#pragma unroll
      for (int i = 0; i < 2; ++i) { xx[i] += dpp<XOR7>(xx[i]); xx[i] += dpp<XOR2>(xx[i]); xx[i] += dpp<XOR1>(xx[i]); }
      SCORE16(xh, kh, 0)
      const float ninf = -__builtin_inff();
      const bool mine = half || c < 16 - ex, extra = half && c >= 16 - 4 * ex;
      xh[0] += dpp<XOR1>(xh[0]); xh[1] += dpp<XOR1>(xh[1]);
      const float s2 = mine ? SCALED(xh[0]) : ninf, s3 = mine ? SCALED(xh[1]) : ninf;
      const float s4 = extra ? SCALED(xx[0]) : ninf, s5 = extra ? SCALED(xx[1]) : ninf;
      float m = wave_max(fmaxf(fmaxf(s2, s3), fmaxf(s4, s5)));
      lds_wait<2>(C_PP + 4 * wave, 1);
      const float me = st[wave + 4];
      m = fmaxf(m, me);
      const float p2 = __expf(s2 - m), p3 = __expf(s3 - m), p4 = __expf(s4 - m), p5 = __expf(s5 - m);
      const float l = wave_sum(fmaf(p4 + p5, 0.25f, p2 + p3)) * 0.5f;
      const u32 pxo = cvt2(p4, p5), pxt = dppu<XOR8>(pxo);
      const u32 px[2] = {c < 8 ? pxo : pxt, c < 8 ? pxt : pxo};
      if (lane == 0) { st[wave] = m; st[8 + wave] = l; }
      pT[(wave * 4 + r) * 16 + 8 + (c >> 1)] = cvt2(p2, p3);
      STAMPT(4, 0); WSTAMP(1);
      if (lds_ticket(C_SC, lane) == 3) {
        const float mw = st[lane & 7], lw = st[8 + (lane & 7)];
        float M = fmaxf(mw, dpp<XOR1>(mw)); M = fmaxf(M, dpp<XOR2>(M)); M = fmaxf(M, dpp<XOR7>(M));
        const float e = __expf(mw - M);
        float L = lw * e;
        L += dpp<XOR1>(L); L += dpp<XOR2>(L); L += dpp<XOR7>(L);
        if (lane == 0) st_gran(rs, G_ML + u * 64, f2u(M), f2u(L), tg);
        if (lane < 8) st[16 + lane] = e * frcp(L);
      }
      GET_P()
#pragma unroll
      for (int i = 0; i < 16; i += 2) { PV2(i) }
      const float f = __expf(me - m);
#pragma unroll
      for (int k = 0; k < 8; ++k) acc[k] *= f;
#pragma unroll
      for (int i = 16; i < NL - PVLAG; i += 2) { PV2(i) }
#pragma unroll
      for (int i = 0; i < 4; i += 2)
#pragma unroll
        for (int k = 0; k < 4; ++k) {
          acc[2 * k] = dot2(__builtin_amdgcn_perm(kx[i + 1][k], kx[i][k], 0x05040100u), px[i / 2], acc[2 * k]);
          acc[2 * k + 1] = dot2(__builtin_amdgcn_perm(kx[i + 1][k], kx[i][k], 0x07060302u), px[i / 2], acc[2 * k + 1]);
        }
    }
#pragma unroll
    for (int i = NL - PVLAG; i < NL; i += 2) { PV2(i) }
#undef PV2
#undef TR_STEP
#undef SCORE16
#undef SCALED
#undef GET_Q
#undef GET_P
#undef LDKV
#pragma unroll
    for (int i = 0; i < 4; ++i) acc[i] = swap32_add(acc[i], acc[i + 4]);
#pragma unroll
    for (int i = 0; i < 2; ++i) acc[i] = swap16_add(acc[i], acc[i + 2]);
    *reinterpret_cast<float2*>(accs + wave * 128 + c * 8 + 2 * r) = float2{acc[0], acc[1]};
    STAMPT(6, 0); STAMPT(28, 448); WSTAMP(2);
  }
  const int role = half ? (g < 74 ? 0 : 1) : (g >= 122 ? 0 : g < 80 ? 2 : 3);
  const int ia = half ? g : g - 48;
  const int ksA = ia & 7, cbA = ia >> 3;
#define HEADA(hh) ((ksA & 3) + (ksA >> 2) * 16 + (hh) * 4)
  const int iB = g - 74, jB = !JMAP ? iB : iB < 48 ? (iB & ~7) | ((iB & 3) << 1) | ((iB >> 2) & 1) : iB < 50 ? 2 * iB - 48 : iB < 52 ? iB + 2 : 2 * iB - 55;
  if (lds_ticket(C_FIN, lane) == 7) {
    if (lane < 32) {
      f32x4 o = {0.f, 0.f, 0.f, 0.f};
#pragma unroll
      for (int w = 0; w < 8; ++w) o += st[16 + w] * *reinterpret_cast<const f32x4*>(accs + w * 128 + 4 * lane);
      st_gran(rs, G_ATT + (u * 32 + lane) * 16, cvt2(o[0], o[1]), cvt2(o[2], o[3]), tg);
    }
    LDS_INC(C_PUB);
    STAMPW(11);
  }
  u32 gaw[2];
  u32x2 xw = {0u, 0u};
  if (role < 2 && wave < 4) {
    if (role == 0) {
      u32 offs[1] = {G_MF + (r * 160 + cbA * 16 + c) * 16};
      u32x4 gm[1];
      if (ksA == 0) xw = *reinterpret_cast<const u32x2*>(a.x.p + r * DM + cbA * 64 + c * 4);
      gather<1>(rs, offs, gm, tg);
      gaw[0] = gm[0][0]; gaw[1] = gm[0][2];
    } else {
      if (MFPOS == 1) lds_wait<2>(C_ISS, 8);
      u32 offs[5];
      u32x4 gm[5];
#pragma unroll
      for (int i = 0; i < 5; ++i) offs[i] = G_MF + (640 + t + 256 * i) * 16;
      gather<5>(rs, offs, gm, tg);
#pragma unroll
      for (int i = 0; i < 5; ++i) {
        const int it = t + 256 * i, row = it / 160, gi = it % 160;
        u32 w0 = gm[i][0], w1 = gm[i][2];
        if (row >= 4) { w0 = cvt2(1.f + lo16(w0), 1.f + hi16(w0)); w1 = cvt2(1.f + lo16(w1), 1.f + hi16(w1)); }
        *reinterpret_cast<u32x2*>(lds + L_XA + (row < 4 ? row + 8 : row) * XS + gi * 8) = u32x2{w0, w1};
      }
    }
  }
  if (role < 2 && wave < 4) {
    if (half) STAMPT(8, 0);
    lds_wait<2>(C_ISS, 8);
    if (role == 0) {
      const bf16* wp = a.wo + (size_t)(HEADA(wave) * 128 + (lane >> 3)) * DM + cbA * 64;
#pragma unroll
      for (int i = 0; i < 16; ++i) {
        if ((i == N1A && (half || N1A == N1E)) || (i == N1E && !half)) lds_wait<2>(C_PUB, 1);
        dma16(wp + (size_t)(8 * i) * DM + (s128e ^ ((i & 1) * 32)), L_W + wave * 16384 + i * 1024);
      }
      if (APOLL == 2) LDS_INC(C_ISS);
    } else {
      const bf16* wp = a.w1 + (size_t)(wave * 160 + (lane >> 2)) * FF + jB * 32 + ((((((lane >> 1) & 1) ^ (lane >> 5)) << 1) | (lane & 1)) * 8);
#pragma unroll
      for (int i = 0; i < 10; ++i) {
        if (i == N1B) lds_wait<2>(C_PUB, 1);
        dma16(wp + (size_t)(16 * i) * FF, L_W + (wave * 10 + i) * 1024);
      }
      if (N1B == 10) lds_wait<2>(C_PUB, 1);
      const bf16* wp2 = a.w2 + (size_t)(jB * 32 + wave * 8 + (lane >> 3)) * DM + (s128e ^ ((wave & 1) * 32));
#pragma unroll
      for (int k = 0; k < 10; ++k) dma16(wp2 + k * 64, L_WM2 + (k * 4 + wave) * 1024);
    }
    STAMPT(16, 0);
  }
  if (role == 0) {
    const int head = HEADA(wave >> 1), kh = wave & 1;
    s16x8 wf[2][4];
#define WFRAGS() _Pragma("unroll") for (int s = 0; s < 2; ++s) _Pragma("unroll") for (int nt = 0; nt < 4; ++nt) \
      wf[s][nt] = wfrag<512>(L_W + wave * 8192 + (tr128 ^ (nt * 32)) + s * 4096);
    u32 offs[3], va[3], vb[3];
#pragma unroll
    for (int e = 0; e < 2; ++e) offs[e] = G_ATT + ((((r * 32 + head) * 2 + e) * 32) + 16 * kh + c) * 16;
    offs[2] = lane < 8 ? G_ML + (u32)((((c & 3) * 32 + head) * 2 + (c >> 2)) * 64) : OOB;
    if (wave < 4) { VM0(); STAMPT(23, 0); lds_arrive(C_W, lane); }
    lds_wait<1>(C_W, 4);
    STAMPT(19, 0);
    WFRAGS()
    STAMPT(21, 0);
    gather_m<3>(rs, offs, va, vb, tg);
    STAMPT(12, 0); STAMPT(22, 448);
    unsigned char* xa = lds + L_XA + wave * 1280;
#pragma unroll
    for (int e = 0; e < 2; ++e) *reinterpret_cast<u32x2*>(xa + (4 * e + r) * 160 + c * 8) = u32x2{va[e], vb[e]};
    float wsc;
    {
      const float M = u2f(va[2]), L = u2f(vb[2]), Mo = dpp<XOR7>(dpp<0x1B>(M)), Lo = dpp<XOR7>(dpp<0x1B>(L));
      const float Mx = fmaxf(M, Mo), le = L * __expf(M - Mx);
      wsc = le * frcp(fmaf(Lo, __expf(Mo - Mx), le));
      wsc = u2f(__builtin_amdgcn_ds_bpermute((c & 7) * 4, f2u(wsc)));
    }
    f32x4 acc[4];
#pragma unroll
    for (int nt = 0; nt < 4; ++nt) acc[nt] = f32x4{0.f, 0.f, 0.f, 0.f};
#pragma unroll
    for (int s = 0; s < 2; ++s) {
      const u32x4 av = *reinterpret_cast<const u32x4*>(xa + (c & 7) * 160 + s * 64 + r * 16);
#pragma unroll
      for (int nt = 0; nt < 4; ++nt) acc[nt] = MFMA(wf[s][nt], av, acc[nt]);
    }
#pragma unroll
    for (int nt = 0; nt < 4; ++nt) {
#pragma unroll
      for (int k = 0; k < 4; ++k) { const float v = acc[nt][k] * wsc; acc[nt][k] = v + dpp<SHL4>(v); }
      if (c < 4) *reinterpret_cast<f32x4*>(red + ((wave * 4 + c) * 16 + nt * 4 + r) * 4) = acc[nt];
    }
    BAR();
    if (wave < NREP) {
      f32x4 s = {0.f, 0.f, 0.f, 0.f};
#pragma unroll
      for (int w = 0; w < 8; ++w) s += *reinterpret_cast<const f32x4*>(red + (w * 64 + lane) * 4);
      st_gran(rs, G_WO + wave * WO_REP + ((ksA * 4 + r) * 160 + cbA * 16 + c) * 16, cvt2(fmaf(s[0], lo16(gaw[0]), lo16(xw[0])), fmaf(s[1], hi16(gaw[0]), hi16(xw[0]))),
              cvt2(fmaf(s[2], lo16(gaw[1]), lo16(xw[1])), fmaf(s[3], hi16(gaw[1]), hi16(xw[1]))), tg);
    }
    STAMPT(13, 0);

  } else if (role == 1) {
    const int b1 = wave >> 1, ch = wave & 1;
    const float b1v = bf2f(a.b1[jB * 32 + (t & 31)]);
    VM0();
    STAMPT(23, 0);
    BAR();
    STAMPT(12, 0);
    const int kq = wave >> 1, nt = wave & 1;
    s16x8 wf[5], wg[5];
    const u32 tr64 = (8 * r + (c >> 2)) * 64 + ((nt ^ (r & 1)) * 32) + (c & 3) * 8;
#pragma unroll
    for (int s = 0; s < 5; ++s) wf[s] = wfrag<256>(L_W + kq * 10240 + tr64 + s * 2048);
#pragma unroll
    for (int k = 0; k < 5; ++k) { const int n2 = wave * 5 + k; wg[k] = wfrag<512>(L_WM2 + (n2 >> 2) * 4096 + (tr128 ^ ((n2 & 3) * 32))); }
    {
      f32x4 acc = {0.f, 0.f, 0.f, 0.f};
#pragma unroll
      for (int s = 0; s < 5; ++s) {
        const u32x4 av = *reinterpret_cast<const u32x4*>(lds + L_XA + (4 + (c & 7)) * XS + kq * 320 + s * 64 + r * 16);
        acc = MFMA(wf[s], av, acc);
      }
      if (c < 8) *reinterpret_cast<f32x4*>(red + ((wave * 8 + c) * 4 + r) * 4) = acc;
    }
    float c1f[5];
#pragma unroll
    for (int i = 0; i < 5; ++i) {
      const int col = (ch * 80 + 16 * i + c) * 4 + r;
      c1f[i] = u2f((u32)*reinterpret_cast<const unsigned short*>(lds + L_XA + (4 + b1) * XS + col * 2) << 16);
    }
    BAR();
    const int fb = (t >> 5) & 3, fc = t & 31;
    float Tc = 0.f, Ts = 0.f;
#pragma unroll
    for (int k = 0; k < 4; ++k) {
      Tc += red[((2 * k + (fc >> 4)) * 8 + fb) * 16 + (fc & 15)];
      Ts += red[((2 * k + (fc >> 4)) * 8 + 4 + fb) * 16 + (fc & 15)];
    }
#pragma unroll
    for (int i = 0; i < 5; ++i) asm volatile("" : "+v"(c1f[i]));
    asm volatile("" : "+v"(Tc), "+v"(Ts));
    u32x4 sel;
#pragma unroll
    for (int d = 0; d < 4; ++d) sel[d] = ((c & 3) == 0 && (d & 1) == (c >> 3)) ? ((c & 4) ? 0x3f800000u : 0x00003f80u) : 0u;
    u32 a_raw = L_W + (((wave >> 1) * 4 + r) * 160 + c) * 16 + (wave & 1) * 8;
    u32 a_sum = L_W + ((r * 4 + b1) * 160 + ch * 80 + c) * 16;
    u32 a_u = L_XA + b1 * XS + ((ch * 80 + c) * 4 + r) * 2;
    u32 a_av = L_XA + (c & 3) * XS + kq * 320 + r * 16;
    u32 a_red = L_RED + ((wave * 4 + c) * 4 + r) * 16;
    u32 a_fin = L_RED + (((fc >> 4) * 4 + fb) * 16 + (fc & 15)) * 4;
    u32 a_out = c < 4 ? G_OUT + (((wave * 20 + r) * 54 + jB) * 4 + c) * 16 : OOB;
    u32 a_fi = L_FI + (c & 3) * 64 + r * 16;
    asm volatile("" : "+v"(sel), "+v"(a_raw), "+v"(a_sum), "+v"(a_u), "+v"(a_av), "+v"(a_red), "+v"(a_fin), "+v"(a_out), "+v"(a_fi));
    STAMPT(18, 0);
    u32x4 pa[10];
    {
      const u32 go = G_WO + (jB & (NREP - 1)) * WO_REP + ((wave * 4 + r) * 160 + c) * 16;
      u32 pend = 0x3ffu;
      SPINLOOP for (unsigned spins = 0; spins < SPIN_CAP; ++spins) {
        const u32x4 sg = ld_gran(rs, lane == 0 ? go + (jB % 10) * 256 : OOB);
        if (__builtin_amdgcn_readfirstlane(sg[1]) == tg) break;
        __builtin_amdgcn_s_sleep(2);
      }
      SPINLOOP for (unsigned spins = 0; spins < SPIN_CAP; ++spins) {
        asm volatile("" ::: "memory");
#pragma unroll
        for (int j = 0; j < 10; ++j) if (pend & (1u << j)) pa[j] = ld_gran(rs, go + j * 256);
#pragma unroll
        for (int j = 0; j < 10; ++j) if (pend & (1u << j)) {
          asm volatile("ds_write2_b32 %0, %1, %2 offset0:%3 offset1:%4" :: "v"(a_raw + (j >> 2) * 1024), "v"(pa[j][0]), "v"(pa[j][2]), "i"((j & 3) * 64), "i"((j & 3) * 64 + 1) : "memory");
          if (__all((pa[j][1] == tg) & (pa[j][3] == tg))) pend &= ~(1u << j);
        }
        if (!pend) break;
      }
    }
    STAMPT(14, 0); WSTAMP(3);
    BAR();
    STAMPT(29, 0);
    float ps = 0.f, pq = 0.f;
    {
      u32x4 v[5];
#pragma unroll
      for (int i = 0; i < 5; ++i) v[i] = *reinterpret_cast<const u32x4*>(lds + a_sum + i * 256);
      asm volatile("" : "+v"(v[0]), "+v"(v[1]), "+v"(v[2]), "+v"(v[3]), "+v"(v[4]));
      f32x4 sm[5];
#pragma unroll
      for (int i = 0; i < 5; ++i) { const f32x4 z = {0.f, 0.f, 0.f, 0.f}; sm[i] = MFMA(sel, v[i], z); }
#pragma unroll
      for (int i = 0; i < 5; ++i) {
        const float x1 = bfr(sm[i][0]);
        ps += x1; pq = fmaf(x1, x1, pq);
        const u32 uw = cvt2(x1 * c1f[i], 0.f);
        *reinterpret_cast<unsigned short*>(lds + a_u + i * 128) = (unsigned short)uw;
      }
    }
    BAR();
    STAMPT(19, 0);
    {
      u32x4 av[5];
#pragma unroll
      for (int s = 0; s < 5; ++s) av[s] = *reinterpret_cast<const u32x4*>(lds + a_av + s * 64);
      const float t1 = ps + dpp<XOR8>(ps), t2 = pq + dpp<XOR8>(pq);
      float z = (c & 8) ? t2 : t1;
      asm volatile("" : "+v"(av[0]), "+v"(av[1]), "+v"(av[2]), "+v"(av[3]), "+v"(av[4]), "+v"(z));
      f32x4 acc = {0.f, 0.f, 0.f, 0.f};
#pragma unroll
      for (int s = 0; s < 5; ++s) acc = MFMA(wf[s], av[s], acc);
      z += dpp<XOR7>(z); z += dpp<XOR2>(z); z += dpp<XOR1>(z);
      z = cross_rows_sum(z);
      if ((lane & 55) == 0) st[24 + 2 * wave + (lane >> 3)] = z;
      if (c < 4) *reinterpret_cast<f32x4*>(lds + a_red) = acc;
    }
    BAR();
    STAMPT(24, 0);
    if (t < 128) {
      const f32x4 sv = *reinterpret_cast<const f32x4*>(st + 24 + 4 * fb);
      float s = 0.f;
#pragma unroll
      for (int k = 0; k < 4; ++k) s += *reinterpret_cast<const float*>(lds + a_fin + k * 512);
      const float mean = (sv[0] + sv[2]) * (1.f / DM);
      const float var = fmaxf((sv[1] + sv[3]) * (1.f / DM) - mean * mean, 0.f);
      const float rstd = __builtin_amdgcn_rsqf(var + 1e-5f);
      const float hh = bfr(bfr(fmaf(rstd, fmaf(-mean, Tc, s), Ts)) + b1v);
      const float e2 = __builtin_amdgcn_exp2f(hh * fmaf(hh * hh, 0.1029432397f, 2.302208198f));
      const float f = fmaf(-hh, frcp(e2 + 1.f), hh);
      *reinterpret_cast<unsigned short*>(lds + L_FI + t * 2) = (unsigned short)cvt2(f, 0.f);
    }
    BAR();
    {
      const u32x4 av = *reinterpret_cast<const u32x4*>(lds + a_fi);
      f32x4 acc[5];
#pragma unroll
      for (int k = 0; k < 5; ++k) { const f32x4 z = {0.f, 0.f, 0.f, 0.f}; acc[k] = MFMA(wg[k], av, z); }
#pragma unroll
      for (int k = 0; k < 5; ++k) st_gran(rs, a_out + k * (4 * 54 * 64), cvt2(acc[k][0], acc[k][1]), cvt2(acc[k][2], acc[k][3]), tg);
    }
    STAMPT(15, 0);

  } else if (role == 2 && wave < 2) {
    const int cg = g * 2 + wave, b = lane & 3, jj = lane >> 2, col = cg * 4 + r;
    const float b2v = bf2f(a.b2[col]);
    float x1v, gbv;
    {
      u32 offs[1], va[1], vb[1];
      offs[0] = jj < 8 ? G_WO + (g & (NREP - 1)) * WO_REP + ((jj * 4 + b) * 160 + cg) * 16 : jj == 8 ? G_MF + ((12 + b) * 160 + cg) * 16 : OOB;
      gather_m<1>(rs, offs, va, vb, tg);
      const u32 gbw0 = __builtin_amdgcn_ds_bpermute((32 + b) * 4, va[0]), gbw1 = __builtin_amdgcn_ds_bpermute((32 + b) * 4, vb[0]);
      if (jj >= 8) { va[0] = 0; vb[0] = 0; }
      float s = swap16_add(swap32_add(lo16(va[0]), lo16(vb[0])), swap32_add(hi16(va[0]), hi16(vb[0])));
      s += dpp<ROR4>(s); s += dpp<XOR8>(s);
      const u32 gbw = (r & 2) ? gbw1 : gbw0;
      gbv = (r & 1) ? hi16(gbw) : lo16(gbw);
      x1v = bfr(s);
    }
    STAMPT(23, 0);
    u32 offs[4], va[4], vb[4];
#pragma unroll
    for (int i = 0; i < 4; ++i) offs[i] = (jj + 16 * i < 54) ? G_OUT + ((cg * 54 + jj + 16 * i) * 4 + b) * 16 : OOB;
    gather_m<4>(rs, offs, va, vb, tg);
    STAMPT(16, 0);
    float s0 = lo16(va[0]), s1 = hi16(va[0]), s2 = lo16(vb[0]), s3 = hi16(vb[0]);
#pragma unroll
    for (int i = 1; i < 4; ++i) { s0 = addlo(va[i], s0); s1 = addhi(va[i], s1); s2 = addlo(vb[i], s2); s3 = addhi(vb[i], s3); }
    float s = swap16_add(swap32_add(s0, s2), swap32_add(s1, s3));
    s += dpp<ROR4>(s); s += dpp<XOR8>(s);
    const float y = bfr(bfr(s) + b2v);
    if ((lane & 12) == 0) a.out[b * DM + col] = __float2bfloat16(x1v + bfr(gbv * y));
    STAMPT(17, 0);

    if (u == 0 && t == 0)
      __hip_atomic_store(reinterpret_cast<u32*>(a.ws + WS_EPOCH), tg, __ATOMIC_RELAXED, __HIP_MEMORY_SCOPE_AGENT);
  }
  STAMP_FLUSH;
}

extern "C" void kernel_launch(void* const* d_in, const int* in_sizes, int n_in, void* d_out, int out_size, void* d_ws,
                              size_t ws_size, hipStream_t stream) {
  Args a;
  a.x = Desc{(const bf16*)d_in[0], NB * DM * 2, 0x00020000u};
  a.kc = (const bf16*)d_in[1];
  a.vc = (const bf16*)d_in[2];
  a.wq = Desc{(const bf16*)d_in[3], DM * QD * 2, 0x00020000u};
  a.wo = (const bf16*)d_in[4];
  a.w1 = (const bf16*)d_in[6];
  a.b1 = (const bf16*)d_in[7];
  a.w2 = (const bf16*)d_in[8];
  a.b2 = (const bf16*)d_in[9];
  a.c = Desc{(const bf16*)d_in[10], NB * DM * 2, 0x00020000u};
  a.wmod = Desc{(const bf16*)d_in[11], DM * MODN * 2, 0x00020000u};
  a.out = (bf16*)d_out;
  a.ws = (unsigned char*)d_ws;
  mega_kernel<<<NWG, NT, 0, stream>>>(a);
}
```

```cpp
#define SPINLOOP _Pragma("nounroll")
#define P_LAG 14
#define P_NPRE 32
#define P_NVE 6
#define P_NVA 6
#define QODD 4
#define QEVEN 4
#define MFPOS 1
#define EA 0
#define EB 0
#define EC 4
#define APOLL 0
#define N1E N1A
#define TAU_A 234
#define TAU_B TAU_A
#define TAU_X TAU_A
#define TAU_E 247
#define PK 0
#define PL 1
#define QPRE 1
#define QHOLD 0
#define CT0 1
#define TOFF 90
#define BLIGHT 1
#define BPRE 0
#define BW1 1
#define BMASK 1
#define CSLEEP 1
#define NREPL 4
#define OUTPRE 1
#define ASL 1
#define AEARLY 1
#define BT 0
#define BSL 2
#define PB 0
#define GOPOS 0
#define JMAP 1
#define N1A 6
#define N1B 0
#define PT 0
#define PTAUX 16
#define P_GATE 8
#include <hip/hip_runtime.h>
#include <hip/hip_bf16.h>
#include <stdint.h>

using bf16 = __hip_bfloat16;
using u32 = unsigned;
using u64 = unsigned long long;
using u32x2 = __attribute__((ext_vector_type(2))) unsigned;
using u32x4 = __attribute__((ext_vector_type(4))) unsigned;
using f32x4 = __attribute__((ext_vector_type(4))) float;
using bf16x2 = __attribute__((ext_vector_type(2))) __bf16;
typedef __attribute__((ext_vector_type(2))) float f32x2;
typedef short s16x8 __attribute__((ext_vector_type(8)));
typedef short s16x4 __attribute__((ext_vector_type(4)));
typedef __attribute__((address_space(3))) s16x4* lds_s4p;
typedef __attribute__((ext_vector_type(8))) __bf16 bf16x8v;

constexpr int NB = 4, DM = 640, NH = 32, DH = 128, SK = 8192, WIN = 2048, FF = 1728, QD = 4096, MODN = 3840;
constexpr int NT = 512, NWG = 256;

constexpr u32 WS_EPOCH = 0;
constexpr u32 G_T = 128;
constexpr u32 G_MOD = 256;
constexpr u32 G_Q = G_MOD + 16 * 960 * 16;
constexpr u32 G_MF = G_Q + 16 * 1024 * 16;
constexpr u32 G_ML = G_MF + 16 * 160 * 16;
constexpr u32 EMPTY = 0xffffffffu;
constexpr int NREP = NREPL;
constexpr u32 G_WO = G_ML + 256 * 64;
constexpr u32 WO_REP = 40 * 1024;
constexpr u32 G_ATT = G_WO + NREP * WO_REP;
constexpr u32 G_OUT = G_ATT + 256 * 256;
constexpr u32 WS_END = G_OUT + 40 * 54 * 128;
constexpr int NCH = (G_OUT - G_WO) / 2048;
constexpr u32 OOB = 0xF0000000u;

#define WSTAMP(k) do {} while (0)
#define STAMPW(i) do {} while (0)
#define STAMPT(i, tt) do {} while (0)
#define STAMP_DECL
#define STAMP_FLUSH do {} while (0)
#define BAR() asm volatile("s_waitcnt lgkmcnt(0)\n\ts_barrier" ::: "memory")
#define VM0() asm volatile("s_waitcnt vmcnt(0)" ::: "memory")

__device__ __forceinline__ float bfr(float x) { return __bfloat162float(__float2bfloat16(x)); }
__device__ __forceinline__ float bf2f(bf16 x) { return __bfloat162float(x); }
__device__ __forceinline__ float lo16(unsigned u) { return __builtin_bit_cast(float, u << 16); }
__device__ __forceinline__ float hi16(unsigned u) { return __builtin_bit_cast(float, u & 0xffff0000u); }
__device__ __forceinline__ u32 f2u(float f) { return __builtin_bit_cast(u32, f); }
__device__ __forceinline__ float u2f(u32 f) { return __builtin_bit_cast(float, f); }
__device__ __forceinline__ u32 cvt2(float a, float b) {
  const f32x2 v = {a, b};
  return __builtin_bit_cast(u32, __builtin_convertvector(v, bf16x2));
}
__device__ __forceinline__ float frcp(float x) { return __builtin_amdgcn_rcpf(x); }
__device__ __forceinline__ float fsqrt(float x) { return __builtin_amdgcn_sqrtf(x); }
__device__ __forceinline__ float fexp(float x) { return __builtin_amdgcn_exp2f(x * 1.4426950408889634f); }
__device__ __forceinline__ float dot2(unsigned k, unsigned q, float acc) {
  return __builtin_amdgcn_fdot2_f32_bf16(__builtin_bit_cast(bf16x2, k), __builtin_bit_cast(bf16x2, q), acc, false);
}
__device__ __forceinline__ float addlo(unsigned w, float acc) { u32 k = 0x00003f80u; asm("" : "+s"(k)); return dot2(w, k, acc); }
__device__ __forceinline__ float addhi(unsigned w, float acc) { return dot2(w, 0x3f800000u, acc); }
template <int CTRL> __device__ __forceinline__ float dpp(float x) {
  return __builtin_bit_cast(float, __builtin_amdgcn_mov_dpp(__builtin_bit_cast(int, x), CTRL, 0xf, 0xf, true));
}
template <int CTRL> __device__ __forceinline__ u32 dppu(u32 x) { return (u32)__builtin_amdgcn_mov_dpp((int)x, CTRL, 0xf, 0xf, true); }
constexpr int XOR1 = 0xB1, XOR2 = 0x4E, XOR7 = 0x141, XOR8 = 0x128, ROR4 = 0x124, ROR2 = 0x122, ROR1 = 0x121, SHL4 = 0x104;
__device__ __forceinline__ float swap32_add(float a, float b) {
  auto s = __builtin_amdgcn_permlane32_swap(f2u(a), f2u(b), false, false);
  return u2f(s[0]) + u2f(s[1]);
}
__device__ __forceinline__ float swap16_add(float a, float b) {
  auto s = __builtin_amdgcn_permlane16_swap(f2u(a), f2u(b), false, false);
  return u2f(s[0]) + u2f(s[1]);
}
__device__ __forceinline__ float cross_rows_sum(float x) { x = swap16_add(x, x); return swap32_add(x, x); }
__device__ __forceinline__ float cross_rows_max(float x) {
  auto s = __builtin_amdgcn_permlane16_swap(f2u(x), f2u(x), false, false);
  x = fmaxf(u2f(s[0]), u2f(s[1]));
  auto s2 = __builtin_amdgcn_permlane32_swap(f2u(x), f2u(x), false, false);
  return fmaxf(u2f(s2[0]), u2f(s2[1]));
}
__device__ __forceinline__ float wave_sum(float x) {
  x += dpp<XOR8>(x); x += dpp<ROR4>(x); x += dpp<ROR2>(x); x += dpp<ROR1>(x);
  return cross_rows_sum(x);
}
__device__ __forceinline__ float wave_max(float x) {
  x = fmaxf(x, dpp<XOR8>(x)); x = fmaxf(x, dpp<ROR4>(x)); x = fmaxf(x, dpp<ROR2>(x)); x = fmaxf(x, dpp<ROR1>(x));
  return cross_rows_max(x);
}

__device__ __forceinline__ void st_gran(__amdgpu_buffer_rsrc_t rs, u32 off, u32 a, u32 b, u32 tg) {
  u32x4 v = {a, tg, b, tg};
  __builtin_amdgcn_raw_buffer_store_b128(v, rs, off, 0, 16);
}
__device__ __forceinline__ u32x4 ld_gran(__amdgpu_buffer_rsrc_t rs, u32 off) {
  return __builtin_amdgcn_raw_buffer_load_b128(rs, off, 0, 16);
}
constexpr unsigned SPIN_CAP = 1u << 18;
template <int N>
__device__ __forceinline__ void gather(__amdgpu_buffer_rsrc_t rs, const u32 (&offs)[N], u32x4 (&g)[N], u32 tg) {
  SPINLOOP for (unsigned spins = 0; spins < SPIN_CAP; ++spins) {
    bool ok = true;
#pragma unroll
    for (int i = 0; i < N; ++i) g[i] = ld_gran(rs, offs[i]);
#pragma unroll
    for (int i = 0; i < N; ++i) ok &= (offs[i] == OOB) | ((g[i][1] == tg) & (g[i][3] == tg));
    if (__all(ok)) break;
    __builtin_amdgcn_s_sleep(1);
  }
}
template <int N>
__device__ __forceinline__ void gather_m(__amdgpu_buffer_rsrc_t rs, u32 (&offs)[N], u32 (&va)[N], u32 (&vb)[N], u32 tg) {
#pragma unroll
  for (int i = 0; i < N; ++i) { va[i] = 0; vb[i] = 0; }
  SPINLOOP for (unsigned spins = 0; spins < SPIN_CAP; ++spins) {
    u32x4 g[N];
#pragma unroll
    for (int i = 0; i < N; ++i) g[i] = ld_gran(rs, offs[i]);
    bool ok = true;
#pragma unroll
    for (int i = 0; i < N; ++i) {
      const bool hit = (offs[i] != OOB) & (g[i][1] == tg) & (g[i][3] == tg);
      va[i] = hit ? g[i][0] : va[i]; vb[i] = hit ? g[i][2] : vb[i];
      offs[i] = hit ? OOB : offs[i];
      ok &= (offs[i] == OOB);
    }
    if (__all(ok)) break;
    __builtin_amdgcn_s_sleep(1);
  }
}

__device__ __forceinline__ bool missing(u32x4 v) { return max(max(v[0], v[1]), max(v[2], v[3])) == EMPTY; }
#define LK(v, o, so, imm, m) "s_mov_b64 exec, %" #m "\n\tbuffer_load_dwordx4 %" #v ", %" #o ", %[rs], " so " offen offset:" #imm " sc1\n\t"
#define LKEND "s_mov_b64 exec, -1\n\ts_waitcnt vmcnt(0)"
template <int N> __device__ __forceinline__ void look(u32x4 rs, u32 off, u32x4 (&v)[N], const u64 (&m)[N]);
template <> __device__ __forceinline__ void look<1>(u32x4 rs, u32 off, u32x4 (&v)[1], const u64 (&m)[1]) {
  asm volatile(LK(0, 1, "0", 0, 2) LKEND : "+v"(v[0]) : "v"(off), "s"(m[0]), [rs] "s"(rs) : "memory");
}
template <> __device__ __forceinline__ void look<5>(u32x4 rs, u32 off, u32x4 (&v)[5], const u64 (&m)[5]) {
  asm volatile(LK(0, 5, "0", 0, 6) LK(1, 5, "0", 1024, 7) LK(2, 5, "0", 2048, 8) LK(3, 5, "0", 3072, 9) LK(4, 5, "%11", 0, 10) LKEND
               : "+v"(v[0]), "+v"(v[1]), "+v"(v[2]), "+v"(v[3]), "+v"(v[4])
               : "v"(off), "s"(m[0]), "s"(m[1]), "s"(m[2]), "s"(m[3]), "s"(m[4]), "s"(4096u), [rs] "s"(rs) : "memory");
}
template <> __device__ __forceinline__ void look<7>(u32x4 rs, u32 off, u32x4 (&v)[7], const u64 (&m)[7]) {
  asm volatile(LK(0, 7, "0", 0, 8) LK(1, 7, "0", 1024, 9) LK(2, 7, "0", 2048, 10) LK(3, 7, "0", 3072, 11)
               LK(4, 7, "%15", 0, 12) LK(5, 7, "%15", 1024, 13) LK(6, 7, "%15", 2048, 14) LKEND
               : "+v"(v[0]), "+v"(v[1]), "+v"(v[2]), "+v"(v[3]), "+v"(v[4]), "+v"(v[5]), "+v"(v[6])
               : "v"(off), "s"(m[0]), "s"(m[1]), "s"(m[2]), "s"(m[3]), "s"(m[4]), "s"(m[5]), "s"(m[6]), "s"(4096u), [rs] "s"(rs) : "memory");
}
template <int N, int SLEEP>
__device__ __forceinline__ void gather_e(u32x4 rs, u32 off, u32x4 (&v)[N], u64 last) {
  u64 m[N];
#pragma unroll
  for (int i = 0; i < N; ++i) { m[i] = i < N - 1 ? ~0ull : last; v[i] = u32x4{0u, 0u, 0u, 0u}; }
  SPINLOOP for (unsigned spins = 0; spins < SPIN_CAP; ++spins) {
    look<N>(rs, off, v, m);
    u64 bad = 0;
#pragma unroll
    for (int i = 0; i < N; ++i) { m[i] = __builtin_amdgcn_ballot_w64(missing(v[i])); bad |= m[i]; }
    if (!bad) break;
    if (SLEEP) __builtin_amdgcn_s_sleep(SLEEP);
  }
}

__device__ __forceinline__ void st_g22(__amdgpu_buffer_rsrc_t rs, u32 off, f32x4 v, u32 tg) {
  const u32 a0 = f2u(v[0]) + 0x200u, a1 = f2u(v[1]) + 0x200u, a2 = f2u(v[2]) + 0x200u, a3 = f2u(v[3]) + 0x200u;
  const u32x4 w = {(a0 >> 10) | ((a1 << 12) & 0xffc00000u), (a1 >> 20) | (tg << 12), (a2 >> 10) | ((a3 << 12) & 0xffc00000u), (a3 >> 20) | (tg << 12)};
  __builtin_amdgcn_raw_buffer_store_b128(w, rs, off, 0, 16);
}
__device__ __forceinline__ f32x4 un_g22(u32x4 w) {
  return f32x4{u2f(w[0] << 10), u2f(__builtin_amdgcn_alignbit(w[1], w[0], 22) << 10), u2f(w[2] << 10), u2f(__builtin_amdgcn_alignbit(w[3], w[2], 22) << 10)};
}
template <int N, int SLEEP>
__device__ __forceinline__ void gather22(__amdgpu_buffer_rsrc_t rs, u32 (&offs)[N], u32x4 (&v)[N], u32 tg) {
#pragma unroll
  for (int i = 0; i < N; ++i) v[i] = u32x4{0u, 0u, 0u, 0u};
  SPINLOOP for (unsigned spins = 0; spins < SPIN_CAP; ++spins) {
    u32x4 g[N];
#pragma unroll
    for (int i = 0; i < N; ++i) g[i] = ld_gran(rs, offs[i]);
    bool ok = true;
#pragma unroll
    for (int i = 0; i < N; ++i) {
      const bool hit = (offs[i] != OOB) & (((g[i][1] ^ (tg << 12)) | (g[i][3] ^ (tg << 12))) < 4096u);
#pragma unroll
      for (int k = 0; k < 4; ++k) v[i][k] = hit ? g[i][k] : v[i][k];
      offs[i] = hit ? OOB : offs[i];
      ok &= (offs[i] == OOB);
    }
    if (__all(ok)) break;
    __builtin_amdgcn_s_sleep(SLEEP);
  }
}

__device__ __forceinline__ u32 next_tag(u32 ep) { const u32 t = ep + 1u; return (t & 0xfffffu) ? t : t + 1u; }

__device__ __forceinline__ void lds_arrive(u32 addr, int lane) {
  asm volatile("s_waitcnt lgkmcnt(0)" ::: "memory");
  if (lane == 0) asm volatile("ds_add_u32 %0, %1" :: "v"(addr), "v"(1u) : "memory");
}
__device__ __forceinline__ u32 lds_ticket(u32 addr, int lane) {
  u32 r = 0;
  asm volatile("s_waitcnt lgkmcnt(0)" ::: "memory");
  if (lane == 0) asm volatile("ds_add_rtn_u32 %0, %1, %2\n\ts_waitcnt lgkmcnt(0)" : "=v"(r) : "v"(addr), "v"(1u) : "memory");
  return __builtin_amdgcn_readfirstlane(r);
}
template <int SLEEP>
__device__ __forceinline__ void lds_wait(u32 addr, u32 target) {
  SPINLOOP for (unsigned spins = 0; spins < (1u << 22); ++spins) {
    u32 v;
    asm volatile("ds_read_b32 %0, %1\n\ts_waitcnt lgkmcnt(0)" : "=v"(v) : "v"(addr) : "memory");
    if (v >= target) break;
    __builtin_amdgcn_s_sleep(SLEEP);
  }
}
__device__ __forceinline__ void dma16(const void* gsrc, u32 lds_dst) {
  u32 keep;
  asm volatile("s_mov_b32 %0, m0\n\ts_mov_b32 m0, %2\n\ts_nop 0\n\tglobal_load_lds_dwordx4 %1, off\n\ts_mov_b32 m0, %0"
               : "=&s"(keep) : "v"(gsrc), "s"(lds_dst) : "memory");
}
__device__ __forceinline__ void dma16nt(const void* gsrc, u32 lds_dst) {
  u32 keep;
  asm volatile("s_mov_b32 %0, m0\n\ts_mov_b32 m0, %2\n\ts_nop 0\n\tglobal_load_lds_dwordx4 %1, off nt\n\ts_mov_b32 m0, %0"
               : "=&s"(keep) : "v"(gsrc), "s"(lds_dst) : "memory");
}
__device__ __forceinline__ u32x4 ldasm(const void* p) {
  u32x4 v;
  asm volatile("global_load_dwordx4 %0, %1, off" : "=v"(v) : "v"(p) : "memory");
  return v;
}
__device__ __forceinline__ u32 ldasm_sc1(const void* p) {
  u32 v;
  asm volatile("global_load_dword %0, %1, off sc1" : "=v"(v) : "v"(p) : "memory");
  return v;
}
__device__ __forceinline__ u32x4 ldw(const bf16* p) { return *reinterpret_cast<const u32x4*>(p); }
template <int D2>
__device__ __forceinline__ s16x8 wfrag(u32 addr) {
  const s16x4 lo = __builtin_amdgcn_ds_read_tr16_b64_v4i16((lds_s4p)(uintptr_t)addr);
  const s16x4 hi = __builtin_amdgcn_ds_read_tr16_b64_v4i16((lds_s4p)(uintptr_t)(addr + D2));
  return s16x8{lo[0], lo[1], lo[2], lo[3], hi[0], hi[1], hi[2], hi[3]};
}
#define MFMA(w_, x_, c_) __builtin_amdgcn_mfma_f32_16x16x32_bf16(__builtin_bit_cast(bf16x8v, w_), __builtin_bit_cast(bf16x8v, x_), c_, 0, 0, 0)

struct Desc { const bf16* p; u32 bytes, flags; };
struct Args {
  Desc wq, x, wmod, c;
  unsigned char* ws;
  const bf16 *kc, *vc, *wo, *w1, *b1, *w2, *b2;
  bf16* out;
};

constexpr int L_W = 0;
constexpr int L_WM2 = 40960;
constexpr int L_XA = 81920;
constexpr int XS = 1312;
constexpr int L_RED = L_XA + 16384;
constexpr int L_MOD = L_RED + 8192;
constexpr int L_XR = L_MOD + 5120;
constexpr int L_PT = L_XR + 5120;
constexpr int L_ACC = L_PT + 2048;
constexpr int L_QS = L_ACC + 4096;
constexpr int L_FI = L_QS + 256;
constexpr int L_ST = L_FI + 256;
constexpr int L_CNT = L_ST + 256;
constexpr int L_END = L_CNT + 64;
static_assert(L_END <= 160 * 1024, "LDS");
constexpr u32 C_Q = L_CNT, C_SC = L_CNT + 4, C_ISS = L_CNT + 8, C_P1 = L_CNT + 20, C_P2 = L_CNT + 24, C_GO = L_CNT + 28, C_W = L_CNT + 12, C_FIN = L_CNT + 32,
              C_KI = L_CNT + 36, C_PP = L_CNT + 40, C_PUB = L_CNT + 56;

constexpr int NL = 32;
constexpr int PVLAG = P_LAG;
constexpr int NPRE = P_NPRE;
constexpr int NVE = P_NVE;
static_assert(PVLAG <= 16 && (PVLAG & 1) == 0 && NVE <= PVLAG && (NVE & 1) == 0, "row groups");

__global__ void __launch_bounds__(NT, 2) mega_kernel(Args a) {
  __shared__ __attribute__((aligned(1024))) unsigned char lds[L_END];
  float* red = reinterpret_cast<float*>(lds + L_RED);
  u32* xraw = reinterpret_cast<u32*>(lds + L_XR);
  u32* pT = reinterpret_cast<u32*>(lds + L_PT);
  float* accs = reinterpret_cast<float*>(lds + L_ACC);
  u32* qs = reinterpret_cast<u32*>(lds + L_QS);
  float* st = reinterpret_cast<float*>(lds + L_ST);
  u32* cnt = reinterpret_cast<u32*>(lds + L_CNT);

  u32x4 rw, rx;
  unsigned char* wsq;
  asm volatile("s_load_dwordx4 %0, %3, %4\n\ts_load_dwordx4 %1, %3, %4 offset:16\n\ts_load_dwordx2 %2, %3, 64"
               : "=&s"(rw), "=&s"(rx), "=&s"(wsq) : "s"(__builtin_amdgcn_kernarg_segment_ptr()), "s"((blockIdx.x & 1) * 32));
  __builtin_amdgcn_sched_barrier(0);
  const int t = threadIdx.x, lane = t & 63;
  const int wave = __builtin_amdgcn_readfirstlane(t >> 6);
  const int u = blockIdx.x;
  const int bh = u >> 1, half = u & 1, b_att = bh >> 5, h_att = bh & 31;
  const int r = lane >> 4, c = lane & 15;
  STAMP_DECL
  STAMPT(0, 0);
  u32 tg;
  const int g = u >> 1, ksp = g & 3, cbp = g >> 2;
  const int s128e = (((((lane & 7) >> 1) ^ ((lane >> 4) & 1)) << 1) | (lane & 1)) * 8;
  const u32 tr128 = (8 * r + (c >> 2)) * 128 + ((((c >> 3) & 1) | ((r & 1) << 1)) * 32) + (c & 3) * 8;
  if (wave < 4) {
    __builtin_amdgcn_s_setprio(3);
    const u32 rb = 8192 - 512 * half;
    const u32 hm = 0u - (u32)half;
    const u32 vo = (u32)(lane >> (4 - half)) * rb + (((lane & (15 >> half)) ^ ((lane >> 3) & (6 - 4 * half))) ^ (half ? (wave & 1) * 4 : (wave >> 1) * 8)) * 16;
    u32 so = (u32)(ksp * 160 + (half ? 8 * (wave & 1) : 4 * wave)) * rb + (cbp << (8 - half)) + ((half && wave < 2 && g < 112) ? 4096 : 0);
    const u32 step = 16 * rb, dstep = 4096 >> half, dst = L_W + wave * 1024 + (half ? (wave < 2 ? 20480 : -2048) : 0);
    const u32 oe = wave * 1280 + lane * 16, oo = (r * 1280 + ksp * 320 + (c + 16 * wave) * 16) | ((u32)(19 - c - 16 * wave) & OOB);
    const u32 oa = (oo & hm) | (oe & ~hm), ob = (wave * 1280 + 1024 + c * 16) | (hm & OOB), oc = oe | (hm & OOB), zero = 0;
    u32 ep, keep;
    u32x4 xv[4];
    const u32 pre = ((u32)u << 31) & (u32)(g - (NCH < 80 ? NCH : 80)) & (u32)(1 - wave), po = G_WO + g * 2048 + (t & 127) * 16;
    const u32 pre2 = g - (NCH - 80), po2 = po + 80 * 2048;
    const u32x4 emp = {EMPTY, EMPTY, EMPTY, EMPTY};
#define DMA1 "buffer_load_dwordx4 %7, %8, %1 offen nt lds\n\t"
#define DMAN "s_add_u32 m0, m0, %11\n\ts_add_u32 %1, %1, %10\n\t" DMA1
    asm volatile("s_mov_b32 %0, m0\n\ts_mov_b32 m0, %9\n\t"
                 "s_cmp_gt_u32 %18, 1\n\ts_waitcnt lgkmcnt(0)\n\ts_cbranch_scc1 1f\n\t"
                 "global_load_dword %2, %12, %13 sc1\n\t"
                 "buffer_load_dwordx4 %3, %14, %17, 0 offen\n\tbuffer_load_dwordx4 %4, %15, %17, 0 offen\n\t"
                 "buffer_load_dwordx4 %5, %16, %17, 0 offen offset:2560\n\tbuffer_load_dwordx4 %6, %15, %17, 0 offen offset:2560\n"
                 "1:\n\t" DMA1 DMAN DMAN DMAN DMAN DMAN DMAN DMAN DMAN DMAN "s_mov_b32 m0, %0\n\t"
                 "s_cmp_eq_u32 %19, 0\n\ts_cbranch_scc1 2f\n\tglobal_store_dwordx4 %20, %21, %13 sc1\n\t"
                 "s_cmp_ge_i32 %22, 0\n\ts_cbranch_scc1 2f\n\tglobal_store_dwordx4 %23, %21, %13 sc1\n2:\n\t"
                 "s_barrier\n\ts_waitcnt vmcnt(10)"
                 : "=&s"(keep), "+s"(so), "=&v"(ep), "=&v"(xv[0]), "=&v"(xv[1]), "=&v"(xv[2]), "=&v"(xv[3])
                 : "v"(vo), "s"(rw), "s"(dst), "s"(step), "s"(dstep), "v"(zero), "s"(wsq), "v"(oa), "v"(ob), "v"(oc), "s"(rx), "s"(wave),
                   "s"(pre), "v"(po), "v"(emp), "s"(pre2), "v"(po2) : "memory", "scc");
#undef DMAN
#undef DMA1
    asm volatile("" : "+s"(wsq));
    const __amdgpu_buffer_rsrc_t rs = __builtin_amdgcn_make_buffer_rsrc(wsq, 0, WS_END, 0x00020000);
    if (half) {
      if (wave >= 2) {
        VM0();
        STAMPT(5, 128);
      } else {
        u32x4 sv;
#pragma unroll
        for (int k = 0; k < 4; ++k) {
          const float c0 = lo16(xv[0][k]), c1 = hi16(xv[0][k]);
          sv[k] = cvt2(c0 * frcp(1.f + fexp(-c0)), c1 * frcp(1.f + fexp(-c1)));
        }
        if (c + 16 * wave < 20) *reinterpret_cast<u32x4*>(lds + L_XA + (r * 20 + c + 16 * wave) * 16) = sv;
        if (t == 0) cnt[4] = next_tag(ep);
        STAMPT(2, 0);
      }
      lds_arrive(C_P1, lane);
      lds_wait<1>(C_P1, 4);
      STAMPT(20, 0);
      tg = __builtin_amdgcn_readfirstlane(cnt[4]);
      if (CT0 && u < 8 && t == 192) { const u32 t0 = cnt[15]; st_gran(rs, G_T + u * 16, t0, t0, tg); }
      u32x4 av[5];
#pragma unroll
      for (int s = 0; s < 5; ++s) av[s] = *reinterpret_cast<const u32x4*>(lds + L_XA + (c & 3) * 320 + s * 64 + r * 16);
      {
        f32x4 acc = {0.f, 0.f, 0.f, 0.f};
#pragma unroll
        for (int s = 0; s < 5; ++s) acc = MFMA(wfrag<512>(L_W + (tr128 ^ (wave * 32)) + s * 4096), av[s], acc);
        if (c < 4) st_g22(rs, G_MOD + ((ksp * 4 + c) * 960 + cbp * 16 + wave * 4 + r) * 16, acc, tg);
      }
      STAMPT(7, 0);
      if (wave < 2) { VM0(); lds_arrive(C_P2, lane); }
      lds_wait<1>(C_P2, 2);
      {
        f32x4 acc = {0.f, 0.f, 0.f, 0.f};
#pragma unroll
        for (int s = 0; s < 5; ++s) acc = MFMA(wfrag<512>(L_W + 20480 + (tr128 ^ (wave * 32)) + s * 4096), av[s], acc);
        if (c < 4 && g < 112) st_g22(rs, G_MOD + ((ksp * 4 + c) * 960 + (cbp + 32) * 16 + wave * 4 + r) * 16, acc, tg);
      }
      STAMPT(9, 0);
    } else {
      if (wave < 2) {
#pragma unroll
        for (int i = 0; i < 2; ++i) {
          const int b = wave + 2 * i;
          float xf[16];
#pragma unroll
          for (int k = 0; k < 4; ++k) { xf[2 * k] = lo16(xv[2 * i][k]); xf[2 * k + 1] = hi16(xv[2 * i][k]); xf[8 + 2 * k] = lo16(xv[2 * i + 1][k]); xf[8 + 2 * k + 1] = hi16(xv[2 * i + 1][k]); }
          float s = 0.f, s2 = 0.f;
#pragma unroll
          for (int k = 0; k < 8; ++k) { s += xf[k]; s2 += xf[8 + k]; }
          if (lane < 16) s += s2;
          const float mean = wave_sum(s) * (1.f / DM);
          float v = 0.f, v2 = 0.f;
#pragma unroll
          for (int k = 0; k < 8; ++k) { const float d = xf[k] - mean; v += d * d; const float d2 = xf[8 + k] - mean; v2 += d2 * d2; }
          if (lane < 16) v += v2;
          v = wave_sum(v);
          if (lane == 0) { st[24 + 2 * b] = bfr(mean); st[24 + 2 * b + 1] = frcp(bfr(fsqrt(bfr(bfr(v * (1.f / DM)) + 1e-5f)))); }
          *reinterpret_cast<u32x4*>(xraw + b * 320 + lane * 4) = xv[2 * i];
          if (lane < 16) *reinterpret_cast<u32x4*>(xraw + b * 320 + 256 + lane * 4) = xv[2 * i + 1];
        }
        if (t == 0) cnt[4] = next_tag(ep);
        lds_arrive(C_P1, lane);
      }
      lds_wait<1>(C_P1, 2);
      STAMPT(20, 0);
      tg = __builtin_amdgcn_readfirstlane(cnt[4]);
      if (CT0 && u < 8 && t == 192) { const u32 t0 = cnt[15]; st_gran(rs, G_T + u * 16, t0, t0, tg); }
      if (wave < 3) {
        const int b = t / 40, j = t % 40;
        u32 offs[8];
        u32x4 gm[8];
#pragma unroll
        for (int sel = 0; sel < 2; ++sel)
#pragma unroll
          for (int ks = 0; ks < 4; ++ks) offs[sel * 4 + ks] = t < 160 ? G_MOD + ((ks * 4 + b) * 960 + sel * 160 + ksp * 40 + j) * 16 : OOB;
        __builtin_amdgcn_s_sleep(16);
        gather22<8, 4>(rs, offs, gm, tg);
        STAMPT(8, 0);
        const f32x4 sa = (un_g22(gm[0]) + un_g22(gm[1])) + (un_g22(gm[2]) + un_g22(gm[3]));
        const f32x4 ca = (un_g22(gm[4]) + un_g22(gm[5])) + (un_g22(gm[6]) + un_g22(gm[7]));
        const int bb = b & 3;
        const float mean = st[24 + 2 * bb], sd = st[24 + 2 * bb + 1];
        const u32x2 xw = *reinterpret_cast<const u32x2*>(xraw + bb * 320 + ksp * 80 + 2 * j);
        float hv[4];
#pragma unroll
        for (int k = 0; k < 4; ++k) {
          const float xf = (k & 1) ? hi16(xw[k >> 1]) : lo16(xw[k >> 1]);
          const float n = bfr(bfr(xf - mean) * sd);
          hv[k] = bfr(n * bfr(1.f + bfr(ca[k]))) + bfr(sa[k]);
        }
        if (t < 160) *reinterpret_cast<u32x2*>(lds + L_XA + t * 8) = u32x2{cvt2(hv[0], hv[1]), cvt2(hv[2], hv[3])};
      }
      VM0();
      lds_arrive(C_P2, lane);
      lds_wait<1>(C_P2, 4);
      u32x4 av[5];
#pragma unroll
      for (int s = 0; s < 5; ++s) av[s] = *reinterpret_cast<const u32x4*>(lds + L_XA + (c & 3) * 320 + s * 64 + r * 16);
#pragma unroll
      for (int e = 0; e < 2; ++e) {
        const int nt = 2 * wave + e;
        const u32 tr256 = (8 * r + (c >> 2)) * 256 + ((((c >> 2) | ((r & 1) << 2)) ^ nt) * 32) + (c & 3) * 8;
        f32x4 acc = {0.f, 0.f, 0.f, 0.f};
#pragma unroll
        for (int s = 0; s < 5; ++s) acc = MFMA(wfrag<1024>(L_W + tr256 + s * 8192), av[s], acc);
        if (c < 4) st_g22(rs, G_Q + ((ksp * 4 + c) * 1024 + cbp * 32 + nt * 4 + r) * 16, acc, tg);
      }
      lds_arrive(C_GO, lane);
      STAMPT(9, 0);
    }
  } else {
    __builtin_amdgcn_s_setprio(2);
    if (t < 256 + 16) cnt[lane] = lane == 15 ? (u32)__builtin_amdgcn_s_memrealtime() : 0;
  }
  u32 kvoff = (u32)(bh * SK + (SK - WIN)) * 256u + (u32)(half * 8 + wave) * (NL * 1024u) + lane * 16, ph[8];
#pragma unroll
  for (int i = 0; i < 8; ++i) ph[i] = ((i + (u >> 3)) & 7) * 4096;
  asm volatile("" : "+v"(kvoff));
  asm volatile("s_waitcnt lgkmcnt(0)" : "+s"(wsq), "+s"(ph[0]), "+s"(ph[1]), "+s"(ph[2]), "+s"(ph[3]), "+s"(ph[4]), "+s"(ph[5]), "+s"(ph[6]), "+s"(ph[7])
               : "s"(rw), "s"(rx));
  const __amdgpu_buffer_rsrc_t rs = __builtin_amdgcn_make_buffer_rsrc(wsq, 0, WS_END, 0x00020000);
  {
    u32x4 kv[NL], kh[16];
    const __amdgpu_buffer_rsrc_t rk = __builtin_amdgcn_make_buffer_rsrc((void*)a.kc, 0, 1u << 28, 0x00020000);
    const __amdgpu_buffer_rsrc_t rv = __builtin_amdgcn_make_buffer_rsrc((void*)a.vc, 0, 1u << 28, 0x00020000);
#define LDKV(rsrc, off, i) __builtin_amdgcn_raw_buffer_load_b128(rsrc, (off) + ((i) & 3) * 1024, ph[(i) >> 2], 2)
#define LDS_INC(addr) do { asm volatile("" ::: "memory"); if (lane == 0) asm volatile("ds_add_u32 %0, %1" :: "v"(addr), "v"(1u) : "memory"); } while (0)
#define TR_STEP(x, HALF, CTRL, BIT) _Pragma("unroll") for (int hh = 0; hh < (HALF); ++hh) { \
    const float lo_ = x[hh], hi_ = x[hh + (HALF)]; const float t1_ = lo_ + dpp<CTRL>(lo_), t2_ = hi_ + dpp<CTRL>(hi_); \
    x[hh] = (c & (BIT)) ? t2_ : t1_; }
#define SCORE16(x, src, o) float x[16]; \
    _Pragma("unroll") for (int i = 0; i < 16; ++i) { float d = 0.f; _Pragma("unroll") for (int k = 0; k < 4; ++k) d = dot2(src[(o) + i][k], qpk[k], d); x[i] = d; } \
    TR_STEP(x, 8, XOR8, 8) TR_STEP(x, 4, XOR7, 4) TR_STEP(x, 2, XOR2, 2)
#define SCALED(v) bfr(bfr(v) * 0.08838834764831845f)
#define PV2(i) _Pragma("unroll") for (int k = 0; k < 4; ++k) { \
      acc[2 * k] = dot2(__builtin_amdgcn_perm(kv[(i) + 1][k], kv[i][k], 0x05040100u), pw[(i) / 2], acc[2 * k]); \
      acc[2 * k + 1] = dot2(__builtin_amdgcn_perm(kv[(i) + 1][k], kv[i][k], 0x07060302u), pw[(i) / 2], acc[2 * k + 1]); }
#define GET_Q() lds_wait<2>(C_Q, 1); u32x4 qpk; \
    asm volatile("ds_read_b128 %0, %2\n\tds_read_b32 %1, %3\n\ts_waitcnt lgkmcnt(0)" : "=&v"(qpk), "=&v"(tg) : "v"((u32)(L_QS + 16 * c)), "v"((u32)(L_CNT + 16)) : "memory"); \
    tg = __builtin_amdgcn_readfirstlane(tg);
#define GET_P() _Pragma("unroll") for (int i4 = 0; i4 < 4; ++i4) { \
      const u32x4 pv = *reinterpret_cast<const u32x4*>(pT + (wave * 4 + r) * 16 + i4 * 4); \
      _Pragma("unroll") for (int j = 0; j < 4; ++j) pw[i4 * 4 + j] = pv[j]; }
    auto gather_q = [&]() __attribute__((always_inline)) {
        u32 offs[2];
        u32x4 gq[2];
#pragma unroll
        for (int e = 0; e < 2; ++e) offs[e] = G_Q + (((2 * (lane >> 5) + e) * 4 + b_att) * 1024 + h_att * 32 + (lane & 31)) * 16;
        {
          gq[0] = ld_gran(rs, offs[0]); gq[1] = ld_gran(rs, offs[1]);
          const u32x4 gt = ld_gran(rs, lane < 8 ? G_T + lane * 16 : OOB);
          LDS_INC(C_KI);
          {
            const u32 own = cnt[15];
            const bool ok = lane < 8 && gt[1] == tg;
            float d = ok ? (float)(int)(gt[0] - own + TOFF) : 0.f;
            d += dpp<XOR1>(d); d += dpp<XOR2>(d); d += dpp<XOR7>(d);
            const int n = __builtin_popcountll(__ballot(ok));
            if (lane == 0 && n) cnt[15] = own + (u32)(int)(d * frcp((float)n));
          }
          const bool hit = (((gq[0][1] ^ (tg << 12)) | (gq[0][3] ^ (tg << 12)) | (gq[1][1] ^ (tg << 12)) | (gq[1][3] ^ (tg << 12))) < 4096u);
          if (!__all(hit)) gather22<2, 4>(rs, offs, gq, tg);
        }
        STAMPW(10);
        f32x4 q4 = un_g22(gq[0]) + un_g22(gq[1]);
#pragma unroll
        for (int k = 0; k < 4; ++k) q4[k] = swap32_add(q4[k], q4[k]);
        if (lane < 32) *reinterpret_cast<u32x2*>(qs + 2 * lane) = u32x2{cvt2(q4[0], q4[1]), cvt2(q4[2], q4[3])};
        lds_arrive(C_Q, lane);
    };
    const int qw = half ? QODD : QEVEN;
    const u32 tau = half ? (g < 74 ? TAU_A : TAU_B) : (g >= 122 ? TAU_X : TAU_E);
#define PACE(n) if ((n) > PB) { const u32 tt_ = t0 + (((n) - PB) * tau >> 4); \
      SPINLOOP while ((int)((u32)__builtin_amdgcn_s_memrealtime() - tt_) < 0) __builtin_amdgcn_s_sleep(1); asm volatile("" ::: "memory"); }
    float acc[8];
#pragma unroll
    for (int k = 0; k < 8; ++k) acc[k] = 0.f;
    u32 pw[16];
    if (__builtin_expect(wave >= 4, 1)) {
      STAMPT(30, 448);
      u32 t0 = (u32)__builtin_amdgcn_s_memrealtime();
      BAR();
      STAMPT(31, 448);
      const u32 kvoff2 = kvoff ^ 131072u;
#pragma unroll
      for (int i = 0; i < 48; ++i) {
        if (i == NPRE && half == 0) lds_wait<2>(C_GO, GOPOS ? 3 : 4);
        if (PK && (i & 3) == 0) PACE(i)
        if (i < NL) kv[i < NL ? i : 0] = LDKV(rk, kvoff, i & 31); else kh[i < NL ? 0 : i - NL] = LDKV(rk, kvoff2, i & 31);
      }
      if (!QPRE || wave != qw) LDS_INC(C_KI);
      STAMPT(25, 448);
      if ((QODD >= 4 || QEVEN >= 4) && wave == qw) {
        __builtin_amdgcn_s_setprio(3);
        lds_wait<2>(C_P1, half ? 4 : 2);
        tg = __builtin_amdgcn_readfirstlane(cnt[4]);
        gather_q();
        __builtin_amdgcn_s_setprio(2);
      }
      GET_Q()
      SCORE16(xa, kv, 0)
      asm volatile("" : "+v"(xa[0]), "+v"(xa[1]), "+v"(kvoff) :: "memory");
#pragma unroll
      for (int i = 0; i < P_NVA; ++i) kv[i] = LDKV(rv, kvoff, i);
      asm volatile("" ::: "memory");
      SCORE16(xb, kv, 16)
      xa[2] = xb[0]; xa[3] = xb[1];
      TR_STEP(xa, 2, XOR1, 1)
      float s0 = SCALED(xa[0]), s1 = SCALED(xa[1]);
      asm volatile("" : "+v"(s0), "+v"(s1), "+v"(kvoff) :: "memory");
#pragma unroll
      for (int i = P_NVA; i < NVE; ++i) kv[i] = LDKV(rv, kvoff, i);
      asm volatile("" ::: "memory");
      SCORE16(xh, kh, 0)
      const float s2 = SCALED(xh[0] + dpp<XOR1>(xh[0])), s3 = SCALED(xh[1] + dpp<XOR1>(xh[1]));
      const float m = wave_max(fmaxf(fmaxf(s0, s1), fmaxf(s2, s3)));
      const float p0 = __expf(s0 - m), p1 = __expf(s1 - m), p2 = __expf(s2 - m), p3 = __expf(s3 - m);
      const float l = wave_sum(fmaf(p2 + p3, 0.5f, p0 + p1));
      if (lane == 0) { st[wave] = m; st[8 + wave] = l; }
      pT[(wave * 4 + r) * 16 + (c & 1) * 8 + (c >> 1)] = cvt2(p0, p1);
      pT[((wave - 4) * 4 + r) * 16 + (c >> 1)] = cvt2(p2, p3);
      lds_arrive(C_PP + 4 * (wave - 4), lane);
      __builtin_amdgcn_s_setprio(0);
      STAMPT(27, 448); WSTAMP(1);
      if (CT0) t0 = __builtin_amdgcn_readfirstlane(cnt[15]);
      GET_P()
#pragma unroll
      for (int i = NVE; i < NL; i += 2) {
        PACE(96 + i)
        kv[i] = LDKV(rv, kvoff, i);
        kv[i + 1] = LDKV(rv, kvoff, i + 1);
        asm volatile("" ::: "memory");
        if (i == NL - 2) { LDS_INC(C_ISS); STAMPT(26, 448); }
        if (i >= PVLAG) {
          PV2(i >= PVLAG ? i - PVLAG : 0)
#pragma unroll
          for (int k = 0; k < 8; ++k) asm volatile("" : "+v"(acc[k]));
        }
      }
    } else {
      const int ex = g < 74 ? EA : g < 122 ? EB : EC;
      u32 kva = (!half && ex > 2) ? OOB : kvoff, kvb = (!half && ex > 0) ? OOB : kvoff;
      const u32 kxa = (half && ex > 2) ? kvoff - 8 * NL * 1024u : OOB, kxb = (half && ex > 0) ? kvoff - 8 * NL * 1024u : OOB;
      u32x4 kx[4];
      if (half && g < 68) __builtin_amdgcn_raw_buffer_store_b128(u32x4{EMPTY, EMPTY, EMPTY, EMPTY}, rs, G_OUT + (g * 256 + t) * 16, 0, 16);
      const u32 t0 = __builtin_amdgcn_readfirstlane(cnt[15]);
#pragma unroll
      for (int i = 0; i < 16; ++i) {
        if (PK && (i & 3) == 0) PACE(48 + i)
        kh[i] = LDKV(rk, i < 12 ? kvoff : i < 14 ? kva : kvb, 16 + i);
      }
#pragma unroll
      for (int i = 0; i < 4; ++i) kx[i] = LDKV(rk, i < 2 ? kxa : kxb, 28 + i);
      LDS_INC(C_KI);
      if (wave == 0) {
        if (half == 0 && g >= 80 && g < 120) {
          const int fb = g - 80;
          u32 of2[4];
          u32x4 gm[4];
#pragma unroll
          for (int ks = 0; ks < 4; ++ks) of2[ks] = G_MOD + ((ks * 4 + r) * 960 + 320 + fb * 16 + c) * 16;
          gather22<4, 4>(rs, of2, gm, tg);
          const f32x4 mv = (un_g22(gm[0]) + un_g22(gm[1])) + (un_g22(gm[2]) + un_g22(gm[3]));
          st_gran(rs, G_MF + (((fb / 10) * 4 + r) * 160 + (fb % 10) * 16 + c) * 16, cvt2(mv[0], mv[1]), cvt2(mv[2], mv[3]), tg);
        }
      }
      __builtin_amdgcn_s_setprio(0);
      lds_wait<2>(C_KI, P_GATE);
      if (QHOLD && wave == qw - 4) lds_wait<2>(C_Q, 1);
      asm volatile("" : "+v"(kvoff), "+v"(kva), "+v"(kvb));
#pragma unroll
      for (int i = 0; i < NL; ++i) {
        if (PL && (i & 3) == 0) PACE(64 + i)
        kv[i] = LDKV(rv, i < 28 ? kvoff : i < 30 ? kva : kvb, i);
      }
      STAMPT(1, 0);
      GET_Q()
      float xx[4];
#pragma unroll
      for (int i = 0; i < 4; ++i) { float d = 0.f;
#pragma unroll
        for (int k = 0; k < 4; ++k) d = dot2(kx[i][k], qpk[k], d);
        xx[i] = d; }
      asm volatile("" : "+v"(xx[0]), "+v"(xx[1]), "+v"(xx[2]), "+v"(xx[3]) :: "memory");
#pragma unroll
      for (int i = 0; i < 4; ++i) kx[i] = LDKV(rv, i < 2 ? kxa : kxb, 28 + i);
      LDS_INC(C_ISS);
      TR_STEP(xx, 2, XOR8, 8)
#pragma unroll
      for (int i = 0; i < 2; ++i) { xx[i] += dpp<XOR7>(xx[i]); xx[i] += dpp<XOR2>(xx[i]); xx[i] += dpp<XOR1>(xx[i]); }
      SCORE16(xh, kh, 0)
      const float ninf = -__builtin_inff();
      const bool mine = half || c < 16 - ex, extra = half && c >= 16 - 4 * ex;
      xh[0] += dpp<XOR1>(xh[0]); xh[1] += dpp<XOR1>(xh[1]);
      const float s2 = mine ? SCALED(xh[0]) : ninf, s3 = mine ? SCALED(xh[1]) : ninf;
      const float s4 = extra ? SCALED(xx[0]) : ninf, s5 = extra ? SCALED(xx[1]) : ninf;
      float m = wave_max(fmaxf(fmaxf(s2, s3), fmaxf(s4, s5)));
      lds_wait<2>(C_PP + 4 * wave, 1);
      const float me = st[wave + 4];
      m = fmaxf(m, me);
      const float p2 = __expf(s2 - m), p3 = __expf(s3 - m), p4 = __expf(s4 - m), p5 = __expf(s5 - m);
      const float l = wave_sum(fmaf(p4 + p5, 0.25f, p2 + p3)) * 0.5f;
      const u32 pxo = cvt2(p4, p5), pxt = dppu<XOR8>(pxo);
      const u32 px[2] = {c < 8 ? pxo : pxt, c < 8 ? pxt : pxo};
      if (lane == 0) { st[wave] = m; st[8 + wave] = l; }
      pT[(wave * 4 + r) * 16 + 8 + (c >> 1)] = cvt2(p2, p3);
      STAMPT(4, 0); WSTAMP(1);
      if (lds_ticket(C_SC, lane) == 3) {
        const float mw = st[lane & 7], lw = st[8 + (lane & 7)];
        float M = fmaxf(mw, dpp<XOR1>(mw)); M = fmaxf(M, dpp<XOR2>(M)); M = fmaxf(M, dpp<XOR7>(M));
        const float e = __expf(mw - M);
        float L = lw * e;
        L += dpp<XOR1>(L); L += dpp<XOR2>(L); L += dpp<XOR7>(L);
        if (lane == 0) st_gran(rs, G_ML + u * 64, f2u(M), f2u(L), tg);
        if (lane < 8) st[16 + lane] = e * frcp(L);
      }
      GET_P()
#pragma unroll
      for (int i = 0; i < 16; i += 2) { PV2(i) }
      const float f = __expf(me - m);
#pragma unroll
      for (int k = 0; k < 8; ++k) acc[k] *= f;
#pragma unroll
      for (int i = 16; i < NL - PVLAG; i += 2) { PV2(i) }
#pragma unroll
      for (int i = 0; i < 4; i += 2)
#pragma unroll
        for (int k = 0; k < 4; ++k) {
          acc[2 * k] = dot2(__builtin_amdgcn_perm(kx[i + 1][k], kx[i][k], 0x05040100u), px[i / 2], acc[2 * k]);
          acc[2 * k + 1] = dot2(__builtin_amdgcn_perm(kx[i + 1][k], kx[i][k], 0x07060302u), px[i / 2], acc[2 * k + 1]);
        }
    }
#pragma unroll
    for (int i = NL - PVLAG; i < NL; i += 2) { PV2(i) }
#undef PV2
#undef TR_STEP
#undef SCORE16
#undef SCALED
#undef GET_Q
#undef GET_P
#undef LDKV
#pragma unroll
    for (int i = 0; i < 4; ++i) acc[i] = swap32_add(acc[i], acc[i + 4]);
#pragma unroll
    for (int i = 0; i < 2; ++i) acc[i] = swap16_add(acc[i], acc[i + 2]);
    *reinterpret_cast<float2*>(accs + wave * 128 + c * 8 + 2 * r) = float2{acc[0], acc[1]};
    STAMPT(6, 0); STAMPT(28, 448); WSTAMP(2);
  }
  const u32x4 rsv = {(u32)(uintptr_t)wsq, (u32)((uintptr_t)wsq >> 32) & 0xffffu, WS_END, 0x00020000u};
  const int role = half ? (g < 74 ? 0 : 1) : (g >= 122 ? 0 : g < 40 ? 2 : 3);
  const int ia = half ? g : g - 48;
  const int ksA = ia & 7, cbA = ia >> 3;
#define HEADA(hh) ((ksA & 3) + (ksA >> 2) * 16 + (hh) * 4)
  const int iB = g - 74, jB = !JMAP ? iB : iB < 48 ? (iB & ~7) | ((iB & 3) << 1) | ((iB >> 2) & 1) : iB < 50 ? 2 * iB - 48 : iB < 52 ? iB + 2 : 2 * iB - 55;
  if (lds_ticket(C_FIN, lane) == 7) {
    if (lane < 32) {
      f32x4 o = {0.f, 0.f, 0.f, 0.f};
#pragma unroll
      for (int w = 0; w < 8; ++w) o += st[16 + w] * *reinterpret_cast<const f32x4*>(accs + w * 128 + 4 * lane);
      const u32x2 ow = {min(cvt2(o[0], o[1]), EMPTY - 1), min(cvt2(o[2], o[3]), EMPTY - 1)};
      __builtin_amdgcn_raw_buffer_store_b64(ow, rs, G_ATT + u * 256 + lane * 8, 0, 16);
    }
    LDS_INC(C_PUB);
    STAMPW(11);
  }
  u32 gaw[2];
  u32x2 xw = {0u, 0u};
  float b1v;
  if (role < 2 && wave < 4) {
    if (role == 0) {
      u32 offs[1] = {G_MF + (r * 160 + cbA * 16 + c) * 16};
      u32x4 gm[1];
      if (ksA == 0) xw = *reinterpret_cast<const u32x2*>(a.x.p + r * DM + cbA * 64 + c * 4);
      gather<1>(rs, offs, gm, tg);
      gaw[0] = gm[0][0]; gaw[1] = gm[0][2];
    } else {
      if (MFPOS == 1) lds_wait<2>(C_ISS, 8);
      b1v = bf2f(a.b1[jB * 32 + (t & 31)]);
      u32 offs[5];
      u32x4 gm[5];
#pragma unroll
      for (int i = 0; i < 5; ++i) offs[i] = G_MF + (640 + t + 256 * i) * 16;
      gather<5>(rs, offs, gm, tg);
#pragma unroll
      for (int i = 0; i < 5; ++i) {
        const int it = t + 256 * i, row = it / 160, gi = it % 160;
        u32 w0 = gm[i][0], w1 = gm[i][2];
        if (row >= 4) { w0 = cvt2(1.f + lo16(w0), 1.f + hi16(w0)); w1 = cvt2(1.f + lo16(w1), 1.f + hi16(w1)); }
        *reinterpret_cast<u32x2*>(lds + L_XA + (row < 4 ? row + 8 : row) * XS + gi * 8) = u32x2{w0, w1};
      }
      asm volatile("" : "+v"(b1v));
    }
  }
  if (role < 2 && wave < 4) {
    if (half) STAMPT(8, 0);
    lds_wait<2>(C_ISS, 8);
    if (role == 0) {
      const bf16* wp = a.wo + (size_t)(HEADA(wave) * 128 + (lane >> 3)) * DM + cbA * 64;
#pragma unroll
      for (int i = 0; i < 16; ++i) {
        if ((i == N1A && (half || N1A == N1E)) || (i == N1E && !half)) lds_wait<2>(C_PUB, 1);
        dma16(wp + (size_t)(8 * i) * DM + (s128e ^ ((i & 1) * 32)), L_W + wave * 16384 + i * 1024);
      }
      LDS_INC(C_ISS);
    } else {
      const bf16* wp = a.w1 + (size_t)(wave * 160 + (lane >> 2)) * FF + jB * 32 + ((((((lane >> 1) & 1) ^ (lane >> 5)) << 1) | (lane & 1)) * 8);
#pragma unroll
      for (int i = 0; i < 10; ++i) {
        if (i == N1B) lds_wait<2>(C_PUB, 1);
        dma16(wp + (size_t)(16 * i) * FF, L_W + (wave * 10 + i) * 1024);
      }
      if (N1B == 10) lds_wait<2>(C_PUB, 1);
      const bf16* wp2 = a.w2 + (size_t)(jB * 32 + wave * 8 + (lane >> 3)) * DM + (s128e ^ ((wave & 1) * 32));
#pragma unroll
      for (int k = 0; k < 10; ++k) dma16(wp2 + k * 64, L_WM2 + (k * 4 + wave) * 1024);
    }
    STAMPT(16, 0);
  }
#define SELECTOR(sel) u32x4 sel; \
    _Pragma("unroll") for (int d = 0; d < 4; ++d) sel[d] = ((c & 2) == 0 && d == (c >> 2)) ? ((c & 1) ? 0x3f800000u : 0x00003f80u) : 0u;
#define PAIRSUM(sm) (((c & 8) ? sm[1] : sm[0]) + dpp<XOR8>((c & 8) ? sm[0] : sm[1]))
  if (role == 0) {
    u32 a_x = L_XA + ((wave & 3) * 8 + (c & 7)) * 272 + r * 16;
    if (wave < 4) {
      float ga4[4] = {lo16(gaw[0]), hi16(gaw[0]), lo16(gaw[1]), hi16(gaw[1])}, x4[4] = {lo16(xw[0]), hi16(xw[0]), lo16(xw[1]), hi16(xw[1])};
      u32 a_red = L_RED + ((wave * 4 + c) * 64 + r * 4) * 4, a_sum = L_RED + lane * 16, a_sc = L_ST + (32 + wave * 8 + (c & 7)) * 4;
      u32 a_out = wave < NREP ? G_WO + wave * WO_REP + ((cbA * 4 + (c >> 2)) * 8 + ksA) * 128 + r * 32 + (c & 3) * 8 : OOB;
      u32 a_w[4];
#pragma unroll
      for (int nt = 0; nt < 4; ++nt) a_w[nt] = L_W + wave * 16384 + (tr128 ^ (nt * 32));
      asm volatile("" : "+v"(ga4[0]), "+v"(ga4[1]), "+v"(ga4[2]), "+v"(ga4[3]), "+v"(x4[0]), "+v"(x4[1]), "+v"(x4[2]), "+v"(x4[3]));
      asm volatile("s_waitcnt vmcnt(4)" : "+v"(a_red), "+v"(a_sum), "+v"(a_sc), "+v"(a_out), "+v"(a_x), "+v"(a_w[0]), "+v"(a_w[1]), "+v"(a_w[2]), "+v"(a_w[3]) :: "memory");
      s16x8 wf[4][4];
#pragma unroll
      for (int s = 0; s < 3; ++s)
#pragma unroll
        for (int nt = 0; nt < 4; ++nt) wf[s][nt] = wfrag<512>(a_w[nt] + s * 4096);
      STAMPT(19, 0);
      lds_wait<1>(C_PP + 4 * wave, 2);
      STAMPT(12, 0);
      u32x4 av[4];
#pragma unroll
      for (int s = 0; s < 4; ++s) av[s] = *reinterpret_cast<const u32x4*>(lds + a_x + s * 64);
      const float wsc = *reinterpret_cast<const float*>(lds + a_sc);
      VM0();
      STAMPT(23, 0);
#pragma unroll
      for (int nt = 0; nt < 4; ++nt) wf[3][nt] = wfrag<512>(a_w[nt] + 3 * 4096);
      __builtin_amdgcn_sched_barrier(0);
      f32x4 acc[4];
#pragma unroll
      for (int nt = 0; nt < 4; ++nt) acc[nt] = f32x4{0.f, 0.f, 0.f, 0.f};
#pragma unroll
      for (int s = 0; s < 4; ++s)
#pragma unroll
        for (int nt = 0; nt < 4; ++nt) acc[nt] = MFMA(wf[s][nt], av[s], acc[nt]);
#pragma unroll
      for (int nt = 0; nt < 4; ++nt) { acc[nt] *= wsc; asm("" : "+v"(acc[nt])); }
#pragma unroll
      for (int nt = 0; nt < 4; ++nt)
#pragma unroll
        for (int k = 0; k < 4; ++k) { float v = acc[nt][k] + dpp<SHL4>(acc[nt][k]); asm("" : "+v"(v)); acc[nt][k] = v; }
      asm volatile("" : "+v"(acc[0]), "+v"(acc[1]), "+v"(acc[2]), "+v"(acc[3]));
      if (c < 4) {
#pragma unroll
        for (int nt = 0; nt < 4; ++nt) *reinterpret_cast<f32x4*>(lds + a_red + nt * 64) = acc[nt];
      }
      BAR();
      STAMPT(21, 0);
      f32x4 pp[4];
#pragma unroll
      for (int w = 0; w < 4; ++w) pp[w] = *reinterpret_cast<const f32x4*>(lds + a_sum + w * 1024);
      asm volatile("" : "+v"(pp[0]), "+v"(pp[1]), "+v"(pp[2]), "+v"(pp[3]));
      const f32x4 s = (pp[0] + pp[1]) + (pp[2] + pp[3]);
      const u32x2 o = {min(cvt2(fmaf(s[0], ga4[0], x4[0]), fmaf(s[1], ga4[1], x4[1])), EMPTY - 1), min(cvt2(fmaf(s[2], ga4[2], x4[2]), fmaf(s[3], ga4[3], x4[3])), EMPTY - 1)};
      __builtin_amdgcn_raw_buffer_store_b64(o, rs, a_out, 0, 16);
    } else {
      const int head = HEADA(wave - 4);
      const u32 blk = ((c & 3) * 32 + head) * 2 + ((c >> 2) & 1);
      u32 oa = G_ATT + blk * 256 + (c >> 3) * 64 + r * 16, om = G_ML + blk * 64;
      a_x += (c >> 3) * 64;
      asm volatile("" : "+v"(oa), "+v"(om), "+v"(a_x));
      u32x4 av[2], ml;
      u64 m0 = ~0ull, m1 = ~0ull, m2 = 0xffull;
      av[0] = av[1] = ml = u32x4{0u, 0u, 0u, 0u};
#define ALOOK() \
        asm volatile(LK(0, 3, "0", 0, 5) LK(1, 4, "0", 0, 6) LK(2, 4, "0", 128, 7) LKEND \
                     : "+v"(ml), "+v"(av[0]), "+v"(av[1]) : "v"(om), "v"(oa), "s"(m2), "s"(m0), "s"(m1), [rs] "s"(rsv) : "memory"); \
        m0 = __builtin_amdgcn_ballot_w64(missing(av[0])); \
        m1 = __builtin_amdgcn_ballot_w64(missing(av[1]));
      lds_wait<2>(C_ISS, 12);
      SPINLOOP for (unsigned spins = 0; spins < SPIN_CAP; ++spins) {
        ALOOK()
        m2 &= __builtin_amdgcn_ballot_w64(ml[1] != tg) | __builtin_amdgcn_ballot_w64(ml[3] != tg);
        if (!m2) break;
        __builtin_amdgcn_s_sleep(ASL);
      }
      {
        const float M = u2f(ml[0]), L = u2f(ml[2]), Mo = dpp<XOR7>(dpp<0x1B>(M)), Lo = dpp<XOR7>(dpp<0x1B>(L));
        const float Mx = fmaxf(M, Mo), le = L * __expf(M - Mx);
        const float wsc = le * frcp(fmaf(Lo, __expf(Mo - Mx), le));
        if (lane < 8) st[wave * 8 + lane] = wsc;
      }
      if (m0 | m1) {
        SPINLOOP for (unsigned spins = 0; spins < SPIN_CAP; ++spins) {
          ALOOK()
          if (!(m0 | m1)) break;
          __builtin_amdgcn_s_sleep(ASL);
        }
      }
#undef ALOOK
      asm volatile("" : "+v"(av[0]), "+v"(av[1]));
      *reinterpret_cast<u32x4*>(lds + a_x) = av[0];
      *reinterpret_cast<u32x4*>(lds + a_x + 128) = av[1];
      LDS_INC(C_PP + 4 * (wave - 4));
      STAMPT(22, 448);
      BAR();
    }
    STAMPT(13, 0);

  } else if (role == 1) {
    const int lb = (c >> 1) & 3, lc = (c & 1) * 8 + 2 * r;
    const int fc = lane & 31, hw = lane >> 5;
    SELECTOR(sel)
    constexpr int L_R2 = L_MOD, L_S2 = L_R2 + 4096;
    const u32 gw = G_WO + (jB & (NREP - 1)) * WO_REP + wave * 5120;
    u32 go = gw + (c >> 3) * 1024 + r * 128 + (c & 7) * 16, go4 = gw + 4096 + lane * 16;
    u32 gl = lane == 0 ? gw + (jB % 5) * 1024 + ((jB + wave) & 7) * 128 : OOB;
    u32 a_u = L_XA + wave * 640 + lb * 160 + ((c >> 3) * 16 + lc) * 2, a_u4 = L_XA + wave * 640 + lb * 160 + (64 + lc + (c >> 3)) * 2;
    u32 a_av = L_XA + wave * 640 + (c & 3) * 160 + r * 16, a_ah = a_av + 128 - r * 8;
    u32 a_red = L_R2 + ((wave * 4 + c) * 32 + r * 4) * 4;
    u32 a_st = L_S2 + ((lb * 2 + (c >> 3)) * 8 + wave) * 4;
    u32 a_fin = L_R2 + ((hw * 16 + (wave & 3)) * 32 + fc) * 4;
    u32 a_fst = L_S2 + (wave & 3) * 64;
    u32 a_out = c < 4 ? G_OUT + (wave * 5 * 54 + jB) * 128 + c * 32 + r * 8 : OOB;
    u32 a_fi = L_FI + (c & 3) * 64 + r * 16, a_fo = L_FI + ((wave & 3) * 32 + fc) * 2;
    u32 a_w1 = L_W + wave * 5120 + (8 * r + (c >> 2)) * 64 + (r & 1) * 32 + (c & 3) * 8;
    u32 a_wh = L_W + wave * 5120 + 4096 + (4 * r + (c >> 2)) * 64 + (r >> 1) * 32 + (c & 3) * 8;
    u32 a_w2[5];
#pragma unroll
    for (int k = 0; k < 5; ++k) { const int n2 = wave * 5 + k; a_w2[k] = L_WM2 + (n2 >> 2) * 4096 + (tr128 ^ ((n2 & 3) * 32)); }
    asm volatile("" : "+v"(sel), "+v"(go), "+v"(go4), "+v"(gl), "+v"(a_u), "+v"(a_u4), "+v"(a_av), "+v"(a_ah), "+v"(a_red), "+v"(a_st), "+v"(a_fin), "+v"(a_fst), "+v"(a_out));
    asm volatile("s_waitcnt vmcnt(10)" : "+v"(a_fi), "+v"(a_fo), "+v"(a_w1), "+v"(a_wh), "+v"(a_w2[0]), "+v"(a_w2[1]), "+v"(a_w2[2]), "+v"(a_w2[3]), "+v"(a_w2[4]) :: "memory");
    STAMPT(23, 0);
    BAR();
    STAMPT(12, 0);
    s16x8 wf[2][2], wg[5];
    s16x4 wh[2];
#pragma unroll
    for (int nt = 0; nt < 2; ++nt) {
#pragma unroll
      for (int s = 0; s < 2; ++s) wf[s][nt] = wfrag<256>((a_w1 ^ (nt * 32)) + s * 2048);
      wh[nt] = __builtin_amdgcn_ds_read_tr16_b64_v4i16((lds_s4p)(uintptr_t)(a_wh ^ (nt * 32)));
    }
    float c1f[5];
    {
      const unsigned char* cp = lds + L_XA + (4 + lb) * XS + wave * 160;
#pragma unroll
      for (int i = 0; i < 2; ++i) { const u32 w = *reinterpret_cast<const u32*>(cp + (32 * i + (c >> 3) * 16 + lc) * 2); c1f[2 * i] = lo16(w); c1f[2 * i + 1] = hi16(w); }
      c1f[4] = u2f((u32)*reinterpret_cast<const unsigned short*>(cp + (64 + lc + (c >> 3)) * 2) << 16);
    }
#define W1MMA(acc) \
      f32x4 acc[2]; \
      _Pragma("unroll") for (int nt = 0; nt < 2; ++nt) { const f32x4 z_ = {0.f, 0.f, 0.f, 0.f}; acc[nt] = MFMA(wf[0][nt], av0, z_); } \
      _Pragma("unroll") for (int nt = 0; nt < 2; ++nt) acc[nt] = MFMA(wf[1][nt], av1, acc[nt]); \
      _Pragma("unroll") for (int nt = 0; nt < 2; ++nt) acc[nt] = __builtin_amdgcn_mfma_f32_16x16x16bf16_1k(wh[nt], av2, acc[nt], 0, 0, 0);
    {
      const unsigned char* p = lds + L_XA + (4 + (c & 7)) * XS + wave * 160;
      const u32x4 av0 = *reinterpret_cast<const u32x4*>(p + r * 16), av1 = *reinterpret_cast<const u32x4*>(p + 64 + r * 16);
      const s16x4 av2 = *reinterpret_cast<const s16x4*>(p + 128 + r * 8);
      W1MMA(acc)
#pragma unroll
      for (int nt = 0; nt < 2; ++nt) if (c < 8) *reinterpret_cast<f32x4*>(red + (wave * 8 + c) * 32 + nt * 16 + r * 4) = acc[nt];
    }
    float Tc = 0.f, Ts = 0.f;
#define TSUM() _Pragma("unroll") for (int k = 0; k < 4; ++k) { Tc += red[((hw * 4 + k) * 8 + (wave & 3)) * 32 + fc]; Ts += red[((hw * 4 + k) * 8 + 4 + (wave & 3)) * 32 + fc]; }
    if (BT) {
      BAR();
      TSUM()
      asm volatile("" : "+v"(Tc), "+v"(Ts));
    }
    VM0();
    BAR();
    STAMPT(29, 0);
    u32x4 sg = ld_gran(rs, gl);
    asm volatile("" ::: "memory");
#pragma unroll
    for (int k = 0; k < 5; ++k) wg[k] = wfrag<512>(a_w2[k]);
    if (!BT) { TSUM() }
#pragma unroll
    for (int i = 0; i < 5; ++i) asm volatile("" : "+v"(c1f[i]), "+v"(wg[i]));
    asm volatile("" : "+v"(Tc), "+v"(Ts));
    STAMPT(18, 0);
    SPINLOOP for (unsigned spins = 0; spins < SPIN_CAP; ++spins) {
      if (__builtin_amdgcn_readfirstlane(sg[0]) != EMPTY) break;
      __builtin_amdgcn_s_sleep(BSL);
      sg = ld_gran(rs, gl);
    }
    u32x4 pa[5];
    {
      u64 m[5];
#pragma unroll
      for (int i = 0; i < 5; ++i) { m[i] = ~0ull; pa[i] = u32x4{0u, 0u, 0u, 0u}; }
      SPINLOOP for (unsigned spins = 0; spins < SPIN_CAP; ++spins) {
        asm volatile(LK(0, 5, "0", 0, 7) LK(1, 5, "0", 512, 8) LK(2, 5, "0", 2048, 9) LK(3, 5, "0", 2560, 10) LK(4, 6, "0", 0, 11) LKEND
                     : "+v"(pa[0]), "+v"(pa[1]), "+v"(pa[2]), "+v"(pa[3]), "+v"(pa[4])
                     : "v"(go), "v"(go4), "s"(m[0]), "s"(m[1]), "s"(m[2]), "s"(m[3]), "s"(m[4]), [rs] "s"(rsv) : "memory");
        u64 bad = 0;
#pragma unroll
        for (int i = 0; i < 5; ++i) { m[i] = __builtin_amdgcn_ballot_w64(missing(pa[i])); bad |= m[i]; }
        if (!bad) break;
      }
    }
    STAMPT(14, 0); WSTAMP(3);
    float xf[5];
    {
      const f32x4 z = {0.f, 0.f, 0.f, 0.f};
      f32x4 s4 = MFMA(sel, pa[4], z), s0 = MFMA(sel, pa[0], z), s1 = MFMA(sel, pa[2], z);
      s0 = MFMA(sel, pa[1], s0); s1 = MFMA(sel, pa[3], s1);
      float e0 = s4[0], e1 = s4[1];
      asm("" : "+v"(e0), "+v"(e1));
      xf[4] = bfr(((c & 8) ? e1 : e0) + dpp<XOR8>((c & 8) ? e0 : e1));
      *reinterpret_cast<unsigned short*>(lds + a_u4) = (unsigned short)cvt2(xf[4] * c1f[4], 0.f);
      const u32 w0 = cvt2(s0[0], s0[1]), w1 = cvt2(s1[0], s1[1]);
      xf[0] = lo16(w0); xf[1] = hi16(w0); xf[2] = lo16(w1); xf[3] = hi16(w1);
      *reinterpret_cast<u32*>(lds + a_u) = cvt2(xf[0] * c1f[0], xf[1] * c1f[1]);
      *reinterpret_cast<u32*>(lds + a_u + 64) = cvt2(xf[2] * c1f[2], xf[3] * c1f[3]);
    }
    STAMPT(19, 0);
    {
      u32x4 av0 = *reinterpret_cast<const u32x4*>(lds + a_av), av1 = *reinterpret_cast<const u32x4*>(lds + a_av + 64);
      s16x4 av2 = *reinterpret_cast<const s16x4*>(lds + a_ah);
      __builtin_amdgcn_sched_barrier(0);
      const float ps = ((xf[0] + xf[1]) + (xf[2] + xf[3])) + xf[4];
      const float pq = fmaf(xf[0], xf[0], fmaf(xf[1], xf[1], xf[4] * xf[4])) + fmaf(xf[2], xf[2], xf[3] * xf[3]);
      const float t1 = ps + dpp<XOR8>(ps), t2 = pq + dpp<XOR8>(pq);
      float z = (c & 8) ? t2 : t1;
      z += dpp<XOR1>(z);
      asm volatile("" : "+v"(z));
      __builtin_amdgcn_sched_barrier(0);
      W1MMA(acc)
      z = cross_rows_sum(z);
      if ((lane & 49) == 0) *reinterpret_cast<float*>(lds + a_st) = z;
      if (c < 4) {
#pragma unroll
        for (int nt = 0; nt < 2; ++nt) *reinterpret_cast<f32x4*>(lds + a_red + nt * 64) = acc[nt];
      }
    }
#undef W1MMA
    BAR();
    STAMPT(24, 0);
    if (wave < 4) {
      f32x4 sv[4];
      float sp[4];
#pragma unroll
      for (int k = 0; k < 4; ++k) sv[k] = *reinterpret_cast<const f32x4*>(lds + a_fst + k * 16);
#pragma unroll
      for (int k = 0; k < 4; ++k) sp[k] = *reinterpret_cast<const float*>(lds + a_fin + k * 512);
      __builtin_amdgcn_sched_barrier(0);
      const f32x4 s1 = sv[0] + sv[1], s2 = sv[2] + sv[3];
      const float mean = ((s1[0] + s1[1]) + (s1[2] + s1[3])) * (1.f / DM);
      const float var = fmaxf(((s2[0] + s2[1]) + (s2[2] + s2[3])) * (1.f / DM) - mean * mean, 0.f);
      const float rstd = __builtin_amdgcn_rsqf(var + 1e-5f);
      const float zp = fmaf(rstd, fmaf(-mean, Tc, (sp[0] + sp[1]) + (sp[2] + sp[3])), Ts);
      const float hh = bfr(bfr(swap32_add(zp, zp)) + b1v);
      const float e2 = __builtin_amdgcn_exp2f(hh * fmaf(hh * hh, 0.1029432397f, 2.302208198f));
      const float f = fmaf(-hh, frcp(e2 + 1.f), hh);
      *reinterpret_cast<unsigned short*>(lds + a_fo) = (unsigned short)cvt2(f, 0.f);
    }
    BAR();
    {
      const u32x4 av = *reinterpret_cast<const u32x4*>(lds + a_fi);
      f32x4 acc[5];
#pragma unroll
      for (int k = 0; k < 5; ++k) { const f32x4 z = {0.f, 0.f, 0.f, 0.f}; acc[k] = MFMA(wg[k], av, z); }
      __builtin_amdgcn_sched_barrier(0);
#pragma unroll
      for (int k = 0; k < 5; ++k) {
        const u32x2 o = {min(cvt2(acc[k][0], acc[k][1]), EMPTY - 1), min(cvt2(acc[k][2], acc[k][3]), EMPTY - 1)};
        __builtin_amdgcn_raw_buffer_store_b64(o, rs, a_out, k * (54 * 128), 16);
      }
    }
    STAMPT(15, 0);

  } else if (role == 2 && wave == 0) {
    const int lb = (c >> 1) & 3, col = g * 16 + (c & 1) * 8 + 2 * r + (c >> 3);
    float b2v = bf2f(a.b2[col]);
    const f32x4 z = {0.f, 0.f, 0.f, 0.f};
    SELECTOR(sel)
    float x1v, gbv;
    {
      u32 offs[1] = {G_MF + ((12 + lb) * 160 + (col >> 2)) * 16};
      u32x4 gm[1];
      gather<1>(rs, offs, gm, tg);
      const u32 gbw = (r & 1) ? gm[0][2] : gm[0][0];
      gbv = (c & 8) ? hi16(gbw) : lo16(gbw);
      gather_e<1, 2>(rsv, G_WO + (g & (NREP - 1)) * WO_REP + g * 1024 + lane * 16, gm, ~0ull);
      const f32x4 sm = MFMA(sel, gm[0], z);
      x1v = bfr(PAIRSUM(sm));
    }
    u32 oo = lb * DM + col;
    asm volatile("" : "+v"(x1v), "+v"(gbv), "+v"(oo), "+v"(sel), "+v"(b2v));
    STAMPT(23, 0);
    u32x4 pa[7];
    gather_e<7, CSLEEP>(rsv, G_OUT + g * (54 * 128) + lane * 16, pa, (1ull << 48) - 1);
    STAMPT(16, 0);
    f32x4 s0 = MFMA(sel, pa[0], z), s1 = MFMA(sel, pa[1], z);
#pragma unroll
    for (int i = 2; i < 7; ++i) { if (i & 1) s1 = MFMA(sel, pa[i], s1); else s0 = MFMA(sel, pa[i], s0); }
    s0 += s1;
    const float y = bfr(bfr(PAIRSUM(s0)) + b2v);
    a.out[oo] = __float2bfloat16(x1v + bfr(gbv * y));
    STAMPT(17, 0);

    if (u == 0 && t == 0)
      __hip_atomic_store(reinterpret_cast<u32*>(a.ws + WS_EPOCH), tg, __ATOMIC_RELAXED, __HIP_MEMORY_SCOPE_AGENT);
  }
  STAMP_FLUSH;
}

extern "C" void kernel_launch(void* const* d_in, const int* in_sizes, int n_in, void* d_out, int out_size, void* d_ws,
                              size_t ws_size, hipStream_t stream) {
  Args a;
  a.x = Desc{(const bf16*)d_in[0], NB * DM * 2, 0x00020000u};
  a.kc = (const bf16*)d_in[1];
  a.vc = (const bf16*)d_in[2];
  a.wq = Desc{(const bf16*)d_in[3], DM * QD * 2, 0x00020000u};
  a.wo = (const bf16*)d_in[4];
  a.w1 = (const bf16*)d_in[6];
  a.b1 = (const bf16*)d_in[7];
  a.w2 = (const bf16*)d_in[8];
  a.b2 = (const bf16*)d_in[9];
  a.c = Desc{(const bf16*)d_in[10], NB * DM * 2, 0x00020000u};
  a.wmod = Desc{(const bf16*)d_in[11], DM * MODN * 2, 0x00020000u};
  a.out = (bf16*)d_out;
  a.ws = (unsigned char*)d_ws;
  mega_kernel<<<NWG, NT, 0, stream>>>(a);
}
```

```cpp
#define SPINLOOP _Pragma("nounroll")
#define P_LAG 14
#define P_NPRE 32
#define P_NVE 6
#define P_NVA 6
#define QODD 4
#define QEVEN 4
#define MFPOS 1
#define EA 0
#define EB 0
#define EC 4
#define APOLL 0
#define N1E N1A
#define TAU_A 234
#define TAU_B TAU_A
#define TAU_X TAU_A
#define TAU_E 247
#define PK 0
#define PL 1
#define QPRE 1
#define QHOLD 0
#define CT0 1
#define TOFF 90
#define BLIGHT 1
#define BPRE 0
#define BW1 1
#define BMASK 1
#define CSLEEP 1
#define NREPL 4
#define OUTPRE 1
#define ASL 1
#define AEARLY 1
#define PDC 4
#define PDA 3
#define PDB 3
#define PSB 1
#define HC 1
#define HCD 120
#define HAG 60
#define AW 4
#define BFW -1
#define FULLF 1
#define WARM 1
#define HT 0
#define HX 10
#define HTD 55
#define HSL 1
#define NANT 1
#define HAW 65
#define BM 1
#define HPD 1
#define BT 0
#define BSL 2
#define PB 0
#define GOPOS 0
#define JMAP 1
#define N1A 10
#define N1B 10
#define PT 0
#define PTAUX 16
#define P_GATE 8
#include <hip/hip_runtime.h>
#include <hip/hip_bf16.h>
#include <stdint.h>

using bf16 = __hip_bfloat16;
using u32 = unsigned;
using u64 = unsigned long long;
using u32x2 = __attribute__((ext_vector_type(2))) unsigned;
using u32x4 = __attribute__((ext_vector_type(4))) unsigned;
using f32x4 = __attribute__((ext_vector_type(4))) float;
using bf16x2 = __attribute__((ext_vector_type(2))) __bf16;
typedef __attribute__((ext_vector_type(2))) float f32x2;
typedef short s16x8 __attribute__((ext_vector_type(8)));
typedef short s16x4 __attribute__((ext_vector_type(4)));
typedef __attribute__((address_space(3))) s16x4* lds_s4p;
typedef __attribute__((ext_vector_type(8))) __bf16 bf16x8v;

constexpr int NB = 4, DM = 640, NH = 32, DH = 128, SK = 8192, WIN = 2048, FF = 1728, QD = 4096, MODN = 3840;
constexpr int NT = 512, NWG = 256;

constexpr u32 WS_EPOCH = 0;
constexpr u32 G_T = 128;
constexpr u32 G_MOD = 256;
constexpr u32 G_Q = G_MOD + 16 * 960 * 16;
constexpr u32 G_MF = G_Q + 16 * 1024 * 16;
constexpr u32 G_HB = G_MF + 16 * 160 * 16;
constexpr u32 G_HA = G_HB + 64 * 128;
constexpr u32 G_HT = G_HA + 80 * 128;
constexpr u32 G_ML = G_HT + 256 * 128;
constexpr u32 EMPTY = 0xffffffffu;
constexpr int NREP = NREPL;
constexpr u32 G_WO = G_ML + 256 * 64;
constexpr u32 WO_REP = 40 * 1024;
constexpr u32 G_ATT = G_WO + NREP * WO_REP;
constexpr u32 G_OUT = G_ATT + 256 * 256;
constexpr u32 WS_END = G_OUT + 40 * 54 * 128;
constexpr int NCH = (G_OUT - G_WO) / 2048;
constexpr u32 OOB = 0xF0000000u;

#define WSTAMP(k) do {} while (0)
#define STAMPW(i) do {} while (0)
#define STAMPT(i, tt) do {} while (0)
#define STAMP_DECL
#define STAMP_FLUSH do {} while (0)
#define FAIL(i) do {} while (0)
#define PAD(name, next) asm volatile(".Lpad_" #name ":\n\ts_cbranch_execz .Lpad_" #next)
#define BAR() asm volatile("s_waitcnt lgkmcnt(0)\n\ts_barrier" ::: "memory")
#define VM0() asm volatile("s_waitcnt vmcnt(0)" ::: "memory")

__device__ __forceinline__ float bfr(float x) { return __bfloat162float(__float2bfloat16(x)); }
__device__ __forceinline__ float bf2f(bf16 x) { return __bfloat162float(x); }
__device__ __forceinline__ float lo16(unsigned u) { return __builtin_bit_cast(float, u << 16); }
__device__ __forceinline__ float hi16(unsigned u) { return __builtin_bit_cast(float, u & 0xffff0000u); }
__device__ __forceinline__ u32 f2u(float f) { return __builtin_bit_cast(u32, f); }
__device__ __forceinline__ float u2f(u32 f) { return __builtin_bit_cast(float, f); }
__device__ __forceinline__ u32 cvt2(float a, float b) {
  const f32x2 v = {a, b};
  return __builtin_bit_cast(u32, __builtin_convertvector(v, bf16x2));
}
__device__ __forceinline__ float frcp(float x) { return __builtin_amdgcn_rcpf(x); }
__device__ __forceinline__ float fsqrt(float x) { return __builtin_amdgcn_sqrtf(x); }
__device__ __forceinline__ float fexp(float x) { return __builtin_amdgcn_exp2f(x * 1.4426950408889634f); }
__device__ __forceinline__ float dot2(unsigned k, unsigned q, float acc) {
  return __builtin_amdgcn_fdot2_f32_bf16(__builtin_bit_cast(bf16x2, k), __builtin_bit_cast(bf16x2, q), acc, false);
}
__device__ __forceinline__ float addlo(unsigned w, float acc) { u32 k = 0x00003f80u; asm("" : "+s"(k)); return dot2(w, k, acc); }
__device__ __forceinline__ float addhi(unsigned w, float acc) { return dot2(w, 0x3f800000u, acc); }
template <int CTRL> __device__ __forceinline__ float dpp(float x) {
  return __builtin_bit_cast(float, __builtin_amdgcn_mov_dpp(__builtin_bit_cast(int, x), CTRL, 0xf, 0xf, true));
}
template <int CTRL> __device__ __forceinline__ u32 dppu(u32 x) { return (u32)__builtin_amdgcn_mov_dpp((int)x, CTRL, 0xf, 0xf, true); }
constexpr int XOR1 = 0xB1, XOR2 = 0x4E, XOR7 = 0x141, XOR8 = 0x128, ROR4 = 0x124, ROR2 = 0x122, ROR1 = 0x121, SHL4 = 0x104;
__device__ __forceinline__ float swap32_add(float a, float b) {
  auto s = __builtin_amdgcn_permlane32_swap(f2u(a), f2u(b), false, false);
  return u2f(s[0]) + u2f(s[1]);
}
__device__ __forceinline__ float swap16_add(float a, float b) {
  auto s = __builtin_amdgcn_permlane16_swap(f2u(a), f2u(b), false, false);
  return u2f(s[0]) + u2f(s[1]);
}
__device__ __forceinline__ float cross_rows_sum(float x) { x = swap16_add(x, x); return swap32_add(x, x); }
__device__ __forceinline__ float cross_rows_max(float x) {
  auto s = __builtin_amdgcn_permlane16_swap(f2u(x), f2u(x), false, false);
  x = fmaxf(u2f(s[0]), u2f(s[1]));
  auto s2 = __builtin_amdgcn_permlane32_swap(f2u(x), f2u(x), false, false);
  return fmaxf(u2f(s2[0]), u2f(s2[1]));
}
__device__ __forceinline__ float wave_sum(float x) {
  x += dpp<XOR8>(x); x += dpp<ROR4>(x); x += dpp<ROR2>(x); x += dpp<ROR1>(x);
  return cross_rows_sum(x);
}
__device__ __forceinline__ float wave_max(float x) {
  x = fmaxf(x, dpp<XOR8>(x)); x = fmaxf(x, dpp<ROR4>(x)); x = fmaxf(x, dpp<ROR2>(x)); x = fmaxf(x, dpp<ROR1>(x));
  return cross_rows_max(x);
}

__device__ __forceinline__ void st_gran(__amdgpu_buffer_rsrc_t rs, u32 off, u32 a, u32 b, u32 tg) {
  u32x4 v = {a, tg, b, tg};
  __builtin_amdgcn_raw_buffer_store_b128(v, rs, off, 0, 16);
}
__device__ __forceinline__ u32x4 ld_gran(__amdgpu_buffer_rsrc_t rs, u32 off) {
  return __builtin_amdgcn_raw_buffer_load_b128(rs, off, 0, 16);
}
constexpr unsigned SPIN_CAP = 1u << 18;
template <int N>
__device__ __forceinline__ void gather(__amdgpu_buffer_rsrc_t rs, const u32 (&offs)[N], u32x4 (&g)[N], u32 tg) {
  SPINLOOP for (unsigned spins = 0; spins < SPIN_CAP; ++spins) {
    bool ok = true;
#pragma unroll
    for (int i = 0; i < N; ++i) g[i] = ld_gran(rs, offs[i]);
#pragma unroll
    for (int i = 0; i < N; ++i) ok &= (offs[i] == OOB) | ((g[i][1] == tg) & (g[i][3] == tg));
    if (__all(ok)) break;
    __builtin_amdgcn_s_sleep(1);
  }
}
template <int N>
__device__ __forceinline__ void gather_m(__amdgpu_buffer_rsrc_t rs, u32 (&offs)[N], u32 (&va)[N], u32 (&vb)[N], u32 tg) {
#pragma unroll
  for (int i = 0; i < N; ++i) { va[i] = 0; vb[i] = 0; }
  SPINLOOP for (unsigned spins = 0; spins < SPIN_CAP; ++spins) {
    u32x4 g[N];
#pragma unroll
    for (int i = 0; i < N; ++i) g[i] = ld_gran(rs, offs[i]);
    bool ok = true;
#pragma unroll
    for (int i = 0; i < N; ++i) {
      const bool hit = (offs[i] != OOB) & (g[i][1] == tg) & (g[i][3] == tg);
      va[i] = hit ? g[i][0] : va[i]; vb[i] = hit ? g[i][2] : vb[i];
      offs[i] = hit ? OOB : offs[i];
      ok &= (offs[i] == OOB);
    }
    if (__all(ok)) break;
    __builtin_amdgcn_s_sleep(1);
  }
}

__device__ __forceinline__ bool missing(u32x4 v) { return max(max(v[0], v[1]), max(v[2], v[3])) == EMPTY; }
#define LK(v, o, so, imm, m) "s_mov_b64 exec, %" #m "\n\tbuffer_load_dwordx4 %" #v ", %" #o ", %[rs], " so " offen offset:" #imm " sc1\n\t"
#define LKEND "s_mov_b64 exec, -1\n\ts_waitcnt vmcnt(%[w])"
template <int W> __device__ __forceinline__ void look(u32x4 rs, u32 off, u32x4 (&v)[1], const u64 (&m)[1]) {
  asm volatile(LK(0, 1, "0", 0, 2) LKEND : "+v"(v[0]) : "v"(off), "s"(m[0]), [rs] "s"(rs), [w] "i"(W) : "memory");
}
template <int W> __device__ __forceinline__ void look(u32x4 rs, u32 off, u32x4 (&v)[5], const u64 (&m)[5]) {
  asm volatile(LK(0, 5, "0", 0, 6) LK(1, 5, "0", 1024, 7) LK(2, 5, "0", 2048, 8) LK(3, 5, "0", 3072, 9) LK(4, 5, "%11", 0, 10) LKEND
               : "+v"(v[0]), "+v"(v[1]), "+v"(v[2]), "+v"(v[3]), "+v"(v[4])
               : "v"(off), "s"(m[0]), "s"(m[1]), "s"(m[2]), "s"(m[3]), "s"(m[4]), "s"(4096u), [rs] "s"(rs), [w] "i"(W) : "memory");
}
template <int W> __device__ __forceinline__ void look(u32x4 rs, u32 off, u32x4 (&v)[7], const u64 (&m)[7]) {
  asm volatile(LK(0, 7, "0", 0, 8) LK(1, 7, "0", 1024, 9) LK(2, 7, "0", 2048, 10) LK(3, 7, "0", 3072, 11)
               LK(4, 7, "%15", 0, 12) LK(5, 7, "%15", 1024, 13) LK(6, 7, "%15", 2048, 14) LKEND
               : "+v"(v[0]), "+v"(v[1]), "+v"(v[2]), "+v"(v[3]), "+v"(v[4]), "+v"(v[5]), "+v"(v[6])
               : "v"(off), "s"(m[0]), "s"(m[1]), "s"(m[2]), "s"(m[3]), "s"(m[4]), "s"(m[5]), "s"(m[6]), "s"(4096u), [rs] "s"(rs), [w] "i"(W) : "memory");
}
template <int N, int SLEEP, int D>
__device__ __forceinline__ void gather_e(u32x4 rs, u32 off, u32x4 (&v)[N], u64 last) {
  u64 m[N];
#pragma unroll
  for (int i = 0; i < N; ++i) { m[i] = i < N - 1 ? ~0ull : last; v[i] = u32x4{0u, 0u, 0u, 0u}; }
  look<0>(rs, off, v, m);
  SPINLOOP for (unsigned spins = 0; spins < SPIN_CAP; ++spins) {
    u64 bad = 0;
#pragma unroll
    for (int i = 0; i < N; ++i) { m[i] = __builtin_amdgcn_ballot_w64(missing(v[i])); bad |= m[i]; }
    if (!bad) break;
    if (SLEEP) __builtin_amdgcn_s_sleep(SLEEP);
    look<N * (D - 1)>(rs, off, v, m);
  }
}

#define HLOOK(W_) asm volatile("s_mov_b64 exec, %2\n\tbuffer_load_dwordx2 %0, %1, %3, 0 offen sc1\n\ts_mov_b64 exec, -1\n\ts_waitcnt vmcnt(%4)" \
                               : "+v"(v) : "v"(off), "s"(m), "s"(rs), "i"(W_) : "memory")
__device__ __forceinline__ void heads_up_start(u32x4 rs, u32 off, u64 m, u32 tg, u32x2& v) { v = u32x2{0u, ~tg}; HLOOK(63); }
template <int SLEEP>
__device__ __forceinline__ void heads_up(u32x4 rs, u32 off, u64 m, u32 tg, u32x2& v) {
  const u64 all = m;
  asm volatile("s_waitcnt vmcnt(0)" : "+v"(v) :: "memory");
  SPINLOOP for (unsigned spins = 0; spins < SPIN_CAP; ++spins) {
    m &= __builtin_amdgcn_ballot_w64(v[1] != tg);
    if (!m) break;
    if (SLEEP) __builtin_amdgcn_s_sleep(SLEEP);
    HLOOK(0);
  }
  SPINLOOP for (unsigned spins = 0; spins < 64; ++spins)
    if (!(all & __builtin_amdgcn_ballot_w64((int)((u32)__builtin_amdgcn_s_memrealtime() - v[0]) < 0))) break;
}
__device__ __forceinline__ void announce(__amdgpu_buffer_rsrc_t rs, u32 off, u32 a_max, u32 a_cnt, u32 n, u32 tb, u32 d, u32 tg) {
  u32 tk = 0, mx;
  const u32 f = (u32)__builtin_amdgcn_s_memrealtime() - tb + d;
  asm volatile("s_mov_b64 exec, 1\n\tds_max_u32 %2, %3\n\tds_add_rtn_u32 %0, %4, %5\n\tds_read_b32 %1, %2\n\ts_mov_b64 exec, -1\n\ts_waitcnt lgkmcnt(0)"
               : "+v"(tk), "=&v"(mx) : "v"(a_max), "v"(f), "v"(a_cnt), "v"(1u) : "memory");
  const u32 lane = threadIdx.x & 63;
  __builtin_amdgcn_raw_buffer_store_b64(u32x2{__builtin_amdgcn_readfirstlane(mx) + tb, tg}, rs,
                                        (lane < 16 && __builtin_amdgcn_readfirstlane(tk) == n - 1) ? off + lane * 8 : OOB, 0, 16);
}

__device__ __forceinline__ void st_g22(__amdgpu_buffer_rsrc_t rs, u32 off, f32x4 v, u32 tg) {
  const u32 a0 = f2u(v[0]) + 0x200u, a1 = f2u(v[1]) + 0x200u, a2 = f2u(v[2]) + 0x200u, a3 = f2u(v[3]) + 0x200u;
  const u32x4 w = {(a0 >> 10) | ((a1 << 12) & 0xffc00000u), (a1 >> 20) | (tg << 12), (a2 >> 10) | ((a3 << 12) & 0xffc00000u), (a3 >> 20) | (tg << 12)};
  __builtin_amdgcn_raw_buffer_store_b128(w, rs, off, 0, 16);
}
__device__ __forceinline__ f32x4 un_g22(u32x4 w) {
  return f32x4{u2f(w[0] << 10), u2f(__builtin_amdgcn_alignbit(w[1], w[0], 22) << 10), u2f(w[2] << 10), u2f(__builtin_amdgcn_alignbit(w[3], w[2], 22) << 10)};
}
template <int N, int SLEEP>
__device__ __forceinline__ void gather22(__amdgpu_buffer_rsrc_t rs, u32 (&offs)[N], u32x4 (&v)[N], u32 tg) {
#pragma unroll
  for (int i = 0; i < N; ++i) v[i] = u32x4{0u, 0u, 0u, 0u};
  SPINLOOP for (unsigned spins = 0; spins < SPIN_CAP; ++spins) {
    u32x4 g[N];
#pragma unroll
    for (int i = 0; i < N; ++i) g[i] = ld_gran(rs, offs[i]);
    bool ok = true;
#pragma unroll
    for (int i = 0; i < N; ++i) {
      const bool hit = (offs[i] != OOB) & (((g[i][1] ^ (tg << 12)) | (g[i][3] ^ (tg << 12))) < 4096u);
#pragma unroll
      for (int k = 0; k < 4; ++k) v[i][k] = hit ? g[i][k] : v[i][k];
      offs[i] = hit ? OOB : offs[i];
      ok &= (offs[i] == OOB);
    }
    if (__all(ok)) break;
    __builtin_amdgcn_s_sleep(SLEEP);
  }
}

__device__ __forceinline__ u32 next_tag(u32 ep) { const u32 t = ep + 1u; return (t & 0xfffffu) ? t : t + 1u; }

__device__ __forceinline__ void lds_arrive(u32 addr, int lane) {
  asm volatile("s_waitcnt lgkmcnt(0)" ::: "memory");
  if (lane == 0) asm volatile("ds_add_u32 %0, %1" :: "v"(addr), "v"(1u) : "memory");
}
__device__ __forceinline__ u32 lds_ticket(u32 addr, int lane) {
  u32 r = 0;
  asm volatile("s_waitcnt lgkmcnt(0)" ::: "memory");
  if (lane == 0) asm volatile("ds_add_rtn_u32 %0, %1, %2\n\ts_waitcnt lgkmcnt(0)" : "=v"(r) : "v"(addr), "v"(1u) : "memory");
  return __builtin_amdgcn_readfirstlane(r);
}
template <int SLEEP>
__device__ __forceinline__ void lds_wait(u32 addr, u32 target) {
  SPINLOOP for (unsigned spins = 0; spins < (1u << 22); ++spins) {
    u32 v;
    asm volatile("ds_read_b32 %0, %1\n\ts_waitcnt lgkmcnt(0)" : "=v"(v) : "v"(addr) : "memory");
    if (v >= target) break;
    __builtin_amdgcn_s_sleep(SLEEP);
  }
}
__device__ __forceinline__ void dma16(const void* gsrc, u32 lds_dst) {
  u32 keep;
  asm volatile("s_mov_b32 %0, m0\n\ts_mov_b32 m0, %2\n\ts_nop 0\n\tglobal_load_lds_dwordx4 %1, off\n\ts_mov_b32 m0, %0"
               : "=&s"(keep) : "v"(gsrc), "s"(lds_dst) : "memory");
}
__device__ __forceinline__ void dma16nt(const void* gsrc, u32 lds_dst) {
  u32 keep;
  asm volatile("s_mov_b32 %0, m0\n\ts_mov_b32 m0, %2\n\ts_nop 0\n\tglobal_load_lds_dwordx4 %1, off nt\n\ts_mov_b32 m0, %0"
               : "=&s"(keep) : "v"(gsrc), "s"(lds_dst) : "memory");
}
__device__ __forceinline__ u32x4 ldasm(const void* p) {
  u32x4 v;
  asm volatile("global_load_dwordx4 %0, %1, off" : "=v"(v) : "v"(p) : "memory");
  return v;
}
__device__ __forceinline__ u32 ldasm_sc1(const void* p) {
  u32 v;
  asm volatile("global_load_dword %0, %1, off sc1" : "=v"(v) : "v"(p) : "memory");
  return v;
}
__device__ __forceinline__ u32x4 ldw(const bf16* p) { return *reinterpret_cast<const u32x4*>(p); }
template <int D2>
__device__ __forceinline__ s16x8 wfrag(u32 addr) {
  const s16x4 lo = __builtin_amdgcn_ds_read_tr16_b64_v4i16((lds_s4p)(uintptr_t)addr);
  const s16x4 hi = __builtin_amdgcn_ds_read_tr16_b64_v4i16((lds_s4p)(uintptr_t)(addr + D2));
  return s16x8{lo[0], lo[1], lo[2], lo[3], hi[0], hi[1], hi[2], hi[3]};
}
#define MFMA(w_, x_, c_) __builtin_amdgcn_mfma_f32_16x16x32_bf16(__builtin_bit_cast(bf16x8v, w_), __builtin_bit_cast(bf16x8v, x_), c_, 0, 0, 0)

struct Desc { const bf16* p; u32 bytes, flags; };
struct Args {
  Desc wq, x, wmod, c;
  unsigned char* ws;
  const bf16 *kc, *vc, *wo, *w1, *b1, *w2, *b2;
  bf16* out;
};

constexpr int L_W = 0;
constexpr int L_WM2 = 40960;
constexpr int L_XA = 81920;
constexpr int XS = 1312;
constexpr int L_RED = L_XA + 16384;
constexpr int L_MOD = L_RED + 8192;
constexpr int L_XR = L_MOD + 5120;
constexpr int L_PT = L_XR + 5120;
constexpr int L_ACC = L_PT + 2048;
constexpr int L_QS = L_ACC + 4096;
constexpr int L_FI = L_QS + 256;
constexpr int L_ST = L_FI + 256;
constexpr int L_CNT = L_ST + 256;
constexpr int L_END = L_CNT + 128;
static_assert(L_END <= 160 * 1024, "LDS");
constexpr u32 C_Q = L_CNT, C_SC = L_CNT + 4, C_ISS = L_CNT + 8, C_P1 = L_CNT + 20, C_P2 = L_CNT + 24, C_GO = L_CNT + 28, C_W = L_CNT + 12, C_FIN = L_CNT + 32,
              C_KI = L_CNT + 36, C_PP = L_CNT + 40, C_PUB = L_CNT + 56, C_HT = L_CNT + 64;

constexpr int NL = 32;
constexpr int PVLAG = P_LAG;
constexpr int NPRE = P_NPRE;
constexpr int NVE = P_NVE;
static_assert(PVLAG <= 16 && (PVLAG & 1) == 0 && NVE <= PVLAG && (NVE & 1) == 0, "row groups");

__global__ void __launch_bounds__(NT, 2) mega_kernel(Args a) {
  __shared__ __attribute__((aligned(1024))) unsigned char lds[L_END];
  float* red = reinterpret_cast<float*>(lds + L_RED);
  u32* xraw = reinterpret_cast<u32*>(lds + L_XR);
  u32* pT = reinterpret_cast<u32*>(lds + L_PT);
  float* accs = reinterpret_cast<float*>(lds + L_ACC);
  u32* qs = reinterpret_cast<u32*>(lds + L_QS);
  float* st = reinterpret_cast<float*>(lds + L_ST);
  u32* cnt = reinterpret_cast<u32*>(lds + L_CNT);

  u32x4 rw, rx;
  unsigned char* wsq;
  asm volatile("s_load_dwordx4 %0, %3, %4\n\ts_load_dwordx4 %1, %3, %4 offset:16\n\ts_load_dwordx2 %2, %3, 64"
               : "=&s"(rw), "=&s"(rx), "=&s"(wsq) : "s"(__builtin_amdgcn_kernarg_segment_ptr()), "s"((blockIdx.x & 1) * 32));
  __builtin_amdgcn_sched_barrier(0);
  const int t = threadIdx.x, lane = t & 63;
  const int wave = __builtin_amdgcn_readfirstlane(t >> 6);
  const int u = blockIdx.x;
  const int bh = u >> 1, half = u & 1, b_att = bh >> 5, h_att = bh & 31;
  const int r = lane >> 4, c = lane & 15;
  STAMP_DECL
  STAMPT(0, 0);
  u32 tg;
  const int g = u >> 1, ksp = g & 3, cbp = g >> 2;
  const int s128e = (((((lane & 7) >> 1) ^ ((lane >> 4) & 1)) << 1) | (lane & 1)) * 8;
  const u32 tr128 = (8 * r + (c >> 2)) * 128 + ((((c >> 3) & 1) | ((r & 1) << 1)) * 32) + (c & 3) * 8;
  if (wave < 4) {
    __builtin_amdgcn_s_setprio(3);
    const u32 rb = 8192 - 512 * half;
    const u32 hm = 0u - (u32)half;
    const u32 vo = (u32)(lane >> (4 - half)) * rb + (((lane & (15 >> half)) ^ ((lane >> 3) & (6 - 4 * half))) ^ (half ? (wave & 1) * 4 : (wave >> 1) * 8)) * 16;
    u32 so = (u32)(ksp * 160 + (half ? 8 * (wave & 1) : 4 * wave)) * rb + (cbp << (8 - half)) + ((half && wave < 2 && g < 112) ? 4096 : 0);
    const u32 step = 16 * rb, dstep = 4096 >> half, dst = L_W + wave * 1024 + (half ? (wave < 2 ? 20480 : -2048) : 0);
    const u32 oe = wave * 1280 + lane * 16, oo = (r * 1280 + ksp * 320 + (c + 16 * wave) * 16) | ((u32)(19 - c - 16 * wave) & OOB);
    const u32 oa = (oo & hm) | (oe & ~hm), ob = (wave * 1280 + 1024 + c * 16) | (hm & OOB), oc = oe | (hm & OOB), zero = 0;
    u32 ep, keep;
    u32x4 xv[4];
    const u32 pre = ((u32)u << 31) & (u32)(g - (NCH < 80 ? NCH : 80)) & (u32)(1 - wave), po = G_WO + g * 2048 + (t & 127) * 16;
    const u32 pre2 = g - (NCH - 80), po2 = po + 80 * 2048;
    const u32x4 emp = {EMPTY, EMPTY, EMPTY, EMPTY};
#define DMA1 "buffer_load_dwordx4 %7, %8, %1 offen nt lds\n\t"
#define DMAN "s_add_u32 m0, m0, %11\n\ts_add_u32 %1, %1, %10\n\t" DMA1
    asm volatile("s_mov_b32 %0, m0\n\ts_mov_b32 m0, %9\n\t"
                 "s_cmp_gt_u32 %18, 1\n\ts_waitcnt lgkmcnt(0)\n\ts_cbranch_scc1 1f\n\t"
                 "global_load_dword %2, %12, %13 sc1\n\t"
                 "buffer_load_dwordx4 %3, %14, %17, 0 offen\n\tbuffer_load_dwordx4 %4, %15, %17, 0 offen\n\t"
                 "buffer_load_dwordx4 %5, %16, %17, 0 offen offset:2560\n\tbuffer_load_dwordx4 %6, %15, %17, 0 offen offset:2560\n"
                 "1:\n\t" DMA1 DMAN DMAN DMAN DMAN DMAN DMAN DMAN DMAN DMAN "s_mov_b32 m0, %0\n\t"
                 "s_cmp_eq_u32 %19, 0\n\ts_cbranch_scc1 2f\n\tglobal_store_dwordx4 %20, %21, %13 sc1\n\t"
                 "s_cmp_ge_i32 %22, 0\n\ts_cbranch_scc1 2f\n\tglobal_store_dwordx4 %23, %21, %13 sc1\n2:\n\t"
                 "s_barrier\n\ts_waitcnt vmcnt(10)"
                 : "=&s"(keep), "+s"(so), "=&v"(ep), "=&v"(xv[0]), "=&v"(xv[1]), "=&v"(xv[2]), "=&v"(xv[3])
                 : "v"(vo), "s"(rw), "s"(dst), "s"(step), "s"(dstep), "v"(zero), "s"(wsq), "v"(oa), "v"(ob), "v"(oc), "s"(rx), "s"(wave),
                   "s"(pre), "v"(po), "v"(emp), "s"(pre2), "v"(po2) : "memory", "scc");
#undef DMAN
#undef DMA1
    asm volatile("" : "+s"(wsq));
    const __amdgpu_buffer_rsrc_t rs = __builtin_amdgcn_make_buffer_rsrc(wsq, 0, WS_END, 0x00020000);
    if (half) {
      if (wave >= 2) {
        VM0();
        STAMPT(5, 128);
      } else {
        u32x4 sv;
#pragma unroll
        for (int k = 0; k < 4; ++k) {
          const float c0 = lo16(xv[0][k]), c1 = hi16(xv[0][k]);
          sv[k] = cvt2(c0 * frcp(1.f + fexp(-c0)), c1 * frcp(1.f + fexp(-c1)));
        }
        if (c + 16 * wave < 20) *reinterpret_cast<u32x4*>(lds + L_XA + (r * 20 + c + 16 * wave) * 16) = sv;
        if (t == 0) cnt[4] = next_tag(ep);
        STAMPT(2, 0);
      }
      lds_arrive(C_P1, lane);
      lds_wait<1>(C_P1, 4);
      STAMPT(20, 0);
      tg = __builtin_amdgcn_readfirstlane(cnt[4]);
      if (CT0 && u < 8 && t == 192) { const u32 t0 = cnt[15]; st_gran(rs, G_T + u * 16, t0, t0, tg); }
      u32x4 av[5];
#pragma unroll
      for (int s = 0; s < 5; ++s) av[s] = *reinterpret_cast<const u32x4*>(lds + L_XA + (c & 3) * 320 + s * 64 + r * 16);
      {
        f32x4 acc = {0.f, 0.f, 0.f, 0.f};
#pragma unroll
        for (int s = 0; s < 5; ++s) acc = MFMA(wfrag<512>(L_W + (tr128 ^ (wave * 32)) + s * 4096), av[s], acc);
        if (c < 4) st_g22(rs, G_MOD + ((ksp * 4 + c) * 960 + cbp * 16 + wave * 4 + r) * 16, acc, tg);
      }
      STAMPT(7, 0);
      if (wave < 2) { VM0(); lds_arrive(C_P2, lane); }
      lds_wait<1>(C_P2, 2);
      {
        f32x4 acc = {0.f, 0.f, 0.f, 0.f};
#pragma unroll
        for (int s = 0; s < 5; ++s) acc = MFMA(wfrag<512>(L_W + 20480 + (tr128 ^ (wave * 32)) + s * 4096), av[s], acc);
        if (c < 4 && g < 112) st_g22(rs, G_MOD + ((ksp * 4 + c) * 960 + (cbp + 32) * 16 + wave * 4 + r) * 16, acc, tg);
      }
      STAMPT(9, 0);
    } else {
      if (wave < 2) {
#pragma unroll
        for (int i = 0; i < 2; ++i) {
          const int b = wave + 2 * i;
          float xf[16];
#pragma unroll
          for (int k = 0; k < 4; ++k) { xf[2 * k] = lo16(xv[2 * i][k]); xf[2 * k + 1] = hi16(xv[2 * i][k]); xf[8 + 2 * k] = lo16(xv[2 * i + 1][k]); xf[8 + 2 * k + 1] = hi16(xv[2 * i + 1][k]); }
          float s = 0.f, s2 = 0.f;
#pragma unroll
          for (int k = 0; k < 8; ++k) { s += xf[k]; s2 += xf[8 + k]; }
          if (lane < 16) s += s2;
          const float mean = wave_sum(s) * (1.f / DM);
          float v = 0.f, v2 = 0.f;
#pragma unroll
          for (int k = 0; k < 8; ++k) { const float d = xf[k] - mean; v += d * d; const float d2 = xf[8 + k] - mean; v2 += d2 * d2; }
          if (lane < 16) v += v2;
          v = wave_sum(v);
          if (lane == 0) { st[24 + 2 * b] = bfr(mean); st[24 + 2 * b + 1] = frcp(bfr(fsqrt(bfr(bfr(v * (1.f / DM)) + 1e-5f)))); }
          *reinterpret_cast<u32x4*>(xraw + b * 320 + lane * 4) = xv[2 * i];
          if (lane < 16) *reinterpret_cast<u32x4*>(xraw + b * 320 + 256 + lane * 4) = xv[2 * i + 1];
        }
        if (t == 0) cnt[4] = next_tag(ep);
        lds_arrive(C_P1, lane);
      }
      lds_wait<1>(C_P1, 2);
      STAMPT(20, 0);
      tg = __builtin_amdgcn_readfirstlane(cnt[4]);
      if (CT0 && u < 8 && t == 192) { const u32 t0 = cnt[15]; st_gran(rs, G_T + u * 16, t0, t0, tg); }
      if (wave < 3) {
        const int b = t / 40, j = t % 40;
        u32 offs[8];
        u32x4 gm[8];
#pragma unroll
        for (int sel = 0; sel < 2; ++sel)
#pragma unroll
          for (int ks = 0; ks < 4; ++ks) offs[sel * 4 + ks] = t < 160 ? G_MOD + ((ks * 4 + b) * 960 + sel * 160 + ksp * 40 + j) * 16 : OOB;
        __builtin_amdgcn_s_sleep(16);
        gather22<8, 4>(rs, offs, gm, tg);
        STAMPT(8, 0);
        const f32x4 sa = (un_g22(gm[0]) + un_g22(gm[1])) + (un_g22(gm[2]) + un_g22(gm[3]));
        const f32x4 ca = (un_g22(gm[4]) + un_g22(gm[5])) + (un_g22(gm[6]) + un_g22(gm[7]));
        const int bb = b & 3;
        const float mean = st[24 + 2 * bb], sd = st[24 + 2 * bb + 1];
        const u32x2 xw = *reinterpret_cast<const u32x2*>(xraw + bb * 320 + ksp * 80 + 2 * j);
        float hv[4];
#pragma unroll
        for (int k = 0; k < 4; ++k) {
          const float xf = (k & 1) ? hi16(xw[k >> 1]) : lo16(xw[k >> 1]);
          const float n = bfr(bfr(xf - mean) * sd);
          hv[k] = bfr(n * bfr(1.f + bfr(ca[k]))) + bfr(sa[k]);
        }
        if (t < 160) *reinterpret_cast<u32x2*>(lds + L_XA + t * 8) = u32x2{cvt2(hv[0], hv[1]), cvt2(hv[2], hv[3])};
      }
      VM0();
      lds_arrive(C_P2, lane);
      lds_wait<1>(C_P2, 4);
      u32x4 av[5];
#pragma unroll
      for (int s = 0; s < 5; ++s) av[s] = *reinterpret_cast<const u32x4*>(lds + L_XA + (c & 3) * 320 + s * 64 + r * 16);
#pragma unroll
      for (int e = 0; e < 2; ++e) {
        const int nt = 2 * wave + e;
        const u32 tr256 = (8 * r + (c >> 2)) * 256 + ((((c >> 2) | ((r & 1) << 2)) ^ nt) * 32) + (c & 3) * 8;
        f32x4 acc = {0.f, 0.f, 0.f, 0.f};
#pragma unroll
        for (int s = 0; s < 5; ++s) acc = MFMA(wfrag<1024>(L_W + tr256 + s * 8192), av[s], acc);
        if (c < 4) st_g22(rs, G_Q + ((ksp * 4 + c) * 1024 + cbp * 32 + nt * 4 + r) * 16, acc, tg);
      }
      lds_arrive(C_GO, lane);
      STAMPT(9, 0);
    }
  } else {
    __builtin_amdgcn_s_setprio(2);
    if (t < 256 + 32) cnt[lane] = lane == 15 ? (u32)__builtin_amdgcn_s_memrealtime() : 0;
  }
  u32 kvoff = (u32)(bh * SK + (SK - WIN)) * 256u + (u32)(half * 8 + wave) * (NL * 1024u) + lane * 16, ph[8];
#pragma unroll
  for (int i = 0; i < 8; ++i) ph[i] = ((i + (u >> 3)) & 7) * 4096;
  asm volatile("" : "+v"(kvoff));
  asm volatile("s_waitcnt lgkmcnt(0)" : "+s"(wsq), "+s"(ph[0]), "+s"(ph[1]), "+s"(ph[2]), "+s"(ph[3]), "+s"(ph[4]), "+s"(ph[5]), "+s"(ph[6]), "+s"(ph[7])
               : "s"(rw), "s"(rx));
  const __amdgpu_buffer_rsrc_t rs = __builtin_amdgcn_make_buffer_rsrc(wsq, 0, WS_END, 0x00020000);
  {
    u32x4 kv[NL], kh[16];
    const __amdgpu_buffer_rsrc_t rk = __builtin_amdgcn_make_buffer_rsrc((void*)a.kc, 0, 1u << 28, 0x00020000);
    const __amdgpu_buffer_rsrc_t rv = __builtin_amdgcn_make_buffer_rsrc((void*)a.vc, 0, 1u << 28, 0x00020000);
#define LDKV(rsrc, off, i) __builtin_amdgcn_raw_buffer_load_b128(rsrc, (off) + ((i) & 3) * 1024, ph[(i) >> 2], 2)
#define LDS_INC(addr) do { asm volatile("" ::: "memory"); if (lane == 0) asm volatile("ds_add_u32 %0, %1" :: "v"(addr), "v"(1u) : "memory"); } while (0)
#define TR_STEP(x, HALF, CTRL, BIT) _Pragma("unroll") for (int hh = 0; hh < (HALF); ++hh) { \
    const float lo_ = x[hh], hi_ = x[hh + (HALF)]; const float t1_ = lo_ + dpp<CTRL>(lo_), t2_ = hi_ + dpp<CTRL>(hi_); \
    x[hh] = (c & (BIT)) ? t2_ : t1_; }
#define SCORE16(x, src, o) float x[16]; \
    _Pragma("unroll") for (int i = 0; i < 16; ++i) { float d = 0.f; _Pragma("unroll") for (int k = 0; k < 4; ++k) d = dot2(src[(o) + i][k], qpk[k], d); x[i] = d; } \
    TR_STEP(x, 8, XOR8, 8) TR_STEP(x, 4, XOR7, 4) TR_STEP(x, 2, XOR2, 2)
#define SCALED(v) bfr(bfr(v) * 0.08838834764831845f)
#define PV2(i) _Pragma("unroll") for (int k = 0; k < 4; ++k) { \
      acc[2 * k] = dot2(__builtin_amdgcn_perm(kv[(i) + 1][k], kv[i][k], 0x05040100u), pw[(i) / 2], acc[2 * k]); \
      acc[2 * k + 1] = dot2(__builtin_amdgcn_perm(kv[(i) + 1][k], kv[i][k], 0x07060302u), pw[(i) / 2], acc[2 * k + 1]); }
#define GET_Q() lds_wait<2>(C_Q, 1); u32x4 qpk; \
    asm volatile("ds_read_b128 %0, %2\n\tds_read_b32 %1, %3\n\ts_waitcnt lgkmcnt(0)" : "=&v"(qpk), "=&v"(tg) : "v"((u32)(L_QS + 16 * c)), "v"((u32)(L_CNT + 16)) : "memory"); \
    tg = __builtin_amdgcn_readfirstlane(tg);
#define GET_P() _Pragma("unroll") for (int i4 = 0; i4 < 4; ++i4) { \
      const u32x4 pv = *reinterpret_cast<const u32x4*>(pT + (wave * 4 + r) * 16 + i4 * 4); \
      _Pragma("unroll") for (int j = 0; j < 4; ++j) pw[i4 * 4 + j] = pv[j]; }
    auto gather_q = [&]() __attribute__((always_inline)) {
        u32 offs[2];
        u32x4 gq[2];
#pragma unroll
        for (int e = 0; e < 2; ++e) offs[e] = G_Q + (((2 * (lane >> 5) + e) * 4 + b_att) * 1024 + h_att * 32 + (lane & 31)) * 16;
        {
          gq[0] = ld_gran(rs, offs[0]); gq[1] = ld_gran(rs, offs[1]);
          const u32x4 gt = ld_gran(rs, lane < 8 ? G_T + lane * 16 : OOB);
          LDS_INC(C_KI);
          {
            const u32 own = cnt[15];
            const bool ok = lane < 8 && gt[1] == tg;
            float d = ok ? __builtin_amdgcn_fmed3f((float)(int)(gt[0] - own + TOFF), -400.f, 400.f) : 0.f;
            d += dpp<XOR1>(d); d += dpp<XOR2>(d); d += dpp<XOR7>(d);
            const int n = __builtin_popcountll(__ballot(ok));
            if (lane == 0 && n) cnt[15] = own + (u32)(int)(d * frcp((float)n));
          }
          const bool hit = (((gq[0][1] ^ (tg << 12)) | (gq[0][3] ^ (tg << 12)) | (gq[1][1] ^ (tg << 12)) | (gq[1][3] ^ (tg << 12))) < 4096u);
          if (!__all(hit)) gather22<2, 4>(rs, offs, gq, tg);
        }
        STAMPW(10);
        f32x4 q4 = un_g22(gq[0]) + un_g22(gq[1]);
#pragma unroll
        for (int k = 0; k < 4; ++k) q4[k] = swap32_add(q4[k], q4[k]);
        if (lane < 32) *reinterpret_cast<u32x2*>(qs + 2 * lane) = u32x2{cvt2(q4[0], q4[1]), cvt2(q4[2], q4[3])};
        lds_arrive(C_Q, lane);
    };
    const int qw = half ? QODD : QEVEN;
    const u32 tau = half ? (g < 74 ? TAU_A : TAU_B) : (g >= 122 ? TAU_X : TAU_E);
#define PACE(n) if ((n) > PB) { const u32 tt_ = t0 + (((n) - PB) * tau >> 4); \
      SPINLOOP while ((int)((u32)__builtin_amdgcn_s_memrealtime() - tt_) < 0) __builtin_amdgcn_s_sleep(1); asm volatile("" ::: "memory"); }
    float acc[8];
#pragma unroll
    for (int k = 0; k < 8; ++k) acc[k] = 0.f;
    u32 pw[16];
    if (__builtin_expect(wave >= 4, 1)) {
      STAMPT(30, 448);
      u32 t0 = (u32)__builtin_amdgcn_s_memrealtime();
      BAR();
      STAMPT(31, 448);
      const u32 kvoff2 = kvoff ^ 131072u;
#pragma unroll
      for (int i = 0; i < 48; ++i) {
        if (i == NPRE && half == 0) lds_wait<2>(C_GO, GOPOS ? 3 : 4);
        if (PK && (i & 3) == 0) PACE(i)
        if (i < NL) kv[i < NL ? i : 0] = LDKV(rk, kvoff, i & 31); else kh[i < NL ? 0 : i - NL] = LDKV(rk, kvoff2, i & 31);
      }
      if (!QPRE || wave != qw) LDS_INC(C_KI);
      STAMPT(25, 448);
      if ((QODD >= 4 || QEVEN >= 4) && wave == qw) {
        __builtin_amdgcn_s_setprio(3);
        lds_wait<2>(C_P1, half ? 4 : 2);
        tg = __builtin_amdgcn_readfirstlane(cnt[4]);
        gather_q();
        __builtin_amdgcn_s_setprio(2);
      }
      GET_Q()
      SCORE16(xa, kv, 0)
      asm volatile("" : "+v"(xa[0]), "+v"(xa[1]), "+v"(kvoff) :: "memory");
#pragma unroll
      for (int i = 0; i < P_NVA; ++i) kv[i] = LDKV(rv, kvoff, i);
      asm volatile("" ::: "memory");
      SCORE16(xb, kv, 16)
      xa[2] = xb[0]; xa[3] = xb[1];
      TR_STEP(xa, 2, XOR1, 1)
      float s0 = SCALED(xa[0]), s1 = SCALED(xa[1]);
      asm volatile("" : "+v"(s0), "+v"(s1), "+v"(kvoff) :: "memory");
#pragma unroll
      for (int i = P_NVA; i < NVE; ++i) kv[i] = LDKV(rv, kvoff, i);
      asm volatile("" ::: "memory");
      SCORE16(xh, kh, 0)
      const float s2 = SCALED(xh[0] + dpp<XOR1>(xh[0])), s3 = SCALED(xh[1] + dpp<XOR1>(xh[1]));
      const float m = wave_max(fmaxf(fmaxf(s0, s1), fmaxf(s2, s3)));
      const float p0 = __expf(s0 - m), p1 = __expf(s1 - m), p2 = __expf(s2 - m), p3 = __expf(s3 - m);
      const float l = wave_sum(fmaf(p2 + p3, 0.5f, p0 + p1));
      if (lane == 0) { st[wave] = m; st[8 + wave] = l; }
      pT[(wave * 4 + r) * 16 + (c & 1) * 8 + (c >> 1)] = cvt2(p0, p1);
      pT[((wave - 4) * 4 + r) * 16 + (c >> 1)] = cvt2(p2, p3);
      lds_arrive(C_PP + 4 * (wave - 4), lane);
      __builtin_amdgcn_s_setprio(0);
      STAMPT(27, 448); WSTAMP(1);
      if (CT0) t0 = __builtin_amdgcn_readfirstlane(cnt[15]);
      GET_P()
#pragma unroll
      for (int i = NVE; i < NL; i += 2) {
        PACE(96 + i)
        kv[i] = LDKV(rv, kvoff, i);
        kv[i + 1] = LDKV(rv, kvoff, i + 1);
        asm volatile("" ::: "memory");
        if (i == NL - 2) { LDS_INC(C_ISS); STAMPT(26, 448); }
        if (i >= PVLAG) {
          PV2(i >= PVLAG ? i - PVLAG : 0)
#pragma unroll
          for (int k = 0; k < 8; ++k) asm volatile("" : "+v"(acc[k]));
        }
      }
    } else {
      const int ex = g < 74 ? EA : g < 122 ? EB : EC;
      u32 kva = (!half && ex > 2) ? OOB : kvoff, kvb = (!half && ex > 0) ? OOB : kvoff;
      const u32 kxa = (half && ex > 2) ? kvoff - 8 * NL * 1024u : OOB, kxb = (half && ex > 0) ? kvoff - 8 * NL * 1024u : OOB;
      u32x4 kx[4];
      if (half && g < 68) __builtin_amdgcn_raw_buffer_store_b128(u32x4{EMPTY, EMPTY, EMPTY, EMPTY}, rs, G_OUT + (g * 256 + t) * 16, 0, 16);
      const u32 t0 = __builtin_amdgcn_readfirstlane(cnt[15]);
#pragma unroll
      for (int i = 0; i < 16; ++i) {
        if (PK && (i & 3) == 0) PACE(48 + i)
        kh[i] = LDKV(rk, i < 12 ? kvoff : i < 14 ? kva : kvb, 16 + i);
      }
#pragma unroll
      for (int i = 0; i < 4; ++i) kx[i] = LDKV(rk, i < 2 ? kxa : kxb, 28 + i);
      LDS_INC(C_KI);
      if (wave == 0) {
        if (half == 0 && g >= 80 && g < 120) {
          const int fb = g - 80;
          u32 of2[4];
          u32x4 gm[4];
#pragma unroll
          for (int ks = 0; ks < 4; ++ks) of2[ks] = G_MOD + ((ks * 4 + r) * 960 + 320 + fb * 16 + c) * 16;
          gather22<4, 4>(rs, of2, gm, tg);
          const f32x4 mv = (un_g22(gm[0]) + un_g22(gm[1])) + (un_g22(gm[2]) + un_g22(gm[3]));
          st_gran(rs, G_MF + (((fb / 10) * 4 + r) * 160 + (fb % 10) * 16 + c) * 16, cvt2(mv[0], mv[1]), cvt2(mv[2], mv[3]), tg);
        }
      }
      __builtin_amdgcn_s_setprio(0);
      lds_wait<2>(C_KI, P_GATE);
      if (QHOLD && wave == qw - 4) lds_wait<2>(C_Q, 1);
      asm volatile("" : "+v"(kvoff), "+v"(kva), "+v"(kvb));
#pragma unroll
      for (int i = 0; i < NL; ++i) {
        if (PL && (i & 3) == 0) PACE(64 + i)
        kv[i] = LDKV(rv, i < 28 ? kvoff : i < 30 ? kva : kvb, i);
      }
      STAMPT(1, 0);
      GET_Q()
      float xx[4];
#pragma unroll
      for (int i = 0; i < 4; ++i) { float d = 0.f;
#pragma unroll
        for (int k = 0; k < 4; ++k) d = dot2(kx[i][k], qpk[k], d);
        xx[i] = d; }
      asm volatile("" : "+v"(xx[0]), "+v"(xx[1]), "+v"(xx[2]), "+v"(xx[3]) :: "memory");
#pragma unroll
      for (int i = 0; i < 4; ++i) kx[i] = LDKV(rv, i < 2 ? kxa : kxb, 28 + i);
      LDS_INC(C_ISS);
      TR_STEP(xx, 2, XOR8, 8)
#pragma unroll
      for (int i = 0; i < 2; ++i) { xx[i] += dpp<XOR7>(xx[i]); xx[i] += dpp<XOR2>(xx[i]); xx[i] += dpp<XOR1>(xx[i]); }
      SCORE16(xh, kh, 0)
      const float ninf = -__builtin_inff();
      const bool mine = half || c < 16 - ex, extra = half && c >= 16 - 4 * ex;
      xh[0] += dpp<XOR1>(xh[0]); xh[1] += dpp<XOR1>(xh[1]);
      const float s2 = mine ? SCALED(xh[0]) : ninf, s3 = mine ? SCALED(xh[1]) : ninf;
      const float s4 = extra ? SCALED(xx[0]) : ninf, s5 = extra ? SCALED(xx[1]) : ninf;
      float m = wave_max(fmaxf(fmaxf(s2, s3), fmaxf(s4, s5)));
      lds_wait<2>(C_PP + 4 * wave, 1);
      const float me = st[wave + 4];
      m = fmaxf(m, me);
      const float p2 = __expf(s2 - m), p3 = __expf(s3 - m), p4 = __expf(s4 - m), p5 = __expf(s5 - m);
      const float l = wave_sum(fmaf(p4 + p5, 0.25f, p2 + p3)) * 0.5f;
      const u32 pxo = cvt2(p4, p5), pxt = dppu<XOR8>(pxo);
      const u32 px[2] = {c < 8 ? pxo : pxt, c < 8 ? pxt : pxo};
      if (lane == 0) { st[wave] = m; st[8 + wave] = l; }
      pT[(wave * 4 + r) * 16 + 8 + (c >> 1)] = cvt2(p2, p3);
      STAMPT(4, 0); WSTAMP(1);
      if (lds_ticket(C_SC, lane) == 3) {
        const float mw = st[lane & 7], lw = st[8 + (lane & 7)];
        float M = fmaxf(mw, dpp<XOR1>(mw)); M = fmaxf(M, dpp<XOR2>(M)); M = fmaxf(M, dpp<XOR7>(M));
        const float e = __expf(mw - M);
        float L = lw * e;
        L += dpp<XOR1>(L); L += dpp<XOR2>(L); L += dpp<XOR7>(L);
        if (lane == 0) st_gran(rs, G_ML + u * 64, f2u(M), f2u(L), tg);
        if (lane < 8) st[16 + lane] = e * frcp(L);
      }
      GET_P()
#pragma unroll
      for (int i = 0; i < 16; i += 2) { PV2(i) }
      const float f = __expf(me - m);
#pragma unroll
      for (int k = 0; k < 8; ++k) acc[k] *= f;
#pragma unroll
      for (int i = 16; i < NL - PVLAG; i += 2) { PV2(i) }
#pragma unroll
      for (int i = 0; i < 4; i += 2)
#pragma unroll
        for (int k = 0; k < 4; ++k) {
          acc[2 * k] = dot2(__builtin_amdgcn_perm(kx[i + 1][k], kx[i][k], 0x05040100u), px[i / 2], acc[2 * k]);
          acc[2 * k + 1] = dot2(__builtin_amdgcn_perm(kx[i + 1][k], kx[i][k], 0x07060302u), px[i / 2], acc[2 * k + 1]);
        }
    }
#pragma unroll
    for (int i = NL - PVLAG; i <= NL - HX; i += 2) { PV2(i) }
    if (!HT) { PV2(NL - HX + 2) } else {
      u32 tk = 0;
      u64 tn;
      asm volatile("s_mov_b64 exec, 1\n\tds_add_rtn_u32 %0, %10, %11\n\ts_mov_b64 exec, -1\n\ts_memrealtime %1"
                   : "+v"(tk), "=s"(tn), "+v"(acc[0]), "+v"(acc[1]), "+v"(acc[2]), "+v"(acc[3]), "+v"(acc[4]), "+v"(acc[5]), "+v"(acc[6]), "+v"(acc[7])
                   : "v"((u32)C_HT), "v"(1u) : "memory");
      PV2(NL - HX + 2)
      asm volatile("s_waitcnt lgkmcnt(0)" : "+v"(tk), "+s"(tn), "+v"(acc[0]), "+v"(acc[1]), "+v"(acc[2]), "+v"(acc[3]), "+v"(acc[4]), "+v"(acc[5]), "+v"(acc[6]), "+v"(acc[7]));
      __builtin_amdgcn_raw_buffer_store_b64(u32x2{(u32)tn + HTD, tg}, rs, tk == 7 ? G_HT + u * 128 : OOB, 0, 16);
      asm volatile("" ::: "memory");
    }
#pragma unroll
    for (int i = NL - HX + 4; i < NL; i += 2) { PV2(i) }
#undef PV2
#undef TR_STEP
#undef SCORE16
#undef SCALED
#undef GET_Q
#undef GET_P
#undef LDKV
#pragma unroll
    for (int i = 0; i < 4; ++i) acc[i] = swap32_add(acc[i], acc[i + 4]);
#pragma unroll
    for (int i = 0; i < 2; ++i) acc[i] = swap16_add(acc[i], acc[i + 2]);
    *reinterpret_cast<float2*>(accs + wave * 128 + c * 8 + 2 * r) = float2{acc[0], acc[1]};
    STAMPT(6, 0); STAMPT(28, 448); WSTAMP(2);
  }
  const u32x4 rsv = {(u32)(uintptr_t)wsq, (u32)((uintptr_t)wsq >> 32) & 0xffffu, WS_END, 0x00020000u};
  const int role = half ? (g < 74 ? 0 : 1) : (g >= 122 ? 0 : g < 40 ? 2 : 3);
  const int ia = half ? g : g - 48;
  const int ksA = ia & 7, cbA = ia >> 3;
#define HEADA(hh) ((ksA & 3) + (ksA >> 2) * 16 + (hh) * 4)
  const int iB = g - 74, jB = !JMAP ? iB : iB < 48 ? (iB & ~7) | ((iB & 3) << 1) | ((iB >> 2) & 1) : iB < 50 ? 2 * iB - 48 : iB < 52 ? iB + 2 : 2 * iB - 55;
  u32x4 ml = {0u, 0u, 0u, 0u};
  const u32 blkA = ((c & 3) * 32 + HEADA(wave & 3)) * 2 + ((c >> 2) & 1);
  u32 om = G_ML + blkA * 64;
#define MLOOK(W_) asm volatile(LK(0, 1, "0", 0, 2) LKEND : "+v"(ml) : "v"(om), "s"(m2), [rs] "s"(rsv), [w] "i"(W_) : "memory");
  u64 m2 = role == 0 && wave >= 4 ? 0xffull : 0;
  const u32 tkf = lds_ticket(C_FIN, lane);
  const bool lastw = tkf == 7;
  if (!lastw) {
    MLOOK(63)
#define GO(n, name) "s_cmp_eq_u32 %0, " #n "\n\ts_cbranch_scc1 .Lpad_" #name "\n\t"
    asm volatile("s_mov_b64 exec, 0\n\t" GO(0, a3) GO(1, a2) GO(2, a1) GO(3, a0) GO(8, b3) GO(9, b2) GO(10, b1) GO(11, b0) GO(16, c1) GO(17, c0)
                 ".Lpad_back:\n\ts_mov_b64 exec, -1" :: "s"(WARM ? role * 8 + tkf : 7) : "scc");
#undef GO
  }
  if (lastw) {
    if (lane < 32) {
      f32x4 o = {0.f, 0.f, 0.f, 0.f};
#pragma unroll
      for (int w = 0; w < 8; ++w) o += st[16 + w] * *reinterpret_cast<const f32x4*>(accs + w * 128 + 4 * lane);
      const u32x2 ow = {min(cvt2(o[0], o[1]), EMPTY - 1), min(cvt2(o[2], o[3]), EMPTY - 1)};
      __builtin_amdgcn_raw_buffer_store_b64(ow, rs, G_ATT + u * 256 + lane * 8, 0, 16);
    }
    MLOOK(63)
    LDS_INC(C_PUB);
    STAMPW(11);
  }
  u32 gaw[2];
  u32x2 xw = {0u, 0u};
  float b1v;
  if (role < 2 && wave < 4) {
    if (role == 0) {
      u32 offs[1] = {G_MF + (r * 160 + cbA * 16 + c) * 16};
      u32x4 gm[1];
      if (ksA == 0) xw = *reinterpret_cast<const u32x2*>(a.x.p + r * DM + cbA * 64 + c * 4);
      gather<1>(rs, offs, gm, tg);
      gaw[0] = gm[0][0]; gaw[1] = gm[0][2];
    } else {
      if (MFPOS == 1) lds_wait<2>(C_ISS, 8);
      b1v = bf2f(a.b1[jB * 32 + (t & 31)]);
      u32 offs[5];
      u32x4 gm[5];
#pragma unroll
      for (int i = 0; i < 5; ++i) offs[i] = G_MF + (640 + t + 256 * i) * 16;
      gather<5>(rs, offs, gm, tg);
#pragma unroll
      for (int i = 0; i < 5; ++i) {
        const int it = t + 256 * i, row = it / 160, gi = it % 160;
        u32 w0 = gm[i][0], w1 = gm[i][2];
        if (row >= 4) { w0 = cvt2(1.f + lo16(w0), 1.f + hi16(w0)); w1 = cvt2(1.f + lo16(w1), 1.f + hi16(w1)); }
        *reinterpret_cast<u32x2*>(lds + L_XA + (row < 4 ? row + 8 : row) * XS + gi * 8) = u32x2{w0, w1};
      }
      asm volatile("" : "+v"(b1v));
    }
  }
  if (role < 2 && wave < 4) {
    if (half) STAMPT(8, 0);
    lds_wait<2>(C_ISS, 8);
    if (role == 0) {
      const bf16* wp = a.wo + (size_t)(HEADA(wave) * 128 + (lane >> 3)) * DM + cbA * 64;
#pragma unroll
      for (int i = 0; i < 16; ++i) {
        if (i == 2) PAD(a0, back);
        if ((i == N1A && (half || N1A == N1E)) || (i == N1E && !half)) lds_wait<2>(C_PUB, 1);
        dma16(wp + (size_t)(8 * i) * DM + (s128e ^ ((i & 1) * 32)), L_W + wave * 16384 + i * 1024);
      }
      LDS_INC(C_ISS);
    } else {
      const bf16* wp = a.w1 + (size_t)(wave * 160 + (lane >> 2)) * FF + jB * 32 + ((((((lane >> 1) & 1) ^ (lane >> 5)) << 1) | (lane & 1)) * 8);
#pragma unroll
      for (int i = 0; i < 10; ++i) {
        if (i == 2) PAD(bd, back);
        if (i == N1B) lds_wait<2>(C_PUB, 1);
        dma16(wp + (size_t)(16 * i) * FF, L_W + (wave * 10 + i) * 1024);
      }
      const bf16* wp2 = a.w2 + (size_t)(jB * 32 + wave * 8 + (lane >> 3)) * DM + (s128e ^ ((wave & 1) * 32));
#pragma unroll
      for (int k = 0; k < 10; ++k) {
        if (k + 10 == N1B) lds_wait<2>(C_PUB, 1);
        dma16(wp2 + k * 64, L_WM2 + (k * 4 + wave) * 1024);
      }
    }
    STAMPT(16, 0);
  }
#define SELECTOR(sel) u32x4 sel; \
    _Pragma("unroll") for (int d = 0; d < 4; ++d) sel[d] = ((c & 2) == 0 && d == (c >> 2)) ? ((c & 1) ? 0x3f800000u : 0x00003f80u) : 0u;
#define PAIRSUM(sm) (((c & 8) ? sm[1] : sm[0]) + dpp<XOR8>((c & 8) ? sm[0] : sm[1]))
  const u32 tb = __builtin_amdgcn_readfirstlane(cnt[15]);
  if (role == 0) {
    PAD(a1, back);
    u32 a_x = L_XA + ((wave & 3) * 8 + (c & 7)) * 272 + r * 16;
    if (wave < 4) {
      PAD(a2, back);
      float ga4[4] = {lo16(gaw[0]), hi16(gaw[0]), lo16(gaw[1]), hi16(gaw[1])}, x4[4] = {lo16(xw[0]), hi16(xw[0]), lo16(xw[1]), hi16(xw[1])};
      u32 a_red = L_RED + ((wave * 4 + c) * 64 + r * 4) * 4, a_sum = L_RED + lane * 16, a_sc = L_ST + (32 + wave * 8 + (c & 7)) * 4;
      u32 a_out = wave < NREP ? G_WO + wave * WO_REP + ((cbA * 4 + (c >> 2)) * 8 + ksA) * 128 + r * 32 + (c & 3) * 8 : OOB;
      u32 a_w[4];
#pragma unroll
      for (int nt = 0; nt < 4; ++nt) a_w[nt] = L_W + wave * 16384 + (tr128 ^ (nt * 32));
      asm volatile("" : "+v"(ga4[0]), "+v"(ga4[1]), "+v"(ga4[2]), "+v"(ga4[3]), "+v"(x4[0]), "+v"(x4[1]), "+v"(x4[2]), "+v"(x4[3]));
      asm volatile("s_waitcnt vmcnt(%0)" :: "i"(AW) : "memory");
      LDS_INC(C_PP + 4 * wave);
      asm volatile("s_waitcnt vmcnt(4)" : "+v"(a_red), "+v"(a_sum), "+v"(a_sc), "+v"(a_out), "+v"(a_x), "+v"(a_w[0]), "+v"(a_w[1]), "+v"(a_w[2]), "+v"(a_w[3]) :: "memory");
      s16x8 wf[4][4];
#pragma unroll
      for (int s = 0; s < 3; ++s)
#pragma unroll
        for (int nt = 0; nt < 4; ++nt) wf[s][nt] = wfrag<512>(a_w[nt] + s * 4096);
      announce(rs, G_HA + ia * 128, C_GO, C_W, 8, tb, HAW, tg);
      STAMPT(19, 0);
      lds_wait<1>(C_PP + 4 * wave, 3);
      STAMPT(12, 0);
      PAD(a3, back);
      u32x4 av[4];
#pragma unroll
      for (int s = 0; s < 4; ++s) av[s] = *reinterpret_cast<const u32x4*>(lds + a_x + s * 64);
      const float wsc = *reinterpret_cast<const float*>(lds + a_sc);
      asm volatile("s_waitcnt vmcnt(1)" ::: "memory");
      STAMPT(23, 0);
#pragma unroll
      for (int nt = 0; nt < 4; ++nt) wf[3][nt] = wfrag<512>(a_w[nt] + 3 * 4096);
      __builtin_amdgcn_sched_barrier(0);
      f32x4 acc[4];
#pragma unroll
      for (int nt = 0; nt < 4; ++nt) acc[nt] = f32x4{0.f, 0.f, 0.f, 0.f};
#pragma unroll
      for (int s = 0; s < 4; ++s)
#pragma unroll
        for (int nt = 0; nt < 4; ++nt) acc[nt] = MFMA(wf[s][nt], av[s], acc[nt]);
#pragma unroll
      for (int nt = 0; nt < 4; ++nt) { acc[nt] *= wsc; asm("" : "+v"(acc[nt])); }
#pragma unroll
      for (int nt = 0; nt < 4; ++nt)
#pragma unroll
        for (int k = 0; k < 4; ++k) { float v = acc[nt][k] + dpp<SHL4>(acc[nt][k]); asm("" : "+v"(v)); acc[nt][k] = v; }
      asm volatile("" : "+v"(acc[0]), "+v"(acc[1]), "+v"(acc[2]), "+v"(acc[3]));
      if (c < 4) {
#pragma unroll
        for (int nt = 0; nt < 4; ++nt) *reinterpret_cast<f32x4*>(lds + a_red + nt * 64) = acc[nt];
      }
      BAR();
      STAMPT(21, 0);
      f32x4 pp[4];
#pragma unroll
      for (int w = 0; w < 4; ++w) pp[w] = *reinterpret_cast<const f32x4*>(lds + a_sum + w * 1024);
      asm volatile("" : "+v"(pp[0]), "+v"(pp[1]), "+v"(pp[2]), "+v"(pp[3]));
      const f32x4 s = (pp[0] + pp[1]) + (pp[2] + pp[3]);
      const u32x2 o = {min(cvt2(fmaf(s[0], ga4[0], x4[0]), fmaf(s[1], ga4[1], x4[1])), EMPTY - 1), min(cvt2(fmaf(s[2], ga4[2], x4[2]), fmaf(s[3], ga4[3], x4[3])), EMPTY - 1)};
      __builtin_amdgcn_raw_buffer_store_b64(o, rs, a_out, 0, 16);
    } else {
      u32 oa = G_ATT + blkA * 256 + (c >> 3) * 64 + r * 16;
      a_x += (c >> 3) * 64;
      u32x4 av[2];
      u64 m0 = ~0ull, m1 = ~0ull;
      av[0] = av[1] = u32x4{0u, 0u, 0u, 0u};
      asm volatile("s_waitcnt vmcnt(0)" : "+v"(oa), "+v"(a_x), "+v"(ml), "+v"(av[0]), "+v"(av[1]) :: "memory");
      SPINLOOP for (unsigned spins = 0; spins < SPIN_CAP; ++spins) {
        m2 &= __builtin_amdgcn_ballot_w64(ml[1] != tg) | __builtin_amdgcn_ballot_w64(ml[3] != tg);
        if (!m2) break;
        if (!spins) FAIL(4);
        __builtin_amdgcn_s_sleep(ASL);
        MLOOK(0)
      }
      {
        const float M = u2f(ml[0]), L = u2f(ml[2]), Mo = dpp<XOR7>(dpp<0x1B>(M)), Lo = dpp<XOR7>(dpp<0x1B>(L));
        const float Mx = fmaxf(M, Mo), le = L * __expf(M - Mx);
        const float wsc = le * frcp(fmaf(Lo, __expf(Mo - Mx), le));
        if (lane < 8) st[wave * 8 + lane] = wsc;
      }
#define ALOOK(W_) \
        asm volatile(LK(0, 2, "0", 0, 3) LK(1, 2, "0", 128, 4) LKEND \
                     : "+v"(av[0]), "+v"(av[1]) : "v"(oa), "s"(m0), "s"(m1), [rs] "s"(rsv), [w] "i"(W_) : "memory"); \
        m0 = __builtin_amdgcn_ballot_w64(missing(av[0])); \
        m1 = __builtin_amdgcn_ballot_w64(missing(av[1]));
      lds_wait<1>(C_PP + 4 * (wave - 4), 2);
      STAMPT(29, 448);
      ALOOK(0)
      STAMPT(18, 448);
      if (m0 | m1) {
        FAIL(1);
        SPINLOOP for (unsigned spins = 0; spins < SPIN_CAP; ++spins) {
          ALOOK(2 * (PDA - 1))
          if (!(m0 | m1)) break;
          __builtin_amdgcn_s_sleep(ASL);
        }
      }
#undef ALOOK
      asm volatile("" : "+v"(av[0]), "+v"(av[1]));
      *reinterpret_cast<u32x4*>(lds + a_x) = av[0];
      *reinterpret_cast<u32x4*>(lds + a_x + 128) = av[1];
      LDS_INC(C_PP + 4 * (wave - 4));
      STAMPT(22, 448);
      announce(rs, G_HA + ia * 128, C_GO, C_W, 8, tb, HAG, tg);
      BAR();
      asm volatile("" :: "v"(av[0]), "v"(av[1]));
    }
    STAMPT(13, 0);

  } else if (role == 1) {
    PAD(b0, bd);
    const int lb = (c >> 1) & 3, lc = (c & 1) * 8 + 2 * r;
    const int fc = lane & 31, hw = lane >> 5;
    SELECTOR(sel)
    constexpr int L_R2 = L_MOD, L_S2 = L_R2 + 4096;
    const u32 gw = G_WO + (jB % (NREP - 1)) * WO_REP + wave * 5120;
    u32 go = gw + (c >> 3) * 1024 + r * 128 + (c & 7) * 16, go4 = gw + 4096 + lane * 16;
    u32 gl = G_HA + (((5 * wave + (lane & 8 ? 4 : 0)) >> 2) * 8 + (lane & 7)) * 128;
    u32 a_u = L_XA + wave * 640 + lb * 160 + ((c >> 3) * 16 + lc) * 2, a_u4 = L_XA + wave * 640 + lb * 160 + (64 + lc + (c >> 3)) * 2;
    u32 a_av = L_XA + wave * 640 + (c & 3) * 160 + r * 16, a_ah = a_av + 128 - r * 8;
    u32 a_red = L_R2 + ((wave * 4 + c) * 32 + r * 4) * 4;
    u32 a_st = L_S2 + ((lb * 2 + (c >> 3)) * 8 + wave) * 4;
    u32 a_fin = L_R2 + ((hw * 16 + (wave & 3)) * 32 + fc) * 4;
    u32 a_fst = L_S2 + (wave & 3) * 64;
    u32 a_out = c < 4 ? G_OUT + (wave * 5 * 54 + jB) * 128 + c * 32 + r * 8 : OOB;
    constexpr int L_F2 = L_PT;
    u32 a_fi = BM ? L_F2 + c * 64 + r * 16 : L_FI + (c & 3) * 64 + r * 16, a_fo = (BM ? L_F2 + 768 : L_FI) + ((wave & 3) * 32 + fc) * 2;
    u32 a_ou4 = G_OUT + ((wave * 5 + (c >> 2)) * 54 + jB) * 128 + (c & 3) * 32 + r * 8;
    asm volatile("" : "+v"(a_ou4));
    u32 a_w1 = L_W + wave * 5120 + (8 * r + (c >> 2)) * 64 + (r & 1) * 32 + (c & 3) * 8;
    u32 a_wh = L_W + wave * 5120 + 4096 + (4 * r + (c >> 2)) * 64 + (r >> 1) * 32 + (c & 3) * 8;
    u32 a_w2[5];
#pragma unroll
    for (int k = 0; k < 5; ++k) { const int n2 = wave * 5 + k; a_w2[k] = L_WM2 + (n2 >> 2) * 4096 + (tr128 ^ ((n2 & 3) * 32)); }
    asm volatile("" : "+v"(sel), "+v"(go), "+v"(go4), "+v"(gl), "+v"(a_u), "+v"(a_u4), "+v"(a_av), "+v"(a_ah), "+v"(a_red), "+v"(a_st), "+v"(a_fin), "+v"(a_fst), "+v"(a_out));
    asm volatile("s_waitcnt vmcnt(10)" : "+v"(a_fi), "+v"(a_fo), "+v"(a_w1), "+v"(a_wh), "+v"(a_w2[0]), "+v"(a_w2[1]), "+v"(a_w2[2]), "+v"(a_w2[3]), "+v"(a_w2[4]) :: "memory");
    STAMPT(23, 0);
    PAD(b1, back);
    BAR();
    STAMPT(12, 0);
    if (BM) *reinterpret_cast<u32*>(lds + L_F2 + t * 4) = 0;
    s16x8 wf[2][2], wg[5];
    s16x4 wh[2];
#pragma unroll
    for (int nt = 0; nt < 2; ++nt) {
#pragma unroll
      for (int s = 0; s < 2; ++s) wf[s][nt] = wfrag<256>((a_w1 ^ (nt * 32)) + s * 2048);
      wh[nt] = __builtin_amdgcn_ds_read_tr16_b64_v4i16((lds_s4p)(uintptr_t)(a_wh ^ (nt * 32)));
    }
    float c1f[5];
    {
      const unsigned char* cp = lds + L_XA + (4 + lb) * XS + wave * 160;
#pragma unroll
      for (int i = 0; i < 2; ++i) { const u32 w = *reinterpret_cast<const u32*>(cp + (32 * i + (c >> 3) * 16 + lc) * 2); c1f[2 * i] = lo16(w); c1f[2 * i + 1] = hi16(w); }
      c1f[4] = u2f((u32)*reinterpret_cast<const unsigned short*>(cp + (64 + lc + (c >> 3)) * 2) << 16);
    }
#define W1MMA(acc) \
      f32x4 acc[2]; \
      _Pragma("unroll") for (int nt = 0; nt < 2; ++nt) { const f32x4 z_ = {0.f, 0.f, 0.f, 0.f}; acc[nt] = MFMA(wf[0][nt], av0, z_); } \
      _Pragma("unroll") for (int nt = 0; nt < 2; ++nt) acc[nt] = MFMA(wf[1][nt], av1, acc[nt]); \
      _Pragma("unroll") for (int nt = 0; nt < 2; ++nt) acc[nt] = __builtin_amdgcn_mfma_f32_16x16x16bf16_1k(wh[nt], av2, acc[nt], 0, 0, 0);
    {
      const unsigned char* p = lds + L_XA + (4 + (c & 7)) * XS + wave * 160;
      const u32x4 av0 = *reinterpret_cast<const u32x4*>(p + r * 16), av1 = *reinterpret_cast<const u32x4*>(p + 64 + r * 16);
      const s16x4 av2 = *reinterpret_cast<const s16x4*>(p + 128 + r * 8);
      W1MMA(acc)
#pragma unroll
      for (int nt = 0; nt < 2; ++nt) if (c < 8) *reinterpret_cast<f32x4*>(red + (wave * 8 + c) * 32 + nt * 16 + r * 4) = acc[nt];
    }
    float Tc = 0.f, Ts = 0.f;
#define TSUM() _Pragma("unroll") for (int k = 0; k < 4; ++k) { Tc += red[((hw * 4 + k) * 8 + (wave & 3)) * 32 + fc]; Ts += red[((hw * 4 + k) * 8 + 4 + (wave & 3)) * 32 + fc]; }
    u32x4 pa[5];
    f32x4 s4, s0, s1;
    {
      u64 m[5], old = 0;
#pragma unroll
      for (int i = 0; i < 5; ++i) { m[i] = ~0ull; pa[i] = u32x4{0u, 0u, 0u, 0u}; }
#pragma unroll
      for (int i = 0; i < 5; ++i) asm volatile("" : "+v"(c1f[i]), "+v"(pa[i]));
      u32x2 sg;
      if (BFW >= 0 && wave < 4) asm volatile("s_waitcnt vmcnt(%0)" :: "i"(BFW) : "memory");
      if (BFW >= 0) BAR();
      heads_up_start(rsv, gl, 0xffffull, tg, sg);
      VM0();
      BAR();
      STAMPT(29, 0);
#define BLOOK(W_) \
        asm volatile(LK(0, 5, "0", 0, 7) LK(1, 5, "0", 512, 8) LK(2, 5, "0", 2048, 9) LK(3, 5, "0", 2560, 10) LK(4, 6, "0", 0, 11) LKEND \
                     : "+v"(pa[0]), "+v"(pa[1]), "+v"(pa[2]), "+v"(pa[3]), "+v"(pa[4]) \
                     : "v"(go), "v"(go4), "s"(m[0]), "s"(m[1]), "s"(m[2]), "s"(m[3]), "s"(m[4]), [rs] "s"(rsv), [w] "i"(W_) : "memory");
      heads_up<0>(rsv, gl, 0xffffull, tg, sg);
      STAMPT(21, 0);
      BLOOK(63)
      announce(rs, G_HB + jB * 128, C_GO, C_W, 8, tb, HCD, tg);
#pragma unroll
      for (int k = 0; k < 5; ++k) wg[k] = wfrag<512>(a_w2[k]);
      PAD(b2, back);
      TSUM()
#pragma unroll
      for (int i = 0; i < 5; ++i) asm volatile("" : "+v"(wg[i]));
      asm volatile("" : "+v"(Tc), "+v"(Ts));
      STAMPT(18, 0);
      asm volatile("s_waitcnt vmcnt(1)" : "+v"(pa[0]), "+v"(pa[1]), "+v"(pa[2]), "+v"(pa[3]), "+v"(pa[4]) :: "memory");
      STAMPT(22, 0);
#define BSUM() { const f32x4 z = {0.f, 0.f, 0.f, 0.f}; \
      s4 = MFMA(sel, pa[4], z); s0 = MFMA(sel, pa[0], z); s1 = MFMA(sel, pa[2], z); s0 = MFMA(sel, pa[1], s0); s1 = MFMA(sel, pa[3], s1); }
      SPINLOOP for (unsigned spins = 0; spins < SPIN_CAP; ++spins) {
        BSUM()
        if (!(__builtin_amdgcn_ballot_w64(__builtin_isunordered(s4[0], s4[1])) | __builtin_amdgcn_ballot_w64(__builtin_isunordered(s0[0], s0[1])) |
              __builtin_amdgcn_ballot_w64(__builtin_isunordered(s1[0], s1[1])))) break;
        u64 bad = 0;
#pragma unroll
        for (int i = 0; i < 5; ++i) { m[i] = __builtin_amdgcn_ballot_w64(missing(pa[i])); bad |= m[i]; }
        if (!(bad | old)) break;
        old = bad;
        if (!spins) FAIL(2);
        BLOOK(5 * (PDB - 1))
      }
    }
    STAMPT(14, 0); WSTAMP(3);
    float xf[5];
    {
      float e0 = s4[0], e1 = s4[1];
      asm("" : "+v"(e0), "+v"(e1));
      xf[4] = bfr(((c & 8) ? e1 : e0) + dpp<XOR8>((c & 8) ? e0 : e1));
      *reinterpret_cast<unsigned short*>(lds + a_u4) = (unsigned short)cvt2(xf[4] * c1f[4], 0.f);
      const u32 w0 = cvt2(s0[0], s0[1]), w1 = cvt2(s1[0], s1[1]);
      xf[0] = lo16(w0); xf[1] = hi16(w0); xf[2] = lo16(w1); xf[3] = hi16(w1);
      *reinterpret_cast<u32*>(lds + a_u) = cvt2(xf[0] * c1f[0], xf[1] * c1f[1]);
      *reinterpret_cast<u32*>(lds + a_u + 64) = cvt2(xf[2] * c1f[2], xf[3] * c1f[3]);
    }
    STAMPT(19, 0);
    {
      u32x4 av0 = *reinterpret_cast<const u32x4*>(lds + a_av), av1 = *reinterpret_cast<const u32x4*>(lds + a_av + 64);
      s16x4 av2 = *reinterpret_cast<const s16x4*>(lds + a_ah);
      __builtin_amdgcn_sched_barrier(0);
      const float ps = ((xf[0] + xf[1]) + (xf[2] + xf[3])) + xf[4];
      const float pq = fmaf(xf[0], xf[0], fmaf(xf[1], xf[1], xf[4] * xf[4])) + fmaf(xf[2], xf[2], xf[3] * xf[3]);
      const float t1 = ps + dpp<XOR8>(ps), t2 = pq + dpp<XOR8>(pq);
      float z = (c & 8) ? t2 : t1;
      z += dpp<XOR1>(z);
      asm volatile("" : "+v"(z));
      __builtin_amdgcn_sched_barrier(0);
      W1MMA(acc)
      z = cross_rows_sum(z);
      if ((lane & 49) == 0) *reinterpret_cast<float*>(lds + a_st) = z;
      if (c < 4) {
#pragma unroll
        for (int nt = 0; nt < 2; ++nt) *reinterpret_cast<f32x4*>(lds + a_red + nt * 64) = acc[nt];
      }
    }
#undef W1MMA
    PAD(b3, back);
    BAR();
    STAMPT(24, 0);
    if (wave < 4) {
      f32x4 sv[4];
      float sp[4];
#pragma unroll
      for (int k = 0; k < 4; ++k) sv[k] = *reinterpret_cast<const f32x4*>(lds + a_fst + k * 16);
#pragma unroll
      for (int k = 0; k < 4; ++k) sp[k] = *reinterpret_cast<const float*>(lds + a_fin + k * 512);
      __builtin_amdgcn_sched_barrier(0);
      const f32x4 s1 = sv[0] + sv[1], s2 = sv[2] + sv[3];
      const float mean = ((s1[0] + s1[1]) + (s1[2] + s1[3])) * (1.f / DM);
      const float var = fmaxf(((s2[0] + s2[1]) + (s2[2] + s2[3])) * (1.f / DM) - mean * mean, 0.f);
      const float rstd = __builtin_amdgcn_rsqf(var + 1e-5f);
      const float zp = fmaf(rstd, fmaf(-mean, Tc, (sp[0] + sp[1]) + (sp[2] + sp[3])), Ts);
      const float hh = bfr(bfr(swap32_add(zp, zp)) + b1v);
      const float e2 = __builtin_amdgcn_exp2f(hh * fmaf(hh * hh, 0.1029432397f, 2.302208198f));
      const float f = fmaf(-hh, frcp(e2 + 1.f), hh);
      *reinterpret_cast<unsigned short*>(lds + a_fo) = (unsigned short)cvt2(f, 0.f);
    }
    BAR();
    if (BM) {
      u32x4 av[4];
#pragma unroll
      for (int k = 0; k < 4; ++k) av[k] = *reinterpret_cast<const u32x4*>(lds + a_fi + (3 - k) * 256);
      const f32x4 z = {0.f, 0.f, 0.f, 0.f};
      f32x4 a4 = MFMA(wg[4], av[0], z), a0 = MFMA(wg[0], av[0], z);
#pragma unroll
      for (int k = 1; k < 4; ++k) a0 = MFMA(wg[k], av[k], a0);
      __builtin_amdgcn_sched_barrier(0);
      const u32x2 o4 = {min(cvt2(a4[0], a4[1]), EMPTY - 1), min(cvt2(a4[2], a4[3]), EMPTY - 1)};
      __builtin_amdgcn_raw_buffer_store_b64(o4, rs, a_out, 4 * (54 * 128), 16);
      const u32x2 o0 = {min(cvt2(a0[0], a0[1]), EMPTY - 1), min(cvt2(a0[2], a0[3]), EMPTY - 1)};
      __builtin_amdgcn_raw_buffer_store_b64(o0, rs, a_ou4, 0, 16);
    } else {
      const u32x4 av = *reinterpret_cast<const u32x4*>(lds + a_fi);
      f32x4 acc[5];
#pragma unroll
      for (int k = 0; k < 5; ++k) { const f32x4 z = {0.f, 0.f, 0.f, 0.f}; acc[k] = MFMA(wg[k], av, z); }
      __builtin_amdgcn_sched_barrier(0);
#pragma unroll
      for (int k = 0; k < 5; ++k) {
        const u32x2 o = {min(cvt2(acc[k][0], acc[k][1]), EMPTY - 1), min(cvt2(acc[k][2], acc[k][3]), EMPTY - 1)};
        __builtin_amdgcn_raw_buffer_store_b64(o, rs, a_out, k * (54 * 128), 16);
      }
    }
    STAMPT(15, 0);
    asm volatile("" :: "v"(pa[0]), "v"(pa[1]), "v"(pa[2]), "v"(pa[3]), "v"(pa[4]));

  } else if (role == 2 && wave == 0) {
    PAD(c0, back);
    const int lb = (c >> 1) & 3, col = g * 16 + (c & 1) * 8 + 2 * r + (c >> 3);
    float b2v = bf2f(a.b2[col]);
    const f32x4 z = {0.f, 0.f, 0.f, 0.f};
    SELECTOR(sel)
    float x1v, gbv;
    {
      u32 offs[1] = {G_MF + ((12 + lb) * 160 + (col >> 2)) * 16};
      u32x4 gm[1];
      gather<1>(rs, offs, gm, tg);
      const u32 gbw = (r & 1) ? gm[0][2] : gm[0][0];
      gbv = (c & 8) ? hi16(gbw) : lo16(gbw);
      gather_e<1, 2, 1>(rsv, G_WO + (NREP - 1) * WO_REP + g * 1024 + lane * 16, gm, ~0ull);
      const f32x4 sm = MFMA(sel, gm[0], z);
      x1v = bfr(PAIRSUM(sm));
    }
    u32 oo = lb * DM + col;
    asm volatile("" : "+v"(x1v), "+v"(gbv), "+v"(oo), "+v"(sel), "+v"(b2v));
    STAMPT(23, 0);
    u32x4 pa[7];
    u32 go = G_OUT + g * (54 * 128) + lane * 16;
#pragma unroll
    for (int i = 0; i < 7; ++i) { pa[i] = u32x4{0u, 0u, 0u, 0u}; asm volatile("" : "+v"(pa[i])); }
    asm volatile("" : "+v"(go));
    PAD(c1, back);
    u32x2 hv;
    if (HC) {
      heads_up_start(rsv, G_HB + lane * 128, (1ull << 54) - 1, tg, hv);
      heads_up<0>(rsv, G_HB + lane * 128, (1ull << 54) - 1, tg, hv);
      STAMPT(14, 0);
    }
    f32x4 s0, s1;
    {
      u64 m[7], old = 0;
#pragma unroll
      for (int i = 0; i < 7; ++i) m[i] = i < 6 ? ~0ull : (1ull << 48) - 1;
      look<0>(rsv, go, pa, m);
#define CSUM() { s0 = MFMA(sel, pa[0], z); s1 = MFMA(sel, pa[1], z); \
      _Pragma("unroll") for (int i = 2; i < 7; ++i) { if (i & 1) s1 = MFMA(sel, pa[i], s1); else s0 = MFMA(sel, pa[i], s0); } \
      s0 += s1; }
      SPINLOOP for (unsigned spins = 0; spins < SPIN_CAP; ++spins) {
        CSUM()
        if (!__builtin_amdgcn_ballot_w64(__builtin_isunordered(s0[0], s0[1]))) break;
        u64 bad = 0;
#pragma unroll
        for (int i = 0; i < 7; ++i) { m[i] = __builtin_amdgcn_ballot_w64(missing(pa[i])); bad |= m[i]; }
        if (!(bad | old)) break;
        old = bad;
        if (!spins) FAIL(3);
        look<7 * (PDC - 1)>(rsv, go, pa, m);
      }
    }
    STAMPT(16, 0);
    const float y = bfr(bfr(PAIRSUM(s0)) + b2v);
    a.out[oo] = __float2bfloat16(x1v + bfr(gbv * y));
    STAMPT(17, 0);
    asm volatile("" :: "v"(pa[0]), "v"(pa[1]), "v"(pa[2]), "v"(pa[3]), "v"(pa[4]), "v"(pa[5]), "v"(pa[6]), "v"(hv));

    if (u == 0 && t == 0)
      __hip_atomic_store(reinterpret_cast<u32*>(a.ws + WS_EPOCH), tg, __ATOMIC_RELAXED, __HIP_MEMORY_SCOPE_AGENT);
  }
  STAMP_FLUSH;
}

extern "C" void kernel_launch(void* const* d_in, const int* in_sizes, int n_in, void* d_out, int out_size, void* d_ws,
                              size_t ws_size, hipStream_t stream) {
  Args a;
  a.x = Desc{(const bf16*)d_in[0], NB * DM * 2, 0x00020000u};
  a.kc = (const bf16*)d_in[1];
  a.vc = (const bf16*)d_in[2];
  a.wq = Desc{(const bf16*)d_in[3], DM * QD * 2, 0x00020000u};
  a.wo = (const bf16*)d_in[4];
  a.w1 = (const bf16*)d_in[6];
  a.b1 = (const bf16*)d_in[7];
  a.w2 = (const bf16*)d_in[8];
  a.b2 = (const bf16*)d_in[9];
  a.c = Desc{(const bf16*)d_in[10], NB * DM * 2, 0x00020000u};
  a.wmod = Desc{(const bf16*)d_in[11], DM * MODN * 2, 0x00020000u};
  a.out = (bf16*)d_out;
  a.ws = (unsigned char*)d_ws;
  mega_kernel<<<NWG, NT, 0, stream>>>(a);
}
```

```cpp
#define SPINLOOP _Pragma("nounroll")
#define P_LAG 14
#define P_NPRE 32
#define P_NVE 6
#define P_NVA 6
#define QODD 4
#define QEVEN 4
#define MFPOS 0
#define EA 0
#define EB 0
#define EC 4
#define APOLL 0
#define N1E N1A
#define TAU_A 234
#define TAU_B TAU_A
#define TAU_X TAU_A
#define TAU_E 247
#define PK 0
#define PL 1
#define QPRE 1
#define QHOLD 0
#define CT0 1
#define TOFF 90
#define BLIGHT 1
#define BPRE 0
#define BW1 1
#define BMASK 1
#define CSLEEP 1
#define CSL 2
#define CPUB 1
#define CXD 20
#define OPOS 1
#define NREPL 1
#define OUTPRE 1
#define ASL 1
#define AEARLY 1
#define PDC 4
#define PDA 3
#define PDB 3
#define PSB 1
#define HC 1
#define HCD 120
#define HAG 60
#define AW 4
#define BFW -1
#define FULLF 1
#define WARM 1
#define HT 0
#define HX 10
#define HTD 55
#define HSL 1
#define NANT 1
#define HAW 65
#define BM 1
#define HPD 1
#define BT 0
#define BSL 2
#define PB 0
#define GOPOS 0
#define JMAP 1
#define N1A 10
#define N1B 10
#define PT 0
#define PTAUX 16
#define LAY 1
#define P_GATE 8
#include <hip/hip_runtime.h>
#include <hip/hip_bf16.h>
#include <stdint.h>

using bf16 = __hip_bfloat16;
using u32 = unsigned;
using u64 = unsigned long long;
using u32x2 = __attribute__((ext_vector_type(2))) unsigned;
using u32x4 = __attribute__((ext_vector_type(4))) unsigned;
using f32x4 = __attribute__((ext_vector_type(4))) float;
using bf16x2 = __attribute__((ext_vector_type(2))) __bf16;
typedef __attribute__((ext_vector_type(2))) float f32x2;
typedef short s16x8 __attribute__((ext_vector_type(8)));
typedef short s16x4 __attribute__((ext_vector_type(4)));
typedef __attribute__((address_space(3))) s16x4* lds_s4p;
typedef __attribute__((ext_vector_type(8))) __bf16 bf16x8v;

constexpr int NB = 4, DM = 640, NH = 32, DH = 128, SK = 8192, WIN = 2048, FF = 1728, QD = 4096, MODN = 3840;
constexpr int NT = 512, NWG = 256;

constexpr u32 WS_EPOCH = 0;
constexpr u32 G_T = 128;
constexpr u32 G_Q = 256;
constexpr u32 G_MOD = G_Q + 8 * 1024 * 16;
constexpr u32 G_MF = G_MOD + 8 * 1024 * 16;
constexpr u32 G_HB = G_MF + 16 * 160 * 16;
constexpr u32 G_HA = G_HB + 64 * 128;
constexpr u32 G_HT = G_HA + 80 * 128;
constexpr u32 G_ML = G_HT + 256 * 128;
constexpr u32 EMPTY = 0xffffffffu;
constexpr int NREP = NREPL;
constexpr u32 G_WO = G_ML + 256 * 64;
constexpr u32 WO_REP = 40 * 1024;
constexpr u32 G_ATT = G_WO + NREP * WO_REP;
constexpr u32 G_OUT = G_ATT + 256 * 256;
constexpr u32 WS_END = G_OUT + 40 * 54 * 128;
constexpr u32 OOB = 0xF0000000u;
#define GIDX(n, ks, b, cg) ((((ks) * ((n) / 4) + ((cg) >> 2)) * 16 + (b) * 4 + ((cg) & 3)) * 16)

#define WSTAMP(k) do {} while (0)
#define STAMPW(i) do {} while (0)
#define STAMPT(i, tt) do {} while (0)
#define STAMP_DECL
#define STAMP_FLUSH do {} while (0)
#define FAIL(i) do {} while (0)
#define PAD(name, next) asm volatile(".Lpad_" #name ":\n\ts_cbranch_execz .Lpad_" #next)
#define BAR() asm volatile("s_waitcnt lgkmcnt(0)\n\ts_barrier" ::: "memory")
#define VM0() asm volatile("s_waitcnt vmcnt(0)" ::: "memory")

__device__ __forceinline__ float bfr(float x) { return __bfloat162float(__float2bfloat16(x)); }
__device__ __forceinline__ float bf2f(bf16 x) { return __bfloat162float(x); }
__device__ __forceinline__ float lo16(unsigned u) { return __builtin_bit_cast(float, u << 16); }
__device__ __forceinline__ float hi16(unsigned u) { return __builtin_bit_cast(float, u & 0xffff0000u); }
__device__ __forceinline__ u32 f2u(float f) { return __builtin_bit_cast(u32, f); }
__device__ __forceinline__ float u2f(u32 f) { return __builtin_bit_cast(float, f); }
__device__ __forceinline__ u32 cvt2(float a, float b) {
  const f32x2 v = {a, b};
  return __builtin_bit_cast(u32, __builtin_convertvector(v, bf16x2));
}
__device__ __forceinline__ float frcp(float x) { return __builtin_amdgcn_rcpf(x); }
__device__ __forceinline__ float fsqrt(float x) { return __builtin_amdgcn_sqrtf(x); }
__device__ __forceinline__ float fexp(float x) { return __builtin_amdgcn_exp2f(x * 1.4426950408889634f); }
__device__ __forceinline__ float dot2(unsigned k, unsigned q, float acc) {
  return __builtin_amdgcn_fdot2_f32_bf16(__builtin_bit_cast(bf16x2, k), __builtin_bit_cast(bf16x2, q), acc, false);
}
__device__ __forceinline__ float addlo(unsigned w, float acc) { u32 k = 0x00003f80u; asm("" : "+s"(k)); return dot2(w, k, acc); }
__device__ __forceinline__ float addhi(unsigned w, float acc) { return dot2(w, 0x3f800000u, acc); }
template <int CTRL> __device__ __forceinline__ float dpp(float x) {
  return __builtin_bit_cast(float, __builtin_amdgcn_mov_dpp(__builtin_bit_cast(int, x), CTRL, 0xf, 0xf, true));
}
template <int CTRL> __device__ __forceinline__ u32 dppu(u32 x) { return (u32)__builtin_amdgcn_mov_dpp((int)x, CTRL, 0xf, 0xf, true); }
constexpr int XOR1 = 0xB1, XOR2 = 0x4E, XOR7 = 0x141, XOR8 = 0x128, ROR4 = 0x124, ROR2 = 0x122, ROR1 = 0x121, SHL4 = 0x104;
__device__ __forceinline__ float swap32_add(float a, float b) {
  auto s = __builtin_amdgcn_permlane32_swap(f2u(a), f2u(b), false, false);
  return u2f(s[0]) + u2f(s[1]);
}
__device__ __forceinline__ float swap16_add(float a, float b) {
  auto s = __builtin_amdgcn_permlane16_swap(f2u(a), f2u(b), false, false);
  return u2f(s[0]) + u2f(s[1]);
}
__device__ __forceinline__ float cross_rows_sum(float x) { x = swap16_add(x, x); return swap32_add(x, x); }
__device__ __forceinline__ float cross_rows_max(float x) {
  auto s = __builtin_amdgcn_permlane16_swap(f2u(x), f2u(x), false, false);
  x = fmaxf(u2f(s[0]), u2f(s[1]));
  auto s2 = __builtin_amdgcn_permlane32_swap(f2u(x), f2u(x), false, false);
  return fmaxf(u2f(s2[0]), u2f(s2[1]));
}
__device__ __forceinline__ float wave_sum(float x) {
  x += dpp<XOR8>(x); x += dpp<ROR4>(x); x += dpp<ROR2>(x); x += dpp<ROR1>(x);
  return cross_rows_sum(x);
}
__device__ __forceinline__ float wave_max(float x) {
  x = fmaxf(x, dpp<XOR8>(x)); x = fmaxf(x, dpp<ROR4>(x)); x = fmaxf(x, dpp<ROR2>(x)); x = fmaxf(x, dpp<ROR1>(x));
  return cross_rows_max(x);
}

__device__ __forceinline__ void st_gran(__amdgpu_buffer_rsrc_t rs, u32 off, u32 a, u32 b, u32 tg) {
  u32x4 v = {a, tg, b, tg};
  __builtin_amdgcn_raw_buffer_store_b128(v, rs, off, 0, 16);
}
__device__ __forceinline__ u32x4 ld_gran(__amdgpu_buffer_rsrc_t rs, u32 off) {
  return __builtin_amdgcn_raw_buffer_load_b128(rs, off, 0, 16);
}
constexpr unsigned SPIN_CAP = 1u << 18;
template <int N>
__device__ __forceinline__ void gather(__amdgpu_buffer_rsrc_t rs, const u32 (&offs)[N], u32x4 (&g)[N], u32 tg) {
  SPINLOOP for (unsigned spins = 0; spins < SPIN_CAP; ++spins) {
    bool ok = true;
#pragma unroll
    for (int i = 0; i < N; ++i) g[i] = ld_gran(rs, offs[i]);
#pragma unroll
    for (int i = 0; i < N; ++i) ok &= (offs[i] == OOB) | ((g[i][1] == tg) & (g[i][3] == tg));
    if (__all(ok)) break;
    __builtin_amdgcn_s_sleep(1);
  }
}
template <int N>
__device__ __forceinline__ void gather_m(__amdgpu_buffer_rsrc_t rs, u32 (&offs)[N], u32 (&va)[N], u32 (&vb)[N], u32 tg) {
#pragma unroll
  for (int i = 0; i < N; ++i) { va[i] = 0; vb[i] = 0; }
  SPINLOOP for (unsigned spins = 0; spins < SPIN_CAP; ++spins) {
    u32x4 g[N];
#pragma unroll
    for (int i = 0; i < N; ++i) g[i] = ld_gran(rs, offs[i]);
    bool ok = true;
#pragma unroll
    for (int i = 0; i < N; ++i) {
      const bool hit = (offs[i] != OOB) & (g[i][1] == tg) & (g[i][3] == tg);
      va[i] = hit ? g[i][0] : va[i]; vb[i] = hit ? g[i][2] : vb[i];
      offs[i] = hit ? OOB : offs[i];
      ok &= (offs[i] == OOB);
    }
    if (__all(ok)) break;
    __builtin_amdgcn_s_sleep(1);
  }
}

__device__ __forceinline__ bool missing(u32x4 v) { return max(max(v[0], v[1]), max(v[2], v[3])) == EMPTY; }
#define LK(v, o, so, imm, m) "s_mov_b64 exec, %" #m "\n\tbuffer_load_dwordx4 %" #v ", %" #o ", %[rs], " so " offen offset:" #imm " sc1\n\t"
#define LKEND "s_mov_b64 exec, -1\n\ts_waitcnt vmcnt(%[w])"
template <int W> __device__ __forceinline__ void look(u32x4 rs, u32 off, u32x4 (&v)[1], const u64 (&m)[1]) {
  asm volatile(LK(0, 1, "0", 0, 2) LKEND : "+v"(v[0]) : "v"(off), "s"(m[0]), [rs] "s"(rs), [w] "i"(W) : "memory");
}
template <int W> __device__ __forceinline__ void look(u32x4 rs, u32 off, u32x4 (&v)[5], const u64 (&m)[5]) {
  asm volatile(LK(0, 5, "0", 0, 6) LK(1, 5, "0", 1024, 7) LK(2, 5, "0", 2048, 8) LK(3, 5, "0", 3072, 9) LK(4, 5, "%11", 0, 10) LKEND
               : "+v"(v[0]), "+v"(v[1]), "+v"(v[2]), "+v"(v[3]), "+v"(v[4])
               : "v"(off), "s"(m[0]), "s"(m[1]), "s"(m[2]), "s"(m[3]), "s"(m[4]), "s"(4096u), [rs] "s"(rs), [w] "i"(W) : "memory");
}
template <int W> __device__ __forceinline__ void look(u32x4 rs, u32 off, u32x4 (&v)[7], const u64 (&m)[7]) {
  asm volatile(LK(0, 7, "0", 0, 8) LK(1, 7, "0", 1024, 9) LK(2, 7, "0", 2048, 10) LK(3, 7, "0", 3072, 11)
               LK(4, 7, "%15", 0, 12) LK(5, 7, "%15", 1024, 13) LK(6, 7, "%15", 2048, 14) LKEND
               : "+v"(v[0]), "+v"(v[1]), "+v"(v[2]), "+v"(v[3]), "+v"(v[4]), "+v"(v[5]), "+v"(v[6])
               : "v"(off), "s"(m[0]), "s"(m[1]), "s"(m[2]), "s"(m[3]), "s"(m[4]), "s"(m[5]), "s"(m[6]), "s"(4096u), [rs] "s"(rs), [w] "i"(W) : "memory");
}
template <int N, int SLEEP, int D>
__device__ __forceinline__ void gather_e(u32x4 rs, u32 off, u32x4 (&v)[N], u64 last) {
  u64 m[N];
#pragma unroll
  for (int i = 0; i < N; ++i) { m[i] = i < N - 1 ? ~0ull : last; v[i] = u32x4{0u, 0u, 0u, 0u}; }
  look<0>(rs, off, v, m);
  SPINLOOP for (unsigned spins = 0; spins < SPIN_CAP; ++spins) {
    u64 bad = 0;
#pragma unroll
    for (int i = 0; i < N; ++i) { m[i] = __builtin_amdgcn_ballot_w64(missing(v[i])); bad |= m[i]; }
    if (!bad) break;
    if (SLEEP) __builtin_amdgcn_s_sleep(SLEEP);
    look<N * (D - 1)>(rs, off, v, m);
  }
}

#define HLOOK(W_) asm volatile("s_mov_b64 exec, %2\n\tbuffer_load_dwordx2 %0, %1, %3, 0 offen sc1\n\ts_mov_b64 exec, -1\n\ts_waitcnt vmcnt(%4)" \
                               : "+v"(v) : "v"(off), "s"(m), "s"(rs), "i"(W_) : "memory")
__device__ __forceinline__ void heads_up_start(u32x4 rs, u32 off, u64 m, u32 tg, u32x2& v) { v = u32x2{0u, ~tg}; HLOOK(63); }
template <int SLEEP>
__device__ __forceinline__ void heads_up(u32x4 rs, u32 off, u64 m, u32 tg, u32x2& v) {
  asm volatile("s_waitcnt vmcnt(0)" : "+v"(v) :: "memory");
  SPINLOOP for (unsigned spins = 0; spins < SPIN_CAP; ++spins) {
    m &= __builtin_amdgcn_ballot_w64(v[1] != tg);
    if (!m) break;
    if (SLEEP) __builtin_amdgcn_s_sleep(SLEEP);
    HLOOK(0);
  }
}
__device__ __forceinline__ void heads_up_time(u64 all, u32x2 v) {
  SPINLOOP for (unsigned spins = 0; spins < 64; ++spins)
    if (!(all & __builtin_amdgcn_ballot_w64((int)((u32)__builtin_amdgcn_s_memrealtime() - v[0]) < 0))) break;
}
__device__ __forceinline__ void announce(__amdgpu_buffer_rsrc_t rs, u32 off, u32 a_max, u32 a_cnt, u32 n, u32 tb, u32 d, u32 tg) {
  u32 tk = 0, mx;
  const u32 f = (u32)__builtin_amdgcn_s_memrealtime() - tb + d;
  asm volatile("s_mov_b64 exec, 1\n\tds_max_u32 %2, %3\n\tds_add_rtn_u32 %0, %4, %5\n\tds_read_b32 %1, %2\n\ts_mov_b64 exec, -1\n\ts_waitcnt lgkmcnt(0)"
               : "+v"(tk), "=&v"(mx) : "v"(a_max), "v"(f), "v"(a_cnt), "v"(1u) : "memory");
  const u32 lane = threadIdx.x & 63;
  __builtin_amdgcn_raw_buffer_store_b64(u32x2{__builtin_amdgcn_readfirstlane(mx) + tb, tg}, rs,
                                        (lane < 16 && __builtin_amdgcn_readfirstlane(tk) == n - 1) ? off + lane * 8 : OOB, 0, 16);
}

__device__ __forceinline__ void st_g22(__amdgpu_buffer_rsrc_t rs, u32 off, f32x4 v, u32 tg) {
  const u32 a0 = f2u(v[0]) + 0x200u, a1 = f2u(v[1]) + 0x200u, a2 = f2u(v[2]) + 0x200u, a3 = f2u(v[3]) + 0x200u;
  const u32x4 w = {(a0 >> 10) | ((a1 << 12) & 0xffc00000u), (a1 >> 20) | (tg << 12), (a2 >> 10) | ((a3 << 12) & 0xffc00000u), (a3 >> 20) | (tg << 12)};
  __builtin_amdgcn_raw_buffer_store_b128(w, rs, off, 0, 16);
}
__device__ __forceinline__ f32x4 un_g22(u32x4 w) {
  return f32x4{u2f(w[0] << 10), u2f(__builtin_amdgcn_alignbit(w[1], w[0], 22) << 10), u2f(w[2] << 10), u2f(__builtin_amdgcn_alignbit(w[3], w[2], 22) << 10)};
}
template <int N, int SLEEP>
__device__ __forceinline__ void gather22(__amdgpu_buffer_rsrc_t rs, u32 (&offs)[N], u32x4 (&v)[N], u32 tg) {
#pragma unroll
  for (int i = 0; i < N; ++i) v[i] = u32x4{0u, 0u, 0u, 0u};
  SPINLOOP for (unsigned spins = 0; spins < SPIN_CAP; ++spins) {
    u32x4 g[N];
#pragma unroll
    for (int i = 0; i < N; ++i) g[i] = ld_gran(rs, offs[i]);
    bool ok = true;
#pragma unroll
    for (int i = 0; i < N; ++i) {
      const bool hit = (offs[i] != OOB) & (((g[i][1] ^ (tg << 12)) | (g[i][3] ^ (tg << 12))) < 4096u);
#pragma unroll
      for (int k = 0; k < 4; ++k) v[i][k] = hit ? g[i][k] : v[i][k];
      offs[i] = hit ? OOB : offs[i];
      ok &= (offs[i] == OOB);
    }
    if (__all(ok)) break;
    __builtin_amdgcn_s_sleep(SLEEP);
  }
}

__device__ __forceinline__ u32 next_tag(u32 ep) { const u32 t = ep + 1u; return (t & 0xfffffu) ? t : t + 1u; }

__device__ __forceinline__ void lds_arrive(u32 addr, int lane) {
  asm volatile("s_waitcnt lgkmcnt(0)" ::: "memory");
  if (lane == 0) asm volatile("ds_add_u32 %0, %1" :: "v"(addr), "v"(1u) : "memory");
}
__device__ __forceinline__ u32 lds_ticket(u32 addr, int lane) {
  u32 r = 0;
  asm volatile("s_waitcnt lgkmcnt(0)" ::: "memory");
  if (lane == 0) asm volatile("ds_add_rtn_u32 %0, %1, %2\n\ts_waitcnt lgkmcnt(0)" : "=v"(r) : "v"(addr), "v"(1u) : "memory");
  return __builtin_amdgcn_readfirstlane(r);
}
template <int SLEEP>
__device__ __forceinline__ void lds_wait(u32 addr, u32 target) {
  SPINLOOP for (unsigned spins = 0; spins < (1u << 22); ++spins) {
    u32 v;
    asm volatile("ds_read_b32 %0, %1\n\ts_waitcnt lgkmcnt(0)" : "=v"(v) : "v"(addr) : "memory");
    if (v >= target) break;
    __builtin_amdgcn_s_sleep(SLEEP);
  }
}
__device__ __forceinline__ void dma16(const void* gsrc, u32 lds_dst) {
  u32 keep;
  asm volatile("s_mov_b32 %0, m0\n\ts_mov_b32 m0, %2\n\ts_nop 0\n\tglobal_load_lds_dwordx4 %1, off\n\ts_mov_b32 m0, %0"
               : "=&s"(keep) : "v"(gsrc), "s"(lds_dst) : "memory");
}
__device__ __forceinline__ void dma16nt(const void* gsrc, u32 lds_dst) {
  u32 keep;
  asm volatile("s_mov_b32 %0, m0\n\ts_mov_b32 m0, %2\n\ts_nop 0\n\tglobal_load_lds_dwordx4 %1, off nt\n\ts_mov_b32 m0, %0"
               : "=&s"(keep) : "v"(gsrc), "s"(lds_dst) : "memory");
}
__device__ __forceinline__ u32x4 ldasm(const void* p) {
  u32x4 v;
  asm volatile("global_load_dwordx4 %0, %1, off" : "=v"(v) : "v"(p) : "memory");
  return v;
}
__device__ __forceinline__ u32 ldasm_sc1(const void* p) {
  u32 v;
  asm volatile("global_load_dword %0, %1, off sc1" : "=v"(v) : "v"(p) : "memory");
  return v;
}
__device__ __forceinline__ u32x4 ldw(const bf16* p) { return *reinterpret_cast<const u32x4*>(p); }
template <int D2>
__device__ __forceinline__ s16x8 wfrag(u32 addr) {
  const s16x4 lo = __builtin_amdgcn_ds_read_tr16_b64_v4i16((lds_s4p)(uintptr_t)addr);
  const s16x4 hi = __builtin_amdgcn_ds_read_tr16_b64_v4i16((lds_s4p)(uintptr_t)(addr + D2));
  return s16x8{lo[0], lo[1], lo[2], lo[3], hi[0], hi[1], hi[2], hi[3]};
}
#define MFMA(w_, x_, c_) __builtin_amdgcn_mfma_f32_16x16x32_bf16(__builtin_bit_cast(bf16x8v, w_), __builtin_bit_cast(bf16x8v, x_), c_, 0, 0, 0)

struct Desc { const bf16* p; u32 bytes, flags; };
struct Args {
  Desc wq, x, wmod, c;
  unsigned char* ws;
  const bf16 *kc, *vc, *wo, *w1, *b1, *w2, *b2;
  bf16* out;
};

constexpr int L_W = 0;
constexpr int L_WM2 = 40960;
constexpr int L_XA = 81920;
constexpr int XS = 1312, XH = 704;
constexpr int L_RED = L_XA + 16384;
constexpr int L_MOD = L_RED + 8192;
constexpr int L_XR = L_MOD + 5120;
constexpr int L_PT = L_XR + 5120;
constexpr int L_ACC = L_PT + 2048;
constexpr int L_QS = L_ACC + 4096;
constexpr int L_FI = L_QS + 256;
constexpr int L_ST = L_FI + 256;
constexpr int L_CNT = L_ST + 256;
constexpr int L_END = L_CNT + 128;
static_assert(L_END <= 160 * 1024, "LDS");
constexpr u32 C_Q = L_CNT, C_SC = L_CNT + 4, C_ISS = L_CNT + 8, C_P1 = L_CNT + 20, C_P2 = L_CNT + 24, C_GO = L_CNT + 28, C_W = L_CNT + 12, C_FIN = L_CNT + 32,
              C_KI = L_CNT + 36, C_PP = L_CNT + 40, C_PUB = L_CNT + 56, C_HT = L_CNT + 64;

constexpr int NL = 32;
constexpr int PVLAG = P_LAG;
constexpr int NPRE = P_NPRE;
constexpr int NVE = P_NVE;
static_assert(PVLAG <= 16 && (PVLAG & 1) == 0 && NVE <= PVLAG && (NVE & 1) == 0, "row groups");

__global__ void __launch_bounds__(NT, 2) mega_kernel(Args a) {
  __shared__ __attribute__((aligned(1024))) unsigned char lds[L_END];
  float* red = reinterpret_cast<float*>(lds + L_RED);
  u32* xraw = reinterpret_cast<u32*>(lds + L_XR);
  u32* pT = reinterpret_cast<u32*>(lds + L_PT);
  float* accs = reinterpret_cast<float*>(lds + L_ACC);
  u32* qs = reinterpret_cast<u32*>(lds + L_QS);
  float* st = reinterpret_cast<float*>(lds + L_ST);
  u32* cnt = reinterpret_cast<u32*>(lds + L_CNT);

  u32x4 rw, rx;
  unsigned char* wsq;
  asm volatile("s_load_dwordx4 %0, %3, %4\n\ts_load_dwordx4 %1, %3, %4 offset:16\n\ts_load_dwordx2 %2, %3, 64"
               : "=&s"(rw), "=&s"(rx), "=&s"(wsq) : "s"(__builtin_amdgcn_kernarg_segment_ptr()), "s"((blockIdx.x & 1) * 32));
  __builtin_amdgcn_sched_barrier(0);
  const int t = threadIdx.x, lane = t & 63;
  const int wave = __builtin_amdgcn_readfirstlane(t >> 6);
  const int u = blockIdx.x;
  const int bh = u >> 1, half = u & 1, b_att = bh >> 5, h_att = bh & 31;
  const int r = lane >> 4, c = lane & 15;
  STAMP_DECL
  STAMPT(0, 0);
  u32 tg;
  const int g = u >> 1, ksp = g & 1, cbp = g >> 1;
  const int s128e = (((((lane & 7) >> 1) ^ ((lane >> 4) & 1)) << 1) | (lane & 1)) * 8;
  const u32 tr128 = (8 * r + (c >> 2)) * 128 + ((((c >> 3) & 1) | ((r & 1) << 1)) * 32) + (c & 3) * 8;
  if (wave < 4) {
    __builtin_amdgcn_s_setprio(3);
    const u32 rb = 8192 - 512 * half;
    const u32 hm = 0u - (u32)half;
    const u32 vo = ((u32)(lane >> 3) * rb + (((lane & 7) ^ ((lane >> 3) & 2)) ^ ((wave & 1) * 4)) * 16) | ((u32)(119 - g) & hm & OOB);
    u32 so = (u32)(ksp * 320 + 8 * wave) * rb + cbp * 128;
    const u32 step = 32 * rb, dstep = 4096, dst = L_W + wave * 1024;
    const u32 oe = wave * 1280 + lane * 16, oo = (r * 1280 + ksp * 640 + (c + 16 * wave) * 16) | ((u32)(39 - c - 16 * wave) & OOB);
    const u32 oa = (oo & hm) | (oe & ~hm), ob = (wave * 1280 + 1024 + c * 16) | (hm & OOB), oc = oe | (hm & OOB), zero = 0;
    u32 ep, keep;
    u32x4 xv[4];
    const u32 pre = ((u32)u << 31) & (u32)(g - 32) & (u32)(1 - wave), po = G_ATT + g * 2048 + (t & 127) * 16;
    const u32x4 emp = {EMPTY, EMPTY, EMPTY, EMPTY};
#define DMA1 "buffer_load_dwordx4 %7, %8, %1 offen nt lds\n\t"
#define DMAN "s_add_u32 m0, m0, %11\n\ts_add_u32 %1, %1, %10\n\t" DMA1
    asm volatile("s_mov_b32 %0, m0\n\ts_mov_b32 m0, %9\n\t"
                 "s_cmp_gt_i32 %18, 1\n\ts_waitcnt lgkmcnt(0)\n\ts_cbranch_scc1 1f\n\t"
                 "global_load_dword %2, %12, %13 sc1\n\t"
                 "buffer_load_dwordx4 %3, %14, %17, 0 offen\n\tbuffer_load_dwordx4 %4, %15, %17, 0 offen\n\t"
                 "buffer_load_dwordx4 %5, %16, %17, 0 offen offset:2560\n\tbuffer_load_dwordx4 %6, %15, %17, 0 offen offset:2560\n"
                 "1:\n\t" DMA1 DMAN DMAN DMAN DMAN DMAN DMAN DMAN DMAN DMAN "s_mov_b32 m0, %0\n\t"
                 "s_cmp_eq_u32 %19, 0\n\ts_cbranch_scc1 2f\n\tglobal_store_dwordx4 %20, %21, %13 sc1\n2:\n\t"
                 "s_barrier\n\ts_waitcnt vmcnt(10)"
                 : "=&s"(keep), "+s"(so), "=&v"(ep), "=&v"(xv[0]), "=&v"(xv[1]), "=&v"(xv[2]), "=&v"(xv[3])
                 : "v"(vo), "s"(rw), "s"(dst), "s"(step), "s"(dstep), "v"(zero), "s"(wsq), "v"(oa), "v"(ob), "v"(oc), "s"(rx), "s"(wave - half),
                   "s"(pre), "v"(po), "v"(emp) : "memory", "scc");
#undef DMAN
#undef DMA1
    asm volatile("" : "+s"(wsq));
    const __amdgpu_buffer_rsrc_t rs = __builtin_amdgcn_make_buffer_rsrc(wsq, 0, WS_END, 0x00020000);
    if (half) {
      if (wave < 3) {
        u32x4 sv;
#pragma unroll
        for (int k = 0; k < 4; ++k) {
          const float c0 = lo16(xv[0][k]), c1 = hi16(xv[0][k]);
          sv[k] = cvt2(c0 * frcp(1.f + fexp(-c0)), c1 * frcp(1.f + fexp(-c1)));
        }
        if (c + 16 * wave < 40) *reinterpret_cast<u32x4*>(lds + L_XA + r * XH + (c + 16 * wave) * 16) = sv;
        if (t == 0) cnt[4] = next_tag(ep);
        lds_arrive(C_P1, lane);
        STAMPT(2, 0);
      }
    } else {
      if (wave < 2) {
#pragma unroll
        for (int i = 0; i < 2; ++i) {
          const int b = wave + 2 * i;
          float xf[16];
#pragma unroll
          for (int k = 0; k < 4; ++k) { xf[2 * k] = lo16(xv[2 * i][k]); xf[2 * k + 1] = hi16(xv[2 * i][k]); xf[8 + 2 * k] = lo16(xv[2 * i + 1][k]); xf[8 + 2 * k + 1] = hi16(xv[2 * i + 1][k]); }
          float s = 0.f, s2 = 0.f;
#pragma unroll
          for (int k = 0; k < 8; ++k) { s += xf[k]; s2 += xf[8 + k]; }
          if (lane < 16) s += s2;
          const float mean = wave_sum(s) * (1.f / DM);
          float v = 0.f, v2 = 0.f;
#pragma unroll
          for (int k = 0; k < 8; ++k) { const float d = xf[k] - mean; v += d * d; const float d2 = xf[8 + k] - mean; v2 += d2 * d2; }
          if (lane < 16) v += v2;
          v = wave_sum(v);
          if (lane == 0) { st[24 + 2 * b] = bfr(mean); st[24 + 2 * b + 1] = frcp(bfr(fsqrt(bfr(bfr(v * (1.f / DM)) + 1e-5f)))); }
          *reinterpret_cast<u32x4*>(xraw + b * 320 + lane * 4) = xv[2 * i];
          if (lane < 16) *reinterpret_cast<u32x4*>(xraw + b * 320 + 256 + lane * 4) = xv[2 * i + 1];
        }
        if (t == 0) cnt[4] = next_tag(ep);
        lds_arrive(C_P1, lane);
      }
      lds_wait<1>(C_P1, 2);
      STAMPT(20, 0);
      tg = __builtin_amdgcn_readfirstlane(cnt[4]);
      if (CT0 && u < 8 && t == 192) { const u32 t0 = cnt[15]; st_gran(rs, G_T + u * 16, t0, t0, tg); }
      if (wave < 3) {
        const int b = LAY == 2 ? t & 3 : (t >> 2) & 3, j = LAY == 2 ? t >> 2 : (t >> 4) * 4 + (t & 3);
        u32 offs[8];
        u32x4 gm[8];
#pragma unroll
        for (int i = 0; i < 8; ++i) offs[i] = t < 160 ? G_MOD + GIDX(1024, i & 1, b, ((i >> 1) & 1) * 160 + (i >> 2) * 40 + ksp * 80 + j) : OOB;
        __builtin_amdgcn_s_sleep(16);
        gather22<8, 4>(rs, offs, gm, tg);
        STAMPT(8, 0);
        const float mean = st[24 + 2 * b], sd = st[24 + 2 * b + 1];
#pragma unroll
        for (int e = 0; e < 2; ++e) {
          const f32x4 sa = un_g22(gm[4 * e]) + un_g22(gm[4 * e + 1]);
          const f32x4 ca = un_g22(gm[4 * e + 2]) + un_g22(gm[4 * e + 3]);
          const u32x2 xw = *reinterpret_cast<const u32x2*>(xraw + b * 320 + ksp * 160 + e * 80 + 2 * j);
          float hv[4];
#pragma unroll
          for (int k = 0; k < 4; ++k) {
            const float xf = (k & 1) ? hi16(xw[k >> 1]) : lo16(xw[k >> 1]);
            const float n = bfr(bfr(xf - mean) * sd);
            hv[k] = bfr(n * bfr(1.f + bfr(ca[k]))) + bfr(sa[k]);
          }
          if (t < 160) *reinterpret_cast<u32x2*>(lds + L_XA + b * XH + (e * 40 + j) * 8) = u32x2{cvt2(hv[0], hv[1]), cvt2(hv[2], hv[3])};
        }
      }
    }
    VM0();
    STAMPT(5, 128);
    lds_arrive(C_P2, lane);
    lds_wait<1>(C_P2, 4);
    STAMPT(7, 0);
    tg = __builtin_amdgcn_readfirstlane(cnt[4]);
    {
      f32x4 acc = {0.f, 0.f, 0.f, 0.f}, acc2 = acc;
#pragma unroll
      for (int s = 0; s < 10; s += 2) {
        acc = MFMA(wfrag<512>(L_W + (tr128 ^ (wave * 32)) + s * 4096), *reinterpret_cast<const u32x4*>(lds + L_XA + (c & 3) * XH + s * 64 + r * 16), acc);
        acc2 = MFMA(wfrag<512>(L_W + (tr128 ^ (wave * 32)) + s * 4096 + 4096), *reinterpret_cast<const u32x4*>(lds + L_XA + (c & 3) * XH + s * 64 + 64 + r * 16), acc2);
      }
      if (c < 4 && u < 240 + 16 - 16 * half) st_g22(rs, G_Q + half * (G_MOD - G_Q) + GIDX(1024, ksp, c, cbp * 16 + wave * 4 + r), acc + acc2, tg);
    }
    lds_arrive(C_GO, lane);
    if (CT0 && half && u < 8 && t == 192) { const u32 t0 = cnt[15]; st_gran(rs, G_T + u * 16, t0, t0, tg); }
    STAMPT(9, 0);
  } else {
    __builtin_amdgcn_s_setprio(2);
    if (t < 256 + 32) cnt[lane] = lane == 15 ? (u32)__builtin_amdgcn_s_memrealtime() : 0;
  }
  u32 kvoff = (u32)(bh * SK + (SK - WIN)) * 256u + (u32)(half * 8 + wave) * (NL * 1024u) + lane * 16, ph[8];
#pragma unroll
  for (int i = 0; i < 8; ++i) ph[i] = ((i + (u >> 3)) & 7) * 4096;
  asm volatile("" : "+v"(kvoff));
  asm volatile("s_waitcnt lgkmcnt(0)" : "+s"(wsq), "+s"(ph[0]), "+s"(ph[1]), "+s"(ph[2]), "+s"(ph[3]), "+s"(ph[4]), "+s"(ph[5]), "+s"(ph[6]), "+s"(ph[7])
               : "s"(rw), "s"(rx));
  const __amdgpu_buffer_rsrc_t rs = __builtin_amdgcn_make_buffer_rsrc(wsq, 0, WS_END, 0x00020000);
  {
    u32x4 kv[NL], kh[16];
    const __amdgpu_buffer_rsrc_t rk = __builtin_amdgcn_make_buffer_rsrc((void*)a.kc, 0, 1u << 28, 0x00020000);
    const __amdgpu_buffer_rsrc_t rv = __builtin_amdgcn_make_buffer_rsrc((void*)a.vc, 0, 1u << 28, 0x00020000);
#define LDKV(rsrc, off, i) __builtin_amdgcn_raw_buffer_load_b128(rsrc, (off) + ((i) & 3) * 1024, ph[(i) >> 2], 2)
#define LDS_INC(addr) do { asm volatile("" ::: "memory"); if (lane == 0) asm volatile("ds_add_u32 %0, %1" :: "v"(addr), "v"(1u) : "memory"); } while (0)
#define TR_STEP(x, HALF, CTRL, BIT) _Pragma("unroll") for (int hh = 0; hh < (HALF); ++hh) { \
    const float lo_ = x[hh], hi_ = x[hh + (HALF)]; const float t1_ = lo_ + dpp<CTRL>(lo_), t2_ = hi_ + dpp<CTRL>(hi_); \
    x[hh] = (c & (BIT)) ? t2_ : t1_; }
#define SCORE16(x, src, o) float x[16]; \
    _Pragma("unroll") for (int i = 0; i < 16; ++i) { float d = 0.f; _Pragma("unroll") for (int k = 0; k < 4; ++k) d = dot2(src[(o) + i][k], qpk[k], d); x[i] = d; } \
    TR_STEP(x, 8, XOR8, 8) TR_STEP(x, 4, XOR7, 4) TR_STEP(x, 2, XOR2, 2)
#define SCALED(v) bfr(bfr(v) * 0.08838834764831845f)
#define PV2(i) _Pragma("unroll") for (int k = 0; k < 4; ++k) { \
      acc[2 * k] = dot2(__builtin_amdgcn_perm(kv[(i) + 1][k], kv[i][k], 0x05040100u), pw[(i) / 2], acc[2 * k]); \
      acc[2 * k + 1] = dot2(__builtin_amdgcn_perm(kv[(i) + 1][k], kv[i][k], 0x07060302u), pw[(i) / 2], acc[2 * k + 1]); }
#define GET_Q() lds_wait<2>(C_Q, 1); u32x4 qpk; \
    asm volatile("ds_read_b128 %0, %2\n\tds_read_b32 %1, %3\n\ts_waitcnt lgkmcnt(0)" : "=&v"(qpk), "=&v"(tg) : "v"((u32)(L_QS + 16 * c)), "v"((u32)(L_CNT + 16)) : "memory"); \
    tg = __builtin_amdgcn_readfirstlane(tg);
#define GET_P() _Pragma("unroll") for (int i4 = 0; i4 < 4; ++i4) { \
      const u32x4 pv = *reinterpret_cast<const u32x4*>(pT + (wave * 4 + r) * 16 + i4 * 4); \
      _Pragma("unroll") for (int j = 0; j < 4; ++j) pw[i4 * 4 + j] = pv[j]; }
    auto gather_q = [&]() __attribute__((always_inline)) {
        u32 offs[1] = {G_Q + GIDX(1024, lane >> 5, b_att, h_att * 32 + (lane & 31))};
        u32x4 gq[1];
        {
          gq[0] = ld_gran(rs, offs[0]);
          const u32x4 gt = ld_gran(rs, lane < 8 ? G_T + lane * 16 : OOB);
          LDS_INC(C_KI);
          {
            const u32 own = cnt[15];
            const bool ok = lane < 8 && gt[1] == tg;
            float d = ok ? __builtin_amdgcn_fmed3f((float)(int)(gt[0] - own + TOFF), -400.f, 400.f) : 0.f;
            d += dpp<XOR1>(d); d += dpp<XOR2>(d); d += dpp<XOR7>(d);
            const int n = __builtin_popcountll(__ballot(ok));
            if (lane == 0 && n) cnt[15] = own + (u32)(int)(d * frcp((float)n));
          }
          const bool hit = (((gq[0][1] ^ (tg << 12)) | (gq[0][3] ^ (tg << 12))) < 4096u);
          if (!__all(hit)) gather22<1, 4>(rs, offs, gq, tg);
        }
        STAMPW(10);
        f32x4 q4 = un_g22(gq[0]);
#pragma unroll
        for (int k = 0; k < 4; ++k) q4[k] = swap32_add(q4[k], q4[k]);
        if (lane < 32) *reinterpret_cast<u32x2*>(qs + 2 * lane) = u32x2{cvt2(q4[0], q4[1]), cvt2(q4[2], q4[3])};
        lds_arrive(C_Q, lane);
    };
    const int qw = half ? QODD : QEVEN;
    const u32 tau = half ? (g < 74 ? TAU_A : TAU_B) : (g >= 122 ? TAU_X : TAU_E);
#define PACE(n) if ((n) > PB) { const u32 tt_ = t0 + (((n) - PB) * tau >> 4); \
      SPINLOOP while ((int)((u32)__builtin_amdgcn_s_memrealtime() - tt_) < 0) __builtin_amdgcn_s_sleep(1); asm volatile("" ::: "memory"); }
    float acc[8];
#pragma unroll
    for (int k = 0; k < 8; ++k) acc[k] = 0.f;
    u32 pw[16];
    if (__builtin_expect(wave >= 4, 1)) {
      STAMPT(30, 448);
      u32 t0 = (u32)__builtin_amdgcn_s_memrealtime();
      BAR();
      STAMPT(31, 448);
      const u32 kvoff2 = kvoff ^ 131072u;
#pragma unroll
      for (int i = 0; i < 48; ++i) {
        if (i == NPRE && half == 0) lds_wait<2>(C_GO, GOPOS ? 3 : 4);
        if (PK && (i & 3) == 0) PACE(i)
        if (i < NL) kv[i < NL ? i : 0] = LDKV(rk, kvoff, i & 31); else kh[i < NL ? 0 : i - NL] = LDKV(rk, kvoff2, i & 31);
      }
      if (!QPRE || wave != qw) LDS_INC(C_KI);
      STAMPT(25, 448);
      if ((QODD >= 4 || QEVEN >= 4) && wave == qw) {
        __builtin_amdgcn_s_setprio(3);
        lds_wait<2>(C_P1, 2 + half);
        tg = __builtin_amdgcn_readfirstlane(cnt[4]);
        gather_q();
        __builtin_amdgcn_s_setprio(2);
      }
      GET_Q()
      SCORE16(xa, kv, 0)
      asm volatile("" : "+v"(xa[0]), "+v"(xa[1]), "+v"(kvoff) :: "memory");
#pragma unroll
      for (int i = 0; i < P_NVA; ++i) kv[i] = LDKV(rv, kvoff, i);
      asm volatile("" ::: "memory");
      SCORE16(xb, kv, 16)
      xa[2] = xb[0]; xa[3] = xb[1];
      TR_STEP(xa, 2, XOR1, 1)
      float s0 = SCALED(xa[0]), s1 = SCALED(xa[1]);
      asm volatile("" : "+v"(s0), "+v"(s1), "+v"(kvoff) :: "memory");
#pragma unroll
      for (int i = P_NVA; i < NVE; ++i) kv[i] = LDKV(rv, kvoff, i);
      asm volatile("" ::: "memory");
      SCORE16(xh, kh, 0)
      const float s2 = SCALED(xh[0] + dpp<XOR1>(xh[0])), s3 = SCALED(xh[1] + dpp<XOR1>(xh[1]));
      const float m = wave_max(fmaxf(fmaxf(s0, s1), fmaxf(s2, s3)));
      const float p0 = __expf(s0 - m), p1 = __expf(s1 - m), p2 = __expf(s2 - m), p3 = __expf(s3 - m);
      const float l = wave_sum(fmaf(p2 + p3, 0.5f, p0 + p1));
      if (lane == 0) { st[wave] = m; st[8 + wave] = l; }
      pT[(wave * 4 + r) * 16 + (c & 1) * 8 + (c >> 1)] = cvt2(p0, p1);
      pT[((wave - 4) * 4 + r) * 16 + (c >> 1)] = cvt2(p2, p3);
      lds_arrive(C_PP + 4 * (wave - 4), lane);
      __builtin_amdgcn_s_setprio(0);
      STAMPT(27, 448); WSTAMP(1);
      if (CT0) t0 = __builtin_amdgcn_readfirstlane(cnt[15]);
      GET_P()
#pragma unroll
      for (int i = NVE; i < NL; i += 2) {
        PACE(96 + i)
        kv[i] = LDKV(rv, kvoff, i);
        kv[i + 1] = LDKV(rv, kvoff, i + 1);
        asm volatile("" ::: "memory");
        if (i == NL - 2) { LDS_INC(C_ISS); STAMPT(26, 448); }
        if (i >= PVLAG) {
          PV2(i >= PVLAG ? i - PVLAG : 0)
#pragma unroll
          for (int k = 0; k < 8; ++k) asm volatile("" : "+v"(acc[k]));
        }
      }
    } else {
      const int ex = g < 74 ? EA : g < 122 ? EB : EC;
      u32 kva = (!half && ex > 2) ? OOB : kvoff, kvb = (!half && ex > 0) ? OOB : kvoff;
      const u32 kxa = (half && ex > 2) ? kvoff - 8 * NL * 1024u : OOB, kxb = (half && ex > 0) ? kvoff - 8 * NL * 1024u : OOB;
      u32x4 kx[4];
      const u32 t0 = __builtin_amdgcn_readfirstlane(cnt[15]);
#pragma unroll
      for (int i = 0; i < 16; ++i) {
        if (PK && (i & 3) == 0) PACE(48 + i)
        kh[i] = LDKV(rk, i < 12 ? kvoff : i < 14 ? kva : kvb, 16 + i);
      }
#pragma unroll
      for (int i = 0; i < 4; ++i) kx[i] = LDKV(rk, i < 2 ? kxa : kxb, 28 + i);
      LDS_INC(C_KI);
      if (wave == 0) {
        if (half == 0 && g >= 80 && g < 120) {
          const int fb = g - 80;
          u32 of2[2];
          u32x4 gm[2];
#pragma unroll
          for (int ks = 0; ks < 2; ++ks) of2[ks] = G_MOD + GIDX(1024, ks, r, 320 + fb * 16 + c);
          gather22<2, 4>(rs, of2, gm, tg);
          const f32x4 mv = un_g22(gm[0]) + un_g22(gm[1]);
          st_gran(rs, G_MF + (((fb / 10) * 4 + r) * 160 + (fb % 10) * 16 + c) * 16, cvt2(mv[0], mv[1]), cvt2(mv[2], mv[3]), tg);
        }
      }
      __builtin_amdgcn_s_setprio(0);
      lds_wait<2>(C_KI, P_GATE);
      if (QHOLD && wave == qw - 4) lds_wait<2>(C_Q, 1);
      asm volatile("" : "+v"(kvoff), "+v"(kva), "+v"(kvb));
#pragma unroll
      for (int i = 0; i < NL; ++i) {
        if (PL && (i & 3) == 0) PACE(64 + i)
        kv[i] = LDKV(rv, i < 28 ? kvoff : i < 30 ? kva : kvb, i);
      }
      STAMPT(1, 0);
      GET_Q()
      float xx[4];
#pragma unroll
      for (int i = 0; i < 4; ++i) { float d = 0.f;
#pragma unroll
        for (int k = 0; k < 4; ++k) d = dot2(kx[i][k], qpk[k], d);
        xx[i] = d; }
      asm volatile("" : "+v"(xx[0]), "+v"(xx[1]), "+v"(xx[2]), "+v"(xx[3]) :: "memory");
#pragma unroll
      for (int i = 0; i < 4; ++i) kx[i] = LDKV(rv, i < 2 ? kxa : kxb, 28 + i);
      LDS_INC(C_ISS);
      TR_STEP(xx, 2, XOR8, 8)
#pragma unroll
      for (int i = 0; i < 2; ++i) { xx[i] += dpp<XOR7>(xx[i]); xx[i] += dpp<XOR2>(xx[i]); xx[i] += dpp<XOR1>(xx[i]); }
      SCORE16(xh, kh, 0)
      const float ninf = -__builtin_inff();
      const bool mine = half || c < 16 - ex, extra = half && c >= 16 - 4 * ex;
      xh[0] += dpp<XOR1>(xh[0]); xh[1] += dpp<XOR1>(xh[1]);
      const float s2 = mine ? SCALED(xh[0]) : ninf, s3 = mine ? SCALED(xh[1]) : ninf;
      const float s4 = extra ? SCALED(xx[0]) : ninf, s5 = extra ? SCALED(xx[1]) : ninf;
      float m = wave_max(fmaxf(fmaxf(s2, s3), fmaxf(s4, s5)));
      lds_wait<2>(C_PP + 4 * wave, 1);
      const float me = st[wave + 4];
      m = fmaxf(m, me);
      const float p2 = __expf(s2 - m), p3 = __expf(s3 - m), p4 = __expf(s4 - m), p5 = __expf(s5 - m);
      const float l = wave_sum(fmaf(p4 + p5, 0.25f, p2 + p3)) * 0.5f;
      const u32 pxo = cvt2(p4, p5), pxt = dppu<XOR8>(pxo);
      const u32 px[2] = {c < 8 ? pxo : pxt, c < 8 ? pxt : pxo};
      if (lane == 0) { st[wave] = m; st[8 + wave] = l; }
      pT[(wave * 4 + r) * 16 + 8 + (c >> 1)] = cvt2(p2, p3);
      STAMPT(4, 0); WSTAMP(1);
      if (lds_ticket(C_SC, lane) == 3) {
        const float mw = st[lane & 7], lw = st[8 + (lane & 7)];
        float M = fmaxf(mw, dpp<XOR1>(mw)); M = fmaxf(M, dpp<XOR2>(M)); M = fmaxf(M, dpp<XOR7>(M));
        const float e = __expf(mw - M);
        float L = lw * e;
        L += dpp<XOR1>(L); L += dpp<XOR2>(L); L += dpp<XOR7>(L);
        if (lane == 0) st_gran(rs, G_ML + u * 64, f2u(M), f2u(L), tg);
        if (lane < 8) st[16 + lane] = e * frcp(L);
      }
      GET_P()
#pragma unroll
      for (int i = 0; i < 16; i += 2) { PV2(i) }
      const float f = __expf(me - m);
#pragma unroll
      for (int k = 0; k < 8; ++k) acc[k] *= f;
#pragma unroll
      for (int i = 16; i < NL - PVLAG; i += 2) { PV2(i) }
#pragma unroll
      for (int i = 0; i < 4; i += 2)
#pragma unroll
        for (int k = 0; k < 4; ++k) {
          acc[2 * k] = dot2(__builtin_amdgcn_perm(kx[i + 1][k], kx[i][k], 0x05040100u), px[i / 2], acc[2 * k]);
          acc[2 * k + 1] = dot2(__builtin_amdgcn_perm(kx[i + 1][k], kx[i][k], 0x07060302u), px[i / 2], acc[2 * k + 1]);
        }
    }
#pragma unroll
    for (int i = NL - PVLAG; i <= NL - HX; i += 2) { PV2(i) }
    if (!HT) { PV2(NL - HX + 2) } else {
      u32 tk = 0;
      u64 tn;
      asm volatile("s_mov_b64 exec, 1\n\tds_add_rtn_u32 %0, %10, %11\n\ts_mov_b64 exec, -1\n\ts_memrealtime %1"
                   : "+v"(tk), "=s"(tn), "+v"(acc[0]), "+v"(acc[1]), "+v"(acc[2]), "+v"(acc[3]), "+v"(acc[4]), "+v"(acc[5]), "+v"(acc[6]), "+v"(acc[7])
                   : "v"((u32)C_HT), "v"(1u) : "memory");
      PV2(NL - HX + 2)
      asm volatile("s_waitcnt lgkmcnt(0)" : "+v"(tk), "+s"(tn), "+v"(acc[0]), "+v"(acc[1]), "+v"(acc[2]), "+v"(acc[3]), "+v"(acc[4]), "+v"(acc[5]), "+v"(acc[6]), "+v"(acc[7]));
      __builtin_amdgcn_raw_buffer_store_b64(u32x2{(u32)tn + HTD, tg}, rs, tk == 7 ? G_HT + u * 128 : OOB, 0, 16);
      asm volatile("" ::: "memory");
    }
#pragma unroll
    for (int i = NL - HX + 4; i < NL; i += 2) { PV2(i) }
#undef PV2
#undef TR_STEP
#undef SCORE16
#undef SCALED
#undef GET_Q
#undef GET_P
#undef LDKV
#pragma unroll
    for (int i = 0; i < 4; ++i) acc[i] = swap32_add(acc[i], acc[i + 4]);
#pragma unroll
    for (int i = 0; i < 2; ++i) acc[i] = swap16_add(acc[i], acc[i + 2]);
    *reinterpret_cast<float2*>(accs + wave * 128 + c * 8 + 2 * r) = float2{acc[0], acc[1]};
    STAMPT(6, 0); STAMPT(28, 448); WSTAMP(2);
  }
  const u32x4 rsv = {(u32)(uintptr_t)wsq, (u32)((uintptr_t)wsq >> 32) & 0xffffu, WS_END, 0x00020000u};
  const int role = half ? (g < 74 ? 0 : 1) : (g >= 122 ? 0 : g < 40 ? 2 : 3);
  const int ia = half ? g : g - 48;
  const int ksA = ia & 7, cbA = ia >> 3;
#define HEADA(hh) ((ksA & 3) + (ksA >> 2) * 16 + (hh) * 4)
  const int iB = g - 74, jB = !JMAP ? iB : iB < 48 ? (iB & ~7) | ((iB & 3) << 1) | ((iB >> 2) & 1) : iB < 50 ? 2 * iB - 48 : iB < 52 ? iB + 2 : 2 * iB - 55;
  u32x4 ml = {0u, 0u, 0u, 0u};
  const u32 blkA = ((c & 3) * 32 + HEADA(wave & 3)) * 2 + ((c >> 2) & 1);
  u32 om = G_ML + blkA * 64;
#define MLOOK(W_) asm volatile(LK(0, 1, "0", 0, 2) LKEND : "+v"(ml) : "v"(om), "s"(m2), [rs] "s"(rsv), [w] "i"(W_) : "memory");
  u64 m2 = role == 0 && wave >= 4 ? 0xffull : 0;
  const u32 tkf = lds_ticket(C_FIN, lane);
  const bool lastw = tkf == 7;
  if (!lastw) {
    MLOOK(63)
#define GO(n, name) "s_cmp_eq_u32 %0, " #n "\n\ts_cbranch_scc1 .Lpad_" #name "\n\t"
    asm volatile("s_mov_b64 exec, 0\n\t" GO(0, a3) GO(1, a2) GO(2, a1) GO(3, a0) GO(8, b3) GO(9, b2) GO(10, b1) GO(11, b0) GO(16, c1) GO(17, c0)
                 ".Lpad_back:\n\ts_mov_b64 exec, -1" :: "s"(WARM ? role * 8 + tkf : 7) : "scc");
#undef GO
  }
  if (lastw) {
    if (lane < 32) {
      f32x4 o = {0.f, 0.f, 0.f, 0.f};
#pragma unroll
      for (int w = 0; w < 8; ++w) o += st[16 + w] * *reinterpret_cast<const f32x4*>(accs + w * 128 + 4 * lane);
      const u32x2 ow = {min(cvt2(o[0], o[1]), EMPTY - 1), min(cvt2(o[2], o[3]), EMPTY - 1)};
      __builtin_amdgcn_raw_buffer_store_b64(ow, rs, G_ATT + u * 256 + lane * 8, 0, 16);
    }
    MLOOK(63)
    LDS_INC(C_PUB);
    STAMPW(11);
  }
  u32 gaw[2];
  u32x2 xw = {0u, 0u};
  float b1v;
  if (role < 2 && wave < 4) {
    if (role == 0) {
      u32 offs[1] = {G_MF + (r * 160 + cbA * 16 + c) * 16};
      u32x4 gm[1];
      if (ksA == 0) xw = *reinterpret_cast<const u32x2*>(a.x.p + r * DM + cbA * 64 + c * 4);
      gather<1>(rs, offs, gm, tg);
      gaw[0] = gm[0][0]; gaw[1] = gm[0][2];
    } else {
      if (MFPOS == 1) lds_wait<2>(C_ISS, 8);
      b1v = bf2f(a.b1[jB * 32 + (t & 31)]);
      u32 offs[5];
      u32x4 gm[5];
#pragma unroll
      for (int i = 0; i < 5; ++i) offs[i] = G_MF + (640 + t + 256 * i) * 16;
      gather<5>(rs, offs, gm, tg);
#pragma unroll
      for (int i = 0; i < 5; ++i) {
        const int it = t + 256 * i, row = it / 160, gi = it % 160;
        u32 w0 = gm[i][0], w1 = gm[i][2];
        if (row >= 4) { w0 = cvt2(1.f + lo16(w0), 1.f + hi16(w0)); w1 = cvt2(1.f + lo16(w1), 1.f + hi16(w1)); }
        *reinterpret_cast<u32x2*>(lds + L_XA + (row < 4 ? row + 8 : row) * XS + gi * 8) = u32x2{w0, w1};
      }
      asm volatile("" : "+v"(b1v));
    }
  }
  if (role < 2 && wave < 4) {
    if (half) STAMPT(8, 0);
    lds_wait<2>(C_ISS, 8);
    if (role == 0) {
      const bf16* wp = a.wo + (size_t)(HEADA(wave) * 128 + (lane >> 3)) * DM + cbA * 64;
#pragma unroll
      for (int i = 0; i < 16; ++i) {
        if (i == 2) PAD(a0, back);
        if (i == N1A) {
          lds_wait<2>(C_PUB, 1);
          __builtin_amdgcn_raw_buffer_store_b128(u32x4{EMPTY, EMPTY, EMPTY, EMPTY}, rs,
              wave < NREP && lane < 32 ? G_WO + wave * WO_REP + ((cbA * 4 + (lane >> 3)) * 8 + ksA) * 128 + (lane & 7) * 16 : OOB, 0, 16);
          asm volatile("" ::: "memory");
        }
        dma16(wp + (size_t)(8 * i) * DM + (s128e ^ ((i & 1) * 32)), L_W + wave * 16384 + i * 1024);
      }
      LDS_INC(C_ISS);
    } else {
      const bf16* wp = a.w1 + (size_t)(wave * 160 + (lane >> 2)) * FF + jB * 32 + ((((((lane >> 1) & 1) ^ (lane >> 5)) << 1) | (lane & 1)) * 8);
#pragma unroll
      for (int i = 0; i < 10; ++i) {
        if (i == 2) PAD(bd, back);
        if (i == N1B) lds_wait<2>(C_PUB, 1);
        dma16(wp + (size_t)(16 * i) * FF, L_W + (wave * 10 + i) * 1024);
      }
      const bf16* wp2 = a.w2 + (size_t)(jB * 32 + wave * 8 + (lane >> 3)) * DM + (s128e ^ ((wave & 1) * 32));
#pragma unroll
      for (int k = 0; k < 10; ++k) {
        if (k + 10 == N1B) lds_wait<2>(C_PUB, 1);
        dma16(wp2 + k * 64, L_WM2 + (k * 4 + wave) * 1024);
      }
    }
    STAMPT(16, 0);
  }
#define SELECTOR(sel) u32x4 sel; \
    _Pragma("unroll") for (int d = 0; d < 4; ++d) sel[d] = ((c & 2) == 0 && d == (c >> 2)) ? ((c & 1) ? 0x3f800000u : 0x00003f80u) : 0u;
#define PAIRSUM(sm) (((c & 8) ? sm[1] : sm[0]) + dpp<XOR8>((c & 8) ? sm[0] : sm[1]))
  const u32 tb = __builtin_amdgcn_readfirstlane(cnt[15]);
  if (role == 0) {
    PAD(a1, back);
    u32 a_x = L_XA + ((wave & 3) * 8 + (c & 7)) * 272 + r * 16;
    if (wave < 4) {
      PAD(a2, back);
      float ga4[4] = {lo16(gaw[0]), hi16(gaw[0]), lo16(gaw[1]), hi16(gaw[1])}, x4[4] = {lo16(xw[0]), hi16(xw[0]), lo16(xw[1]), hi16(xw[1])};
      u32 a_red = L_RED + ((wave * 4 + c) * 64 + r * 4) * 4, a_sum = L_RED + lane * 16, a_sc = L_ST + (32 + wave * 8 + (c & 7)) * 4;
      u32 a_out = wave < NREP ? G_WO + wave * WO_REP + ((cbA * 4 + (c >> 2)) * 8 + ksA) * 128 + r * 32 + (c & 3) * 8 : OOB;
      u32 a_w[4];
#pragma unroll
      for (int nt = 0; nt < 4; ++nt) a_w[nt] = L_W + wave * 16384 + (tr128 ^ (nt * 32));
      asm volatile("" : "+v"(ga4[0]), "+v"(ga4[1]), "+v"(ga4[2]), "+v"(ga4[3]), "+v"(x4[0]), "+v"(x4[1]), "+v"(x4[2]), "+v"(x4[3]));
      asm volatile("s_waitcnt vmcnt(%0)" :: "i"(AW) : "memory");
      LDS_INC(C_PP + 4 * wave);
      asm volatile("s_waitcnt vmcnt(4)" : "+v"(a_red), "+v"(a_sum), "+v"(a_sc), "+v"(a_out), "+v"(a_x), "+v"(a_w[0]), "+v"(a_w[1]), "+v"(a_w[2]), "+v"(a_w[3]) :: "memory");
      s16x8 wf[4][4];
#pragma unroll
      for (int s = 0; s < 3; ++s)
#pragma unroll
        for (int nt = 0; nt < 4; ++nt) wf[s][nt] = wfrag<512>(a_w[nt] + s * 4096);
      announce(rs, G_HA + ia * 128, C_GO, C_W, 8, tb, HAW, tg);
      STAMPT(19, 0);
      lds_wait<1>(C_PP + 4 * wave, 3);
      STAMPT(12, 0);
      PAD(a3, back);
      u32x4 av[4];
#pragma unroll
      for (int s = 0; s < 4; ++s) av[s] = *reinterpret_cast<const u32x4*>(lds + a_x + s * 64);
      const float wsc = *reinterpret_cast<const float*>(lds + a_sc);
      asm volatile("s_waitcnt vmcnt(1)" ::: "memory");
      STAMPT(23, 0);
#pragma unroll
      for (int nt = 0; nt < 4; ++nt) wf[3][nt] = wfrag<512>(a_w[nt] + 3 * 4096);
      __builtin_amdgcn_sched_barrier(0);
      f32x4 acc[4];
#pragma unroll
      for (int nt = 0; nt < 4; ++nt) acc[nt] = f32x4{0.f, 0.f, 0.f, 0.f};
#pragma unroll
      for (int s = 0; s < 4; ++s)
#pragma unroll
        for (int nt = 0; nt < 4; ++nt) acc[nt] = MFMA(wf[s][nt], av[s], acc[nt]);
#pragma unroll
      for (int nt = 0; nt < 4; ++nt) { acc[nt] *= wsc; asm("" : "+v"(acc[nt])); }
#pragma unroll
      for (int nt = 0; nt < 4; ++nt)
#pragma unroll
        for (int k = 0; k < 4; ++k) { float v = acc[nt][k] + dpp<SHL4>(acc[nt][k]); asm("" : "+v"(v)); acc[nt][k] = v; }
      asm volatile("" : "+v"(acc[0]), "+v"(acc[1]), "+v"(acc[2]), "+v"(acc[3]));
      if (c < 4) {
#pragma unroll
        for (int nt = 0; nt < 4; ++nt) *reinterpret_cast<f32x4*>(lds + a_red + nt * 64) = acc[nt];
      }
      BAR();
      STAMPT(21, 0);
      f32x4 pp[4];
#pragma unroll
      for (int w = 0; w < 4; ++w) pp[w] = *reinterpret_cast<const f32x4*>(lds + a_sum + w * 1024);
      asm volatile("" : "+v"(pp[0]), "+v"(pp[1]), "+v"(pp[2]), "+v"(pp[3]));
      const f32x4 s = (pp[0] + pp[1]) + (pp[2] + pp[3]);
      const u32x2 o = {min(cvt2(fmaf(s[0], ga4[0], x4[0]), fmaf(s[1], ga4[1], x4[1])), EMPTY - 1), min(cvt2(fmaf(s[2], ga4[2], x4[2]), fmaf(s[3], ga4[3], x4[3])), EMPTY - 1)};
      __builtin_amdgcn_raw_buffer_store_b64(o, rs, a_out, 0, 16);
    } else {
      u32 oa = G_ATT + blkA * 256 + (c >> 3) * 64 + r * 16;
      a_x += (c >> 3) * 64;
      u32x4 av[2];
      u64 m0 = ~0ull, m1 = ~0ull;
      av[0] = av[1] = u32x4{0u, 0u, 0u, 0u};
      asm volatile("s_waitcnt vmcnt(0)" : "+v"(oa), "+v"(a_x), "+v"(ml), "+v"(av[0]), "+v"(av[1]) :: "memory");
      SPINLOOP for (unsigned spins = 0; spins < SPIN_CAP; ++spins) {
        m2 &= __builtin_amdgcn_ballot_w64(ml[1] != tg) | __builtin_amdgcn_ballot_w64(ml[3] != tg);
        if (!m2) break;
        if (!spins) FAIL(4);
        __builtin_amdgcn_s_sleep(ASL);
        MLOOK(0)
      }
      {
        const float M = u2f(ml[0]), L = u2f(ml[2]), Mo = dpp<XOR7>(dpp<0x1B>(M)), Lo = dpp<XOR7>(dpp<0x1B>(L));
        const float Mx = fmaxf(M, Mo), le = L * __expf(M - Mx);
        const float wsc = le * frcp(fmaf(Lo, __expf(Mo - Mx), le));
        if (lane < 8) st[wave * 8 + lane] = wsc;
      }
#define ALOOK(W_) \
        asm volatile(LK(0, 2, "0", 0, 3) LK(1, 2, "0", 128, 4) LKEND \
                     : "+v"(av[0]), "+v"(av[1]) : "v"(oa), "s"(m0), "s"(m1), [rs] "s"(rsv), [w] "i"(W_) : "memory"); \
        m0 = __builtin_amdgcn_ballot_w64(missing(av[0])); \
        m1 = __builtin_amdgcn_ballot_w64(missing(av[1]));
      lds_wait<1>(C_PP + 4 * (wave - 4), 2);
      STAMPT(29, 448);
      ALOOK(0)
      STAMPT(18, 448);
      if (m0 | m1) {
        FAIL(1);
        SPINLOOP for (unsigned spins = 0; spins < SPIN_CAP; ++spins) {
          ALOOK(2 * (PDA - 1))
          if (!(m0 | m1)) break;
          __builtin_amdgcn_s_sleep(ASL);
        }
      }
#undef ALOOK
      asm volatile("" : "+v"(av[0]), "+v"(av[1]));
      *reinterpret_cast<u32x4*>(lds + a_x) = av[0];
      *reinterpret_cast<u32x4*>(lds + a_x + 128) = av[1];
      LDS_INC(C_PP + 4 * (wave - 4));
      STAMPT(22, 448);
      announce(rs, G_HA + ia * 128, C_GO, C_W, 8, tb, HAG, tg);
      BAR();
      asm volatile("" :: "v"(av[0]), "v"(av[1]));
    }
    STAMPT(13, 0);

  } else if (role == 1) {
    PAD(b0, bd);
    const int lb = (c >> 1) & 3, lc = (c & 1) * 8 + 2 * r;
    const int fc = lane & 31, hw = lane >> 5;
    SELECTOR(sel)
    constexpr int L_R2 = L_MOD, L_S2 = L_R2 + 4096;
    const u32 gw = G_WO + (jB % NREP) * WO_REP + wave * 5120;
    u32 go = gw + (c >> 3) * 1024 + r * 128 + (c & 7) * 16, go4 = gw + 4096 + lane * 16;
    u32 gl = G_HA + (((5 * wave + (lane & 8 ? 4 : 0)) >> 2) * 8 + (lane & 7)) * 128;
    u32 a_u = L_XA + wave * 640 + lb * 160 + ((c >> 3) * 16 + lc) * 2, a_u4 = L_XA + wave * 640 + lb * 160 + (64 + lc + (c >> 3)) * 2;
    u32 a_av = L_XA + wave * 640 + (c & 3) * 160 + r * 16, a_ah = a_av + 128 - r * 8;
    u32 a_red = L_R2 + ((wave * 4 + c) * 32 + r * 4) * 4;
    u32 a_st = L_S2 + ((lb * 2 + (c >> 3)) * 8 + wave) * 4;
    u32 a_fin = L_R2 + ((hw * 16 + (wave & 3)) * 32 + fc) * 4;
    u32 a_fst = L_S2 + (wave & 3) * 64;
    u32 a_out = c < 4 ? G_OUT + (wave * 5 * 54 + jB) * 128 + c * 32 + r * 8 : OOB;
    constexpr int L_F2 = L_PT;
    u32 a_fi = BM ? L_F2 + c * 64 + r * 16 : L_FI + (c & 3) * 64 + r * 16, a_fo = (BM ? L_F2 + 768 : L_FI) + ((wave & 3) * 32 + fc) * 2;
    u32 a_ou4 = G_OUT + ((wave * 5 + (c >> 2)) * 54 + jB) * 128 + (c & 3) * 32 + r * 8;
    asm volatile("" : "+v"(a_ou4));
    u32 a_pre = lane < 40 ? G_OUT + ((wave * 5 + (lane >> 3)) * 54 + jB) * 128 + (lane & 7) * 16 : OOB;
    u32x4 emp = {EMPTY, EMPTY, EMPTY, EMPTY};
    asm volatile("" : "+v"(a_pre), "+v"(emp));
#define OPRE() asm volatile("buffer_store_dwordx4 %0, %1, %2, 0 offen sc1" :: "v"(emp), "v"(a_pre), "s"(rsv) : "memory")
    if (OPOS == 0) OPRE();
    u32 a_w1 = L_W + wave * 5120 + (8 * r + (c >> 2)) * 64 + (r & 1) * 32 + (c & 3) * 8;
    u32 a_wh = L_W + wave * 5120 + 4096 + (4 * r + (c >> 2)) * 64 + (r >> 1) * 32 + (c & 3) * 8;
    u32 a_w2[5];
#pragma unroll
    for (int k = 0; k < 5; ++k) { const int n2 = wave * 5 + k; a_w2[k] = L_WM2 + (n2 >> 2) * 4096 + (tr128 ^ ((n2 & 3) * 32)); }
    asm volatile("" : "+v"(sel), "+v"(go), "+v"(go4), "+v"(gl), "+v"(a_u), "+v"(a_u4), "+v"(a_av), "+v"(a_ah), "+v"(a_red), "+v"(a_st), "+v"(a_fin), "+v"(a_fst), "+v"(a_out));
    asm volatile("s_waitcnt vmcnt(%9)" : "+v"(a_fi), "+v"(a_fo), "+v"(a_w1), "+v"(a_wh), "+v"(a_w2[0]), "+v"(a_w2[1]), "+v"(a_w2[2]), "+v"(a_w2[3]), "+v"(a_w2[4]) : "i"(OPOS == 0 ? 11 : 10) : "memory");
    STAMPT(23, 0);
    PAD(b1, back);
    BAR();
    STAMPT(12, 0);
    if (OPOS == 1) OPRE();
    if (BM) *reinterpret_cast<u32*>(lds + L_F2 + t * 4) = 0;
    s16x8 wf[2][2], wg[5];
    s16x4 wh[2];
#pragma unroll
    for (int nt = 0; nt < 2; ++nt) {
#pragma unroll
      for (int s = 0; s < 2; ++s) wf[s][nt] = wfrag<256>((a_w1 ^ (nt * 32)) + s * 2048);
      wh[nt] = __builtin_amdgcn_ds_read_tr16_b64_v4i16((lds_s4p)(uintptr_t)(a_wh ^ (nt * 32)));
    }
    float c1f[5];
    {
      const unsigned char* cp = lds + L_XA + (4 + lb) * XS + wave * 160;
#pragma unroll
      for (int i = 0; i < 2; ++i) { const u32 w = *reinterpret_cast<const u32*>(cp + (32 * i + (c >> 3) * 16 + lc) * 2); c1f[2 * i] = lo16(w); c1f[2 * i + 1] = hi16(w); }
      c1f[4] = u2f((u32)*reinterpret_cast<const unsigned short*>(cp + (64 + lc + (c >> 3)) * 2) << 16);
    }
#define W1MMA(acc) \
      f32x4 acc[2]; \
      _Pragma("unroll") for (int nt = 0; nt < 2; ++nt) { const f32x4 z_ = {0.f, 0.f, 0.f, 0.f}; acc[nt] = MFMA(wf[0][nt], av0, z_); } \
      _Pragma("unroll") for (int nt = 0; nt < 2; ++nt) acc[nt] = MFMA(wf[1][nt], av1, acc[nt]); \
      _Pragma("unroll") for (int nt = 0; nt < 2; ++nt) acc[nt] = __builtin_amdgcn_mfma_f32_16x16x16bf16_1k(wh[nt], av2, acc[nt], 0, 0, 0);
    {
      const unsigned char* p = lds + L_XA + (4 + (c & 7)) * XS + wave * 160;
      const u32x4 av0 = *reinterpret_cast<const u32x4*>(p + r * 16), av1 = *reinterpret_cast<const u32x4*>(p + 64 + r * 16);
      const s16x4 av2 = *reinterpret_cast<const s16x4*>(p + 128 + r * 8);
      W1MMA(acc)
#pragma unroll
      for (int nt = 0; nt < 2; ++nt) if (c < 8) *reinterpret_cast<f32x4*>(red + (wave * 8 + c) * 32 + nt * 16 + r * 4) = acc[nt];
    }
    float Tc = 0.f, Ts = 0.f;
#define TSUM() _Pragma("unroll") for (int k = 0; k < 4; ++k) { Tc += red[((hw * 4 + k) * 8 + (wave & 3)) * 32 + fc]; Ts += red[((hw * 4 + k) * 8 + 4 + (wave & 3)) * 32 + fc]; }
    u32x4 pa[5];
    f32x4 s4, s0, s1;
    {
      u64 m[5], old = 0;
#pragma unroll
      for (int i = 0; i < 5; ++i) { m[i] = ~0ull; pa[i] = u32x4{0u, 0u, 0u, 0u}; }
#pragma unroll
      for (int i = 0; i < 5; ++i) asm volatile("" : "+v"(c1f[i]), "+v"(pa[i]));
      u32x2 sg;
      if (BFW >= 0 && wave < 4) asm volatile("s_waitcnt vmcnt(%0)" :: "i"(BFW) : "memory");
      if (BFW >= 0) BAR();
      heads_up_start(rsv, gl, 0xffffull, tg, sg);
      VM0();
      BAR();
      STAMPT(29, 0);
#define BLOOK(W_) \
        asm volatile(LK(0, 5, "0", 0, 7) LK(1, 5, "0", 512, 8) LK(2, 5, "0", 2048, 9) LK(3, 5, "0", 2560, 10) LK(4, 6, "0", 0, 11) LKEND \
                     : "+v"(pa[0]), "+v"(pa[1]), "+v"(pa[2]), "+v"(pa[3]), "+v"(pa[4]) \
                     : "v"(go), "v"(go4), "s"(m[0]), "s"(m[1]), "s"(m[2]), "s"(m[3]), "s"(m[4]), [rs] "s"(rsv), [w] "i"(W_) : "memory");
      heads_up<0>(rsv, gl, 0xffffull, tg, sg);
      heads_up_time(0xffffull, sg);
      STAMPT(21, 0);
      BLOOK(63)
      announce(rs, G_HB + jB * 128, C_GO, C_W, 8, tb, HCD, tg);
#pragma unroll
      for (int k = 0; k < 5; ++k) wg[k] = wfrag<512>(a_w2[k]);
      PAD(b2, back);
      TSUM()
#pragma unroll
      for (int i = 0; i < 5; ++i) asm volatile("" : "+v"(wg[i]));
      asm volatile("" : "+v"(Tc), "+v"(Ts));
      STAMPT(18, 0);
      asm volatile("s_waitcnt vmcnt(1)" : "+v"(pa[0]), "+v"(pa[1]), "+v"(pa[2]), "+v"(pa[3]), "+v"(pa[4]) :: "memory");
      STAMPT(22, 0);
#define BSUM() { const f32x4 z = {0.f, 0.f, 0.f, 0.f}; \
      s4 = MFMA(sel, pa[4], z); s0 = MFMA(sel, pa[0], z); s1 = MFMA(sel, pa[2], z); s0 = MFMA(sel, pa[1], s0); s1 = MFMA(sel, pa[3], s1); }
      SPINLOOP for (unsigned spins = 0; spins < SPIN_CAP; ++spins) {
        BSUM()
        if (!(__builtin_amdgcn_ballot_w64(__builtin_isunordered(s4[0], s4[1])) | __builtin_amdgcn_ballot_w64(__builtin_isunordered(s0[0], s0[1])) |
              __builtin_amdgcn_ballot_w64(__builtin_isunordered(s1[0], s1[1])))) break;
        u64 bad = 0;
#pragma unroll
        for (int i = 0; i < 5; ++i) { m[i] = __builtin_amdgcn_ballot_w64(missing(pa[i])); bad |= m[i]; }
        if (!(bad | old)) break;
        old = bad;
        if (!spins) FAIL(2);
        BLOOK(5 * (PDB - 1))
      }
    }
    STAMPT(14, 0); WSTAMP(3);
    float xf[5];
    {
      float e0 = s4[0], e1 = s4[1];
      asm("" : "+v"(e0), "+v"(e1));
      xf[4] = bfr(((c & 8) ? e1 : e0) + dpp<XOR8>((c & 8) ? e0 : e1));
      *reinterpret_cast<unsigned short*>(lds + a_u4) = (unsigned short)cvt2(xf[4] * c1f[4], 0.f);
      const u32 w0 = cvt2(s0[0], s0[1]), w1 = cvt2(s1[0], s1[1]);
      xf[0] = lo16(w0); xf[1] = hi16(w0); xf[2] = lo16(w1); xf[3] = hi16(w1);
      *reinterpret_cast<u32*>(lds + a_u) = cvt2(xf[0] * c1f[0], xf[1] * c1f[1]);
      *reinterpret_cast<u32*>(lds + a_u + 64) = cvt2(xf[2] * c1f[2], xf[3] * c1f[3]);
    }
    STAMPT(19, 0);
    {
      u32x4 av0 = *reinterpret_cast<const u32x4*>(lds + a_av), av1 = *reinterpret_cast<const u32x4*>(lds + a_av + 64);
      s16x4 av2 = *reinterpret_cast<const s16x4*>(lds + a_ah);
      __builtin_amdgcn_sched_barrier(0);
      const float ps = ((xf[0] + xf[1]) + (xf[2] + xf[3])) + xf[4];
      const float pq = fmaf(xf[0], xf[0], fmaf(xf[1], xf[1], xf[4] * xf[4])) + fmaf(xf[2], xf[2], xf[3] * xf[3]);
      const float t1 = ps + dpp<XOR8>(ps), t2 = pq + dpp<XOR8>(pq);
      float z = (c & 8) ? t2 : t1;
      z += dpp<XOR1>(z);
      asm volatile("" : "+v"(z));
      __builtin_amdgcn_sched_barrier(0);
      W1MMA(acc)
      z = cross_rows_sum(z);
      if ((lane & 49) == 0) *reinterpret_cast<float*>(lds + a_st) = z;
      if (c < 4) {
#pragma unroll
        for (int nt = 0; nt < 2; ++nt) *reinterpret_cast<f32x4*>(lds + a_red + nt * 64) = acc[nt];
      }
    }
#undef W1MMA
    PAD(b3, back);
    BAR();
    STAMPT(24, 0);
    if (wave < 4) {
      f32x4 sv[4];
      float sp[4];
#pragma unroll
      for (int k = 0; k < 4; ++k) sv[k] = *reinterpret_cast<const f32x4*>(lds + a_fst + k * 16);
#pragma unroll
      for (int k = 0; k < 4; ++k) sp[k] = *reinterpret_cast<const float*>(lds + a_fin + k * 512);
      __builtin_amdgcn_sched_barrier(0);
      const f32x4 s1 = sv[0] + sv[1], s2 = sv[2] + sv[3];
      const float mean = ((s1[0] + s1[1]) + (s1[2] + s1[3])) * (1.f / DM);
      const float var = fmaxf(((s2[0] + s2[1]) + (s2[2] + s2[3])) * (1.f / DM) - mean * mean, 0.f);
      const float rstd = __builtin_amdgcn_rsqf(var + 1e-5f);
      const float zp = fmaf(rstd, fmaf(-mean, Tc, (sp[0] + sp[1]) + (sp[2] + sp[3])), Ts);
      const float hh = bfr(bfr(swap32_add(zp, zp)) + b1v);
      const float e2 = __builtin_amdgcn_exp2f(hh * fmaf(hh * hh, 0.1029432397f, 2.302208198f));
      const float f = fmaf(-hh, frcp(e2 + 1.f), hh);
      *reinterpret_cast<unsigned short*>(lds + a_fo) = (unsigned short)cvt2(f, 0.f);
    }
    BAR();
    if (BM) {
      u32x4 av[4];
#pragma unroll
      for (int k = 0; k < 4; ++k) av[k] = *reinterpret_cast<const u32x4*>(lds + a_fi + (3 - k) * 256);
      const f32x4 z = {0.f, 0.f, 0.f, 0.f};
      f32x4 a4 = MFMA(wg[4], av[0], z), a0 = MFMA(wg[0], av[0], z);
#pragma unroll
      for (int k = 1; k < 4; ++k) a0 = MFMA(wg[k], av[k], a0);
      __builtin_amdgcn_sched_barrier(0);
      const u32x2 o4 = {min(cvt2(a4[0], a4[1]), EMPTY - 1), min(cvt2(a4[2], a4[3]), EMPTY - 1)};
      __builtin_amdgcn_raw_buffer_store_b64(o4, rs, a_out, 4 * (54 * 128), 16);
      const u32x2 o0 = {min(cvt2(a0[0], a0[1]), EMPTY - 1), min(cvt2(a0[2], a0[3]), EMPTY - 1)};
      __builtin_amdgcn_raw_buffer_store_b64(o0, rs, a_ou4, 0, 16);
    } else {
      const u32x4 av = *reinterpret_cast<const u32x4*>(lds + a_fi);
      f32x4 acc[5];
#pragma unroll
      for (int k = 0; k < 5; ++k) { const f32x4 z = {0.f, 0.f, 0.f, 0.f}; acc[k] = MFMA(wg[k], av, z); }
      __builtin_amdgcn_sched_barrier(0);
#pragma unroll
      for (int k = 0; k < 5; ++k) {
        const u32x2 o = {min(cvt2(acc[k][0], acc[k][1]), EMPTY - 1), min(cvt2(acc[k][2], acc[k][3]), EMPTY - 1)};
        __builtin_amdgcn_raw_buffer_store_b64(o, rs, a_out, k * (54 * 128), 16);
      }
    }
    STAMPT(15, 0);
    asm volatile("" :: "v"(pa[0]), "v"(pa[1]), "v"(pa[2]), "v"(pa[3]), "v"(pa[4]));

  } else if (role == 2 && wave == 0) {
    PAD(c0, back);
    const int lb = (c >> 1) & 3, col = g * 16 + (c & 1) * 8 + 2 * r + (c >> 3);
    float b2v = bf2f(a.b2[col]);
    const f32x4 z = {0.f, 0.f, 0.f, 0.f};
    SELECTOR(sel)
    float x1v, gbv;
    {
      u32 offs[1] = {G_MF + ((12 + lb) * 160 + (col >> 2)) * 16};
      u32x4 gm[1];
      gather<1>(rs, offs, gm, tg);
      const u32 gbw = (r & 1) ? gm[0][2] : gm[0][0];
      gbv = (c & 8) ? hi16(gbw) : lo16(gbw);
    }
    u32 oo = lb * DM + col;
    asm volatile("" : "+v"(gbv), "+v"(oo), "+v"(sel), "+v"(b2v));
    u32x4 pa[7];
    u32 go = G_OUT + g * (54 * 128) + lane * 16, gx = G_WO + g * 1024 + lane * 16;
#pragma unroll
    for (int i = 0; i < 7; ++i) { pa[i] = u32x4{0u, 0u, 0u, 0u}; asm volatile("" : "+v"(pa[i])); }
    asm volatile("" : "+v"(go), "+v"(gx));
    u32x2 hv;
    if (CPUB) lds_wait<2>(C_PUB, 1);
    heads_up_start(rsv, G_HA + ((g >> 2) * 8 + lane) * 128, 0xffull, tg, hv);
    heads_up<CSL>(rsv, G_HA + ((g >> 2) * 8 + lane) * 128, 0xffull, tg, hv);
    hv[0] += CXD;
    heads_up_time(0xffull, hv);
    {
      u32x4 gm[1];
      gather_e<1, 2, 1>(rsv, gx, gm, ~0ull);
      const f32x4 sm = MFMA(sel, gm[0], z);
      x1v = bfr(PAIRSUM(sm));
      asm volatile("" : "+v"(x1v));
    }
    STAMPT(23, 0);
    PAD(c1, back);
    heads_up_start(rsv, G_HB + lane * 128, (1ull << 54) - 1, tg, hv);
    heads_up<0>(rsv, G_HB + lane * 128, (1ull << 54) - 1, tg, hv);
    heads_up_time((1ull << 54) - 1, hv);
    STAMPT(14, 0);
    f32x4 s0, s1;
    {
      u64 m[7], old = 0;
#pragma unroll
      for (int i = 0; i < 7; ++i) m[i] = i < 6 ? ~0ull : (1ull << 48) - 1;
      look<0>(rsv, go, pa, m);
#define CSUM() { s0 = MFMA(sel, pa[0], z); s1 = MFMA(sel, pa[1], z); \
      _Pragma("unroll") for (int i = 2; i < 7; ++i) { if (i & 1) s1 = MFMA(sel, pa[i], s1); else s0 = MFMA(sel, pa[i], s0); } \
      s0 += s1; }
      SPINLOOP for (unsigned spins = 0; spins < SPIN_CAP; ++spins) {
        CSUM()
        if (!__builtin_amdgcn_ballot_w64(__builtin_isunordered(s0[0], s0[1]))) break;
        u64 bad = 0;
#pragma unroll
        for (int i = 0; i < 7; ++i) { m[i] = __builtin_amdgcn_ballot_w64(missing(pa[i])); bad |= m[i]; }
        if (!(bad | old)) break;
        old = bad;
        if (!spins) FAIL(3);
        look<7 * (PDC - 1)>(rsv, go, pa, m);
      }
    }
    STAMPT(16, 0);
    const float y = bfr(bfr(PAIRSUM(s0)) + b2v);
    a.out[oo] = __float2bfloat16(x1v + bfr(gbv * y));
    STAMPT(17, 0);
    asm volatile("" :: "v"(pa[0]), "v"(pa[1]), "v"(pa[2]), "v"(pa[3]), "v"(pa[4]), "v"(pa[5]), "v"(pa[6]), "v"(hv));

    if (u == 0 && t == 0)
      __hip_atomic_store(reinterpret_cast<u32*>(a.ws + WS_EPOCH), tg, __ATOMIC_RELAXED, __HIP_MEMORY_SCOPE_AGENT);
  }
  STAMP_FLUSH;
}

extern "C" void kernel_launch(void* const* d_in, const int* in_sizes, int n_in, void* d_out, int out_size, void* d_ws,
                              size_t ws_size, hipStream_t stream) {
  Args a;
  a.x = Desc{(const bf16*)d_in[0], NB * DM * 2, 0x00020000u};
  a.kc = (const bf16*)d_in[1];
  a.vc = (const bf16*)d_in[2];
  a.wq = Desc{(const bf16*)d_in[3], DM * QD * 2, 0x00020000u};
  a.wo = (const bf16*)d_in[4];
  a.w1 = (const bf16*)d_in[6];
  a.b1 = (const bf16*)d_in[7];
  a.w2 = (const bf16*)d_in[8];
  a.b2 = (const bf16*)d_in[9];
  a.c = Desc{(const bf16*)d_in[10], NB * DM * 2, 0x00020000u};
  a.wmod = Desc{(const bf16*)d_in[11], DM * MODN * 2, 0x00020000u};
  a.out = (bf16*)d_out;
  a.ws = (unsigned char*)d_ws;
  mega_kernel<<<NWG, NT, 0, stream>>>(a);
}
```

```cpp
#define SPINLOOP _Pragma("nounroll")
#define P_LAG 14
#define P_NPRE 32
#define P_NVE 6
#define P_NVA 6
#define QODD 4
#define QEVEN 4
#define MFPOS 0
#define EA 0
#define EB 0
#define EC 4
#define APOLL 0
#define N1E N1A
#define TAU_A 234
#define TAU_B TAU_A
#define TAU_X TAU_A
#define TAU_E 247
#define PK 0
#define PL 1
#define QPRE 1
#define QHOLD 0
#define CT0 1
#define TOFF 60
#define BLIGHT 1
#define BPRE 0
#define BW1 1
#define BMASK 1
#define CSLEEP 1
#define CSL 2
#define CPUB 1
#define CXD 20
#define OPOS 1
#define NREPL 1
#define OUTPRE 1
#define ASL 1
#define AEARLY 1
#define PDC 4
#define PDA 3
#define PDB 3
#define PSB 1
#define HC 1
#define HCD 120
#define HAG 60
#define AW 4
#define BFW -1
#define FULLF 1
#define WARM 1
#define HT 0
#define HX 10
#define HTD 55
#define HSL 1
#define NANT 1
#define HAW 65
#define BM 1
#define HPD 1
#define BT 0
#define BSL 2
#define PB 0
#define GOPOS 2
#define JMAP 1
#define N1A 10
#define N1B 10
#define PT 0
#define PTAUX 16
#define LAY 1
#define OUTM 1
#define EPE 0
#define DPOS 2
#define DAUX 16
#define FAUX 16
#define KOUT 1
#define NK0 12
#define GT 540
#define PLO 0
#define PLS 16
#define P_GATE 8
#include <hip/hip_runtime.h>
#include <hip/hip_bf16.h>
#include <stdint.h>

using bf16 = __hip_bfloat16;
using u32 = unsigned;
using u64 = unsigned long long;
using u32x2 = __attribute__((ext_vector_type(2))) unsigned;
using u32x4 = __attribute__((ext_vector_type(4))) unsigned;
using f32x4 = __attribute__((ext_vector_type(4))) float;
using bf16x2 = __attribute__((ext_vector_type(2))) __bf16;
typedef __attribute__((ext_vector_type(2))) float f32x2;
typedef short s16x8 __attribute__((ext_vector_type(8)));
typedef short s16x4 __attribute__((ext_vector_type(4)));
typedef __attribute__((address_space(3))) s16x4* lds_s4p;
typedef __attribute__((ext_vector_type(8))) __bf16 bf16x8v;

constexpr int NB = 4, DM = 640, NH = 32, DH = 128, SK = 8192, WIN = 2048, FF = 1728, QD = 4096, MODN = 3840;
constexpr int NT = 512, NWG = 256;

constexpr u32 WS_EPOCH = 0;
constexpr u32 G_T = 128;
constexpr u32 G_Q = 256;
constexpr u32 G_MOD = G_Q + 8 * 1024 * 16;
constexpr u32 G_MF = G_MOD + 8 * 1024 * 16;
constexpr u32 G_HB = G_MF + 16 * 160 * 16;
constexpr u32 G_HA = G_HB + 64 * 128;
constexpr u32 G_HT = G_HA + 80 * 128;
constexpr u32 G_ML = G_HT + 256 * 128;
constexpr u32 EMPTY = 0xffffffffu;
constexpr int NREP = NREPL;
constexpr u32 G_WO = G_ML + 256 * 64;
constexpr u32 WO_REP = 40 * 1024;
constexpr u32 G_ATT = G_WO + NREP * WO_REP;
constexpr u32 G_OUT = G_ATT + 256 * 256;
constexpr u32 WS_END = G_OUT + 40 * 54 * 128;
constexpr u32 OOB = 0xF0000000u;
#define GIDX(n, ks, b, cg) ((((ks) * ((n) / 4) + ((cg) >> 2)) * 16 + (b) * 4 + ((cg) & 3)) * 16)

#define WSTAMP(k) do {} while (0)
#define STAMPW(i) do {} while (0)
#define STAMPT(i, tt) do {} while (0)
#define STAMP_DECL
#define STAMP_FLUSH do {} while (0)
#define FAIL(i) do {} while (0)
#define PAD(name, next) asm volatile(".Lpad_" #name ":\n\ts_cbranch_execz .Lpad_" #next)
#define BAR() asm volatile("s_waitcnt lgkmcnt(0)\n\ts_barrier" ::: "memory")
#define VM0() asm volatile("s_waitcnt vmcnt(0)" ::: "memory")

__device__ __forceinline__ float bfr(float x) { return __bfloat162float(__float2bfloat16(x)); }
__device__ __forceinline__ float bf2f(bf16 x) { return __bfloat162float(x); }
__device__ __forceinline__ float lo16(unsigned u) { return __builtin_bit_cast(float, u << 16); }
__device__ __forceinline__ float hi16(unsigned u) { return __builtin_bit_cast(float, u & 0xffff0000u); }
__device__ __forceinline__ u32 f2u(float f) { return __builtin_bit_cast(u32, f); }
__device__ __forceinline__ float u2f(u32 f) { return __builtin_bit_cast(float, f); }
__device__ __forceinline__ u32 cvt2(float a, float b) {
  const f32x2 v = {a, b};
  return __builtin_bit_cast(u32, __builtin_convertvector(v, bf16x2));
}
__device__ __forceinline__ float frcp(float x) { return __builtin_amdgcn_rcpf(x); }
__device__ __forceinline__ float fsqrt(float x) { return __builtin_amdgcn_sqrtf(x); }
__device__ __forceinline__ float fexp(float x) { return __builtin_amdgcn_exp2f(x * 1.4426950408889634f); }
__device__ __forceinline__ float dot2(unsigned k, unsigned q, float acc) {
  return __builtin_amdgcn_fdot2_f32_bf16(__builtin_bit_cast(bf16x2, k), __builtin_bit_cast(bf16x2, q), acc, false);
}
__device__ __forceinline__ float addlo(unsigned w, float acc) { u32 k = 0x00003f80u; asm("" : "+s"(k)); return dot2(w, k, acc); }
__device__ __forceinline__ float addhi(unsigned w, float acc) { return dot2(w, 0x3f800000u, acc); }
template <int CTRL> __device__ __forceinline__ float dpp(float x) {
  return __builtin_bit_cast(float, __builtin_amdgcn_mov_dpp(__builtin_bit_cast(int, x), CTRL, 0xf, 0xf, true));
}
template <int CTRL> __device__ __forceinline__ u32 dppu(u32 x) { return (u32)__builtin_amdgcn_mov_dpp((int)x, CTRL, 0xf, 0xf, true); }
constexpr int XOR1 = 0xB1, XOR2 = 0x4E, XOR7 = 0x141, XOR8 = 0x128, ROR4 = 0x124, ROR2 = 0x122, ROR1 = 0x121, SHL4 = 0x104;
__device__ __forceinline__ float swap32_add(float a, float b) {
  auto s = __builtin_amdgcn_permlane32_swap(f2u(a), f2u(b), false, false);
  return u2f(s[0]) + u2f(s[1]);
}
__device__ __forceinline__ float swap16_add(float a, float b) {
  auto s = __builtin_amdgcn_permlane16_swap(f2u(a), f2u(b), false, false);
  return u2f(s[0]) + u2f(s[1]);
}
__device__ __forceinline__ float cross_rows_sum(float x) { x = swap16_add(x, x); return swap32_add(x, x); }
__device__ __forceinline__ float cross_rows_max(float x) {
  auto s = __builtin_amdgcn_permlane16_swap(f2u(x), f2u(x), false, false);
  x = fmaxf(u2f(s[0]), u2f(s[1]));
  auto s2 = __builtin_amdgcn_permlane32_swap(f2u(x), f2u(x), false, false);
  return fmaxf(u2f(s2[0]), u2f(s2[1]));
}
__device__ __forceinline__ float wave_sum(float x) {
  x += dpp<XOR8>(x); x += dpp<ROR4>(x); x += dpp<ROR2>(x); x += dpp<ROR1>(x);
  return cross_rows_sum(x);
}
__device__ __forceinline__ float wave_max(float x) {
  x = fmaxf(x, dpp<XOR8>(x)); x = fmaxf(x, dpp<ROR4>(x)); x = fmaxf(x, dpp<ROR2>(x)); x = fmaxf(x, dpp<ROR1>(x));
  return cross_rows_max(x);
}

__device__ __forceinline__ void st_gran(__amdgpu_buffer_rsrc_t rs, u32 off, u32 a, u32 b, u32 tg) {
  u32x4 v = {a, tg, b, tg};
  __builtin_amdgcn_raw_buffer_store_b128(v, rs, off, 0, 16);
}
__device__ __forceinline__ u32x4 ld_gran(__amdgpu_buffer_rsrc_t rs, u32 off) {
  return __builtin_amdgcn_raw_buffer_load_b128(rs, off, 0, 16);
}
constexpr unsigned SPIN_CAP = 1u << 18;
template <int N>
__device__ __forceinline__ void gather(__amdgpu_buffer_rsrc_t rs, const u32 (&offs)[N], u32x4 (&g)[N], u32 tg) {
  SPINLOOP for (unsigned spins = 0; spins < SPIN_CAP; ++spins) {
    bool ok = true;
#pragma unroll
    for (int i = 0; i < N; ++i) g[i] = ld_gran(rs, offs[i]);
#pragma unroll
    for (int i = 0; i < N; ++i) ok &= (offs[i] == OOB) | ((g[i][1] == tg) & (g[i][3] == tg));
    if (__all(ok)) break;
    __builtin_amdgcn_s_sleep(1);
  }
}
template <int N>
__device__ __forceinline__ void gather_m(__amdgpu_buffer_rsrc_t rs, u32 (&offs)[N], u32 (&va)[N], u32 (&vb)[N], u32 tg) {
#pragma unroll
  for (int i = 0; i < N; ++i) { va[i] = 0; vb[i] = 0; }
  SPINLOOP for (unsigned spins = 0; spins < SPIN_CAP; ++spins) {
    u32x4 g[N];
#pragma unroll
    for (int i = 0; i < N; ++i) g[i] = ld_gran(rs, offs[i]);
    bool ok = true;
#pragma unroll
    for (int i = 0; i < N; ++i) {
      const bool hit = (offs[i] != OOB) & (g[i][1] == tg) & (g[i][3] == tg);
      va[i] = hit ? g[i][0] : va[i]; vb[i] = hit ? g[i][2] : vb[i];
      offs[i] = hit ? OOB : offs[i];
      ok &= (offs[i] == OOB);
    }
    if (__all(ok)) break;
    __builtin_amdgcn_s_sleep(1);
  }
}

__device__ __forceinline__ bool missing(u32x4 v) { return max(max(v[0], v[1]), max(v[2], v[3])) == EMPTY; }
#define LK(v, o, so, imm, m) "s_mov_b64 exec, %" #m "\n\tbuffer_load_dwordx4 %" #v ", %" #o ", %[rs], " so " offen offset:" #imm " sc1\n\t"
#define LKEND "s_mov_b64 exec, -1\n\ts_waitcnt vmcnt(%[w])"
template <int W> __device__ __forceinline__ void look(u32x4 rs, u32 off, u32x4 (&v)[1], const u64 (&m)[1]) {
  asm volatile(LK(0, 1, "0", 0, 2) LKEND : "+v"(v[0]) : "v"(off), "s"(m[0]), [rs] "s"(rs), [w] "i"(W) : "memory");
}
template <int W> __device__ __forceinline__ void look(u32x4 rs, u32 off, u32x4 (&v)[5], const u64 (&m)[5]) {
  asm volatile(LK(0, 5, "0", 0, 6) LK(1, 5, "0", 1024, 7) LK(2, 5, "0", 2048, 8) LK(3, 5, "0", 3072, 9) LK(4, 5, "%11", 0, 10) LKEND
               : "+v"(v[0]), "+v"(v[1]), "+v"(v[2]), "+v"(v[3]), "+v"(v[4])
               : "v"(off), "s"(m[0]), "s"(m[1]), "s"(m[2]), "s"(m[3]), "s"(m[4]), "s"(4096u), [rs] "s"(rs), [w] "i"(W) : "memory");
}
template <int W> __device__ __forceinline__ void look(u32x4 rs, u32 off, u32x4 (&v)[7], const u64 (&m)[7]) {
  asm volatile(LK(0, 7, "0", 0, 8) LK(1, 7, "0", 1024, 9) LK(2, 7, "0", 2048, 10) LK(3, 7, "0", 3072, 11)
               LK(4, 7, "%15", 0, 12) LK(5, 7, "%15", 1024, 13) LK(6, 7, "%15", 2048, 14) LKEND
               : "+v"(v[0]), "+v"(v[1]), "+v"(v[2]), "+v"(v[3]), "+v"(v[4]), "+v"(v[5]), "+v"(v[6])
               : "v"(off), "s"(m[0]), "s"(m[1]), "s"(m[2]), "s"(m[3]), "s"(m[4]), "s"(m[5]), "s"(m[6]), "s"(4096u), [rs] "s"(rs), [w] "i"(W) : "memory");
}
template <int N, int SLEEP, int D>
__device__ __forceinline__ void gather_e(u32x4 rs, u32 off, u32x4 (&v)[N], u64 last) {
  u64 m[N];
#pragma unroll
  for (int i = 0; i < N; ++i) { m[i] = i < N - 1 ? ~0ull : last; v[i] = u32x4{0u, 0u, 0u, 0u}; }
  look<0>(rs, off, v, m);
  SPINLOOP for (unsigned spins = 0; spins < SPIN_CAP; ++spins) {
    u64 bad = 0;
#pragma unroll
    for (int i = 0; i < N; ++i) { m[i] = __builtin_amdgcn_ballot_w64(missing(v[i])); bad |= m[i]; }
    if (!bad) break;
    if (SLEEP) __builtin_amdgcn_s_sleep(SLEEP);
    look<N * (D - 1)>(rs, off, v, m);
  }
}

#define HLOOK(W_) asm volatile("s_mov_b64 exec, %2\n\tbuffer_load_dwordx2 %0, %1, %3, 0 offen sc1\n\ts_mov_b64 exec, -1\n\ts_waitcnt vmcnt(%4)" \
                               : "+v"(v) : "v"(off), "s"(m), "s"(rs), "i"(W_) : "memory")
__device__ __forceinline__ void heads_up_start(u32x4 rs, u32 off, u64 m, u32 tg, u32x2& v) { v = u32x2{0u, ~tg}; HLOOK(63); }
template <int SLEEP>
__device__ __forceinline__ void heads_up(u32x4 rs, u32 off, u64 m, u32 tg, u32x2& v) {
  asm volatile("s_waitcnt vmcnt(0)" : "+v"(v) :: "memory");
  SPINLOOP for (unsigned spins = 0; spins < SPIN_CAP; ++spins) {
    m &= __builtin_amdgcn_ballot_w64(v[1] != tg);
    if (!m) break;
    if (SLEEP) __builtin_amdgcn_s_sleep(SLEEP);
    HLOOK(0);
  }
}
__device__ __forceinline__ void heads_up_time(u64 all, u32x2 v) {
  SPINLOOP for (unsigned spins = 0; spins < 64; ++spins)
    if (!(all & __builtin_amdgcn_ballot_w64((int)((u32)__builtin_amdgcn_s_memrealtime() - v[0]) < 0))) break;
}
__device__ __forceinline__ void announce(__amdgpu_buffer_rsrc_t rs, u32 off, u32 a_max, u32 a_cnt, u32 n, u32 tb, u32 d, u32 tg) {
  u32 tk = 0, mx;
  const u32 f = (u32)__builtin_amdgcn_s_memrealtime() - tb + d;
  asm volatile("s_mov_b64 exec, 1\n\tds_max_u32 %2, %3\n\tds_add_rtn_u32 %0, %4, %5\n\tds_read_b32 %1, %2\n\ts_mov_b64 exec, -1\n\ts_waitcnt lgkmcnt(0)"
               : "+v"(tk), "=&v"(mx) : "v"(a_max), "v"(f), "v"(a_cnt), "v"(1u) : "memory");
  const u32 lane = threadIdx.x & 63;
  __builtin_amdgcn_raw_buffer_store_b64(u32x2{__builtin_amdgcn_readfirstlane(mx) + tb, tg}, rs,
                                        (lane < 16 && __builtin_amdgcn_readfirstlane(tk) == n - 1) ? off + lane * 8 : OOB, 0, 16);
}

__device__ __forceinline__ void st_g22(__amdgpu_buffer_rsrc_t rs, u32 off, f32x4 v, u32 tg) {
  const u32 a0 = f2u(v[0]) + 0x200u, a1 = f2u(v[1]) + 0x200u, a2 = f2u(v[2]) + 0x200u, a3 = f2u(v[3]) + 0x200u;
  const u32x4 w = {(a0 >> 10) | ((a1 << 12) & 0xffc00000u), (a1 >> 20) | (tg << 12), (a2 >> 10) | ((a3 << 12) & 0xffc00000u), (a3 >> 20) | (tg << 12)};
  __builtin_amdgcn_raw_buffer_store_b128(w, rs, off, 0, 16);
}
__device__ __forceinline__ f32x4 un_g22(u32x4 w) {
  return f32x4{u2f(w[0] << 10), u2f(__builtin_amdgcn_alignbit(w[1], w[0], 22) << 10), u2f(w[2] << 10), u2f(__builtin_amdgcn_alignbit(w[3], w[2], 22) << 10)};
}
template <int N, int SLEEP>
__device__ __forceinline__ void gather22(__amdgpu_buffer_rsrc_t rs, u32 (&offs)[N], u32x4 (&v)[N], u32 tg, u32 a_inc = 0) {
#pragma unroll
  for (int i = 0; i < N; ++i) v[i] = u32x4{0u, 0u, 0u, 0u};
  SPINLOOP for (unsigned spins = 0; spins < SPIN_CAP; ++spins) {
    u32x4 g[N];
#pragma unroll
    for (int i = 0; i < N; ++i) g[i] = ld_gran(rs, offs[i]);
    if (a_inc) asm volatile("s_mov_b64 exec, 1\n\tds_add_u32 %0, %1\n\ts_mov_b64 exec, -1" :: "v"(a_inc), "v"(1u) : "memory");
    bool ok = true;
#pragma unroll
    for (int i = 0; i < N; ++i) {
      const bool hit = (offs[i] != OOB) & (((g[i][1] ^ (tg << 12)) | (g[i][3] ^ (tg << 12))) < 4096u);
#pragma unroll
      for (int k = 0; k < 4; ++k) v[i][k] = hit ? g[i][k] : v[i][k];
      offs[i] = hit ? OOB : offs[i];
      ok &= (offs[i] == OOB);
    }
    if (__all(ok)) break;
    __builtin_amdgcn_s_sleep(SLEEP);
  }
}

__device__ __forceinline__ u32 next_tag(u32 ep) { const u32 t = ep + 1u; return (t & 0xfffffu) ? t : t + 1u; }

__device__ __forceinline__ void lds_arrive(u32 addr, int lane) {
  asm volatile("s_waitcnt lgkmcnt(0)" ::: "memory");
  if (lane == 0) asm volatile("ds_add_u32 %0, %1" :: "v"(addr), "v"(1u) : "memory");
}
__device__ __forceinline__ u32 lds_ticket(u32 addr, int lane) {
  u32 r = 0;
  asm volatile("s_waitcnt lgkmcnt(0)" ::: "memory");
  if (lane == 0) asm volatile("ds_add_rtn_u32 %0, %1, %2\n\ts_waitcnt lgkmcnt(0)" : "=v"(r) : "v"(addr), "v"(1u) : "memory");
  return __builtin_amdgcn_readfirstlane(r);
}
template <int SLEEP>
__device__ __forceinline__ void lds_wait(u32 addr, u32 target) {
  SPINLOOP for (unsigned spins = 0; spins < (1u << 22); ++spins) {
    u32 v;
    asm volatile("ds_read_b32 %0, %1\n\ts_waitcnt lgkmcnt(0)" : "=v"(v) : "v"(addr) : "memory");
    if (v >= target) break;
    __builtin_amdgcn_s_sleep(SLEEP);
  }
}
__device__ __forceinline__ void dma16(const void* gsrc, u32 lds_dst) {
  u32 keep;
  asm volatile("s_mov_b32 %0, m0\n\ts_mov_b32 m0, %2\n\ts_nop 0\n\tglobal_load_lds_dwordx4 %1, off\n\ts_mov_b32 m0, %0"
               : "=&s"(keep) : "v"(gsrc), "s"(lds_dst) : "memory");
}
__device__ __forceinline__ void dma16nt(const void* gsrc, u32 lds_dst) {
  u32 keep;
  asm volatile("s_mov_b32 %0, m0\n\ts_mov_b32 m0, %2\n\ts_nop 0\n\tglobal_load_lds_dwordx4 %1, off nt\n\ts_mov_b32 m0, %0"
               : "=&s"(keep) : "v"(gsrc), "s"(lds_dst) : "memory");
}
__device__ __forceinline__ u32x4 ldasm(const void* p) {
  u32x4 v;
  asm volatile("global_load_dwordx4 %0, %1, off" : "=v"(v) : "v"(p) : "memory");
  return v;
}
__device__ __forceinline__ u32 ldasm_sc1(const void* p) {
  u32 v;
  asm volatile("global_load_dword %0, %1, off sc1" : "=v"(v) : "v"(p) : "memory");
  return v;
}
__device__ __forceinline__ u32x4 ldw(const bf16* p) { return *reinterpret_cast<const u32x4*>(p); }
template <int D2>
__device__ __forceinline__ s16x8 wfrag(u32 addr) {
  const s16x4 lo = __builtin_amdgcn_ds_read_tr16_b64_v4i16((lds_s4p)(uintptr_t)addr);
  const s16x4 hi = __builtin_amdgcn_ds_read_tr16_b64_v4i16((lds_s4p)(uintptr_t)(addr + D2));
  return s16x8{lo[0], lo[1], lo[2], lo[3], hi[0], hi[1], hi[2], hi[3]};
}
#define MFMA(w_, x_, c_) __builtin_amdgcn_mfma_f32_16x16x32_bf16(__builtin_bit_cast(bf16x8v, w_), __builtin_bit_cast(bf16x8v, x_), c_, 0, 0, 0)

struct Desc { const bf16* p; u32 bytes, flags; };
struct Args {
  Desc wq, x, wmod, c;
  unsigned char* ws;
  const bf16 *kc, *vc, *wo, *w1, *b1, *w2, *b2;
  bf16* out;
};

static_assert(offsetof(Args, ws) == 64 && offsetof(Args, out) == 128, "argument offsets");

constexpr int L_W = 0;
constexpr int L_WM2 = 40960;
constexpr int L_XA = 81920;
constexpr int XS = 1312, XH = 704;
constexpr int L_RED = L_XA + 16384;
constexpr int L_MOD = L_RED + 8192;
constexpr int L_XR = L_MOD + 5120;
constexpr int L_PT = L_XR + 5120;
constexpr int L_ACC = L_PT + 2048;
constexpr int L_QS = L_ACC + 4096;
constexpr int L_FI = L_QS + 256;
constexpr int L_ST = L_FI + 256;
constexpr int L_CNT = L_ST + 256;
constexpr int L_END = L_CNT + 128;
static_assert(L_END <= 160 * 1024, "LDS");
constexpr u32 C_Q = L_CNT, C_SC = L_CNT + 4, C_ISS = L_CNT + 8, C_P1 = L_CNT + 20, C_P2 = L_CNT + 24, C_GO = L_CNT + 28, C_W = L_CNT + 12, C_FIN = L_CNT + 32,
              C_KI = L_CNT + 36, C_PP = L_CNT + 40, C_PUB = L_CNT + 56, C_HT = L_CNT + 64;

constexpr int NL = 32;
constexpr int PVLAG = P_LAG;
constexpr int NPRE = P_NPRE;
constexpr int NVE = P_NVE;
static_assert(PVLAG <= 16 && (PVLAG & 1) == 0 && NVE <= PVLAG && (NVE & 1) == 0, "row groups");

__global__ void __launch_bounds__(NT, 2) mega_kernel(Args a) {
  __shared__ __attribute__((aligned(1024))) unsigned char lds[L_END];
  float* red = reinterpret_cast<float*>(lds + L_RED);
  u32* xraw = reinterpret_cast<u32*>(lds + L_XR);
  u32* pT = reinterpret_cast<u32*>(lds + L_PT);
  float* accs = reinterpret_cast<float*>(lds + L_ACC);
  u32* qs = reinterpret_cast<u32*>(lds + L_QS);
  float* st = reinterpret_cast<float*>(lds + L_ST);
  u32* cnt = reinterpret_cast<u32*>(lds + L_CNT);

  u32x4 rw, rx;
  unsigned char* wsq;
  const void* kcq;
  asm volatile("s_load_dwordx4 %0, %4, %5\n\ts_load_dwordx4 %1, %4, %5 offset:16\n\ts_load_dwordx2 %2, %4, 64\n\ts_load_dwordx2 %3, %4, 72"
               : "=&s"(rw), "=&s"(rx), "=&s"(wsq), "=&s"(kcq) : "s"(__builtin_amdgcn_kernarg_segment_ptr()), "s"((blockIdx.x & 1) * 32));
  __builtin_amdgcn_sched_barrier(0);
  const int t = threadIdx.x, lane = t & 63;
  const int wave = __builtin_amdgcn_readfirstlane(t >> 6);
  const int u = blockIdx.x;
  const int bh = u >> 1, half = u & 1, b_att = bh >> 5, h_att = bh & 31;
  const int r = lane >> 4, c = lane & 15;
  STAMP_DECL
  STAMPT(0, 0);
  u32 tg;
  const int g = u >> 1, ksp = g & 1, cbp = g >> 1;
  const int s128e = (((((lane & 7) >> 1) ^ ((lane >> 4) & 1)) << 1) | (lane & 1)) * 8;
  const u32 tr128 = (8 * r + (c >> 2)) * 128 + ((((c >> 3) & 1) | ((r & 1) << 1)) * 32) + (c & 3) * 8;
  if (wave < 4) {
    __builtin_amdgcn_s_setprio(3);
    const u32 rb = 8192 - 512 * half;
    const u32 hm = 0u - (u32)half;
    const u32 vo = ((u32)(lane >> 3) * rb + (((lane & 7) ^ ((lane >> 3) & 2)) ^ ((wave & 1) * 4)) * 16) | ((u32)(119 - g) & hm & OOB);
    u32 so = (u32)(ksp * 320 + 8 * wave) * rb + cbp * 128;
    const u32 step = 32 * rb, dstep = 4096, dst = L_W + wave * 1024;
    const u32 oe = wave * 1280 + lane * 16, oo = (r * 1280 + ksp * 640 + (c + 16 * wave) * 16) | ((u32)(39 - c - 16 * wave) & OOB);
    const u32 oa = (oo & hm) | (oe & ~hm), ob = (wave * 1280 + 1024 + c * 16) | (hm & OOB), oc = oe | (hm & OOB), zero = 0;
    u32 ep, keep;
    u32x4 xv[4];
    const u32 pre = ((u32)u << 31) & (u32)(g - 32) & (u32)(1 - wave), po = G_ATT + g * 2048 + (t & 127) * 16;
    const u32x4 emp = {EMPTY, EMPTY, EMPTY, EMPTY};
#define DMA1 "buffer_load_dwordx4 %7, %8, %1 offen nt lds\n\t"
#define DMAN "s_add_u32 m0, m0, %11\n\ts_add_u32 %1, %1, %10\n\t" DMA1
    asm volatile("s_mov_b32 %0, m0\n\ts_mov_b32 m0, %9\n\t"
                 "s_cmp_gt_i32 %18, 1\n\ts_waitcnt lgkmcnt(0)\n\ts_cbranch_scc1 1f\n\t"
                 "global_load_dword %2, %12, %13 sc1\n\t"
                 "buffer_load_dwordx4 %3, %14, %17, 0 offen\n\tbuffer_load_dwordx4 %4, %15, %17, 0 offen\n\t"
                 "buffer_load_dwordx4 %5, %16, %17, 0 offen offset:2560\n\tbuffer_load_dwordx4 %6, %15, %17, 0 offen offset:2560\n"
                 "1:\n\t" DMA1 DMAN DMAN DMAN DMAN DMAN DMAN DMAN DMAN DMAN "s_mov_b32 m0, %0\n\t"
                 "s_cmp_eq_u32 %19, 0\n\ts_cbranch_scc1 2f\n\tglobal_store_dwordx4 %20, %21, %13 sc1\n2:\n\t"
                 "s_barrier\n\ts_waitcnt vmcnt(10)"
                 : "=&s"(keep), "+s"(so), "=&v"(ep), "=&v"(xv[0]), "=&v"(xv[1]), "=&v"(xv[2]), "=&v"(xv[3])
                 : "v"(vo), "s"(rw), "s"(dst), "s"(step), "s"(dstep), "v"(zero), "s"(wsq), "v"(oa), "v"(ob), "v"(oc), "s"(rx), "s"(wave - half),
                   "s"(pre), "v"(po), "v"(emp) : "memory", "scc");
#undef DMAN
#undef DMA1
    asm volatile("" : "+s"(wsq));
    const __amdgpu_buffer_rsrc_t rs = __builtin_amdgcn_make_buffer_rsrc(wsq, 0, WS_END, 0x00020000);
    if (half) {
      if (wave < 3) {
        u32x4 sv;
#pragma unroll
        for (int k = 0; k < 4; ++k) {
          const float c0 = lo16(xv[0][k]), c1 = hi16(xv[0][k]);
          sv[k] = cvt2(c0 * frcp(1.f + fexp(-c0)), c1 * frcp(1.f + fexp(-c1)));
        }
        if (c + 16 * wave < 40) *reinterpret_cast<u32x4*>(lds + L_XA + r * XH + (c + 16 * wave) * 16) = sv;
        if (t == 0) cnt[4] = next_tag(ep);
        lds_arrive(C_P1, lane);
        STAMPT(2, 0);
      }
    } else {
      if (wave < 2) {
#pragma unroll
        for (int i = 0; i < 2; ++i) {
          const int b = wave + 2 * i;
          float xf[16];
#pragma unroll
          for (int k = 0; k < 4; ++k) { xf[2 * k] = lo16(xv[2 * i][k]); xf[2 * k + 1] = hi16(xv[2 * i][k]); xf[8 + 2 * k] = lo16(xv[2 * i + 1][k]); xf[8 + 2 * k + 1] = hi16(xv[2 * i + 1][k]); }
          float s = 0.f, s2 = 0.f;
#pragma unroll
          for (int k = 0; k < 8; ++k) { s += xf[k]; s2 += xf[8 + k]; }
          if (lane < 16) s += s2;
          const float mean = wave_sum(s) * (1.f / DM);
          float v = 0.f, v2 = 0.f;
#pragma unroll
          for (int k = 0; k < 8; ++k) { const float d = xf[k] - mean; v += d * d; const float d2 = xf[8 + k] - mean; v2 += d2 * d2; }
          if (lane < 16) v += v2;
          v = wave_sum(v);
          if (lane == 0) { st[24 + 2 * b] = bfr(mean); st[24 + 2 * b + 1] = frcp(bfr(fsqrt(bfr(bfr(v * (1.f / DM)) + 1e-5f)))); }
          *reinterpret_cast<u32x4*>(xraw + b * 320 + lane * 4) = xv[2 * i];
          if (lane < 16) *reinterpret_cast<u32x4*>(xraw + b * 320 + 256 + lane * 4) = xv[2 * i + 1];
        }
        if (t == 0) cnt[4] = next_tag(ep);
        lds_arrive(C_P1, lane);
      }
      lds_wait<1>(C_P1, 2);
      STAMPT(20, 0);
      tg = __builtin_amdgcn_readfirstlane(cnt[4]);
      if (CT0 && u < 8 && t == 192) { const u32 t0 = cnt[15]; st_gran(rs, G_T + u * 16, t0, t0, tg); }
      if (wave < 3) {
        const int b = LAY == 2 ? t & 3 : (t >> 2) & 3, j = LAY == 2 ? t >> 2 : (t >> 4) * 4 + (t & 3);
        u32 offs[8];
        u32x4 gm[8];
#pragma unroll
        for (int i = 0; i < 8; ++i) offs[i] = t < 160 ? G_MOD + GIDX(1024, i & 1, b, ((i >> 1) & 1) * 160 + (i >> 2) * 40 + ksp * 80 + j) : OOB;
        const float mean = st[24 + 2 * b], sd = st[24 + 2 * b + 1];
        float nv[8];
#pragma unroll
        for (int e = 0; e < 2; ++e) {
          const u32x2 xw = *reinterpret_cast<const u32x2*>(xraw + b * 320 + ksp * 160 + e * 80 + 2 * j);
#pragma unroll
          for (int k = 0; k < 4; ++k) {
            const float xf = (k & 1) ? hi16(xw[k >> 1]) : lo16(xw[k >> 1]);
            nv[4 * e + k] = bfr(bfr(xf - mean) * sd);
            asm volatile("" : "+v"(nv[4 * e + k]));
          }
        }
        {
          const u32 tt = __builtin_amdgcn_readfirstlane(cnt[15]) + GT;
          SPINLOOP while ((int)((u32)__builtin_amdgcn_s_memrealtime() - tt) < 0) __builtin_amdgcn_s_sleep(1);
        }
        STAMPT(14, 0);
        gather22<8, 4>(rs, offs, gm, tg, GOPOS == 2 ? C_GO : 0);
        STAMPT(8, 0);
#pragma unroll
        for (int e = 0; e < 2; ++e) {
          const f32x4 sa = un_g22(gm[4 * e]) + un_g22(gm[4 * e + 1]);
          const f32x4 ca = un_g22(gm[4 * e + 2]) + un_g22(gm[4 * e + 3]);
          const u32 s01 = cvt2(sa[0], sa[1]), s23 = cvt2(sa[2], sa[3]), c01 = cvt2(ca[0], ca[1]), c23 = cvt2(ca[2], ca[3]);
          const u32 d01 = cvt2(1.f + lo16(c01), 1.f + hi16(c01)), d23 = cvt2(1.f + lo16(c23), 1.f + hi16(c23));
          const u32 p01 = cvt2(nv[4 * e] * lo16(d01), nv[4 * e + 1] * hi16(d01)), p23 = cvt2(nv[4 * e + 2] * lo16(d23), nv[4 * e + 3] * hi16(d23));
          const float hv[4] = {lo16(p01) + lo16(s01), hi16(p01) + hi16(s01), lo16(p23) + lo16(s23), hi16(p23) + hi16(s23)};
          if (t < 160) *reinterpret_cast<u32x2*>(lds + L_XA + b * XH + (e * 40 + j) * 8) = u32x2{cvt2(hv[0], hv[1]), cvt2(hv[2], hv[3])};
        }
      }
    }
    VM0();
    STAMPT(5, 128);
    lds_arrive(C_P2, lane);
    lds_wait<1>(C_P2, 4);
    STAMPT(7, 0);
    tg = __builtin_amdgcn_readfirstlane(cnt[4]);
    {
      f32x4 acc = {0.f, 0.f, 0.f, 0.f}, acc2 = acc;
#pragma unroll
      for (int s = 0; s < 10; s += 2) {
        acc = MFMA(wfrag<512>(L_W + (tr128 ^ (wave * 32)) + s * 4096), *reinterpret_cast<const u32x4*>(lds + L_XA + (c & 3) * XH + s * 64 + r * 16), acc);
        acc2 = MFMA(wfrag<512>(L_W + (tr128 ^ (wave * 32)) + s * 4096 + 4096), *reinterpret_cast<const u32x4*>(lds + L_XA + (c & 3) * XH + s * 64 + 64 + r * 16), acc2);
      }
      if (c < 4 && u < 240 + 16 - 16 * half) st_g22(rs, G_Q + half * (G_MOD - G_Q) + GIDX(1024, ksp, c, cbp * 16 + wave * 4 + r), acc + acc2, tg);
    }
    if (CT0 && half && u < 8 && t == 192) { const u32 t0 = cnt[15]; st_gran(rs, G_T + u * 16, t0, t0, tg); }
    STAMPT(9, 0);
  } else {
    __builtin_amdgcn_s_setprio(2);
    if (t < 256 + 32) cnt[lane] = lane == 15 ? (u32)__builtin_amdgcn_s_memrealtime() : 0;
    const u32 ko = (u32)(bh * SK + (SK - WIN)) * 256u + (u32)(half * 8 + wave) * (NL * 1024u) + lane * 16;
    STAMPT(30, 448);
#define LK4(a, b, c, d, o) "global_load_dwordx4 " a ", %" #o ", %[k] nt\n\tglobal_load_dwordx4 " b ", %" #o ", %[k] offset:1024 nt\n\t" \
                           "global_load_dwordx4 " c ", %" #o ", %[k] offset:2048 nt\n\tglobal_load_dwordx4 " d ", %" #o ", %[k] offset:3072 nt\n\t"
    asm volatile("s_waitcnt lgkmcnt(0)\n\ts_barrier\n\t"
                 LK4("v[128:131]", "v[132:135]", "v[136:139]", "v[140:143]", 0) LK4("v[144:147]", "v[148:151]", "v[152:155]", "v[156:159]", 1)
                 LK4("v[160:163]", "v[164:167]", "v[168:171]", "v[172:175]", 2)
                 :: "v"(ko + ((u >> 3) & 7) * 4096), "v"(ko + (((u >> 3) + 1) & 7) * 4096),
                   "v"(ko + (((u >> 3) + 2) & 7) * 4096),
                   [k] "s"(kcq) : "memory", "v128", "v129", "v130", "v131", "v132", "v133", "v134", "v135", "v136", "v137", "v138", "v139", "v140", "v141", "v142", "v143", "v144", "v145", "v146", "v147", "v148", "v149", "v150", "v151", "v152", "v153", "v154", "v155", "v156", "v157", "v158", "v159", "v160", "v161", "v162", "v163", "v164", "v165", "v166", "v167", "v168", "v169", "v170", "v171", "v172", "v173", "v174", "v175", "v176", "v177", "v178", "v179", "v180", "v181", "v182", "v183", "v184", "v185", "v186", "v187", "v188", "v189", "v190", "v191");
#undef LK4
    STAMPT(31, 448);
  }
  u32 kvoff = (u32)(bh * SK + (SK - WIN)) * 256u + (u32)(half * 8 + wave) * (NL * 1024u) + lane * 16, ph[8];
#pragma unroll
  for (int i = 0; i < 8; ++i) ph[i] = ((i + (u >> 3)) & 7) * 4096;
  asm volatile("" : "+v"(kvoff));
  asm volatile("s_waitcnt lgkmcnt(0)" : "+s"(wsq), "+s"(ph[0]), "+s"(ph[1]), "+s"(ph[2]), "+s"(ph[3]), "+s"(ph[4]), "+s"(ph[5]), "+s"(ph[6]), "+s"(ph[7])
               : "s"(rw), "s"(rx), "s"(kcq));
  const __amdgpu_buffer_rsrc_t rs = __builtin_amdgcn_make_buffer_rsrc(wsq, 0, WS_END, 0x00020000);
  {
    u32x4 kv[NL], kh[16];
    const __amdgpu_buffer_rsrc_t rk = __builtin_amdgcn_make_buffer_rsrc((void*)a.kc, 0, 1u << 28, 0x00020000);
    const __amdgpu_buffer_rsrc_t rv = __builtin_amdgcn_make_buffer_rsrc((void*)a.vc, 0, 1u << 28, 0x00020000);
#define LDKV(rsrc, off, i) __builtin_amdgcn_raw_buffer_load_b128(rsrc, (off) + ((i) & 3) * 1024, ph[(i) >> 2], 2)
#define LDS_INC(addr) do { asm volatile("" ::: "memory"); if (lane == 0) asm volatile("ds_add_u32 %0, %1" :: "v"(addr), "v"(1u) : "memory"); } while (0)
#define TR_STEP(x, HALF, CTRL, BIT) _Pragma("unroll") for (int hh = 0; hh < (HALF); ++hh) { \
    const float lo_ = x[hh], hi_ = x[hh + (HALF)]; const float t1_ = lo_ + dpp<CTRL>(lo_), t2_ = hi_ + dpp<CTRL>(hi_); \
    x[hh] = (c & (BIT)) ? t2_ : t1_; }
#define SCORE16(x, src, o) float x[16]; \
    _Pragma("unroll") for (int i = 0; i < 16; ++i) { float d = 0.f; _Pragma("unroll") for (int k = 0; k < 4; ++k) d = dot2(src[(o) + i][k], qpk[k], d); x[i] = d; } \
    TR_STEP(x, 8, XOR8, 8) TR_STEP(x, 4, XOR7, 4) TR_STEP(x, 2, XOR2, 2)
#define SCALED(v) bfr(bfr(v) * 0.08838834764831845f)
#define PV2(i) _Pragma("unroll") for (int k = 0; k < 4; ++k) { \
      acc[2 * k] = dot2(__builtin_amdgcn_perm(kv[(i) + 1][k], kv[i][k], 0x05040100u), pw[(i) / 2], acc[2 * k]); \
      acc[2 * k + 1] = dot2(__builtin_amdgcn_perm(kv[(i) + 1][k], kv[i][k], 0x07060302u), pw[(i) / 2], acc[2 * k + 1]); }
#define GET_Q() lds_wait<2>(C_Q, 1); u32x4 qpk; \
    asm volatile("ds_read_b128 %0, %2\n\tds_read_b32 %1, %3\n\ts_waitcnt lgkmcnt(0)" : "=&v"(qpk), "=&v"(tg) : "v"((u32)(L_QS + 16 * c)), "v"((u32)(L_CNT + 16)) : "memory"); \
    tg = __builtin_amdgcn_readfirstlane(tg);
#define GET_P() _Pragma("unroll") for (int i4 = 0; i4 < 4; ++i4) { \
      const u32x4 pv = *reinterpret_cast<const u32x4*>(pT + (wave * 4 + r) * 16 + i4 * 4); \
      _Pragma("unroll") for (int j = 0; j < 4; ++j) pw[i4 * 4 + j] = pv[j]; }
    auto gather_q = [&]() __attribute__((always_inline)) {
        u32 offs[1] = {G_Q + GIDX(1024, lane >> 5, b_att, h_att * 32 + (lane & 31))};
        u32x4 gq[1];
        {
          gq[0] = ld_gran(rs, offs[0]);
          const u32x4 gt = ld_gran(rs, lane < 8 ? G_T + lane * 16 : OOB);
          LDS_INC(C_KI);
          {
            const u32 own = cnt[15];
            const bool ok = lane < 8 && gt[1] == tg;
            float d = ok ? __builtin_amdgcn_fmed3f((float)(int)(gt[0] - own + TOFF), -400.f, 400.f) : 0.f;
            d += dpp<XOR1>(d); d += dpp<XOR2>(d); d += dpp<XOR7>(d);
            const int n = __builtin_popcountll(__ballot(ok));
            if (lane == 0 && n) cnt[15] = own + (u32)(int)(d * frcp((float)n));
          }
          const bool hit = (((gq[0][1] ^ (tg << 12)) | (gq[0][3] ^ (tg << 12))) < 4096u);
          if (!__all(hit)) gather22<1, 4>(rs, offs, gq, tg);
        }
        STAMPW(10);
        f32x4 q4 = un_g22(gq[0]);
#pragma unroll
        for (int k = 0; k < 4; ++k) q4[k] = swap32_add(q4[k], q4[k]);
        if (lane < 32) *reinterpret_cast<u32x2*>(qs + 2 * lane) = u32x2{cvt2(q4[0], q4[1]), cvt2(q4[2], q4[3])};
        lds_arrive(C_Q, lane);
    };
    const int qw = half ? QODD : QEVEN;
    const u32 tau = half ? (g < 74 ? TAU_A : TAU_B) : (g >= 122 ? TAU_X : TAU_E);
#define PACE(n) if ((n) > PB) { const u32 tt_ = t0 + (((n) - PB) * tau >> 4); \
      SPINLOOP while ((int)((u32)__builtin_amdgcn_s_memrealtime() - tt_) < 0) __builtin_amdgcn_s_sleep(1); asm volatile("" ::: "memory"); }
    float acc[8];
#pragma unroll
    for (int k = 0; k < 8; ++k) acc[k] = 0.f;
    u32 pw[16];
    if (__builtin_expect(wave >= 4, 1)) {
      asm volatile("" : "={v[128:131]}"(kv[0]), "={v[132:135]}"(kv[1]), "={v[136:139]}"(kv[2]), "={v[140:143]}"(kv[3]),
                        "={v[144:147]}"(kv[4]), "={v[148:151]}"(kv[5]), "={v[152:155]}"(kv[6]), "={v[156:159]}"(kv[7])
                        , "={v[160:163]}"(kv[8]), "={v[164:167]}"(kv[9]), "={v[168:171]}"(kv[10]), "={v[172:175]}"(kv[11])
                   );
      u32 t0 = 0;
      STAMPT(24, 448);
      const u32 kvoff2 = kvoff ^ 131072u;
#pragma unroll
      for (int i = NK0; i < 48; ++i) {
        if (i == NPRE) { STAMPT(12, 448); STAMPT(13, 256); }
        if (i == NPRE && half == 0) lds_wait<2>(C_GO, GOPOS ? 3 : 4);
        if (PK && (i & 3) == 0) PACE(i)
        if (i < NL) kv[i < NL ? i : 0] = LDKV(rk, kvoff, i & 31); else kh[i < NL ? 0 : i - NL] = LDKV(rk, kvoff2, i & 31);
      }
      if (!QPRE || wave != qw) LDS_INC(C_KI);
#pragma unroll
      for (int i = 0; i < NK0; i += 4) asm volatile("s_waitcnt vmcnt(%4)" : "+v"(kv[i]), "+v"(kv[i + 1]), "+v"(kv[i + 2]), "+v"(kv[i + 3]) : "i"(48 - NK0) : "memory");
      STAMPT(25, 448);
      if ((QODD >= 4 || QEVEN >= 4) && wave == qw) {
        __builtin_amdgcn_s_setprio(3);
        lds_wait<2>(C_P1, 2 + half);
        tg = __builtin_amdgcn_readfirstlane(cnt[4]);
        gather_q();
        __builtin_amdgcn_s_setprio(2);
      }
      GET_Q()
      SCORE16(xa, kv, 0)
      asm volatile("" : "+v"(xa[0]), "+v"(xa[1]), "+v"(kvoff) :: "memory");
#pragma unroll
      for (int i = 0; i < P_NVA; ++i) kv[i] = LDKV(rv, kvoff, i);
      asm volatile("" ::: "memory");
      SCORE16(xb, kv, 16)
      xa[2] = xb[0]; xa[3] = xb[1];
      TR_STEP(xa, 2, XOR1, 1)
      float s0 = SCALED(xa[0]), s1 = SCALED(xa[1]);
      asm volatile("" : "+v"(s0), "+v"(s1), "+v"(kvoff) :: "memory");
#pragma unroll
      for (int i = P_NVA; i < NVE; ++i) kv[i] = LDKV(rv, kvoff, i);
      asm volatile("" ::: "memory");
      SCORE16(xh, kh, 0)
      const float s2 = SCALED(xh[0] + dpp<XOR1>(xh[0])), s3 = SCALED(xh[1] + dpp<XOR1>(xh[1]));
      const float m = wave_max(fmaxf(fmaxf(s0, s1), fmaxf(s2, s3)));
      const float p0 = __expf(s0 - m), p1 = __expf(s1 - m), p2 = __expf(s2 - m), p3 = __expf(s3 - m);
      const float l = wave_sum(fmaf(p2 + p3, 0.5f, p0 + p1));
      if (lane == 0) { st[wave] = m; st[8 + wave] = l; }
      pT[(wave * 4 + r) * 16 + (c & 1) * 8 + (c >> 1)] = cvt2(p0, p1);
      pT[((wave - 4) * 4 + r) * 16 + (c >> 1)] = cvt2(p2, p3);
      lds_arrive(C_PP + 4 * (wave - 4), lane);
      __builtin_amdgcn_s_setprio(0);
      STAMPT(27, 448); WSTAMP(1);
      if (CT0) t0 = __builtin_amdgcn_readfirstlane(cnt[15]);
      GET_P()
#pragma unroll
      for (int i = NVE; i < NL; i += 2) {
        PACE(96 + i)
        kv[i] = LDKV(rv, kvoff, i);
        kv[i + 1] = LDKV(rv, kvoff, i + 1);
        asm volatile("" ::: "memory");
        if (i == NL - 2) { LDS_INC(C_ISS); STAMPT(26, 448); }
        if (i >= PVLAG) {
          PV2(i >= PVLAG ? i - PVLAG : 0)
#pragma unroll
          for (int k = 0; k < 8; ++k) asm volatile("" : "+v"(acc[k]));
        }
      }
    } else {
      const int ex = g < 74 ? EA : g < 122 ? EB : EC;
      u32 kva = (!half && ex > 2) ? OOB : kvoff, kvb = (!half && ex > 0) ? OOB : kvoff;
      const u32 kxa = (half && ex > 2) ? kvoff - 8 * NL * 1024u : OOB, kxb = (half && ex > 0) ? kvoff - 8 * NL * 1024u : OOB;
      u32x4 kx[4];
      const u32 t0 = __builtin_amdgcn_readfirstlane(cnt[15]);
#pragma unroll
      for (int i = 0; i < 16; ++i) {
        if (PK && (i & 3) == 0) PACE(48 + i)
        kh[i] = LDKV(rk, i < 12 ? kvoff : i < 14 ? kva : kvb, 16 + i);
      }
#pragma unroll
      for (int i = 0; i < 4; ++i) kx[i] = LDKV(rk, i < 2 ? kxa : kxb, 28 + i);
      LDS_INC(C_KI);
      if (wave == 0) {
        if (half == 0 && g >= 80 && g < 120) {
          const int fb = g - 80;
          u32 of2[2];
          u32x4 gm[2];
#pragma unroll
          for (int ks = 0; ks < 2; ++ks) of2[ks] = G_MOD + GIDX(1024, ks, r, 320 + fb * 16 + c);
          gather22<2, 4>(rs, of2, gm, tg);
          const f32x4 mv = un_g22(gm[0]) + un_g22(gm[1]);
          st_gran(rs, G_MF + (((fb / 10) * 4 + r) * 160 + (fb % 10) * 16 + c) * 16, cvt2(mv[0], mv[1]), cvt2(mv[2], mv[3]), tg);
        }
      }
      __builtin_amdgcn_s_setprio(0);
      lds_wait<2>(C_KI, P_GATE);
      if (QHOLD && wave == qw - 4) lds_wait<2>(C_Q, 1);
      asm volatile("" : "+v"(kvoff), "+v"(kva), "+v"(kvb));
#pragma unroll
      for (int i = 0; i < NL; ++i) {
        if (PL && (i & 3) == 0) PACE(64 + PLO + i * PLS / 16)
        kv[i] = LDKV(rv, i < 28 ? kvoff : i < 30 ? kva : kvb, i);
      }
      STAMPT(1, 0);
      GET_Q()
      float xx[4];
#pragma unroll
      for (int i = 0; i < 4; ++i) { float d = 0.f;
#pragma unroll
        for (int k = 0; k < 4; ++k) d = dot2(kx[i][k], qpk[k], d);
        xx[i] = d; }
      asm volatile("" : "+v"(xx[0]), "+v"(xx[1]), "+v"(xx[2]), "+v"(xx[3]) :: "memory");
#pragma unroll
      for (int i = 0; i < 4; ++i) kx[i] = LDKV(rv, i < 2 ? kxa : kxb, 28 + i);
      LDS_INC(C_ISS);
      TR_STEP(xx, 2, XOR8, 8)
#pragma unroll
      for (int i = 0; i < 2; ++i) { xx[i] += dpp<XOR7>(xx[i]); xx[i] += dpp<XOR2>(xx[i]); xx[i] += dpp<XOR1>(xx[i]); }
      SCORE16(xh, kh, 0)
      const float ninf = -__builtin_inff();
      const bool mine = half || c < 16 - ex, extra = half && c >= 16 - 4 * ex;
      xh[0] += dpp<XOR1>(xh[0]); xh[1] += dpp<XOR1>(xh[1]);
      const float s2 = mine ? SCALED(xh[0]) : ninf, s3 = mine ? SCALED(xh[1]) : ninf;
      const float s4 = extra ? SCALED(xx[0]) : ninf, s5 = extra ? SCALED(xx[1]) : ninf;
      float m = wave_max(fmaxf(fmaxf(s2, s3), fmaxf(s4, s5)));
      lds_wait<2>(C_PP + 4 * wave, 1);
      const float me = st[wave + 4];
      m = fmaxf(m, me);
      const float p2 = __expf(s2 - m), p3 = __expf(s3 - m), p4 = __expf(s4 - m), p5 = __expf(s5 - m);
      const float l = wave_sum(fmaf(p4 + p5, 0.25f, p2 + p3)) * 0.5f;
      const u32 pxo = cvt2(p4, p5), pxt = dppu<XOR8>(pxo);
      const u32 px[2] = {c < 8 ? pxo : pxt, c < 8 ? pxt : pxo};
      if (lane == 0) { st[wave] = m; st[8 + wave] = l; }
      pT[(wave * 4 + r) * 16 + 8 + (c >> 1)] = cvt2(p2, p3);
      STAMPT(4, 0); WSTAMP(1);
      if (lds_ticket(C_SC, lane) == 3) {
        const float mw = st[lane & 7], lw = st[8 + (lane & 7)];
        float M = fmaxf(mw, dpp<XOR1>(mw)); M = fmaxf(M, dpp<XOR2>(M)); M = fmaxf(M, dpp<XOR7>(M));
        const float e = __expf(mw - M);
        float L = lw * e;
        L += dpp<XOR1>(L); L += dpp<XOR2>(L); L += dpp<XOR7>(L);
        if (lane == 0) st_gran(rs, G_ML + u * 64, f2u(M), f2u(L), tg);
        if (lane < 8) st[16 + lane] = e * frcp(L);
      }
      GET_P()
#pragma unroll
      for (int i = 0; i < 16; i += 2) { PV2(i) }
      const float f = __expf(me - m);
#pragma unroll
      for (int k = 0; k < 8; ++k) acc[k] *= f;
#pragma unroll
      for (int i = 16; i < NL - PVLAG; i += 2) { PV2(i) }
#pragma unroll
      for (int i = 0; i < 4; i += 2)
#pragma unroll
        for (int k = 0; k < 4; ++k) {
          acc[2 * k] = dot2(__builtin_amdgcn_perm(kx[i + 1][k], kx[i][k], 0x05040100u), px[i / 2], acc[2 * k]);
          acc[2 * k + 1] = dot2(__builtin_amdgcn_perm(kx[i + 1][k], kx[i][k], 0x07060302u), px[i / 2], acc[2 * k + 1]);
        }
    }
#pragma unroll
    for (int i = NL - PVLAG; i <= NL - HX; i += 2) { PV2(i) }
    if (!HT) { PV2(NL - HX + 2) } else {
      u32 tk = 0;
      u64 tn;
      asm volatile("s_mov_b64 exec, 1\n\tds_add_rtn_u32 %0, %10, %11\n\ts_mov_b64 exec, -1\n\ts_memrealtime %1"
                   : "+v"(tk), "=s"(tn), "+v"(acc[0]), "+v"(acc[1]), "+v"(acc[2]), "+v"(acc[3]), "+v"(acc[4]), "+v"(acc[5]), "+v"(acc[6]), "+v"(acc[7])
                   : "v"((u32)C_HT), "v"(1u) : "memory");
      PV2(NL - HX + 2)
      asm volatile("s_waitcnt lgkmcnt(0)" : "+v"(tk), "+s"(tn), "+v"(acc[0]), "+v"(acc[1]), "+v"(acc[2]), "+v"(acc[3]), "+v"(acc[4]), "+v"(acc[5]), "+v"(acc[6]), "+v"(acc[7]));
      __builtin_amdgcn_raw_buffer_store_b64(u32x2{(u32)tn + HTD, tg}, rs, tk == 7 ? G_HT + u * 128 : OOB, 0, 16);
      asm volatile("" ::: "memory");
    }
#pragma unroll
    for (int i = NL - HX + 4; i < NL; i += 2) { PV2(i) }
#undef PV2
#undef TR_STEP
#undef SCORE16
#undef SCALED
#undef GET_Q
#undef GET_P
#undef LDKV
#pragma unroll
    for (int i = 0; i < 4; ++i) acc[i] = swap32_add(acc[i], acc[i + 4]);
#pragma unroll
    for (int i = 0; i < 2; ++i) acc[i] = swap16_add(acc[i], acc[i + 2]);
    *reinterpret_cast<float2*>(accs + wave * 128 + c * 8 + 2 * r) = float2{acc[0], acc[1]};
    STAMPT(6, 0); STAMPT(28, 448); WSTAMP(2);
  }
  const u32x4 rsv = {(u32)(uintptr_t)wsq, (u32)((uintptr_t)wsq >> 32) & 0xffffu, WS_END, 0x00020000u};
  const int role = half ? (g < 74 ? 0 : 1) : (g >= 122 ? 0 : g < 40 ? 2 : 3);
  const int ia = half ? g : g - 48;
  const int ksA = ia & 7, cbA = ia >> 3;
#define HEADA(hh) ((ksA & 3) + (ksA >> 2) * 16 + (hh) * 4)
  const int iB = g - 74, jB = !JMAP ? iB : iB < 48 ? (iB & ~7) | ((iB & 3) << 1) | ((iB >> 2) & 1) : iB < 50 ? 2 * iB - 48 : iB < 52 ? iB + 2 : 2 * iB - 55;
  u32x4 ml = {0u, 0u, 0u, 0u};
  const u32 blkA = ((c & 3) * 32 + HEADA(wave & 3)) * 2 + ((c >> 2) & 1);
  u32 om = G_ML + blkA * 64;
#define MLOOK(W_) asm volatile(LK(0, 1, "0", 0, 2) LKEND : "+v"(ml) : "v"(om), "s"(m2), [rs] "s"(rsv), [w] "i"(W_) : "memory");
  u64 m2 = role == 0 && wave >= 4 ? 0xffull : 0;
  const u32 tkf = lds_ticket(C_FIN, lane);
  const bool lastw = tkf == 7;
  if (!lastw) {
    MLOOK(63)
#define GO(n, name) "s_cmp_eq_u32 %0, " #n "\n\ts_cbranch_scc1 .Lpad_" #name "\n\t"
    asm volatile("s_mov_b64 exec, 0\n\t" GO(0, a3) GO(1, a2) GO(2, a1) GO(3, a0) GO(8, b3) GO(9, b2) GO(10, b1) GO(11, b0) GO(16, c1) GO(17, c0)
                 ".Lpad_back:\n\ts_mov_b64 exec, -1" :: "s"(WARM ? role * 8 + tkf : 7) : "scc");
#undef GO
  }
  if (lastw) {
    if (lane < 32) {
      f32x4 o = {0.f, 0.f, 0.f, 0.f};
#pragma unroll
      for (int w = 0; w < 8; ++w) o += st[16 + w] * *reinterpret_cast<const f32x4*>(accs + w * 128 + 4 * lane);
      const u32x2 ow = {min(cvt2(o[0], o[1]), EMPTY - 1), min(cvt2(o[2], o[3]), EMPTY - 1)};
      __builtin_amdgcn_raw_buffer_store_b64(ow, rs, G_ATT + u * 256 + lane * 8, 0, 16);
    }
    MLOOK(63)
    LDS_INC(C_PUB);
    STAMPW(11);
  }
  u32 gaw[2];
  u32x2 xw = {0u, 0u};
  float b1v;
  if (role < 2 && wave < 4) {
    if (role == 0) {
      u32 offs[1] = {G_MF + (r * 160 + cbA * 16 + c) * 16};
      u32x4 gm[1];
      if (ksA == 0) xw = *reinterpret_cast<const u32x2*>(a.x.p + r * DM + cbA * 64 + c * 4);
      gather<1>(rs, offs, gm, tg);
      gaw[0] = gm[0][0]; gaw[1] = gm[0][2];
    } else {
      if (MFPOS == 1) lds_wait<2>(C_ISS, 8);
      b1v = bf2f(a.b1[jB * 32 + (t & 31)]);
      u32 offs[5];
      u32x4 gm[5];
#pragma unroll
      for (int i = 0; i < 5; ++i) offs[i] = G_MF + (640 + t + 256 * i) * 16;
      gather<5>(rs, offs, gm, tg);
#pragma unroll
      for (int i = 0; i < 5; ++i) {
        const int it = t + 256 * i, row = it / 160, gi = it % 160;
        u32 w0 = gm[i][0], w1 = gm[i][2];
        if (row >= 4) { w0 = cvt2(1.f + lo16(w0), 1.f + hi16(w0)); w1 = cvt2(1.f + lo16(w1), 1.f + hi16(w1)); }
        *reinterpret_cast<u32x2*>(lds + L_XA + (row < 4 ? row + 8 : row) * XS + gi * 8) = u32x2{w0, w1};
      }
      asm volatile("" : "+v"(b1v));
    }
  }
  if (role < 2 && wave < 4) {
    if (half) STAMPT(8, 0);
    lds_wait<2>(C_ISS, 8);
    if (role == 0) {
      const bf16* wp = a.wo + (size_t)(HEADA(wave) * 128 + (lane >> 3)) * DM + cbA * 64;
#pragma unroll
      for (int i = 0; i < 16; ++i) {
        if (i == 2) PAD(a0, back);
        if (i == N1A) {
          lds_wait<2>(C_PUB, 1);
          __builtin_amdgcn_raw_buffer_store_b128(u32x4{EMPTY, EMPTY, EMPTY, EMPTY}, rs,
              wave < NREP && lane < 32 ? G_WO + wave * WO_REP + ((cbA * 4 + (lane >> 3)) * 8 + ksA) * 128 + (lane & 7) * 16 : OOB, 0, 16);
          asm volatile("" ::: "memory");
        }
        dma16(wp + (size_t)(8 * i) * DM + (s128e ^ ((i & 1) * 32)), L_W + wave * 16384 + i * 1024);
      }
      LDS_INC(C_ISS);
    } else {
      const bf16* wp = a.w1 + (size_t)(wave * 160 + (lane >> 2)) * FF + jB * 32 + ((((((lane >> 1) & 1) ^ (lane >> 5)) << 1) | (lane & 1)) * 8);
#pragma unroll
      for (int i = 0; i < 10; ++i) {
        if (i == 2) PAD(bd, back);
        if (i == N1B) lds_wait<2>(C_PUB, 1);
        dma16(wp + (size_t)(16 * i) * FF, L_W + (wave * 10 + i) * 1024);
      }
      const bf16* wp2 = a.w2 + (size_t)(jB * 32 + wave * 8 + (lane >> 3)) * DM + (s128e ^ ((wave & 1) * 32));
#pragma unroll
      for (int k = 0; k < 10; ++k) {
        if (k + 10 == N1B) lds_wait<2>(C_PUB, 1);
        dma16(wp2 + k * 64, L_WM2 + (k * 4 + wave) * 1024);
      }
    }
    STAMPT(16, 0);
  }
#define SELECTOR(sel) u32x4 sel; \
    _Pragma("unroll") for (int d = 0; d < 4; ++d) sel[d] = ((c & 2) == 0 && d == (c >> 2)) ? ((c & 1) ? 0x3f800000u : 0x00003f80u) : 0u;
#define PAIRSUM(sm) (((c & 8) ? sm[1] : sm[0]) + dpp<XOR8>((c & 8) ? sm[0] : sm[1]))
  const u32 tb = __builtin_amdgcn_readfirstlane(cnt[15]);
  if (role == 0) {
    PAD(a1, back);
    u32 a_x = L_XA + ((wave & 3) * 8 + (c & 7)) * 272 + r * 16;
    if (wave < 4) {
      PAD(a2, back);
      float ga4[4] = {lo16(gaw[0]), hi16(gaw[0]), lo16(gaw[1]), hi16(gaw[1])}, x4[4] = {lo16(xw[0]), hi16(xw[0]), lo16(xw[1]), hi16(xw[1])};
      u32 a_red = L_RED + ((wave * 4 + c) * 64 + r * 4) * 4, a_sum = L_RED + lane * 16, a_sc = L_ST + (32 + wave * 8 + (c & 7)) * 4;
      u32 a_out = wave < NREP ? G_WO + wave * WO_REP + ((cbA * 4 + (c >> 2)) * 8 + ksA) * 128 + r * 32 + (c & 3) * 8 : OOB;
      u32 a_w[4];
#pragma unroll
      for (int nt = 0; nt < 4; ++nt) a_w[nt] = L_W + wave * 16384 + (tr128 ^ (nt * 32));
      asm volatile("" : "+v"(ga4[0]), "+v"(ga4[1]), "+v"(ga4[2]), "+v"(ga4[3]), "+v"(x4[0]), "+v"(x4[1]), "+v"(x4[2]), "+v"(x4[3]));
      asm volatile("s_waitcnt vmcnt(%0)" :: "i"(AW) : "memory");
      LDS_INC(C_PP + 4 * wave);
      asm volatile("s_waitcnt vmcnt(4)" : "+v"(a_red), "+v"(a_sum), "+v"(a_sc), "+v"(a_out), "+v"(a_x), "+v"(a_w[0]), "+v"(a_w[1]), "+v"(a_w[2]), "+v"(a_w[3]) :: "memory");
      s16x8 wf[4][4];
#pragma unroll
      for (int s = 0; s < 3; ++s)
#pragma unroll
        for (int nt = 0; nt < 4; ++nt) wf[s][nt] = wfrag<512>(a_w[nt] + s * 4096);
      announce(rs, G_HA + ia * 128, C_GO, C_W, 8, tb, HAW, tg);
      STAMPT(19, 0);
      lds_wait<1>(C_PP + 4 * wave, 3);
      STAMPT(12, 0);
      PAD(a3, back);
      u32x4 av[4];
#pragma unroll
      for (int s = 0; s < 4; ++s) av[s] = *reinterpret_cast<const u32x4*>(lds + a_x + s * 64);
      const float wsc = *reinterpret_cast<const float*>(lds + a_sc);
      asm volatile("s_waitcnt vmcnt(1)" ::: "memory");
      STAMPT(23, 0);
#pragma unroll
      for (int nt = 0; nt < 4; ++nt) wf[3][nt] = wfrag<512>(a_w[nt] + 3 * 4096);
      __builtin_amdgcn_sched_barrier(0);
      f32x4 acc[4];
#pragma unroll
      for (int nt = 0; nt < 4; ++nt) acc[nt] = f32x4{0.f, 0.f, 0.f, 0.f};
#pragma unroll
      for (int s = 0; s < 4; ++s)
#pragma unroll
        for (int nt = 0; nt < 4; ++nt) acc[nt] = MFMA(wf[s][nt], av[s], acc[nt]);
#pragma unroll
      for (int nt = 0; nt < 4; ++nt) { acc[nt] *= wsc; asm("" : "+v"(acc[nt])); }
#pragma unroll
      for (int nt = 0; nt < 4; ++nt)
#pragma unroll
        for (int k = 0; k < 4; ++k) { float v = acc[nt][k] + dpp<SHL4>(acc[nt][k]); asm("" : "+v"(v)); acc[nt][k] = v; }
      asm volatile("" : "+v"(acc[0]), "+v"(acc[1]), "+v"(acc[2]), "+v"(acc[3]));
      if (c < 4) {
#pragma unroll
        for (int nt = 0; nt < 4; ++nt) *reinterpret_cast<f32x4*>(lds + a_red + nt * 64) = acc[nt];
      }
      BAR();
      STAMPT(21, 0);
      f32x4 pp[4];
#pragma unroll
      for (int w = 0; w < 4; ++w) pp[w] = *reinterpret_cast<const f32x4*>(lds + a_sum + w * 1024);
      asm volatile("" : "+v"(pp[0]), "+v"(pp[1]), "+v"(pp[2]), "+v"(pp[3]));
      const f32x4 s = (pp[0] + pp[1]) + (pp[2] + pp[3]);
      const u32x2 o = {min(cvt2(fmaf(s[0], ga4[0], x4[0]), fmaf(s[1], ga4[1], x4[1])), EMPTY - 1), min(cvt2(fmaf(s[2], ga4[2], x4[2]), fmaf(s[3], ga4[3], x4[3])), EMPTY - 1)};
      __builtin_amdgcn_raw_buffer_store_b64(o, rs, a_out, 0, 16);
    } else {
      u32 oa = G_ATT + blkA * 256 + (c >> 3) * 64 + r * 16;
      a_x += (c >> 3) * 64;
      u32x4 av[2];
      u64 m0 = ~0ull, m1 = ~0ull;
      av[0] = av[1] = u32x4{0u, 0u, 0u, 0u};
      asm volatile("s_waitcnt vmcnt(0)" : "+v"(oa), "+v"(a_x), "+v"(ml), "+v"(av[0]), "+v"(av[1]) :: "memory");
      SPINLOOP for (unsigned spins = 0; spins < SPIN_CAP; ++spins) {
        m2 &= __builtin_amdgcn_ballot_w64(ml[1] != tg) | __builtin_amdgcn_ballot_w64(ml[3] != tg);
        if (!m2) break;
        if (!spins) FAIL(4);
        __builtin_amdgcn_s_sleep(ASL);
        MLOOK(0)
      }
      {
        const float M = u2f(ml[0]), L = u2f(ml[2]), Mo = dpp<XOR7>(dpp<0x1B>(M)), Lo = dpp<XOR7>(dpp<0x1B>(L));
        const float Mx = fmaxf(M, Mo), le = L * __expf(M - Mx);
        const float wsc = le * frcp(fmaf(Lo, __expf(Mo - Mx), le));
        if (lane < 8) st[wave * 8 + lane] = wsc;
      }
#define ALOOK(W_) \
        asm volatile(LK(0, 2, "0", 0, 3) LK(1, 2, "0", 128, 4) LKEND \
                     : "+v"(av[0]), "+v"(av[1]) : "v"(oa), "s"(m0), "s"(m1), [rs] "s"(rsv), [w] "i"(W_) : "memory"); \
        m0 = __builtin_amdgcn_ballot_w64(missing(av[0])); \
        m1 = __builtin_amdgcn_ballot_w64(missing(av[1]));
      lds_wait<1>(C_PP + 4 * (wave - 4), 2);
      STAMPT(29, 448);
      ALOOK(0)
      STAMPT(18, 448);
      if (m0 | m1) {
        FAIL(1);
        SPINLOOP for (unsigned spins = 0; spins < SPIN_CAP; ++spins) {
          ALOOK(2 * (PDA - 1))
          if (!(m0 | m1)) break;
          __builtin_amdgcn_s_sleep(ASL);
        }
      }
#undef ALOOK
      asm volatile("" : "+v"(av[0]), "+v"(av[1]));
      *reinterpret_cast<u32x4*>(lds + a_x) = av[0];
      *reinterpret_cast<u32x4*>(lds + a_x + 128) = av[1];
      LDS_INC(C_PP + 4 * (wave - 4));
      STAMPT(22, 448);
      announce(rs, G_HA + ia * 128, C_GO, C_W, 8, tb, HAG, tg);
      BAR();
      asm volatile("" :: "v"(av[0]), "v"(av[1]));
    }
    STAMPT(13, 0);

  } else if (role == 1) {
    PAD(b0, bd);
    const int lb = (c >> 1) & 3, lc = (c & 1) * 8 + 2 * r;
    const int fc = lane & 31, hw = lane >> 5;
    SELECTOR(sel)
    constexpr int L_R2 = L_MOD, L_S2 = L_R2 + 4096;
    const u32 gw = G_WO + (jB % NREP) * WO_REP + wave * 5120;
    u32 go = gw + (c >> 3) * 1024 + r * 128 + (c & 7) * 16, go4 = gw + 4096 + lane * 16;
    u32 gl = G_HA + (((5 * wave + (lane & 8 ? 4 : 0)) >> 2) * 8 + (lane & 7)) * 128;
    u32 a_u = L_XA + wave * 640 + lb * 160 + ((c >> 3) * 16 + lc) * 2, a_u4 = L_XA + wave * 640 + lb * 160 + (64 + lc + (c >> 3)) * 2;
    u32 a_av = L_XA + wave * 640 + (c & 3) * 160 + r * 16, a_ah = a_av + 128 - r * 8;
    u32 a_red = L_R2 + ((wave * 4 + c) * 32 + r * 4) * 4;
    u32 a_st = L_S2 + ((lb * 2 + (c >> 3)) * 8 + wave) * 4;
    u32 a_fin = L_R2 + ((hw * 16 + (wave & 3)) * 32 + fc) * 4;
    u32 a_fst = L_S2 + (wave & 3) * 64;
    u32 a_out = c < 4 ? G_OUT + (wave * 5 * 54 + jB) * 128 + c * 32 + r * 8 : OOB;
    constexpr int L_F2 = L_PT;
    u32 a_fi = BM ? L_F2 + c * 64 + r * 16 : L_FI + (c & 3) * 64 + r * 16, a_fo = (BM ? L_F2 + 768 : L_FI) + ((wave & 3) * 32 + fc) * 2;
    u32 a_ou4 = G_OUT + ((wave * 5 + (c >> 2)) * 54 + jB) * 128 + (c & 3) * 32 + r * 8;
    asm volatile("" : "+v"(a_ou4));
    u32 a_pre = lane < 40 ? G_OUT + ((wave * 5 + (lane >> 3)) * 54 + jB) * 128 + (lane & 7) * 16 : OOB;
    u32x4 emp = {EMPTY, EMPTY, EMPTY, EMPTY};
    asm volatile("" : "+v"(a_pre), "+v"(emp));
#define OPRE() asm volatile("buffer_store_dwordx4 %0, %1, %2, 0 offen sc1" :: "v"(emp), "v"(a_pre), "s"(rsv) : "memory")
    if (OPOS == 0) OPRE();
    u32 a_w1 = L_W + wave * 5120 + (8 * r + (c >> 2)) * 64 + (r & 1) * 32 + (c & 3) * 8;
    u32 a_wh = L_W + wave * 5120 + 4096 + (4 * r + (c >> 2)) * 64 + (r >> 1) * 32 + (c & 3) * 8;
    u32 a_w2[5];
#pragma unroll
    for (int k = 0; k < 5; ++k) { const int n2 = wave * 5 + k; a_w2[k] = L_WM2 + (n2 >> 2) * 4096 + (tr128 ^ ((n2 & 3) * 32)); }
    asm volatile("" : "+v"(sel), "+v"(go), "+v"(go4), "+v"(gl), "+v"(a_u), "+v"(a_u4), "+v"(a_av), "+v"(a_ah), "+v"(a_red), "+v"(a_st), "+v"(a_fin), "+v"(a_fst), "+v"(a_out));
    asm volatile("s_waitcnt vmcnt(%9)" : "+v"(a_fi), "+v"(a_fo), "+v"(a_w1), "+v"(a_wh), "+v"(a_w2[0]), "+v"(a_w2[1]), "+v"(a_w2[2]), "+v"(a_w2[3]), "+v"(a_w2[4]) : "i"(OPOS == 0 ? 11 : 10) : "memory");
    STAMPT(23, 0);
    PAD(b1, back);
    BAR();
    STAMPT(12, 0);
    if (OPOS == 1) OPRE();
    if (BM) *reinterpret_cast<u32*>(lds + L_F2 + t * 4) = 0;
    s16x8 wf[2][2], wg[5];
    s16x4 wh[2];
#pragma unroll
    for (int nt = 0; nt < 2; ++nt) {
#pragma unroll
      for (int s = 0; s < 2; ++s) wf[s][nt] = wfrag<256>((a_w1 ^ (nt * 32)) + s * 2048);
      wh[nt] = __builtin_amdgcn_ds_read_tr16_b64_v4i16((lds_s4p)(uintptr_t)(a_wh ^ (nt * 32)));
    }
    float c1f[5];
    {
      const unsigned char* cp = lds + L_XA + (4 + lb) * XS + wave * 160;
#pragma unroll
      for (int i = 0; i < 2; ++i) { const u32 w = *reinterpret_cast<const u32*>(cp + (32 * i + (c >> 3) * 16 + lc) * 2); c1f[2 * i] = lo16(w); c1f[2 * i + 1] = hi16(w); }
      c1f[4] = u2f((u32)*reinterpret_cast<const unsigned short*>(cp + (64 + lc + (c >> 3)) * 2) << 16);
    }
#define W1MMA(acc) \
      f32x4 acc[2]; \
      _Pragma("unroll") for (int nt = 0; nt < 2; ++nt) { const f32x4 z_ = {0.f, 0.f, 0.f, 0.f}; acc[nt] = MFMA(wf[0][nt], av0, z_); } \
      _Pragma("unroll") for (int nt = 0; nt < 2; ++nt) acc[nt] = MFMA(wf[1][nt], av1, acc[nt]); \
      _Pragma("unroll") for (int nt = 0; nt < 2; ++nt) acc[nt] = __builtin_amdgcn_mfma_f32_16x16x16bf16_1k(wh[nt], av2, acc[nt], 0, 0, 0);
    {
      const unsigned char* p = lds + L_XA + (4 + (c & 7)) * XS + wave * 160;
      const u32x4 av0 = *reinterpret_cast<const u32x4*>(p + r * 16), av1 = *reinterpret_cast<const u32x4*>(p + 64 + r * 16);
      const s16x4 av2 = *reinterpret_cast<const s16x4*>(p + 128 + r * 8);
      W1MMA(acc)
#pragma unroll
      for (int nt = 0; nt < 2; ++nt) if (c < 8) *reinterpret_cast<f32x4*>(red + (wave * 8 + c) * 32 + nt * 16 + r * 4) = acc[nt];
    }
    float Tc = 0.f, Ts = 0.f;
#define TSUM() _Pragma("unroll") for (int k = 0; k < 4; ++k) { Tc += red[((hw * 4 + k) * 8 + (wave & 3)) * 32 + fc]; Ts += red[((hw * 4 + k) * 8 + 4 + (wave & 3)) * 32 + fc]; }
    u32x4 pa[5];
    f32x4 s4, s0, s1;
    {
      u64 m[5], old = 0;
#pragma unroll
      for (int i = 0; i < 5; ++i) { m[i] = ~0ull; pa[i] = u32x4{0u, 0u, 0u, 0u}; }
#pragma unroll
      for (int i = 0; i < 5; ++i) asm volatile("" : "+v"(c1f[i]), "+v"(pa[i]));
      u32x2 sg;
      if (BFW >= 0 && wave < 4) asm volatile("s_waitcnt vmcnt(%0)" :: "i"(BFW) : "memory");
      if (BFW >= 0) BAR();
      heads_up_start(rsv, gl, 0xffffull, tg, sg);
      VM0();
      BAR();
      STAMPT(29, 0);
#define BLOOK(W_) \
        asm volatile(LK(0, 5, "0", 0, 7) LK(1, 5, "0", 512, 8) LK(2, 5, "0", 2048, 9) LK(3, 5, "0", 2560, 10) LK(4, 6, "0", 0, 11) LKEND \
                     : "+v"(pa[0]), "+v"(pa[1]), "+v"(pa[2]), "+v"(pa[3]), "+v"(pa[4]) \
                     : "v"(go), "v"(go4), "s"(m[0]), "s"(m[1]), "s"(m[2]), "s"(m[3]), "s"(m[4]), [rs] "s"(rsv), [w] "i"(W_) : "memory");
      heads_up<0>(rsv, gl, 0xffffull, tg, sg);
      heads_up_time(0xffffull, sg);
      STAMPT(21, 0);
      BLOOK(63)
      announce(rs, G_HB + jB * 128, C_GO, C_W, 8, tb, HCD, tg);
#pragma unroll
      for (int k = 0; k < 5; ++k) wg[k] = wfrag<512>(a_w2[k]);
      PAD(b2, back);
      TSUM()
#pragma unroll
      for (int i = 0; i < 5; ++i) asm volatile("" : "+v"(wg[i]));
      asm volatile("" : "+v"(Tc), "+v"(Ts));
      STAMPT(18, 0);
      asm volatile("s_waitcnt vmcnt(1)" : "+v"(pa[0]), "+v"(pa[1]), "+v"(pa[2]), "+v"(pa[3]), "+v"(pa[4]) :: "memory");
      STAMPT(22, 0);
#define BSUM() { const f32x4 z = {0.f, 0.f, 0.f, 0.f}; \
      s4 = MFMA(sel, pa[4], z); s0 = MFMA(sel, pa[0], z); s1 = MFMA(sel, pa[2], z); s0 = MFMA(sel, pa[1], s0); s1 = MFMA(sel, pa[3], s1); }
      SPINLOOP for (unsigned spins = 0; spins < SPIN_CAP; ++spins) {
        BSUM()
        if (!(__builtin_amdgcn_ballot_w64(__builtin_isunordered(s4[0], s4[1])) | __builtin_amdgcn_ballot_w64(__builtin_isunordered(s0[0], s0[1])) |
              __builtin_amdgcn_ballot_w64(__builtin_isunordered(s1[0], s1[1])))) break;
        u64 bad = 0;
#pragma unroll
        for (int i = 0; i < 5; ++i) { m[i] = __builtin_amdgcn_ballot_w64(missing(pa[i])); bad |= m[i]; }
        if (!(bad | old)) break;
        old = bad;
        if (!spins) FAIL(2);
        BLOOK(5 * (PDB - 1))
      }
    }
    STAMPT(14, 0); WSTAMP(3);
    float xf[5];
    {
      float e0 = s4[0], e1 = s4[1];
      asm("" : "+v"(e0), "+v"(e1));
      xf[4] = bfr(((c & 8) ? e1 : e0) + dpp<XOR8>((c & 8) ? e0 : e1));
      *reinterpret_cast<unsigned short*>(lds + a_u4) = (unsigned short)cvt2(xf[4] * c1f[4], 0.f);
      const u32 w0 = cvt2(s0[0], s0[1]), w1 = cvt2(s1[0], s1[1]);
      xf[0] = lo16(w0); xf[1] = hi16(w0); xf[2] = lo16(w1); xf[3] = hi16(w1);
      *reinterpret_cast<u32*>(lds + a_u) = cvt2(xf[0] * c1f[0], xf[1] * c1f[1]);
      *reinterpret_cast<u32*>(lds + a_u + 64) = cvt2(xf[2] * c1f[2], xf[3] * c1f[3]);
    }
    STAMPT(19, 0);
    {
      u32x4 av0 = *reinterpret_cast<const u32x4*>(lds + a_av), av1 = *reinterpret_cast<const u32x4*>(lds + a_av + 64);
      s16x4 av2 = *reinterpret_cast<const s16x4*>(lds + a_ah);
      __builtin_amdgcn_sched_barrier(0);
      const float ps = ((xf[0] + xf[1]) + (xf[2] + xf[3])) + xf[4];
      const float pq = fmaf(xf[0], xf[0], fmaf(xf[1], xf[1], xf[4] * xf[4])) + fmaf(xf[2], xf[2], xf[3] * xf[3]);
      const float t1 = ps + dpp<XOR8>(ps), t2 = pq + dpp<XOR8>(pq);
      float z = (c & 8) ? t2 : t1;
      z += dpp<XOR1>(z);
      asm volatile("" : "+v"(z));
      __builtin_amdgcn_sched_barrier(0);
      W1MMA(acc)
      z = cross_rows_sum(z);
      if ((lane & 49) == 0) *reinterpret_cast<float*>(lds + a_st) = z;
      if (c < 4) {
#pragma unroll
        for (int nt = 0; nt < 2; ++nt) *reinterpret_cast<f32x4*>(lds + a_red + nt * 64) = acc[nt];
      }
    }
#undef W1MMA
    PAD(b3, back);
    BAR();
    STAMPT(24, 0);
    if (wave < 4) {
      f32x4 sv[4];
      float sp[4];
#pragma unroll
      for (int k = 0; k < 4; ++k) sv[k] = *reinterpret_cast<const f32x4*>(lds + a_fst + k * 16);
#pragma unroll
      for (int k = 0; k < 4; ++k) sp[k] = *reinterpret_cast<const float*>(lds + a_fin + k * 512);
      __builtin_amdgcn_sched_barrier(0);
      const f32x4 s1 = sv[0] + sv[1], s2 = sv[2] + sv[3];
      const float mean = ((s1[0] + s1[1]) + (s1[2] + s1[3])) * (1.f / DM);
      const float var = fmaxf(((s2[0] + s2[1]) + (s2[2] + s2[3])) * (1.f / DM) - mean * mean, 0.f);
      const float rstd = __builtin_amdgcn_rsqf(var + 1e-5f);
      const float zp = fmaf(rstd, fmaf(-mean, Tc, (sp[0] + sp[1]) + (sp[2] + sp[3])), Ts);
      const float hh = bfr(bfr(swap32_add(zp, zp)) + b1v);
      const float e2 = __builtin_amdgcn_exp2f(hh * fmaf(hh * hh, 0.1029432397f, 2.302208198f));
      const float f = fmaf(-hh, frcp(e2 + 1.f), hh);
      *reinterpret_cast<unsigned short*>(lds + a_fo) = (unsigned short)cvt2(f, 0.f);
    }
    BAR();
    if (BM) {
      u32x4 av[4];
#pragma unroll
      for (int k = 0; k < 4; ++k) av[k] = *reinterpret_cast<const u32x4*>(lds + a_fi + (3 - k) * 256);
      const f32x4 z = {0.f, 0.f, 0.f, 0.f};
      f32x4 a4 = MFMA(wg[4], av[0], z), a0 = MFMA(wg[0], av[0], z);
#pragma unroll
      for (int k = 1; k < 4; ++k) a0 = MFMA(wg[k], av[k], a0);
      __builtin_amdgcn_sched_barrier(0);
      const u32x2 o4 = {min(cvt2(a4[0], a4[1]), EMPTY - 1), min(cvt2(a4[2], a4[3]), EMPTY - 1)};
      __builtin_amdgcn_raw_buffer_store_b64(o4, rs, a_out, 4 * (54 * 128), 16);
      const u32x2 o0 = {min(cvt2(a0[0], a0[1]), EMPTY - 1), min(cvt2(a0[2], a0[3]), EMPTY - 1)};
      __builtin_amdgcn_raw_buffer_store_b64(o0, rs, a_ou4, 0, 16);
    } else {
      const u32x4 av = *reinterpret_cast<const u32x4*>(lds + a_fi);
      f32x4 acc[5];
#pragma unroll
      for (int k = 0; k < 5; ++k) { const f32x4 z = {0.f, 0.f, 0.f, 0.f}; acc[k] = MFMA(wg[k], av, z); }
      __builtin_amdgcn_sched_barrier(0);
#pragma unroll
      for (int k = 0; k < 5; ++k) {
        const u32x2 o = {min(cvt2(acc[k][0], acc[k][1]), EMPTY - 1), min(cvt2(acc[k][2], acc[k][3]), EMPTY - 1)};
        __builtin_amdgcn_raw_buffer_store_b64(o, rs, a_out, k * (54 * 128), 16);
      }
    }
    STAMPT(15, 0);
    asm volatile("" :: "v"(pa[0]), "v"(pa[1]), "v"(pa[2]), "v"(pa[3]), "v"(pa[4]));

  } else if (role == 2 && wave == 0) {
    PAD(c0, back);
    const int lb = (c >> 1) & 3, col = g * 16 + (c & 1) * 8 + 2 * r + (c >> 3);
    bf16* outp;
    asm volatile("s_load_dwordx2 %0, %1, 128\n\ts_waitcnt lgkmcnt(0)" : "=s"(outp) : "s"(__builtin_amdgcn_kernarg_segment_ptr()));
    const __amdgpu_buffer_rsrc_t ro = __builtin_amdgcn_make_buffer_rsrc(outp, 0, NB * DM * 2, 0x00020000);
#define DUMMY() __builtin_amdgcn_raw_buffer_store_b16((unsigned short)0, ro, (lb * DM + col) * 2, 0, DAUX)
    if (DPOS == 0) DUMMY();
    float b2v = bf2f(a.b2[col]);
    const f32x4 z = {0.f, 0.f, 0.f, 0.f};
    SELECTOR(sel)
    float x1v, gbv;
    {
      u32 offs[1] = {G_MF + ((12 + lb) * 160 + (col >> 2)) * 16};
      u32x4 gm[1];
      gather<1>(rs, offs, gm, tg);
      const u32 gbw = (r & 1) ? gm[0][2] : gm[0][0];
      gbv = (c & 8) ? hi16(gbw) : lo16(gbw);
    }
    u32 oo = lb * DM + col;
    asm volatile("" : "+v"(gbv), "+v"(oo), "+v"(sel), "+v"(b2v));
    u32x4 pa[7];
    u32 go = G_OUT + g * (54 * 128) + lane * 16, gx = G_WO + g * 1024 + lane * 16;
#pragma unroll
    for (int i = 0; i < 7; ++i) { pa[i] = u32x4{0u, 0u, 0u, 0u}; asm volatile("" : "+v"(pa[i])); }
    asm volatile("" : "+v"(go), "+v"(gx));
    u32x2 hv;
    if (CPUB) lds_wait<2>(C_PUB, 1);
    if (DPOS == 1) DUMMY();
    heads_up_start(rsv, G_HA + ((g >> 2) * 8 + lane) * 128, 0xffull, tg, hv);
    heads_up<CSL>(rsv, G_HA + ((g >> 2) * 8 + lane) * 128, 0xffull, tg, hv);
    if (DPOS == 2) DUMMY();
    if (EPE == 1) __builtin_amdgcn_raw_buffer_store_b32(tg, rs, u == 0 && lane == 0 ? WS_EPOCH : OOB, 0, 16);
    hv[0] += CXD;
    heads_up_time(0xffull, hv);
    {
      u32x4 gm[1];
      gather_e<1, 2, 1>(rsv, gx, gm, ~0ull);
      const f32x4 sm = MFMA(sel, gm[0], z);
      x1v = bfr(PAIRSUM(sm));
      asm volatile("" : "+v"(x1v));
    }
    STAMPT(23, 0);
    if (DPOS == 3) DUMMY();
    if (EPE == 2) __builtin_amdgcn_raw_buffer_store_b32(tg, rs, u == 0 && lane == 0 ? WS_EPOCH : OOB, 0, 16);
    PAD(c1, back);
    heads_up_start(rsv, G_HB + lane * 128, (1ull << 54) - 1, tg, hv);
    heads_up<0>(rsv, G_HB + lane * 128, (1ull << 54) - 1, tg, hv);
    heads_up_time((1ull << 54) - 1, hv);
    STAMPT(14, 0);
    f32x4 s0, s1;
    {
      u64 m[7], old = 0;
#pragma unroll
      for (int i = 0; i < 7; ++i) m[i] = i < 6 ? ~0ull : (1ull << 48) - 1;
      look<0>(rsv, go, pa, m);
#define CSUM() { s0 = MFMA(sel, pa[0], z); s1 = MFMA(sel, pa[1], z); \
      _Pragma("unroll") for (int i = 2; i < 7; ++i) { if (i & 1) s1 = MFMA(sel, pa[i], s1); else s0 = MFMA(sel, pa[i], s0); } \
      s0 += s1; }
      SPINLOOP for (unsigned spins = 0; spins < SPIN_CAP; ++spins) {
        CSUM()
        if (!__builtin_amdgcn_ballot_w64(__builtin_isunordered(s0[0], s0[1]))) break;
        u64 bad = 0;
#pragma unroll
        for (int i = 0; i < 7; ++i) { m[i] = __builtin_amdgcn_ballot_w64(missing(pa[i])); bad |= m[i]; }
        if (!(bad | old)) break;
        old = bad;
        if (!spins) FAIL(3);
        look<7 * (PDC - 1)>(rsv, go, pa, m);
      }
    }
    STAMPT(16, 0);
    const float y = bfr(bfr(PAIRSUM(s0)) + b2v);
    if (OUTM & 1) __builtin_amdgcn_raw_buffer_store_b16((unsigned short)cvt2(x1v + bfr(gbv * y), 0.f), ro, oo * 2, 0, FAUX);
    else outp[oo] = __float2bfloat16(x1v + bfr(gbv * y));
    STAMPT(17, 0);
    asm volatile("" :: "v"(pa[0]), "v"(pa[1]), "v"(pa[2]), "v"(pa[3]), "v"(pa[4]), "v"(pa[5]), "v"(pa[6]), "v"(hv));

    if (!EPE && u == 0 && t == 0)
      __hip_atomic_store(reinterpret_cast<u32*>(a.ws + WS_EPOCH), tg, __ATOMIC_RELAXED, __HIP_MEMORY_SCOPE_AGENT);
  }
  else if (EPE == 3 && u == 80 && wave == 0) {
    u32x2 hv;
    lds_wait<2>(C_PUB, 1);
    heads_up_start(rsv, G_HA + lane * 128, 0xffull, tg, hv);
    heads_up<8>(rsv, G_HA + lane * 128, 0xffull, tg, hv);
    __builtin_amdgcn_raw_buffer_store_b32(tg, rs, lane < 32 ? WS_EPOCH + lane * 4 : OOB, 0, 16);
  }
  STAMP_FLUSH;
}

extern "C" void kernel_launch(void* const* d_in, const int* in_sizes, int n_in, void* d_out, int out_size, void* d_ws,
                              size_t ws_size, hipStream_t stream) {
  Args a;
  a.x = Desc{(const bf16*)d_in[0], NB * DM * 2, 0x00020000u};
  a.kc = (const bf16*)d_in[1];
  a.vc = (const bf16*)d_in[2];
  a.wq = Desc{(const bf16*)d_in[3], DM * QD * 2, 0x00020000u};
  a.wo = (const bf16*)d_in[4];
  a.w1 = (const bf16*)d_in[6];
  a.b1 = (const bf16*)d_in[7];
  a.w2 = (const bf16*)d_in[8];
  a.b2 = (const bf16*)d_in[9];
  a.c = Desc{(const bf16*)d_in[10], NB * DM * 2, 0x00020000u};
  a.wmod = Desc{(const bf16*)d_in[11], DM * MODN * 2, 0x00020000u};
  a.out = (bf16*)d_out;
  a.ws = (unsigned char*)d_ws;
  mega_kernel<<<NWG, NT, 0, stream>>>(a);
}
```

```cpp
#define SPINLOOP _Pragma("nounroll")
#define P_LAG 14
#define P_NPRE 32
#define P_NVE 6
#define P_NVA 6
#define QODD 4
#define QEVEN 4
#define MFPOS 0
#define EA 0
#define EB 0
#define EC 4
#define APOLL 0
#define N1E N1A
#define TAU_A 234
#define TAU_B TAU_A
#define TAU_X TAU_A
#define TAU_E 247
#define PK 0
#define PL 1
#define QPRE 1
#define QHOLD 0
#define CT0 1
#define TOFF 65
#define BLIGHT 1
#define BPRE 0
#define BW1 1
#define BMASK 1
#define CSLEEP 1
#define CSL 2
#define CPUB 1
#define CXD 20
#define OPOS 1
#define NREPL 1
#define OUTPRE 1
#define ASL 1
#define AEARLY 1
#define PDC 4
#define PDA 3
#define PDB 3
#define PSB 1
#define HC 1
#define HCD 120
#define HAG 60
#define AW 4
#define BFW -1
#define FULLF 1
#define WARM 1
#define HT 0
#define HX 10
#define HTD 55
#define HSL 1
#define NANT 1
#define HAW 65
#define BM 1
#define HPD 1
#define BT 0
#define BSL 2
#define PB 0
#define GOPOS 2
#define JMAP 1
#define N1A 10
#define N1B 10
#define PT 0
#define PTAUX 16
#define LAY 1
#define OUTM 1
#define EPE 0
#define DPOS 2
#define DAUX 16
#define FAUX 16
#define KOUT 1
#define NK0 12
#define GT 540
#define PLO 0
#define PLS 16
#define ANCH 1
#define AOFF 55
#define AGG 1
#define P_GATE 8
#include <hip/hip_runtime.h>
#include <hip/hip_bf16.h>
#include <stdint.h>

using bf16 = __hip_bfloat16;
using u32 = unsigned;
using u64 = unsigned long long;
using u32x2 = __attribute__((ext_vector_type(2))) unsigned;
using u32x4 = __attribute__((ext_vector_type(4))) unsigned;
using f32x4 = __attribute__((ext_vector_type(4))) float;
using bf16x2 = __attribute__((ext_vector_type(2))) __bf16;
typedef __attribute__((ext_vector_type(2))) float f32x2;
typedef short s16x8 __attribute__((ext_vector_type(8)));
typedef short s16x4 __attribute__((ext_vector_type(4)));
typedef __attribute__((address_space(3))) s16x4* lds_s4p;
typedef __attribute__((ext_vector_type(8))) __bf16 bf16x8v;

constexpr int NB = 4, DM = 640, NH = 32, DH = 128, SK = 8192, WIN = 2048, FF = 1728, QD = 4096, MODN = 3840;
constexpr int NT = 512, NWG = 256;

constexpr u32 WS_EPOCH = 0;
constexpr u32 G_T = 128;
constexpr u32 G_Q = 256;
constexpr u32 G_MOD = G_Q + 8 * 1024 * 16;
constexpr u32 G_MF = G_MOD + 8 * 1024 * 16;
constexpr u32 G_HB = G_MF + 16 * 160 * 16;
constexpr u32 G_HA = G_HB + 64 * 128;
constexpr u32 G_HT = G_HA + 80 * 128;
constexpr u32 G_ML = G_HT + 256 * 128;
constexpr u32 EMPTY = 0xffffffffu;
constexpr int NREP = NREPL;
constexpr u32 G_WO = G_ML + 256 * 64;
constexpr u32 WO_REP = 40 * 1024;
constexpr u32 G_ATT = G_WO + NREP * WO_REP;
constexpr u32 G_OUT = G_ATT + 256 * 256;
constexpr u32 WS_END = G_OUT + 40 * 54 * 128;
constexpr u32 OOB = 0xF0000000u;
#define GIDX(n, ks, b, cg) ((((ks) * ((n) / 4) + ((cg) >> 2)) * 16 + (b) * 4 + ((cg) & 3)) * 16)

#define WSTAMP(k) do {} while (0)
#define STAMPW(i) do {} while (0)
#define STAMPT(i, tt) do {} while (0)
#define STAMP_DECL
#define STAMP_FLUSH do {} while (0)
#define FAIL(i) do {} while (0)
#define PAD(name, next) asm volatile(".Lpad_" #name ":\n\ts_cbranch_execz .Lpad_" #next)
#define STR_(x) #x
#define STR(x) STR_(x)
#define BAR() asm volatile("s_waitcnt lgkmcnt(0)\n\ts_barrier" ::: "memory")
#define VM0() asm volatile("s_waitcnt vmcnt(0)" ::: "memory")

__device__ __forceinline__ float bfr(float x) { return __bfloat162float(__float2bfloat16(x)); }
__device__ __forceinline__ float bf2f(bf16 x) { return __bfloat162float(x); }
__device__ __forceinline__ float lo16(unsigned u) { return __builtin_bit_cast(float, u << 16); }
__device__ __forceinline__ float hi16(unsigned u) { return __builtin_bit_cast(float, u & 0xffff0000u); }
__device__ __forceinline__ u32 f2u(float f) { return __builtin_bit_cast(u32, f); }
__device__ __forceinline__ float u2f(u32 f) { return __builtin_bit_cast(float, f); }
__device__ __forceinline__ u32 cvt2(float a, float b) {
  const f32x2 v = {a, b};
  return __builtin_bit_cast(u32, __builtin_convertvector(v, bf16x2));
}
__device__ __forceinline__ float frcp(float x) { return __builtin_amdgcn_rcpf(x); }
__device__ __forceinline__ float fsqrt(float x) { return __builtin_amdgcn_sqrtf(x); }
__device__ __forceinline__ float fexp(float x) { return __builtin_amdgcn_exp2f(x * 1.4426950408889634f); }
__device__ __forceinline__ float dot2(unsigned k, unsigned q, float acc) {
  return __builtin_amdgcn_fdot2_f32_bf16(__builtin_bit_cast(bf16x2, k), __builtin_bit_cast(bf16x2, q), acc, false);
}
__device__ __forceinline__ float addlo(unsigned w, float acc) { u32 k = 0x00003f80u; asm("" : "+s"(k)); return dot2(w, k, acc); }
__device__ __forceinline__ float addhi(unsigned w, float acc) { return dot2(w, 0x3f800000u, acc); }
template <int CTRL> __device__ __forceinline__ float dpp(float x) {
  return __builtin_bit_cast(float, __builtin_amdgcn_mov_dpp(__builtin_bit_cast(int, x), CTRL, 0xf, 0xf, true));
}
template <int CTRL> __device__ __forceinline__ u32 dppu(u32 x) { return (u32)__builtin_amdgcn_mov_dpp((int)x, CTRL, 0xf, 0xf, true); }
constexpr int XOR1 = 0xB1, XOR2 = 0x4E, XOR7 = 0x141, XOR8 = 0x128, ROR4 = 0x124, ROR2 = 0x122, ROR1 = 0x121, SHL4 = 0x104;
__device__ __forceinline__ float swap32_add(float a, float b) {
  auto s = __builtin_amdgcn_permlane32_swap(f2u(a), f2u(b), false, false);
  return u2f(s[0]) + u2f(s[1]);
}
__device__ __forceinline__ float swap16_add(float a, float b) {
  auto s = __builtin_amdgcn_permlane16_swap(f2u(a), f2u(b), false, false);
  return u2f(s[0]) + u2f(s[1]);
}
__device__ __forceinline__ float cross_rows_sum(float x) { x = swap16_add(x, x); return swap32_add(x, x); }
__device__ __forceinline__ float cross_rows_max(float x) {
  auto s = __builtin_amdgcn_permlane16_swap(f2u(x), f2u(x), false, false);
  x = fmaxf(u2f(s[0]), u2f(s[1]));
  auto s2 = __builtin_amdgcn_permlane32_swap(f2u(x), f2u(x), false, false);
  return fmaxf(u2f(s2[0]), u2f(s2[1]));
}
__device__ __forceinline__ float wave_sum(float x) {
  x += dpp<XOR8>(x); x += dpp<ROR4>(x); x += dpp<ROR2>(x); x += dpp<ROR1>(x);
  return cross_rows_sum(x);
}
__device__ __forceinline__ float wave_max(float x) {
  x = fmaxf(x, dpp<XOR8>(x)); x = fmaxf(x, dpp<ROR4>(x)); x = fmaxf(x, dpp<ROR2>(x)); x = fmaxf(x, dpp<ROR1>(x));
  return cross_rows_max(x);
}

__device__ __forceinline__ void st_gran(__amdgpu_buffer_rsrc_t rs, u32 off, u32 a, u32 b, u32 tg) {
  u32x4 v = {a, tg, b, tg};
  __builtin_amdgcn_raw_buffer_store_b128(v, rs, off, 0, 16);
}
__device__ __forceinline__ u32x4 ld_gran(__amdgpu_buffer_rsrc_t rs, u32 off) {
  return __builtin_amdgcn_raw_buffer_load_b128(rs, off, 0, 16);
}
constexpr unsigned SPIN_CAP = 1u << 18;
template <int N>
__device__ __forceinline__ void gather(__amdgpu_buffer_rsrc_t rs, const u32 (&offs)[N], u32x4 (&g)[N], u32 tg) {
  SPINLOOP for (unsigned spins = 0; spins < SPIN_CAP; ++spins) {
    bool ok = true;
#pragma unroll
    for (int i = 0; i < N; ++i) g[i] = ld_gran(rs, offs[i]);
#pragma unroll
    for (int i = 0; i < N; ++i) ok &= (offs[i] == OOB) | ((g[i][1] == tg) & (g[i][3] == tg));
    if (__all(ok)) break;
    __builtin_amdgcn_s_sleep(1);
  }
}
template <int N>
__device__ __forceinline__ void gather_m(__amdgpu_buffer_rsrc_t rs, u32 (&offs)[N], u32 (&va)[N], u32 (&vb)[N], u32 tg) {
#pragma unroll
  for (int i = 0; i < N; ++i) { va[i] = 0; vb[i] = 0; }
  SPINLOOP for (unsigned spins = 0; spins < SPIN_CAP; ++spins) {
    u32x4 g[N];
#pragma unroll
    for (int i = 0; i < N; ++i) g[i] = ld_gran(rs, offs[i]);
    bool ok = true;
#pragma unroll
    for (int i = 0; i < N; ++i) {
      const bool hit = (offs[i] != OOB) & (g[i][1] == tg) & (g[i][3] == tg);
      va[i] = hit ? g[i][0] : va[i]; vb[i] = hit ? g[i][2] : vb[i];
      offs[i] = hit ? OOB : offs[i];
      ok &= (offs[i] == OOB);
    }
    if (__all(ok)) break;
    __builtin_amdgcn_s_sleep(1);
  }
}

__device__ __forceinline__ bool missing(u32x4 v) { return max(max(v[0], v[1]), max(v[2], v[3])) == EMPTY; }
#define LK(v, o, so, imm, m) "s_mov_b64 exec, %" #m "\n\tbuffer_load_dwordx4 %" #v ", %" #o ", %[rs], " so " offen offset:" #imm " sc1\n\t"
#define LKEND "s_mov_b64 exec, -1\n\ts_waitcnt vmcnt(%[w])"
template <int W> __device__ __forceinline__ void look(u32x4 rs, u32 off, u32x4 (&v)[1], const u64 (&m)[1]) {
  asm volatile(LK(0, 1, "0", 0, 2) LKEND : "+v"(v[0]) : "v"(off), "s"(m[0]), [rs] "s"(rs), [w] "i"(W) : "memory");
}
template <int W> __device__ __forceinline__ void look(u32x4 rs, u32 off, u32x4 (&v)[5], const u64 (&m)[5]) {
  asm volatile(LK(0, 5, "0", 0, 6) LK(1, 5, "0", 1024, 7) LK(2, 5, "0", 2048, 8) LK(3, 5, "0", 3072, 9) LK(4, 5, "%11", 0, 10) LKEND
               : "+v"(v[0]), "+v"(v[1]), "+v"(v[2]), "+v"(v[3]), "+v"(v[4])
               : "v"(off), "s"(m[0]), "s"(m[1]), "s"(m[2]), "s"(m[3]), "s"(m[4]), "s"(4096u), [rs] "s"(rs), [w] "i"(W) : "memory");
}
template <int W> __device__ __forceinline__ void look(u32x4 rs, u32 off, u32x4 (&v)[7], const u64 (&m)[7]) {
  asm volatile(LK(0, 7, "0", 0, 8) LK(1, 7, "0", 1024, 9) LK(2, 7, "0", 2048, 10) LK(3, 7, "0", 3072, 11)
               LK(4, 7, "%15", 0, 12) LK(5, 7, "%15", 1024, 13) LK(6, 7, "%15", 2048, 14) LKEND
               : "+v"(v[0]), "+v"(v[1]), "+v"(v[2]), "+v"(v[3]), "+v"(v[4]), "+v"(v[5]), "+v"(v[6])
               : "v"(off), "s"(m[0]), "s"(m[1]), "s"(m[2]), "s"(m[3]), "s"(m[4]), "s"(m[5]), "s"(m[6]), "s"(4096u), [rs] "s"(rs), [w] "i"(W) : "memory");
}
template <int N, int SLEEP, int D>
__device__ __forceinline__ void gather_e(u32x4 rs, u32 off, u32x4 (&v)[N], u64 last) {
  u64 m[N];
#pragma unroll
  for (int i = 0; i < N; ++i) { m[i] = i < N - 1 ? ~0ull : last; v[i] = u32x4{0u, 0u, 0u, 0u}; }
  look<0>(rs, off, v, m);
  SPINLOOP for (unsigned spins = 0; spins < SPIN_CAP; ++spins) {
    u64 bad = 0;
#pragma unroll
    for (int i = 0; i < N; ++i) { m[i] = __builtin_amdgcn_ballot_w64(missing(v[i])); bad |= m[i]; }
    if (!bad) break;
    if (SLEEP) __builtin_amdgcn_s_sleep(SLEEP);
    look<N * (D - 1)>(rs, off, v, m);
  }
}

#define HLOOK(W_) asm volatile("s_mov_b64 exec, %2\n\tbuffer_load_dwordx2 %0, %1, %3, 0 offen sc1\n\ts_mov_b64 exec, -1\n\ts_waitcnt vmcnt(%4)" \
                               : "+v"(v) : "v"(off), "s"(m), "s"(rs), "i"(W_) : "memory")
__device__ __forceinline__ void heads_up_start(u32x4 rs, u32 off, u64 m, u32 tg, u32x2& v) { v = u32x2{0u, ~tg}; HLOOK(63); }
template <int SLEEP>
__device__ __forceinline__ void heads_up(u32x4 rs, u32 off, u64 m, u32 tg, u32x2& v) {
  asm volatile("s_waitcnt vmcnt(0)" : "+v"(v) :: "memory");
  SPINLOOP for (unsigned spins = 0; spins < SPIN_CAP; ++spins) {
    m &= __builtin_amdgcn_ballot_w64(v[1] != tg);
    if (!m) break;
    if (SLEEP) __builtin_amdgcn_s_sleep(SLEEP);
    HLOOK(0);
  }
}
__device__ __forceinline__ void heads_up_time(u64 all, u32x2 v) {
  SPINLOOP for (unsigned spins = 0; spins < 64; ++spins)
    if (!(all & __builtin_amdgcn_ballot_w64((int)((u32)__builtin_amdgcn_s_memrealtime() - v[0]) < 0))) break;
}
__device__ __forceinline__ void announce(__amdgpu_buffer_rsrc_t rs, u32 off, u32 a_max, u32 a_cnt, u32 n, u32 tb, u32 d, u32 tg) {
  u32 tk = 0, mx;
  const u32 f = (u32)__builtin_amdgcn_s_memrealtime() - tb + d;
  asm volatile("s_mov_b64 exec, 1\n\tds_max_u32 %2, %3\n\tds_add_rtn_u32 %0, %4, %5\n\tds_read_b32 %1, %2\n\ts_mov_b64 exec, -1\n\ts_waitcnt lgkmcnt(0)"
               : "+v"(tk), "=&v"(mx) : "v"(a_max), "v"(f), "v"(a_cnt), "v"(1u) : "memory");
  const u32 lane = threadIdx.x & 63;
  __builtin_amdgcn_raw_buffer_store_b64(u32x2{__builtin_amdgcn_readfirstlane(mx) + tb, tg}, rs,
                                        (lane < 16 && __builtin_amdgcn_readfirstlane(tk) == n - 1) ? off + lane * 8 : OOB, 0, 16);
}

__device__ __forceinline__ void st_g22(__amdgpu_buffer_rsrc_t rs, u32 off, f32x4 v, u32 tg) {
  const u32 a0 = f2u(v[0]) + 0x200u, a1 = f2u(v[1]) + 0x200u, a2 = f2u(v[2]) + 0x200u, a3 = f2u(v[3]) + 0x200u;
  const u32x4 w = {(a0 >> 10) | ((a1 << 12) & 0xffc00000u), (a1 >> 20) | (tg << 12), (a2 >> 10) | ((a3 << 12) & 0xffc00000u), (a3 >> 20) | (tg << 12)};
  __builtin_amdgcn_raw_buffer_store_b128(w, rs, off, 0, 16);
}
__device__ __forceinline__ f32x4 un_g22(u32x4 w) {
  return f32x4{u2f(w[0] << 10), u2f(__builtin_amdgcn_alignbit(w[1], w[0], 22) << 10), u2f(w[2] << 10), u2f(__builtin_amdgcn_alignbit(w[3], w[2], 22) << 10)};
}
template <int N, int SLEEP>
__device__ __forceinline__ void gather22(__amdgpu_buffer_rsrc_t rs, u32 (&offs)[N], u32x4 (&v)[N], u32 tg, u32 a_inc = 0) {
#pragma unroll
  for (int i = 0; i < N; ++i) v[i] = u32x4{0u, 0u, 0u, 0u};
  SPINLOOP for (unsigned spins = 0; spins < SPIN_CAP; ++spins) {
    u32x4 g[N];
#pragma unroll
    for (int i = 0; i < N; ++i) g[i] = ld_gran(rs, offs[i]);
    if (a_inc) asm volatile("s_mov_b64 exec, 1\n\tds_add_u32 %0, %1\n\ts_mov_b64 exec, -1" :: "v"(a_inc), "v"(1u) : "memory");
    bool ok = true;
#pragma unroll
    for (int i = 0; i < N; ++i) {
      const bool hit = (offs[i] != OOB) & (((g[i][1] ^ (tg << 12)) | (g[i][3] ^ (tg << 12))) < 4096u);
#pragma unroll
      for (int k = 0; k < 4; ++k) v[i][k] = hit ? g[i][k] : v[i][k];
      offs[i] = hit ? OOB : offs[i];
      ok &= (offs[i] == OOB);
    }
    if (__all(ok)) break;
    __builtin_amdgcn_s_sleep(SLEEP);
  }
}

__device__ __forceinline__ u32 next_tag(u32 ep) { const u32 t = ep + 1u; return (t & 0xfffffu) ? t : t + 1u; }

__device__ __forceinline__ void lds_arrive(u32 addr, int lane) {
  asm volatile("s_waitcnt lgkmcnt(0)" ::: "memory");
  if (lane == 0) asm volatile("ds_add_u32 %0, %1" :: "v"(addr), "v"(1u) : "memory");
}
__device__ __forceinline__ u32 lds_ticket(u32 addr, int lane) {
  u32 r = 0;
  asm volatile("s_waitcnt lgkmcnt(0)" ::: "memory");
  if (lane == 0) asm volatile("ds_add_rtn_u32 %0, %1, %2\n\ts_waitcnt lgkmcnt(0)" : "=v"(r) : "v"(addr), "v"(1u) : "memory");
  return __builtin_amdgcn_readfirstlane(r);
}
template <int SLEEP>
__device__ __forceinline__ void lds_wait(u32 addr, u32 target) {
  SPINLOOP for (unsigned spins = 0; spins < (1u << 22); ++spins) {
    u32 v;
    asm volatile("ds_read_b32 %0, %1\n\ts_waitcnt lgkmcnt(0)" : "=v"(v) : "v"(addr) : "memory");
    if (v >= target) break;
    __builtin_amdgcn_s_sleep(SLEEP);
  }
}
__device__ __forceinline__ void dma16(const void* gsrc, u32 lds_dst) {
  u32 keep;
  asm volatile("s_mov_b32 %0, m0\n\ts_mov_b32 m0, %2\n\ts_nop 0\n\tglobal_load_lds_dwordx4 %1, off\n\ts_mov_b32 m0, %0"
               : "=&s"(keep) : "v"(gsrc), "s"(lds_dst) : "memory");
}
__device__ __forceinline__ void dma16nt(const void* gsrc, u32 lds_dst) {
  u32 keep;
  asm volatile("s_mov_b32 %0, m0\n\ts_mov_b32 m0, %2\n\ts_nop 0\n\tglobal_load_lds_dwordx4 %1, off nt\n\ts_mov_b32 m0, %0"
               : "=&s"(keep) : "v"(gsrc), "s"(lds_dst) : "memory");
}
__device__ __forceinline__ u32x4 ldasm(const void* p) {
  u32x4 v;
  asm volatile("global_load_dwordx4 %0, %1, off" : "=v"(v) : "v"(p) : "memory");
  return v;
}
__device__ __forceinline__ u32 ldasm_sc1(const void* p) {
  u32 v;
  asm volatile("global_load_dword %0, %1, off sc1" : "=v"(v) : "v"(p) : "memory");
  return v;
}
__device__ __forceinline__ u32x4 ldw(const bf16* p) { return *reinterpret_cast<const u32x4*>(p); }
template <int D2>
__device__ __forceinline__ s16x8 wfrag(u32 addr) {
  const s16x4 lo = __builtin_amdgcn_ds_read_tr16_b64_v4i16((lds_s4p)(uintptr_t)addr);
  const s16x4 hi = __builtin_amdgcn_ds_read_tr16_b64_v4i16((lds_s4p)(uintptr_t)(addr + D2));
  return s16x8{lo[0], lo[1], lo[2], lo[3], hi[0], hi[1], hi[2], hi[3]};
}
#define MFMA(w_, x_, c_) __builtin_amdgcn_mfma_f32_16x16x32_bf16(__builtin_bit_cast(bf16x8v, w_), __builtin_bit_cast(bf16x8v, x_), c_, 0, 0, 0)

struct Desc { const bf16* p; u32 bytes, flags; };
struct Args {
  Desc wq, x, wmod, c;
  unsigned char* ws;
  const bf16 *kc, *vc, *wo, *w1, *b1, *w2, *b2;
  bf16* out;
};

static_assert(offsetof(Args, ws) == 64 && offsetof(Args, out) == 128, "argument offsets");

constexpr int L_W = 0;
constexpr int L_WM2 = 40960;
constexpr int L_XA = 81920;
constexpr int XS = 1312, XH = 704;
constexpr int L_RED = L_XA + 16384;
constexpr int L_MOD = L_RED + 8192;
constexpr int L_XR = L_MOD + 5120;
constexpr int L_PT = L_XR + 5120;
constexpr int L_ACC = L_PT + 2048;
constexpr int L_QS = L_ACC + 4096;
constexpr int L_FI = L_QS + 256;
constexpr int L_ST = L_FI + 256;
constexpr int L_CNT = L_ST + 256;
constexpr int L_END = L_CNT + 128;
static_assert(L_END <= 160 * 1024, "LDS");
constexpr u32 C_Q = L_CNT, C_SC = L_CNT + 4, C_ISS = L_CNT + 8, C_P1 = L_CNT + 20, C_P2 = L_CNT + 24, C_GO = L_CNT + 28, C_W = L_CNT + 12, C_FIN = L_CNT + 32,
              C_KI = L_CNT + 36, C_PP = L_CNT + 40, C_PUB = L_CNT + 56, C_HT = L_CNT + 64;

constexpr int NL = 32;
constexpr int PVLAG = P_LAG;
constexpr int NPRE = P_NPRE;
constexpr int NVE = P_NVE;
static_assert(PVLAG <= 16 && (PVLAG & 1) == 0 && NVE <= PVLAG && (NVE & 1) == 0, "row groups");

__global__ void __launch_bounds__(NT, 2) mega_kernel(Args a) {
  __shared__ __attribute__((aligned(1024))) unsigned char lds[L_END];
  float* red = reinterpret_cast<float*>(lds + L_RED);
  u32* xraw = reinterpret_cast<u32*>(lds + L_XR);
  u32* pT = reinterpret_cast<u32*>(lds + L_PT);
  float* accs = reinterpret_cast<float*>(lds + L_ACC);
  u32* qs = reinterpret_cast<u32*>(lds + L_QS);
  float* st = reinterpret_cast<float*>(lds + L_ST);
  u32* cnt = reinterpret_cast<u32*>(lds + L_CNT);

  u32x4 rw, rx;
  unsigned char* wsq;
  const void* kcq;
  asm volatile("s_load_dwordx4 %0, %4, %5\n\ts_load_dwordx4 %1, %4, %5 offset:16\n\ts_load_dwordx2 %2, %4, 64\n\ts_load_dwordx2 %3, %4, 72"
               : "=&s"(rw), "=&s"(rx), "=&s"(wsq), "=&s"(kcq) : "s"(__builtin_amdgcn_kernarg_segment_ptr()), "s"((blockIdx.x & 1) * 32));
  __builtin_amdgcn_sched_barrier(0);
  const int t = threadIdx.x, lane = t & 63;
  const int wave = __builtin_amdgcn_readfirstlane(t >> 6);
  const int u = blockIdx.x;
  const int bh = u >> 1, half = u & 1, b_att = bh >> 5, h_att = bh & 31;
  const int r = lane >> 4, c = lane & 15;
  STAMP_DECL
  STAMPT(0, 0);
  u32 tg;
  const int g = u >> 1, ksp = g & 1, cbp = g >> 1;
  const int s128e = (((((lane & 7) >> 1) ^ ((lane >> 4) & 1)) << 1) | (lane & 1)) * 8;
  const u32 tr128 = (8 * r + (c >> 2)) * 128 + ((((c >> 3) & 1) | ((r & 1) << 1)) * 32) + (c & 3) * 8;
  if (wave < 4) {
    __builtin_amdgcn_s_setprio(3);
    const u32 rb = 8192 - 512 * half;
    const u32 hm = 0u - (u32)half;
    const u32 vo = ((u32)(lane >> 3) * rb + (((lane & 7) ^ ((lane >> 3) & 2)) ^ ((wave & 1) * 4)) * 16) | ((u32)(119 - g) & hm & OOB);
    u32 so = (u32)(ksp * 320 + 8 * wave) * rb + cbp * 128;
    const u32 step = 32 * rb, dstep = 4096, dst = L_W + wave * 1024;
    const u32 oe = wave * 1280 + lane * 16, oo = (r * 1280 + ksp * 640 + (c + 16 * wave) * 16) | ((u32)(39 - c - 16 * wave) & OOB);
    const u32 oa = (oo & hm) | (oe & ~hm), ob = (wave * 1280 + 1024 + c * 16) | (hm & OOB), oc = oe | (hm & OOB), zero = 0;
    u32 ep, keep;
    u32x4 xv[4];
    const u32 pre = ((u32)u << 31) & (u32)(g - 32) & (u32)(1 - wave), po = G_ATT + g * 2048 + (t & 127) * 16;
    const u32x4 emp = {EMPTY, EMPTY, EMPTY, EMPTY};
#define DMA1 "buffer_load_dwordx4 %7, %8, %1 offen nt lds\n\t"
#define DMAN "s_add_u32 m0, m0, %11\n\ts_add_u32 %1, %1, %10\n\t" DMA1
    asm volatile("s_mov_b32 %0, m0\n\ts_mov_b32 m0, %9\n\t"
                 "s_cmp_gt_i32 %18, 1\n\ts_waitcnt lgkmcnt(0)\n\ts_cbranch_scc1 1f\n\t"
                 "global_load_dword %2, %12, %13 sc1\n\t"
                 "buffer_load_dwordx4 %3, %14, %17, 0 offen\n\tbuffer_load_dwordx4 %4, %15, %17, 0 offen\n\t"
                 "buffer_load_dwordx4 %5, %16, %17, 0 offen offset:2560\n\tbuffer_load_dwordx4 %6, %15, %17, 0 offen offset:2560\n"
                 "1:\n\t" DMA1 DMAN DMAN DMAN DMAN DMAN DMAN DMAN DMAN DMAN "s_mov_b32 m0, %0\n\t"
                 "s_cmp_eq_u32 %19, 0\n\ts_cbranch_scc1 2f\n\tglobal_store_dwordx4 %20, %21, %13 sc1\n2:\n\t"
#define B0W "s_barrier\n\ts_memrealtime vcc\n\ts_waitcnt vmcnt(10) lgkmcnt(0)\n\ts_sub_u32 %0, vcc_lo, " STR(AOFF)
                 B0W
                 : "=&s"(keep), "+s"(so), "=&v"(ep), "=&v"(xv[0]), "=&v"(xv[1]), "=&v"(xv[2]), "=&v"(xv[3])
                 : "v"(vo), "s"(rw), "s"(dst), "s"(step), "s"(dstep), "v"(zero), "s"(wsq), "v"(oa), "v"(ob), "v"(oc), "s"(rx), "s"(wave - half),
                   "s"(pre), "v"(po), "v"(emp) : "memory", "scc", "vcc");
#undef DMAN
#undef DMA1
    asm volatile("" : "+s"(wsq));
    const __amdgpu_buffer_rsrc_t rs = __builtin_amdgcn_make_buffer_rsrc(wsq, 0, WS_END, 0x00020000);
    if (half) {
      if (wave < 3) {
        u32x4 sv;
#pragma unroll
        for (int k = 0; k < 4; ++k) {
          const float c0 = lo16(xv[0][k]), c1 = hi16(xv[0][k]);
          sv[k] = cvt2(c0 * frcp(1.f + fexp(-c0)), c1 * frcp(1.f + fexp(-c1)));
        }
        if (c + 16 * wave < 40) *reinterpret_cast<u32x4*>(lds + L_XA + r * XH + (c + 16 * wave) * 16) = sv;
        if (t == 0) { cnt[4] = next_tag(ep); cnt[15] = keep; }
        lds_arrive(C_P1, lane);
        STAMPT(2, 0);
      }
    } else {
      if (wave < 2) {
#pragma unroll
        for (int i = 0; i < 2; ++i) {
          const int b = wave + 2 * i;
          float xf[16];
#pragma unroll
          for (int k = 0; k < 4; ++k) { xf[2 * k] = lo16(xv[2 * i][k]); xf[2 * k + 1] = hi16(xv[2 * i][k]); xf[8 + 2 * k] = lo16(xv[2 * i + 1][k]); xf[8 + 2 * k + 1] = hi16(xv[2 * i + 1][k]); }
          float s = 0.f, s2 = 0.f;
#pragma unroll
          for (int k = 0; k < 8; ++k) { s += xf[k]; s2 += xf[8 + k]; }
          if (lane < 16) s += s2;
          const float mean = wave_sum(s) * (1.f / DM);
          float v = 0.f, v2 = 0.f;
#pragma unroll
          for (int k = 0; k < 8; ++k) { const float d = xf[k] - mean; v += d * d; const float d2 = xf[8 + k] - mean; v2 += d2 * d2; }
          if (lane < 16) v += v2;
          v = wave_sum(v);
          if (lane == 0) { st[24 + 2 * b] = bfr(mean); st[24 + 2 * b + 1] = frcp(bfr(fsqrt(bfr(bfr(v * (1.f / DM)) + 1e-5f)))); }
          *reinterpret_cast<u32x4*>(xraw + b * 320 + lane * 4) = xv[2 * i];
          if (lane < 16) *reinterpret_cast<u32x4*>(xraw + b * 320 + 256 + lane * 4) = xv[2 * i + 1];
        }
        if (t == 0) { cnt[4] = next_tag(ep); cnt[15] = keep; }
        lds_arrive(C_P1, lane);
      }
      lds_wait<1>(C_P1, 2);
      STAMPT(20, 0);
      tg = __builtin_amdgcn_readfirstlane(cnt[4]);
      if (CT0 && u < 8 && t == 192) { const u32 t0 = cnt[15]; st_gran(rs, G_T + u * 16, t0, t0, tg); }
      if (wave < 3) {
        const int b = LAY == 2 ? t & 3 : (t >> 2) & 3, j = LAY == 2 ? t >> 2 : (t >> 4) * 4 + (t & 3);
        u32 offs[8];
        u32x4 gm[8];
#pragma unroll
        for (int i = 0; i < 8; ++i) offs[i] = t < 160 ? G_MOD + GIDX(1024, i & 1, b, ((i >> 1) & 1) * 160 + (i >> 2) * 40 + ksp * 80 + j) : OOB;
        const float mean = st[24 + 2 * b], sd = st[24 + 2 * b + 1];
        float nv[8];
#pragma unroll
        for (int e = 0; e < 2; ++e) {
          const u32x2 xw = *reinterpret_cast<const u32x2*>(xraw + b * 320 + ksp * 160 + e * 80 + 2 * j);
#pragma unroll
          for (int k = 0; k < 4; ++k) {
            const float xf = (k & 1) ? hi16(xw[k >> 1]) : lo16(xw[k >> 1]);
            nv[4 * e + k] = bfr(bfr(xf - mean) * sd);
            asm volatile("" : "+v"(nv[4 * e + k]));
          }
        }
        {
          const u32 tt = __builtin_amdgcn_readfirstlane(cnt[15]) + GT;
          SPINLOOP while ((int)((u32)__builtin_amdgcn_s_memrealtime() - tt) < 0) __builtin_amdgcn_s_sleep(1);
        }
        STAMPT(14, 0);
        gather22<8, 4>(rs, offs, gm, tg, GOPOS == 2 ? C_GO : 0);
        STAMPT(8, 0);
#pragma unroll
        for (int e = 0; e < 2; ++e) {
          const f32x4 sa = un_g22(gm[4 * e]) + un_g22(gm[4 * e + 1]);
          const f32x4 ca = un_g22(gm[4 * e + 2]) + un_g22(gm[4 * e + 3]);
          const u32 s01 = cvt2(sa[0], sa[1]), s23 = cvt2(sa[2], sa[3]), c01 = cvt2(ca[0], ca[1]), c23 = cvt2(ca[2], ca[3]);
          const u32 d01 = cvt2(1.f + lo16(c01), 1.f + hi16(c01)), d23 = cvt2(1.f + lo16(c23), 1.f + hi16(c23));
          const u32 p01 = cvt2(nv[4 * e] * lo16(d01), nv[4 * e + 1] * hi16(d01)), p23 = cvt2(nv[4 * e + 2] * lo16(d23), nv[4 * e + 3] * hi16(d23));
          const float hv[4] = {lo16(p01) + lo16(s01), hi16(p01) + hi16(s01), lo16(p23) + lo16(s23), hi16(p23) + hi16(s23)};
          if (t < 160) *reinterpret_cast<u32x2*>(lds + L_XA + b * XH + (e * 40 + j) * 8) = u32x2{cvt2(hv[0], hv[1]), cvt2(hv[2], hv[3])};
        }
      }
    }
    VM0();
    STAMPT(5, 128);
    lds_arrive(C_P2, lane);
    lds_wait<1>(C_P2, 4);
    STAMPT(7, 0);
    tg = __builtin_amdgcn_readfirstlane(cnt[4]);
    {
      f32x4 acc = {0.f, 0.f, 0.f, 0.f}, acc2 = acc;
#pragma unroll
      for (int s = 0; s < 10; s += 2) {
        acc = MFMA(wfrag<512>(L_W + (tr128 ^ (wave * 32)) + s * 4096), *reinterpret_cast<const u32x4*>(lds + L_XA + (c & 3) * XH + s * 64 + r * 16), acc);
        acc2 = MFMA(wfrag<512>(L_W + (tr128 ^ (wave * 32)) + s * 4096 + 4096), *reinterpret_cast<const u32x4*>(lds + L_XA + (c & 3) * XH + s * 64 + 64 + r * 16), acc2);
      }
      if (c < 4 && u < 240 + 16 - 16 * half) st_g22(rs, G_Q + half * (G_MOD - G_Q) + GIDX(1024, ksp, c, cbp * 16 + wave * 4 + r), acc + acc2, tg);
    }
    if (CT0 && half && u < 8 && t == 192) { const u32 t0 = cnt[15]; st_gran(rs, G_T + u * 16, t0, t0, tg); }
    STAMPT(9, 0);
  } else {
    __builtin_amdgcn_s_setprio(2);
    if (t < 256 + 32) cnt[lane] = 0;
    const u32 ko = (u32)(bh * SK + (SK - WIN)) * 256u + (u32)(half * 8 + wave) * (NL * 1024u) + lane * 16;
    STAMPT(30, 448);
#define LK4(a, b, c, d, o) "global_load_dwordx4 " a ", %" #o ", %[k] nt\n\tglobal_load_dwordx4 " b ", %" #o ", %[k] offset:1024 nt\n\t" \
                           "global_load_dwordx4 " c ", %" #o ", %[k] offset:2048 nt\n\tglobal_load_dwordx4 " d ", %" #o ", %[k] offset:3072 nt\n\t"
    asm volatile("s_waitcnt lgkmcnt(0)\n\ts_barrier\n\t"
                 LK4("v[128:131]", "v[132:135]", "v[136:139]", "v[140:143]", 0) LK4("v[144:147]", "v[148:151]", "v[152:155]", "v[156:159]", 1)
                 LK4("v[160:163]", "v[164:167]", "v[168:171]", "v[172:175]", 2)
                 :: "v"(ko + ((u >> 3) & 7) * 4096), "v"(ko + (((u >> 3) + 1) & 7) * 4096),
                   "v"(ko + (((u >> 3) + 2) & 7) * 4096),
                   [k] "s"(kcq) : "memory", "v128", "v129", "v130", "v131", "v132", "v133", "v134", "v135", "v136", "v137", "v138", "v139", "v140", "v141", "v142", "v143", "v144", "v145", "v146", "v147", "v148", "v149", "v150", "v151", "v152", "v153", "v154", "v155", "v156", "v157", "v158", "v159", "v160", "v161", "v162", "v163", "v164", "v165", "v166", "v167", "v168", "v169", "v170", "v171", "v172", "v173", "v174", "v175", "v176", "v177", "v178", "v179", "v180", "v181", "v182", "v183", "v184", "v185", "v186", "v187", "v188", "v189", "v190", "v191");
#undef LK4
    STAMPT(31, 448);
  }
  u32 kvoff = (u32)(bh * SK + (SK - WIN)) * 256u + (u32)(half * 8 + wave) * (NL * 1024u) + lane * 16, ph[8];
#pragma unroll
  for (int i = 0; i < 8; ++i) ph[i] = ((i + (u >> 3)) & 7) * 4096;
  asm volatile("" : "+v"(kvoff));
  asm volatile("s_waitcnt lgkmcnt(0)" : "+s"(wsq), "+s"(ph[0]), "+s"(ph[1]), "+s"(ph[2]), "+s"(ph[3]), "+s"(ph[4]), "+s"(ph[5]), "+s"(ph[6]), "+s"(ph[7])
               : "s"(rw), "s"(rx), "s"(kcq));
  const __amdgpu_buffer_rsrc_t rs = __builtin_amdgcn_make_buffer_rsrc(wsq, 0, WS_END, 0x00020000);
  {
    u32x4 kv[NL], kh[16];
    const __amdgpu_buffer_rsrc_t rk = __builtin_amdgcn_make_buffer_rsrc((void*)a.kc, 0, 1u << 28, 0x00020000);
    const __amdgpu_buffer_rsrc_t rv = __builtin_amdgcn_make_buffer_rsrc((void*)a.vc, 0, 1u << 28, 0x00020000);
#define LDKV(rsrc, off, i) __builtin_amdgcn_raw_buffer_load_b128(rsrc, (off) + ((i) & 3) * 1024, ph[(i) >> 2], 2)
#define LDS_INC(addr) do { asm volatile("" ::: "memory"); if (lane == 0) asm volatile("ds_add_u32 %0, %1" :: "v"(addr), "v"(1u) : "memory"); } while (0)
#define TR_STEP(x, HALF, CTRL, BIT) _Pragma("unroll") for (int hh = 0; hh < (HALF); ++hh) { \
    const float lo_ = x[hh], hi_ = x[hh + (HALF)]; const float t1_ = lo_ + dpp<CTRL>(lo_), t2_ = hi_ + dpp<CTRL>(hi_); \
    x[hh] = (c & (BIT)) ? t2_ : t1_; }
#define SCORE16(x, src, o) float x[16]; \
    _Pragma("unroll") for (int i = 0; i < 16; ++i) { float d = 0.f; _Pragma("unroll") for (int k = 0; k < 4; ++k) d = dot2(src[(o) + i][k], qpk[k], d); x[i] = d; } \
    TR_STEP(x, 8, XOR8, 8) TR_STEP(x, 4, XOR7, 4) TR_STEP(x, 2, XOR2, 2)
#define SCALED(v) bfr(bfr(v) * 0.08838834764831845f)
#define PV2(i) _Pragma("unroll") for (int k = 0; k < 4; ++k) { \
      acc[2 * k] = dot2(__builtin_amdgcn_perm(kv[(i) + 1][k], kv[i][k], 0x05040100u), pw[(i) / 2], acc[2 * k]); \
      acc[2 * k + 1] = dot2(__builtin_amdgcn_perm(kv[(i) + 1][k], kv[i][k], 0x07060302u), pw[(i) / 2], acc[2 * k + 1]); }
#define GET_Q() lds_wait<2>(C_Q, 1); u32x4 qpk; \
    asm volatile("ds_read_b128 %0, %2\n\tds_read_b32 %1, %3\n\ts_waitcnt lgkmcnt(0)" : "=&v"(qpk), "=&v"(tg) : "v"((u32)(L_QS + 16 * c)), "v"((u32)(L_CNT + 16)) : "memory"); \
    tg = __builtin_amdgcn_readfirstlane(tg);
#define GET_P() _Pragma("unroll") for (int i4 = 0; i4 < 4; ++i4) { \
      const u32x4 pv = *reinterpret_cast<const u32x4*>(pT + (wave * 4 + r) * 16 + i4 * 4); \
      _Pragma("unroll") for (int j = 0; j < 4; ++j) pw[i4 * 4 + j] = pv[j]; }
    auto gather_q = [&]() __attribute__((always_inline)) {
        u32 offs[1] = {G_Q + GIDX(1024, lane >> 5, b_att, h_att * 32 + (lane & 31))};
        u32x4 gq[1];
        {
          gq[0] = ld_gran(rs, offs[0]);
          const u32x4 gt = ld_gran(rs, lane < 8 ? G_T + lane * 16 : OOB);
          LDS_INC(C_KI);
          {
            const u32 own = cnt[15];
            const bool ok = lane < 8 && gt[1] == tg;
            float d = ok ? __builtin_amdgcn_fmed3f((float)(int)(gt[0] - own + TOFF), -400.f, 400.f) : -500.f;
            d = fmaxf(d, dpp<XOR1>(d)); d = fmaxf(d, dpp<XOR2>(d)); d = fmaxf(d, dpp<XOR7>(d));
            if (lane == 0 && d > -450.f) cnt[15] = own + (u32)(int)d;
          }
          const bool hit = (((gq[0][1] ^ (tg << 12)) | (gq[0][3] ^ (tg << 12))) < 4096u);
          if (!__all(hit)) gather22<1, 4>(rs, offs, gq, tg);
        }
        STAMPW(10);
        f32x4 q4 = un_g22(gq[0]);
#pragma unroll
        for (int k = 0; k < 4; ++k) q4[k] = swap32_add(q4[k], q4[k]);
        if (lane < 32) *reinterpret_cast<u32x2*>(qs + 2 * lane) = u32x2{cvt2(q4[0], q4[1]), cvt2(q4[2], q4[3])};
        lds_arrive(C_Q, lane);
    };
    const int qw = half ? QODD : QEVEN;
    const u32 tau = half ? (g < 74 ? TAU_A : TAU_B) : (g >= 122 ? TAU_X : TAU_E);
#define PACE(n) if ((n) > PB) { const u32 tt_ = t0 + (((n) - PB) * tau >> 4); \
      SPINLOOP while ((int)((u32)__builtin_amdgcn_s_memrealtime() - tt_) < 0) __builtin_amdgcn_s_sleep(1); asm volatile("" ::: "memory"); }
    float acc[8];
#pragma unroll
    for (int k = 0; k < 8; ++k) acc[k] = 0.f;
    u32 pw[16];
    if (__builtin_expect(wave >= 4, 1)) {
      asm volatile("" : "={v[128:131]}"(kv[0]), "={v[132:135]}"(kv[1]), "={v[136:139]}"(kv[2]), "={v[140:143]}"(kv[3]),
                        "={v[144:147]}"(kv[4]), "={v[148:151]}"(kv[5]), "={v[152:155]}"(kv[6]), "={v[156:159]}"(kv[7])
                        , "={v[160:163]}"(kv[8]), "={v[164:167]}"(kv[9]), "={v[168:171]}"(kv[10]), "={v[172:175]}"(kv[11])
                   );
      u32 t0 = 0;
      STAMPT(24, 448);
      const u32 kvoff2 = kvoff ^ 131072u;
#pragma unroll
      for (int i = NK0; i < 48; ++i) {
        if (i == NPRE) { STAMPT(12, 448); STAMPT(13, 256); }
        if (i == NPRE && half == 0) lds_wait<2>(C_GO, GOPOS ? 3 : 4);
        if (PK && (i & 3) == 0) PACE(i)
        if (i < NL) kv[i < NL ? i : 0] = LDKV(rk, kvoff, i & 31); else kh[i < NL ? 0 : i - NL] = LDKV(rk, kvoff2, i & 31);
      }
      if (!QPRE || wave != qw) LDS_INC(C_KI);
#pragma unroll
      for (int i = 0; i < NK0; i += 4) asm volatile("s_waitcnt vmcnt(%4)" : "+v"(kv[i]), "+v"(kv[i + 1]), "+v"(kv[i + 2]), "+v"(kv[i + 3]) : "i"(48 - NK0) : "memory");
      STAMPT(25, 448);
      if ((QODD >= 4 || QEVEN >= 4) && wave == qw) {
        __builtin_amdgcn_s_setprio(3);
        lds_wait<2>(C_P1, 2 + half);
        tg = __builtin_amdgcn_readfirstlane(cnt[4]);
        gather_q();
        __builtin_amdgcn_s_setprio(2);
      }
      GET_Q()
      SCORE16(xa, kv, 0)
      asm volatile("" : "+v"(xa[0]), "+v"(xa[1]), "+v"(kvoff) :: "memory");
#pragma unroll
      for (int i = 0; i < P_NVA; ++i) kv[i] = LDKV(rv, kvoff, i);
      asm volatile("" ::: "memory");
      SCORE16(xb, kv, 16)
      xa[2] = xb[0]; xa[3] = xb[1];
      TR_STEP(xa, 2, XOR1, 1)
      float s0 = SCALED(xa[0]), s1 = SCALED(xa[1]);
      asm volatile("" : "+v"(s0), "+v"(s1), "+v"(kvoff) :: "memory");
#pragma unroll
      for (int i = P_NVA; i < NVE; ++i) kv[i] = LDKV(rv, kvoff, i);
      asm volatile("" ::: "memory");
      SCORE16(xh, kh, 0)
      const float s2 = SCALED(xh[0] + dpp<XOR1>(xh[0])), s3 = SCALED(xh[1] + dpp<XOR1>(xh[1]));
      const float m = wave_max(fmaxf(fmaxf(s0, s1), fmaxf(s2, s3)));
      const float p0 = __expf(s0 - m), p1 = __expf(s1 - m), p2 = __expf(s2 - m), p3 = __expf(s3 - m);
      const float l = wave_sum(fmaf(p2 + p3, 0.5f, p0 + p1));
      if (lane == 0) { st[wave] = m; st[8 + wave] = l; }
      pT[(wave * 4 + r) * 16 + (c & 1) * 8 + (c >> 1)] = cvt2(p0, p1);
      pT[((wave - 4) * 4 + r) * 16 + (c >> 1)] = cvt2(p2, p3);
      lds_arrive(C_PP + 4 * (wave - 4), lane);
      __builtin_amdgcn_s_setprio(0);
      STAMPT(27, 448); WSTAMP(1);
      if (CT0) t0 = __builtin_amdgcn_readfirstlane(cnt[15]);
      GET_P()
#pragma unroll
      for (int i = NVE; i < NL; i += 2) {
        PACE(96 + i)
        kv[i] = LDKV(rv, kvoff, i);
        kv[i + 1] = LDKV(rv, kvoff, i + 1);
        asm volatile("" ::: "memory");
        if (i == NL - 2) { LDS_INC(C_ISS); STAMPT(26, 448); }
        if (i >= PVLAG) {
          PV2(i >= PVLAG ? i - PVLAG : 0)
#pragma unroll
          for (int k = 0; k < 8; ++k) asm volatile("" : "+v"(acc[k]));
        }
      }
    } else {
      const int ex = g < 74 ? EA : g < 122 ? EB : EC;
      u32 kva = (!half && ex > 2) ? OOB : kvoff, kvb = (!half && ex > 0) ? OOB : kvoff;
      const u32 kxa = (half && ex > 2) ? kvoff - 8 * NL * 1024u : OOB, kxb = (half && ex > 0) ? kvoff - 8 * NL * 1024u : OOB;
      u32x4 kx[4];
      const u32 t0 = __builtin_amdgcn_readfirstlane(cnt[15]);
#pragma unroll
      for (int i = 0; i < 16; ++i) {
        if (PK && (i & 3) == 0) PACE(48 + i)
        kh[i] = LDKV(rk, i < 12 ? kvoff : i < 14 ? kva : kvb, 16 + i);
      }
#pragma unroll
      for (int i = 0; i < 4; ++i) kx[i] = LDKV(rk, i < 2 ? kxa : kxb, 28 + i);
      LDS_INC(C_KI);
      if (wave == 0) {
        if (half == 0 && g >= 80 && g < 120) {
          const int fb = g - 80;
          u32 of2[2];
          u32x4 gm[2];
#pragma unroll
          for (int ks = 0; ks < 2; ++ks) of2[ks] = G_MOD + GIDX(1024, ks, r, 320 + fb * 16 + c);
          gather22<2, 4>(rs, of2, gm, tg);
          const f32x4 mv = un_g22(gm[0]) + un_g22(gm[1]);
          st_gran(rs, G_MF + (((fb / 10) * 4 + r) * 160 + (fb % 10) * 16 + c) * 16, cvt2(mv[0], mv[1]), cvt2(mv[2], mv[3]), tg);
        }
      }
      __builtin_amdgcn_s_setprio(0);
      lds_wait<2>(C_KI, P_GATE);
      if (QHOLD && wave == qw - 4) lds_wait<2>(C_Q, 1);
      asm volatile("" : "+v"(kvoff), "+v"(kva), "+v"(kvb));
#pragma unroll
      for (int i = 0; i < NL; ++i) {
        if (PL && (i & 3) == 0) PACE(64 + PLO + i * PLS / 16)
        kv[i] = LDKV(rv, i < 28 ? kvoff : i < 30 ? kva : kvb, i);
      }
      STAMPT(1, 0);
      GET_Q()
      float xx[4];
#pragma unroll
      for (int i = 0; i < 4; ++i) { float d = 0.f;
#pragma unroll
        for (int k = 0; k < 4; ++k) d = dot2(kx[i][k], qpk[k], d);
        xx[i] = d; }
      asm volatile("" : "+v"(xx[0]), "+v"(xx[1]), "+v"(xx[2]), "+v"(xx[3]) :: "memory");
#pragma unroll
      for (int i = 0; i < 4; ++i) kx[i] = LDKV(rv, i < 2 ? kxa : kxb, 28 + i);
      LDS_INC(C_ISS);
      TR_STEP(xx, 2, XOR8, 8)
#pragma unroll
      for (int i = 0; i < 2; ++i) { xx[i] += dpp<XOR7>(xx[i]); xx[i] += dpp<XOR2>(xx[i]); xx[i] += dpp<XOR1>(xx[i]); }
      SCORE16(xh, kh, 0)
      const float ninf = -__builtin_inff();
      const bool mine = half || c < 16 - ex, extra = half && c >= 16 - 4 * ex;
      xh[0] += dpp<XOR1>(xh[0]); xh[1] += dpp<XOR1>(xh[1]);
      const float s2 = mine ? SCALED(xh[0]) : ninf, s3 = mine ? SCALED(xh[1]) : ninf;
      const float s4 = extra ? SCALED(xx[0]) : ninf, s5 = extra ? SCALED(xx[1]) : ninf;
      float m = wave_max(fmaxf(fmaxf(s2, s3), fmaxf(s4, s5)));
      lds_wait<2>(C_PP + 4 * wave, 1);
      const float me = st[wave + 4];
      m = fmaxf(m, me);
      const float p2 = __expf(s2 - m), p3 = __expf(s3 - m), p4 = __expf(s4 - m), p5 = __expf(s5 - m);
      const float l = wave_sum(fmaf(p4 + p5, 0.25f, p2 + p3)) * 0.5f;
      const u32 pxo = cvt2(p4, p5), pxt = dppu<XOR8>(pxo);
      const u32 px[2] = {c < 8 ? pxo : pxt, c < 8 ? pxt : pxo};
      if (lane == 0) { st[wave] = m; st[8 + wave] = l; }
      pT[(wave * 4 + r) * 16 + 8 + (c >> 1)] = cvt2(p2, p3);
      STAMPT(4, 0); WSTAMP(1);
      if (lds_ticket(C_SC, lane) == 3) {
        const float mw = st[lane & 7], lw = st[8 + (lane & 7)];
        float M = fmaxf(mw, dpp<XOR1>(mw)); M = fmaxf(M, dpp<XOR2>(M)); M = fmaxf(M, dpp<XOR7>(M));
        const float e = __expf(mw - M);
        float L = lw * e;
        L += dpp<XOR1>(L); L += dpp<XOR2>(L); L += dpp<XOR7>(L);
        if (lane == 0) st_gran(rs, G_ML + u * 64, f2u(M), f2u(L), tg);
        if (lane < 8) st[16 + lane] = e * frcp(L);
      }
      GET_P()
#pragma unroll
      for (int i = 0; i < 16; i += 2) { PV2(i) }
      const float f = __expf(me - m);
#pragma unroll
      for (int k = 0; k < 8; ++k) acc[k] *= f;
#pragma unroll
      for (int i = 16; i < NL - PVLAG; i += 2) { PV2(i) }
#pragma unroll
      for (int i = 0; i < 4; i += 2)
#pragma unroll
        for (int k = 0; k < 4; ++k) {
          acc[2 * k] = dot2(__builtin_amdgcn_perm(kx[i + 1][k], kx[i][k], 0x05040100u), px[i / 2], acc[2 * k]);
          acc[2 * k + 1] = dot2(__builtin_amdgcn_perm(kx[i + 1][k], kx[i][k], 0x07060302u), px[i / 2], acc[2 * k + 1]);
        }
    }
#pragma unroll
    for (int i = NL - PVLAG; i <= NL - HX; i += 2) { PV2(i) }
    if (!HT) { PV2(NL - HX + 2) } else {
      u32 tk = 0;
      u64 tn;
      asm volatile("s_mov_b64 exec, 1\n\tds_add_rtn_u32 %0, %10, %11\n\ts_mov_b64 exec, -1\n\ts_memrealtime %1"
                   : "+v"(tk), "=s"(tn), "+v"(acc[0]), "+v"(acc[1]), "+v"(acc[2]), "+v"(acc[3]), "+v"(acc[4]), "+v"(acc[5]), "+v"(acc[6]), "+v"(acc[7])
                   : "v"((u32)C_HT), "v"(1u) : "memory");
      PV2(NL - HX + 2)
      asm volatile("s_waitcnt lgkmcnt(0)" : "+v"(tk), "+s"(tn), "+v"(acc[0]), "+v"(acc[1]), "+v"(acc[2]), "+v"(acc[3]), "+v"(acc[4]), "+v"(acc[5]), "+v"(acc[6]), "+v"(acc[7]));
      __builtin_amdgcn_raw_buffer_store_b64(u32x2{(u32)tn + HTD, tg}, rs, tk == 7 ? G_HT + u * 128 : OOB, 0, 16);
      asm volatile("" ::: "memory");
    }
#pragma unroll
    for (int i = NL - HX + 4; i < NL; i += 2) { PV2(i) }
#undef PV2
#undef TR_STEP
#undef SCORE16
#undef SCALED
#undef GET_Q
#undef GET_P
#undef LDKV
#pragma unroll
    for (int i = 0; i < 4; ++i) acc[i] = swap32_add(acc[i], acc[i + 4]);
#pragma unroll
    for (int i = 0; i < 2; ++i) acc[i] = swap16_add(acc[i], acc[i + 2]);
    *reinterpret_cast<float2*>(accs + wave * 128 + c * 8 + 2 * r) = float2{acc[0], acc[1]};
    STAMPT(6, 0); WSTAMP(2);
  }
  const u32x4 rsv = {(u32)(uintptr_t)wsq, (u32)((uintptr_t)wsq >> 32) & 0xffffu, WS_END, 0x00020000u};
  const int role = half ? (g < 74 ? 0 : 1) : (g >= 122 ? 0 : g < 40 ? 2 : 3);
  const int ia = half ? g : g - 48;
  const int ksA = ia & 7, cbA = ia >> 3;
#define HEADA(hh) ((ksA & 3) + (ksA >> 2) * 16 + (hh) * 4)
  const int iB = g - 74, jB = !JMAP ? iB : iB < 48 ? (iB & ~7) | ((iB & 3) << 1) | ((iB >> 2) & 1) : iB < 50 ? 2 * iB - 48 : iB < 52 ? iB + 2 : 2 * iB - 55;
  u32x4 ml = {0u, 0u, 0u, 0u};
  const u32 blkA = ((c & 3) * 32 + HEADA(wave & 3)) * 2 + ((c >> 2) & 1);
  u32 om = G_ML + blkA * 64;
#define MLOOK(W_) asm volatile(LK(0, 1, "0", 0, 2) LKEND : "+v"(ml) : "v"(om), "s"(m2), [rs] "s"(rsv), [w] "i"(W_) : "memory");
  u64 m2 = role == 0 && wave >= 4 ? 0xffull : 0;
  const u32 tkf = lds_ticket(C_FIN, lane);
  const bool lastw = tkf == 7;
  if (!lastw) {
    MLOOK(63)
#define GO(n, name) "s_cmp_eq_u32 %0, " #n "\n\ts_cbranch_scc1 .Lpad_" #name "\n\t"
    asm volatile("s_mov_b64 exec, 0\n\t" GO(0, a3) GO(1, a2) GO(2, a1) GO(3, a0) GO(8, b3) GO(9, b2) GO(10, b1) GO(11, b0) GO(16, c1) GO(17, c0)
                 ".Lpad_back:\n\ts_mov_b64 exec, -1" :: "s"(WARM ? role * 8 + tkf : 7) : "scc");
#undef GO
  }
  if (lastw) {
    if (lane < 32) {
      f32x4 o = {0.f, 0.f, 0.f, 0.f};
#pragma unroll
      for (int w = 0; w < 8; ++w) o += st[16 + w] * *reinterpret_cast<const f32x4*>(accs + w * 128 + 4 * lane);
      const u32x2 ow = {min(cvt2(o[0], o[1]), EMPTY - 1), min(cvt2(o[2], o[3]), EMPTY - 1)};
      __builtin_amdgcn_raw_buffer_store_b64(ow, rs, G_ATT + u * 256 + lane * 8, 0, 16);
    }
    MLOOK(63)
    LDS_INC(C_PUB);
    STAMPW(11);
  }
  u32 gaw[2];
  u32x2 xw = {0u, 0u};
  float b1v;
  if (role < 2 && wave < 4) {
    if (role == 0) {
      u32 offs[1] = {G_MF + (r * 160 + cbA * 16 + c) * 16};
      u32x4 gm[1];
      if (ksA == 0) xw = *reinterpret_cast<const u32x2*>(a.x.p + r * DM + cbA * 64 + c * 4);
      gather<1>(rs, offs, gm, tg);
      gaw[0] = gm[0][0]; gaw[1] = gm[0][2];
    } else {
      if (MFPOS == 1) lds_wait<2>(C_ISS, 8);
      b1v = bf2f(a.b1[jB * 32 + (t & 31)]);
      u32 offs[5];
      u32x4 gm[5];
#pragma unroll
      for (int i = 0; i < 5; ++i) offs[i] = G_MF + (640 + t + 256 * i) * 16;
      gather<5>(rs, offs, gm, tg);
#pragma unroll
      for (int i = 0; i < 5; ++i) {
        const int it = t + 256 * i, row = it / 160, gi = it % 160;
        u32 w0 = gm[i][0], w1 = gm[i][2];
        if (row >= 4) { w0 = cvt2(1.f + lo16(w0), 1.f + hi16(w0)); w1 = cvt2(1.f + lo16(w1), 1.f + hi16(w1)); }
        *reinterpret_cast<u32x2*>(lds + L_XA + (row < 4 ? row + 8 : row) * XS + gi * 8) = u32x2{w0, w1};
      }
      asm volatile("" : "+v"(b1v));
    }
  }
  if (role < 2 && wave < 4) {
    if (half) STAMPT(8, 0);
    lds_wait<2>(C_ISS, 8);
    if (role == 0) {
      const bf16* wp = a.wo + (size_t)(HEADA(wave) * 128 + (lane >> 3)) * DM + cbA * 64;
#pragma unroll
      for (int i = 0; i < 16; ++i) {
        if (i == 2) PAD(a0, back);
        if (i == N1A) {
          lds_wait<2>(C_PUB, 1);
          __builtin_amdgcn_raw_buffer_store_b128(u32x4{EMPTY, EMPTY, EMPTY, EMPTY}, rs,
              wave < NREP && lane < 32 ? G_WO + wave * WO_REP + ((cbA * 4 + (lane >> 3)) * 8 + ksA) * 128 + (lane & 7) * 16 : OOB, 0, 16);
          asm volatile("" ::: "memory");
        }
        dma16(wp + (size_t)(8 * i) * DM + (s128e ^ ((i & 1) * 32)), L_W + wave * 16384 + i * 1024);
      }
      LDS_INC(C_ISS);
    } else {
      const bf16* wp = a.w1 + (size_t)(wave * 160 + (lane >> 2)) * FF + jB * 32 + ((((((lane >> 1) & 1) ^ (lane >> 5)) << 1) | (lane & 1)) * 8);
#pragma unroll
      for (int i = 0; i < 10; ++i) {
        if (i == 2) PAD(bd, back);
        if (i == N1B) lds_wait<2>(C_PUB, 1);
        dma16(wp + (size_t)(16 * i) * FF, L_W + (wave * 10 + i) * 1024);
      }
      const bf16* wp2 = a.w2 + (size_t)(jB * 32 + wave * 8 + (lane >> 3)) * DM + (s128e ^ ((wave & 1) * 32));
#pragma unroll
      for (int k = 0; k < 10; ++k) {
        if (k + 10 == N1B) lds_wait<2>(C_PUB, 1);
        dma16(wp2 + k * 64, L_WM2 + (k * 4 + wave) * 1024);
      }
    }
    STAMPT(16, 0);
  }
#define SELECTOR(sel) u32x4 sel; \
    _Pragma("unroll") for (int d = 0; d < 4; ++d) sel[d] = ((c & 2) == 0 && d == (c >> 2)) ? ((c & 1) ? 0x3f800000u : 0x00003f80u) : 0u;
#define PAIRSUM(sm) (((c & 8) ? sm[1] : sm[0]) + dpp<XOR8>((c & 8) ? sm[0] : sm[1]))
  const u32 tb = __builtin_amdgcn_readfirstlane(cnt[15]);
  if (role == 0) {
    PAD(a1, back);
    u32 a_x = L_XA + ((wave & 3) * 8 + (c & 7)) * 272 + r * 16;
    if (wave < 4) {
      PAD(a2, back);
      float ga4[4] = {lo16(gaw[0]), hi16(gaw[0]), lo16(gaw[1]), hi16(gaw[1])}, x4[4] = {lo16(xw[0]), hi16(xw[0]), lo16(xw[1]), hi16(xw[1])};
      u32 a_red = L_RED + ((wave * 4 + c) * 64 + r * 4) * 4, a_sum = L_RED + lane * 16, a_sc = L_ST + (32 + wave * 8 + (c & 7)) * 4;
      u32 a_out = wave < NREP ? G_WO + wave * WO_REP + ((cbA * 4 + (c >> 2)) * 8 + ksA) * 128 + r * 32 + (c & 3) * 8 : OOB;
      u32 a_w[4];
#pragma unroll
      for (int nt = 0; nt < 4; ++nt) a_w[nt] = L_W + wave * 16384 + (tr128 ^ (nt * 32));
      asm volatile("" : "+v"(ga4[0]), "+v"(ga4[1]), "+v"(ga4[2]), "+v"(ga4[3]), "+v"(x4[0]), "+v"(x4[1]), "+v"(x4[2]), "+v"(x4[3]));
      asm volatile("s_waitcnt vmcnt(%0)" :: "i"(AW) : "memory");
      LDS_INC(C_PP + 4 * wave);
      asm volatile("s_waitcnt vmcnt(4)" : "+v"(a_red), "+v"(a_sum), "+v"(a_sc), "+v"(a_out), "+v"(a_x), "+v"(a_w[0]), "+v"(a_w[1]), "+v"(a_w[2]), "+v"(a_w[3]) :: "memory");
      s16x8 wf[4][4];
#pragma unroll
      for (int s = 0; s < 3; ++s)
#pragma unroll
        for (int nt = 0; nt < 4; ++nt) wf[s][nt] = wfrag<512>(a_w[nt] + s * 4096);
      announce(rs, G_HA + ia * 128, C_GO, C_W, 8, tb, HAW, tg);
      STAMPT(19, 0);
      lds_wait<1>(C_PP + 4 * wave, 3);
      STAMPT(12, 0);
      PAD(a3, back);
      u32x4 av[4];
#pragma unroll
      for (int s = 0; s < 4; ++s) av[s] = *reinterpret_cast<const u32x4*>(lds + a_x + s * 64);
      const float wsc = *reinterpret_cast<const float*>(lds + a_sc);
      asm volatile("s_waitcnt vmcnt(1)" ::: "memory");
      STAMPT(23, 0);
#pragma unroll
      for (int nt = 0; nt < 4; ++nt) wf[3][nt] = wfrag<512>(a_w[nt] + 3 * 4096);
      __builtin_amdgcn_sched_barrier(0);
      f32x4 acc[4];
#pragma unroll
      for (int nt = 0; nt < 4; ++nt) acc[nt] = f32x4{0.f, 0.f, 0.f, 0.f};
#pragma unroll
      for (int s = 0; s < 4; ++s)
#pragma unroll
        for (int nt = 0; nt < 4; ++nt) acc[nt] = MFMA(wf[s][nt], av[s], acc[nt]);
#pragma unroll
      for (int nt = 0; nt < 4; ++nt) { acc[nt] *= wsc; asm("" : "+v"(acc[nt])); }
#pragma unroll
      for (int nt = 0; nt < 4; ++nt)
#pragma unroll
        for (int k = 0; k < 4; ++k) { float v = acc[nt][k] + dpp<SHL4>(acc[nt][k]); asm("" : "+v"(v)); acc[nt][k] = v; }
      asm volatile("" : "+v"(acc[0]), "+v"(acc[1]), "+v"(acc[2]), "+v"(acc[3]));
      if (c < 4) {
#pragma unroll
        for (int nt = 0; nt < 4; ++nt) *reinterpret_cast<f32x4*>(lds + a_red + nt * 64) = acc[nt];
      }
      BAR();
      STAMPT(21, 0);
      f32x4 pp[4];
#pragma unroll
      for (int w = 0; w < 4; ++w) pp[w] = *reinterpret_cast<const f32x4*>(lds + a_sum + w * 1024);
      asm volatile("" : "+v"(pp[0]), "+v"(pp[1]), "+v"(pp[2]), "+v"(pp[3]));
      const f32x4 s = (pp[0] + pp[1]) + (pp[2] + pp[3]);
      const u32x2 o = {min(cvt2(fmaf(s[0], ga4[0], x4[0]), fmaf(s[1], ga4[1], x4[1])), EMPTY - 1), min(cvt2(fmaf(s[2], ga4[2], x4[2]), fmaf(s[3], ga4[3], x4[3])), EMPTY - 1)};
      __builtin_amdgcn_raw_buffer_store_b64(o, rs, a_out, 0, 16);
    } else {
      u32 oa = G_ATT + blkA * 256 + (c >> 3) * 64 + r * 16;
      a_x += (c >> 3) * 64;
      u32x4 av[2];
      u64 m0 = ~0ull, m1 = ~0ull;
      av[0] = av[1] = u32x4{0u, 0u, 0u, 0u};
      asm volatile("s_waitcnt vmcnt(0)" : "+v"(oa), "+v"(a_x), "+v"(ml), "+v"(av[0]), "+v"(av[1]) :: "memory");
      SPINLOOP for (unsigned spins = 0; spins < SPIN_CAP; ++spins) {
        m2 &= __builtin_amdgcn_ballot_w64(ml[1] != tg) | __builtin_amdgcn_ballot_w64(ml[3] != tg);
        if (!m2) break;
        if (!spins) FAIL(4);
        __builtin_amdgcn_s_sleep(ASL);
        MLOOK(0)
      }
      {
        const float M = u2f(ml[0]), L = u2f(ml[2]), Mo = dpp<XOR7>(dpp<0x1B>(M)), Lo = dpp<XOR7>(dpp<0x1B>(L));
        const float Mx = fmaxf(M, Mo), le = L * __expf(M - Mx);
        const float wsc = le * frcp(fmaf(Lo, __expf(Mo - Mx), le));
        if (lane < 8) st[wave * 8 + lane] = wsc;
      }
#define ALOOK(W_) \
        asm volatile(LK(0, 2, "0", 0, 3) LK(1, 2, "0", 128, 4) LKEND \
                     : "+v"(av[0]), "+v"(av[1]) : "v"(oa), "s"(m0), "s"(m1), [rs] "s"(rsv), [w] "i"(W_) : "memory"); \
        m0 = __builtin_amdgcn_ballot_w64(missing(av[0])); \
        m1 = __builtin_amdgcn_ballot_w64(missing(av[1]));
      lds_wait<1>(C_PP + 4 * (wave - 4), 2);
      STAMPT(29, 448);
      ALOOK(0)
      STAMPT(18, 448);
      if (m0 | m1) {
        FAIL(1);
        SPINLOOP for (unsigned spins = 0; spins < SPIN_CAP; ++spins) {
          ALOOK(2 * (PDA - 1))
          if (!(m0 | m1)) break;
          __builtin_amdgcn_s_sleep(ASL);
        }
      }
#undef ALOOK
      asm volatile("" : "+v"(av[0]), "+v"(av[1]));
      *reinterpret_cast<u32x4*>(lds + a_x) = av[0];
      *reinterpret_cast<u32x4*>(lds + a_x + 128) = av[1];
      LDS_INC(C_PP + 4 * (wave - 4));
      STAMPT(22, 448);
      announce(rs, G_HA + ia * 128, C_GO, C_W, 8, tb, HAG, tg);
      BAR();
      asm volatile("" :: "v"(av[0]), "v"(av[1]));
    }
    STAMPT(13, 0);

  } else if (role == 1) {
    PAD(b0, bd);
    const int lb = (c >> 1) & 3, lc = (c & 1) * 8 + 2 * r;
    const int fc = lane & 31, hw = lane >> 5;
    SELECTOR(sel)
    constexpr int L_R2 = L_MOD, L_S2 = L_R2 + 4096;
    const u32 gw = G_WO + (jB % NREP) * WO_REP + wave * 5120;
    u32 go = gw + (c >> 3) * 1024 + r * 128 + (c & 7) * 16, go4 = gw + 4096 + lane * 16;
    u32 gl = G_HA + (((5 * wave + (lane & 8 ? 4 : 0)) >> 2) * 8 + (lane & 7)) * 128;
    u32 a_u = L_XA + wave * 640 + lb * 160 + ((c >> 3) * 16 + lc) * 2, a_u4 = L_XA + wave * 640 + lb * 160 + (64 + lc + (c >> 3)) * 2;
    u32 a_av = L_XA + wave * 640 + (c & 3) * 160 + r * 16, a_ah = a_av + 128 - r * 8;
    u32 a_red = L_R2 + ((wave * 4 + c) * 32 + r * 4) * 4;
    u32 a_st = L_S2 + ((lb * 2 + (c >> 3)) * 8 + wave) * 4;
    u32 a_fin = L_R2 + ((hw * 16 + (wave & 3)) * 32 + fc) * 4;
    u32 a_fst = L_S2 + (wave & 3) * 64;
    u32 a_out = c < 4 ? G_OUT + (wave * 5 * 54 + jB) * 128 + c * 32 + r * 8 : OOB;
    constexpr int L_F2 = L_PT;
    u32 a_fi = BM ? L_F2 + c * 64 + r * 16 : L_FI + (c & 3) * 64 + r * 16, a_fo = (BM ? L_F2 + 768 : L_FI) + ((wave & 3) * 32 + fc) * 2;
    u32 a_ou4 = G_OUT + ((wave * 5 + (c >> 2)) * 54 + jB) * 128 + (c & 3) * 32 + r * 8;
    asm volatile("" : "+v"(a_ou4));
    u32 a_pre = lane < 40 ? G_OUT + ((wave * 5 + (lane >> 3)) * 54 + jB) * 128 + (lane & 7) * 16 : OOB;
    u32x4 emp = {EMPTY, EMPTY, EMPTY, EMPTY};
    asm volatile("" : "+v"(a_pre), "+v"(emp));
#define OPRE() asm volatile("buffer_store_dwordx4 %0, %1, %2, 0 offen sc1" :: "v"(emp), "v"(a_pre), "s"(rsv) : "memory")
    if (OPOS == 0) OPRE();
    u32 a_w1 = L_W + wave * 5120 + (8 * r + (c >> 2)) * 64 + (r & 1) * 32 + (c & 3) * 8;
    u32 a_wh = L_W + wave * 5120 + 4096 + (4 * r + (c >> 2)) * 64 + (r >> 1) * 32 + (c & 3) * 8;
    u32 a_w2[5];
#pragma unroll
    for (int k = 0; k < 5; ++k) { const int n2 = wave * 5 + k; a_w2[k] = L_WM2 + (n2 >> 2) * 4096 + (tr128 ^ ((n2 & 3) * 32)); }
    asm volatile("" : "+v"(sel), "+v"(go), "+v"(go4), "+v"(gl), "+v"(a_u), "+v"(a_u4), "+v"(a_av), "+v"(a_ah), "+v"(a_red), "+v"(a_st), "+v"(a_fin), "+v"(a_fst), "+v"(a_out));
    asm volatile("s_waitcnt vmcnt(%9)" : "+v"(a_fi), "+v"(a_fo), "+v"(a_w1), "+v"(a_wh), "+v"(a_w2[0]), "+v"(a_w2[1]), "+v"(a_w2[2]), "+v"(a_w2[3]), "+v"(a_w2[4]) : "i"(OPOS == 0 ? 11 : 10) : "memory");
    STAMPT(23, 0);
    PAD(b1, back);
    BAR();
    STAMPT(12, 0);
    if (OPOS == 1) OPRE();
    if (BM) *reinterpret_cast<u32*>(lds + L_F2 + t * 4) = 0;
    s16x8 wf[2][2], wg[5];
    s16x4 wh[2];
#pragma unroll
    for (int nt = 0; nt < 2; ++nt) {
#pragma unroll
      for (int s = 0; s < 2; ++s) wf[s][nt] = wfrag<256>((a_w1 ^ (nt * 32)) + s * 2048);
      wh[nt] = __builtin_amdgcn_ds_read_tr16_b64_v4i16((lds_s4p)(uintptr_t)(a_wh ^ (nt * 32)));
    }
    float c1f[5];
    {
      const unsigned char* cp = lds + L_XA + (4 + lb) * XS + wave * 160;
#pragma unroll
      for (int i = 0; i < 2; ++i) { const u32 w = *reinterpret_cast<const u32*>(cp + (32 * i + (c >> 3) * 16 + lc) * 2); c1f[2 * i] = lo16(w); c1f[2 * i + 1] = hi16(w); }
      c1f[4] = u2f((u32)*reinterpret_cast<const unsigned short*>(cp + (64 + lc + (c >> 3)) * 2) << 16);
    }
#define W1MMA(acc) \
      f32x4 acc[2]; \
      _Pragma("unroll") for (int nt = 0; nt < 2; ++nt) { const f32x4 z_ = {0.f, 0.f, 0.f, 0.f}; acc[nt] = MFMA(wf[0][nt], av0, z_); } \
      _Pragma("unroll") for (int nt = 0; nt < 2; ++nt) acc[nt] = MFMA(wf[1][nt], av1, acc[nt]); \
      _Pragma("unroll") for (int nt = 0; nt < 2; ++nt) acc[nt] = __builtin_amdgcn_mfma_f32_16x16x16bf16_1k(wh[nt], av2, acc[nt], 0, 0, 0);
    {
      const unsigned char* p = lds + L_XA + (4 + (c & 7)) * XS + wave * 160;
      const u32x4 av0 = *reinterpret_cast<const u32x4*>(p + r * 16), av1 = *reinterpret_cast<const u32x4*>(p + 64 + r * 16);
      const s16x4 av2 = *reinterpret_cast<const s16x4*>(p + 128 + r * 8);
      W1MMA(acc)
#pragma unroll
      for (int nt = 0; nt < 2; ++nt) if (c < 8) *reinterpret_cast<f32x4*>(red + (wave * 8 + c) * 32 + nt * 16 + r * 4) = acc[nt];
    }
    float Tc = 0.f, Ts = 0.f;
#define TSUM() _Pragma("unroll") for (int k = 0; k < 4; ++k) { Tc += red[((hw * 4 + k) * 8 + (wave & 3)) * 32 + fc]; Ts += red[((hw * 4 + k) * 8 + 4 + (wave & 3)) * 32 + fc]; }
    u32x4 pa[5];
    f32x4 s4, s0, s1;
    {
      u64 m[5], old = 0;
#pragma unroll
      for (int i = 0; i < 5; ++i) { m[i] = ~0ull; pa[i] = u32x4{0u, 0u, 0u, 0u}; }
#pragma unroll
      for (int i = 0; i < 5; ++i) asm volatile("" : "+v"(c1f[i]), "+v"(pa[i]));
      u32x2 sg;
      if (BFW >= 0 && wave < 4) asm volatile("s_waitcnt vmcnt(%0)" :: "i"(BFW) : "memory");
      if (BFW >= 0) BAR();
      heads_up_start(rsv, gl, 0xffffull, tg, sg);
      VM0();
      BAR();
      STAMPT(29, 0);
#define BLOOK(W_) \
        asm volatile(LK(0, 5, "0", 0, 7) LK(1, 5, "0", 512, 8) LK(2, 5, "0", 2048, 9) LK(3, 5, "0", 2560, 10) LK(4, 6, "0", 0, 11) LKEND \
                     : "+v"(pa[0]), "+v"(pa[1]), "+v"(pa[2]), "+v"(pa[3]), "+v"(pa[4]) \
                     : "v"(go), "v"(go4), "s"(m[0]), "s"(m[1]), "s"(m[2]), "s"(m[3]), "s"(m[4]), [rs] "s"(rsv), [w] "i"(W_) : "memory");
      heads_up<0>(rsv, gl, 0xffffull, tg, sg);
      heads_up_time(0xffffull, sg);
      STAMPT(21, 0);
      BLOOK(63)
      announce(rs, G_HB + jB * 128, C_GO, C_W, 8, tb, HCD, tg);
#pragma unroll
      for (int k = 0; k < 5; ++k) wg[k] = wfrag<512>(a_w2[k]);
      PAD(b2, back);
      TSUM()
#pragma unroll
      for (int i = 0; i < 5; ++i) asm volatile("" : "+v"(wg[i]));
      asm volatile("" : "+v"(Tc), "+v"(Ts));
      STAMPT(18, 0);
      asm volatile("s_waitcnt vmcnt(1)" : "+v"(pa[0]), "+v"(pa[1]), "+v"(pa[2]), "+v"(pa[3]), "+v"(pa[4]) :: "memory");
      STAMPT(22, 0);
#define BSUM() { const f32x4 z = {0.f, 0.f, 0.f, 0.f}; \
      s4 = MFMA(sel, pa[4], z); s0 = MFMA(sel, pa[0], z); s1 = MFMA(sel, pa[2], z); s0 = MFMA(sel, pa[1], s0); s1 = MFMA(sel, pa[3], s1); }
      SPINLOOP for (unsigned spins = 0; spins < SPIN_CAP; ++spins) {
        BSUM()
        if (!(__builtin_amdgcn_ballot_w64(__builtin_isunordered(s4[0], s4[1])) | __builtin_amdgcn_ballot_w64(__builtin_isunordered(s0[0], s0[1])) |
              __builtin_amdgcn_ballot_w64(__builtin_isunordered(s1[0], s1[1])))) break;
        u64 bad = 0;
#pragma unroll
        for (int i = 0; i < 5; ++i) { m[i] = __builtin_amdgcn_ballot_w64(missing(pa[i])); bad |= m[i]; }
        if (!(bad | old)) break;
        old = bad;
        if (!spins) FAIL(2);
        BLOOK(5 * (PDB - 1))
      }
    }
    STAMPT(14, 0); WSTAMP(3);
    float xf[5];
    {
      float e0 = s4[0], e1 = s4[1];
      asm("" : "+v"(e0), "+v"(e1));
      xf[4] = bfr(((c & 8) ? e1 : e0) + dpp<XOR8>((c & 8) ? e0 : e1));
      *reinterpret_cast<unsigned short*>(lds + a_u4) = (unsigned short)cvt2(xf[4] * c1f[4], 0.f);
      const u32 w0 = cvt2(s0[0], s0[1]), w1 = cvt2(s1[0], s1[1]);
      xf[0] = lo16(w0); xf[1] = hi16(w0); xf[2] = lo16(w1); xf[3] = hi16(w1);
      *reinterpret_cast<u32*>(lds + a_u) = cvt2(xf[0] * c1f[0], xf[1] * c1f[1]);
      *reinterpret_cast<u32*>(lds + a_u + 64) = cvt2(xf[2] * c1f[2], xf[3] * c1f[3]);
    }
    STAMPT(19, 0);
    {
      u32x4 av0 = *reinterpret_cast<const u32x4*>(lds + a_av), av1 = *reinterpret_cast<const u32x4*>(lds + a_av + 64);
      s16x4 av2 = *reinterpret_cast<const s16x4*>(lds + a_ah);
      __builtin_amdgcn_sched_barrier(0);
      const float ps = ((xf[0] + xf[1]) + (xf[2] + xf[3])) + xf[4];
      const float pq = fmaf(xf[0], xf[0], fmaf(xf[1], xf[1], xf[4] * xf[4])) + fmaf(xf[2], xf[2], xf[3] * xf[3]);
      const float t1 = ps + dpp<XOR8>(ps), t2 = pq + dpp<XOR8>(pq);
      float z = (c & 8) ? t2 : t1;
      z += dpp<XOR1>(z);
      asm volatile("" : "+v"(z));
      __builtin_amdgcn_sched_barrier(0);
      W1MMA(acc)
      z = cross_rows_sum(z);
      if ((lane & 49) == 0) *reinterpret_cast<float*>(lds + a_st) = z;
      if (c < 4) {
#pragma unroll
        for (int nt = 0; nt < 2; ++nt) *reinterpret_cast<f32x4*>(lds + a_red + nt * 64) = acc[nt];
      }
    }
#undef W1MMA
    PAD(b3, back);
    BAR();
    STAMPT(24, 0);
    if (wave < 4) {
      f32x4 sv[4];
      float sp[4];
#pragma unroll
      for (int k = 0; k < 4; ++k) sv[k] = *reinterpret_cast<const f32x4*>(lds + a_fst + k * 16);
#pragma unroll
      for (int k = 0; k < 4; ++k) sp[k] = *reinterpret_cast<const float*>(lds + a_fin + k * 512);
      __builtin_amdgcn_sched_barrier(0);
      const f32x4 s1 = sv[0] + sv[1], s2 = sv[2] + sv[3];
      const float mean = ((s1[0] + s1[1]) + (s1[2] + s1[3])) * (1.f / DM);
      const float var = fmaxf(((s2[0] + s2[1]) + (s2[2] + s2[3])) * (1.f / DM) - mean * mean, 0.f);
      const float rstd = __builtin_amdgcn_rsqf(var + 1e-5f);
      const float zp = fmaf(rstd, fmaf(-mean, Tc, (sp[0] + sp[1]) + (sp[2] + sp[3])), Ts);
      const float hh = bfr(bfr(swap32_add(zp, zp)) + b1v);
      const float e2 = __builtin_amdgcn_exp2f(hh * fmaf(hh * hh, 0.1029432397f, 2.302208198f));
      const float f = fmaf(-hh, frcp(e2 + 1.f), hh);
      *reinterpret_cast<unsigned short*>(lds + a_fo) = (unsigned short)cvt2(f, 0.f);
    }
    BAR();
    if (BM) {
      u32x4 av[4];
#pragma unroll
      for (int k = 0; k < 4; ++k) av[k] = *reinterpret_cast<const u32x4*>(lds + a_fi + (3 - k) * 256);
      const f32x4 z = {0.f, 0.f, 0.f, 0.f};
      f32x4 a4 = MFMA(wg[4], av[0], z), a0 = MFMA(wg[0], av[0], z);
#pragma unroll
      for (int k = 1; k < 4; ++k) a0 = MFMA(wg[k], av[k], a0);
      __builtin_amdgcn_sched_barrier(0);
      const u32x2 o4 = {min(cvt2(a4[0], a4[1]), EMPTY - 1), min(cvt2(a4[2], a4[3]), EMPTY - 1)};
      __builtin_amdgcn_raw_buffer_store_b64(o4, rs, a_out, 4 * (54 * 128), 16);
      const u32x2 o0 = {min(cvt2(a0[0], a0[1]), EMPTY - 1), min(cvt2(a0[2], a0[3]), EMPTY - 1)};
      __builtin_amdgcn_raw_buffer_store_b64(o0, rs, a_ou4, 0, 16);
    } else {
      const u32x4 av = *reinterpret_cast<const u32x4*>(lds + a_fi);
      f32x4 acc[5];
#pragma unroll
      for (int k = 0; k < 5; ++k) { const f32x4 z = {0.f, 0.f, 0.f, 0.f}; acc[k] = MFMA(wg[k], av, z); }
      __builtin_amdgcn_sched_barrier(0);
#pragma unroll
      for (int k = 0; k < 5; ++k) {
        const u32x2 o = {min(cvt2(acc[k][0], acc[k][1]), EMPTY - 1), min(cvt2(acc[k][2], acc[k][3]), EMPTY - 1)};
        __builtin_amdgcn_raw_buffer_store_b64(o, rs, a_out, k * (54 * 128), 16);
      }
    }
    STAMPT(15, 0);
    asm volatile("" :: "v"(pa[0]), "v"(pa[1]), "v"(pa[2]), "v"(pa[3]), "v"(pa[4]));

  } else if (role == 2 && wave == 0) {
    PAD(c0, back);
    const int lb = (c >> 1) & 3, col = g * 16 + (c & 1) * 8 + 2 * r + (c >> 3);
    bf16* outp;
    asm volatile("s_load_dwordx2 %0, %1, 128\n\ts_waitcnt lgkmcnt(0)" : "=s"(outp) : "s"(__builtin_amdgcn_kernarg_segment_ptr()));
    const __amdgpu_buffer_rsrc_t ro = __builtin_amdgcn_make_buffer_rsrc(outp, 0, NB * DM * 2, 0x00020000);
#define DUMMY() __builtin_amdgcn_raw_buffer_store_b16((unsigned short)0, ro, (lb * DM + col) * 2, 0, DAUX)
    if (DPOS == 0) DUMMY();
    float b2v = bf2f(a.b2[col]);
    const f32x4 z = {0.f, 0.f, 0.f, 0.f};
    SELECTOR(sel)
    float x1v, gbv;
    {
      u32 offs[1] = {G_MF + ((12 + lb) * 160 + (col >> 2)) * 16};
      u32x4 gm[1];
      gather<1>(rs, offs, gm, tg);
      const u32 gbw = (r & 1) ? gm[0][2] : gm[0][0];
      gbv = (c & 8) ? hi16(gbw) : lo16(gbw);
    }
    u32 oo = lb * DM + col;
    asm volatile("" : "+v"(gbv), "+v"(oo), "+v"(sel), "+v"(b2v));
    u32x4 pa[7];
    u32 go = G_OUT + g * (54 * 128) + lane * 16, gx = G_WO + g * 1024 + lane * 16;
#pragma unroll
    for (int i = 0; i < 7; ++i) { pa[i] = u32x4{0u, 0u, 0u, 0u}; asm volatile("" : "+v"(pa[i])); }
    asm volatile("" : "+v"(go), "+v"(gx));
    u32x2 hv;
    if (CPUB) lds_wait<2>(C_PUB, 1);
    if (DPOS == 1) DUMMY();
    heads_up_start(rsv, G_HA + ((g >> 2) * 8 + lane) * 128, 0xffull, tg, hv);
    heads_up<CSL>(rsv, G_HA + ((g >> 2) * 8 + lane) * 128, 0xffull, tg, hv);
    if (DPOS == 2) DUMMY();
    if (EPE == 1) __builtin_amdgcn_raw_buffer_store_b32(tg, rs, u == 0 && lane == 0 ? WS_EPOCH : OOB, 0, 16);
    hv[0] += CXD;
    heads_up_time(0xffull, hv);
    {
      u32x4 gm[1];
      gather_e<1, 2, 1>(rsv, gx, gm, ~0ull);
      const f32x4 sm = MFMA(sel, gm[0], z);
      x1v = bfr(PAIRSUM(sm));
      asm volatile("" : "+v"(x1v));
    }
    STAMPT(23, 0);
    if (DPOS == 3) DUMMY();
    if (EPE == 2) __builtin_amdgcn_raw_buffer_store_b32(tg, rs, u == 0 && lane == 0 ? WS_EPOCH : OOB, 0, 16);
    PAD(c1, back);
    heads_up_start(rsv, G_HB + lane * 128, (1ull << 54) - 1, tg, hv);
    heads_up<0>(rsv, G_HB + lane * 128, (1ull << 54) - 1, tg, hv);
    heads_up_time((1ull << 54) - 1, hv);
    STAMPT(14, 0);
    f32x4 s0, s1;
    {
      u64 m[7], old = 0;
#pragma unroll
      for (int i = 0; i < 7; ++i) m[i] = i < 6 ? ~0ull : (1ull << 48) - 1;
      look<0>(rsv, go, pa, m);
#define CSUM() { s0 = MFMA(sel, pa[0], z); s1 = MFMA(sel, pa[1], z); \
      _Pragma("unroll") for (int i = 2; i < 7; ++i) { if (i & 1) s1 = MFMA(sel, pa[i], s1); else s0 = MFMA(sel, pa[i], s0); } \
      s0 += s1; }
      SPINLOOP for (unsigned spins = 0; spins < SPIN_CAP; ++spins) {
        CSUM()
        if (!__builtin_amdgcn_ballot_w64(__builtin_isunordered(s0[0], s0[1]))) break;
        u64 bad = 0;
#pragma unroll
        for (int i = 0; i < 7; ++i) { m[i] = __builtin_amdgcn_ballot_w64(missing(pa[i])); bad |= m[i]; }
        if (!(bad | old)) break;
        old = bad;
        if (!spins) FAIL(3);
        look<7 * (PDC - 1)>(rsv, go, pa, m);
      }
    }
    STAMPT(16, 0);
    const float y = bfr(bfr(PAIRSUM(s0)) + b2v);
    if (OUTM & 1) __builtin_amdgcn_raw_buffer_store_b16((unsigned short)cvt2(x1v + bfr(gbv * y), 0.f), ro, oo * 2, 0, FAUX);
    else outp[oo] = __float2bfloat16(x1v + bfr(gbv * y));
    STAMPT(17, 0);
    asm volatile("" :: "v"(pa[0]), "v"(pa[1]), "v"(pa[2]), "v"(pa[3]), "v"(pa[4]), "v"(pa[5]), "v"(pa[6]), "v"(hv));

    if (!EPE && u == 0 && t == 0)
      __hip_atomic_store(reinterpret_cast<u32*>(a.ws + WS_EPOCH), tg, __ATOMIC_RELAXED, __HIP_MEMORY_SCOPE_AGENT);
  }
  else if (EPE == 3 && u == 80 && wave == 0) {
    u32x2 hv;
    lds_wait<2>(C_PUB, 1);
    heads_up_start(rsv, G_HA + lane * 128, 0xffull, tg, hv);
    heads_up<8>(rsv, G_HA + lane * 128, 0xffull, tg, hv);
    __builtin_amdgcn_raw_buffer_store_b32(tg, rs, lane < 32 ? WS_EPOCH + lane * 4 : OOB, 0, 16);
  }
  STAMP_FLUSH;
}

extern "C" void kernel_launch(void* const* d_in, const int* in_sizes, int n_in, void* d_out, int out_size, void* d_ws,
                              size_t ws_size, hipStream_t stream) {
  Args a;
  a.x = Desc{(const bf16*)d_in[0], NB * DM * 2, 0x00020000u};
  a.kc = (const bf16*)d_in[1];
  a.vc = (const bf16*)d_in[2];
  a.wq = Desc{(const bf16*)d_in[3], DM * QD * 2, 0x00020000u};
  a.wo = (const bf16*)d_in[4];
  a.w1 = (const bf16*)d_in[6];
  a.b1 = (const bf16*)d_in[7];
  a.w2 = (const bf16*)d_in[8];
  a.b2 = (const bf16*)d_in[9];
  a.c = Desc{(const bf16*)d_in[10], NB * DM * 2, 0x00020000u};
  a.wmod = Desc{(const bf16*)d_in[11], DM * MODN * 2, 0x00020000u};
  a.out = (bf16*)d_out;
  a.ws = (unsigned char*)d_ws;
  mega_kernel<<<NWG, NT, 0, stream>>>(a);
}
```

```cpp
#define SPINLOOP _Pragma("nounroll")
#define P_LAG 14
#define P_NPRE 32
#define P_NVE 6
#define P_NVA 6
#define QODD 4
#define QEVEN 4
#define MFPOS 0
#define EA 0
#define EB 0
#define EC 4
#define APOLL 0
#define N1E N1A
#define TAU_A 234
#define TAU_B 243
#define TAU_X TAU_A
#define TAU_E 243
#define PK 0
#define PL 1
#define QPRE 1
#define QHOLD 0
#define CT0 1
#define TOFF 55
#define BLIGHT 1
#define BPRE 0
#define BW1 1
#define BMASK 1
#define CSLEEP 1
#define CSL 2
#define CPUB 1
#define CXD 20
#define OPOS 1
#define NREPL 1
#define OUTPRE 1
#define ASL 1
#define AEARLY 1
#define PDC 4
#define PDA 3
#define PDB 3
#define PSB 1
#define HC 1
#define HCD 120
#define HAG 60
#define AW 4
#define BFW -1
#define FULLF 1
#define WARM 1
#define HT 0
#define HX 10
#define HTD 55
#define HSL 1
#define NANT 1
#define HAW 65
#define BM 1
#define HPD 1
#define BT 0
#define BSL 2
#define PB 0
#define GOPOS 2
#define JMAP 1
#define N1A 10
#define N1B 5
#define PT 0
#define PTAUX 16
#define LAY 1
#define OUTM 1
#define EPE 0
#define DPOS 2
#define DAUX 16
#define FAUX 16
#define KOUT 1
#define NK0 12
#define GT 540
#define PLO 0
#define PLS 16
#define ANCH 1
#define AOFF 55
#define AGG 1
#define PHW 0
#define CPF 1
#define CLEN 0x7f00
#define AGT 0
#define GPAD 1
#define MFE 1
#define E3POS 10
#define E4POS 22
#define L1POS 28
#define L4POS 4
#define T1POS 18
#define HEARLY 0
#define BAR2L 0
#define ROT1 5
#define W2F 0
#define HGT 300
#define P_GATE 8
#include <hip/hip_runtime.h>
#include <hip/hip_bf16.h>
#include <stdint.h>

using bf16 = __hip_bfloat16;
using u32 = unsigned;
using u64 = unsigned long long;
using u32x2 = __attribute__((ext_vector_type(2))) unsigned;
using u32x4 = __attribute__((ext_vector_type(4))) unsigned;
using f32x4 = __attribute__((ext_vector_type(4))) float;
using bf16x2 = __attribute__((ext_vector_type(2))) __bf16;
typedef __attribute__((ext_vector_type(2))) float f32x2;
typedef short s16x8 __attribute__((ext_vector_type(8)));
typedef short s16x4 __attribute__((ext_vector_type(4)));
typedef __attribute__((address_space(3))) s16x4* lds_s4p;
typedef __attribute__((ext_vector_type(8))) __bf16 bf16x8v;

constexpr int NB = 4, DM = 640, NH = 32, DH = 128, SK = 8192, WIN = 2048, FF = 1728, QD = 4096, MODN = 3840;
constexpr int NT = 512, NWG = 256;

constexpr u32 WS_EPOCH = 0;
constexpr u32 G_T = 128;
constexpr u32 G_Q = 256;
constexpr u32 G_MOD = G_Q + 8 * 1024 * 16;
constexpr u32 G_MF = G_MOD + 8 * 1024 * 16;
constexpr u32 G_HB = G_MF + 16 * 160 * 16;
constexpr u32 G_HA = G_HB + 64 * 128;
constexpr u32 G_HT = G_HA + 80 * 128;
constexpr u32 G_ML = G_HT + 256 * 128;
constexpr u32 EMPTY = 0xffffffffu;
constexpr int NREP = NREPL;
constexpr u32 G_WO = G_ML + 256 * 64;
constexpr u32 WO_REP = 40 * 1024;
constexpr u32 G_ATT = G_WO + NREP * WO_REP;
constexpr u32 G_OUT = G_ATT + 256 * 256;
constexpr u32 WS_END = G_OUT + 40 * 54 * 128;
constexpr u32 OOB = 0xF0000000u;
#define GIDX(n, ks, b, cg) ((((ks) * ((n) / 4) + ((cg) >> 2)) * 16 + (b) * 4 + ((cg) & 3)) * 16)

#define WSTAMP(k) do {} while (0)
#define STAMPW(i) do {} while (0)
#define STAMPT(i, tt) do {} while (0)
#define STAMP_DECL
#define STAMP_FLUSH do {} while (0)
#define FAIL(i) do {} while (0)
#define PAD(name, next) asm volatile(".Lpad_" #name ":\n\ts_cbranch_execz .Lpad_" #next)
#define PADX(name, next, x) asm volatile(".Lpad_" #name ":\n\ts_cbranch_execz .Lpad_" #next : "+v"(x))
#define PADA(name, next) asm volatile(".Lpad_" #name ":\n\ts_cbranch_execz .Lpad_" #next : "+v"(acc[0]), "+v"(acc[1]), "+v"(acc[2]), "+v"(acc[3]), "+v"(acc[4]), "+v"(acc[5]), "+v"(acc[6]), "+v"(acc[7]))
#define STR_(x) #x
#define STR(x) STR_(x)
#define BAR() asm volatile("s_waitcnt lgkmcnt(0)\n\ts_barrier" ::: "memory")
#define VM0() asm volatile("s_waitcnt vmcnt(0)" ::: "memory")

__device__ __forceinline__ float bfr(float x) { return __bfloat162float(__float2bfloat16(x)); }
__device__ __forceinline__ float bf2f(bf16 x) { return __bfloat162float(x); }
__device__ __forceinline__ float lo16(unsigned u) { return __builtin_bit_cast(float, u << 16); }
__device__ __forceinline__ float hi16(unsigned u) { return __builtin_bit_cast(float, u & 0xffff0000u); }
__device__ __forceinline__ u32 f2u(float f) { return __builtin_bit_cast(u32, f); }
__device__ __forceinline__ float u2f(u32 f) { return __builtin_bit_cast(float, f); }
__device__ __forceinline__ u32 cvt2(float a, float b) {
  const f32x2 v = {a, b};
  return __builtin_bit_cast(u32, __builtin_convertvector(v, bf16x2));
}
__device__ __forceinline__ float frcp(float x) { return __builtin_amdgcn_rcpf(x); }
__device__ __forceinline__ float fsqrt(float x) { return __builtin_amdgcn_sqrtf(x); }
__device__ __forceinline__ float fexp(float x) { return __builtin_amdgcn_exp2f(x * 1.4426950408889634f); }
__device__ __forceinline__ float dot2(unsigned k, unsigned q, float acc) {
  return __builtin_amdgcn_fdot2_f32_bf16(__builtin_bit_cast(bf16x2, k), __builtin_bit_cast(bf16x2, q), acc, false);
}
__device__ __forceinline__ float addlo(unsigned w, float acc) { u32 k = 0x00003f80u; asm("" : "+s"(k)); return dot2(w, k, acc); }
__device__ __forceinline__ float addhi(unsigned w, float acc) { return dot2(w, 0x3f800000u, acc); }
template <int CTRL> __device__ __forceinline__ float dpp(float x) {
  return __builtin_bit_cast(float, __builtin_amdgcn_mov_dpp(__builtin_bit_cast(int, x), CTRL, 0xf, 0xf, true));
}
template <int CTRL> __device__ __forceinline__ u32 dppu(u32 x) { return (u32)__builtin_amdgcn_mov_dpp((int)x, CTRL, 0xf, 0xf, true); }
constexpr int XOR1 = 0xB1, XOR2 = 0x4E, XOR7 = 0x141, XOR8 = 0x128, ROR4 = 0x124, ROR2 = 0x122, ROR1 = 0x121, SHL4 = 0x104;
__device__ __forceinline__ float swap32_add(float a, float b) {
  auto s = __builtin_amdgcn_permlane32_swap(f2u(a), f2u(b), false, false);
  return u2f(s[0]) + u2f(s[1]);
}
__device__ __forceinline__ float swap16_add(float a, float b) {
  auto s = __builtin_amdgcn_permlane16_swap(f2u(a), f2u(b), false, false);
  return u2f(s[0]) + u2f(s[1]);
}
__device__ __forceinline__ float cross_rows_sum(float x) { x = swap16_add(x, x); return swap32_add(x, x); }
__device__ __forceinline__ float cross_rows_max(float x) {
  auto s = __builtin_amdgcn_permlane16_swap(f2u(x), f2u(x), false, false);
  x = fmaxf(u2f(s[0]), u2f(s[1]));
  auto s2 = __builtin_amdgcn_permlane32_swap(f2u(x), f2u(x), false, false);
  return fmaxf(u2f(s2[0]), u2f(s2[1]));
}
__device__ __forceinline__ float wave_sum(float x) {
  x += dpp<XOR8>(x); x += dpp<ROR4>(x); x += dpp<ROR2>(x); x += dpp<ROR1>(x);
  return cross_rows_sum(x);
}
__device__ __forceinline__ float wave_max(float x) {
  x = fmaxf(x, dpp<XOR8>(x)); x = fmaxf(x, dpp<ROR4>(x)); x = fmaxf(x, dpp<ROR2>(x)); x = fmaxf(x, dpp<ROR1>(x));
  return cross_rows_max(x);
}

__device__ __forceinline__ void st_gran(__amdgpu_buffer_rsrc_t rs, u32 off, u32 a, u32 b, u32 tg) {
  u32x4 v = {a, tg, b, tg};
  __builtin_amdgcn_raw_buffer_store_b128(v, rs, off, 0, 16);
}
__device__ __forceinline__ u32x4 ld_gran(__amdgpu_buffer_rsrc_t rs, u32 off) {
  return __builtin_amdgcn_raw_buffer_load_b128(rs, off, 0, 16);
}
constexpr unsigned SPIN_CAP = 1u << 18;
template <int N>
__device__ __forceinline__ void gather(__amdgpu_buffer_rsrc_t rs, const u32 (&offs)[N], u32x4 (&g)[N], u32 tg) {
  SPINLOOP for (unsigned spins = 0; spins < SPIN_CAP; ++spins) {
    bool ok = true;
#pragma unroll
    for (int i = 0; i < N; ++i) g[i] = ld_gran(rs, offs[i]);
#pragma unroll
    for (int i = 0; i < N; ++i) ok &= (offs[i] == OOB) | ((g[i][1] == tg) & (g[i][3] == tg));
    if (__all(ok)) break;
    __builtin_amdgcn_s_sleep(1);
  }
}
template <int N>
__device__ __forceinline__ void gather_m(__amdgpu_buffer_rsrc_t rs, u32 (&offs)[N], u32 (&va)[N], u32 (&vb)[N], u32 tg) {
#pragma unroll
  for (int i = 0; i < N; ++i) { va[i] = 0; vb[i] = 0; }
  SPINLOOP for (unsigned spins = 0; spins < SPIN_CAP; ++spins) {
    u32x4 g[N];
#pragma unroll
    for (int i = 0; i < N; ++i) g[i] = ld_gran(rs, offs[i]);
    bool ok = true;
#pragma unroll
    for (int i = 0; i < N; ++i) {
      const bool hit = (offs[i] != OOB) & (g[i][1] == tg) & (g[i][3] == tg);
      va[i] = hit ? g[i][0] : va[i]; vb[i] = hit ? g[i][2] : vb[i];
      offs[i] = hit ? OOB : offs[i];
      ok &= (offs[i] == OOB);
    }
    if (__all(ok)) break;
    __builtin_amdgcn_s_sleep(1);
  }
}

__device__ __forceinline__ bool missing(u32x4 v) { return max(max(v[0], v[1]), max(v[2], v[3])) == EMPTY; }
#define LK(v, o, so, imm, m) "s_mov_b64 exec, %" #m "\n\tbuffer_load_dwordx4 %" #v ", %" #o ", %[rs], " so " offen offset:" #imm " sc1\n\t"
#define LKEND "s_mov_b64 exec, -1\n\ts_waitcnt vmcnt(%[w])"
template <int W> __device__ __forceinline__ void look(u32x4 rs, u32 off, u32x4 (&v)[1], const u64 (&m)[1]) {
  asm volatile(LK(0, 1, "0", 0, 2) LKEND : "+v"(v[0]) : "v"(off), "s"(m[0]), [rs] "s"(rs), [w] "i"(W) : "memory");
}
template <int W> __device__ __forceinline__ void look(u32x4 rs, u32 off, u32x4 (&v)[5], const u64 (&m)[5]) {
  asm volatile(LK(0, 5, "0", 0, 6) LK(1, 5, "0", 1024, 7) LK(2, 5, "0", 2048, 8) LK(3, 5, "0", 3072, 9) LK(4, 5, "%11", 0, 10) LKEND
               : "+v"(v[0]), "+v"(v[1]), "+v"(v[2]), "+v"(v[3]), "+v"(v[4])
               : "v"(off), "s"(m[0]), "s"(m[1]), "s"(m[2]), "s"(m[3]), "s"(m[4]), "s"(4096u), [rs] "s"(rs), [w] "i"(W) : "memory");
}
template <int W> __device__ __forceinline__ void look(u32x4 rs, u32 off, u32x4 (&v)[7], const u64 (&m)[7]) {
  asm volatile(LK(0, 7, "0", 0, 8) LK(1, 7, "0", 1024, 9) LK(2, 7, "0", 2048, 10) LK(3, 7, "0", 3072, 11)
               LK(4, 7, "%15", 0, 12) LK(5, 7, "%15", 1024, 13) LK(6, 7, "%15", 2048, 14) LKEND
               : "+v"(v[0]), "+v"(v[1]), "+v"(v[2]), "+v"(v[3]), "+v"(v[4]), "+v"(v[5]), "+v"(v[6])
               : "v"(off), "s"(m[0]), "s"(m[1]), "s"(m[2]), "s"(m[3]), "s"(m[4]), "s"(m[5]), "s"(m[6]), "s"(4096u), [rs] "s"(rs), [w] "i"(W) : "memory");
}
template <int N, int SLEEP, int D>
__device__ __forceinline__ void gather_e(u32x4 rs, u32 off, u32x4 (&v)[N], u64 last) {
  u64 m[N];
#pragma unroll
  for (int i = 0; i < N; ++i) { m[i] = i < N - 1 ? ~0ull : last; v[i] = u32x4{0u, 0u, 0u, 0u}; }
  look<0>(rs, off, v, m);
  SPINLOOP for (unsigned spins = 0; spins < SPIN_CAP; ++spins) {
    u64 bad = 0;
#pragma unroll
    for (int i = 0; i < N; ++i) { m[i] = __builtin_amdgcn_ballot_w64(missing(v[i])); bad |= m[i]; }
    if (!bad) break;
    if (SLEEP) __builtin_amdgcn_s_sleep(SLEEP);
    look<N * (D - 1)>(rs, off, v, m);
  }
}

#define HLOOK(W_) asm volatile("s_mov_b64 exec, %2\n\tbuffer_load_dwordx2 %0, %1, %3, 0 offen sc1\n\ts_mov_b64 exec, -1\n\ts_waitcnt vmcnt(%4)" \
                               : "+v"(v) : "v"(off), "s"(m), "s"(rs), "i"(W_) : "memory")
__device__ __forceinline__ void heads_up_start(u32x4 rs, u32 off, u64 m, u32 tg, u32x2& v) { v = u32x2{0u, ~tg}; HLOOK(63); }
template <int SLEEP, int W0 = 0>
__device__ __forceinline__ void heads_up(u32x4 rs, u32 off, u64 m, u32 tg, u32x2& v) {
  asm volatile("s_waitcnt vmcnt(%1)" : "+v"(v) : "i"(W0) : "memory");
  SPINLOOP for (unsigned spins = 0; spins < SPIN_CAP; ++spins) {
    m &= __builtin_amdgcn_ballot_w64(v[1] != tg);
    if (!m) break;
    if (SLEEP) __builtin_amdgcn_s_sleep(SLEEP);
    HLOOK(0);
  }
}
__device__ __forceinline__ void heads_up_time(u64 all, u32x2 v) {
  SPINLOOP for (unsigned spins = 0; spins < 64; ++spins)
    if (!(all & __builtin_amdgcn_ballot_w64((int)((u32)__builtin_amdgcn_s_memrealtime() - v[0]) < 0))) break;
}
__device__ __forceinline__ void announce(__amdgpu_buffer_rsrc_t rs, u32 off, u32 a_max, u32 a_cnt, u32 n, u32 tb, u32 d, u32 tg) {
  u32 tk = 0, mx;
  const u32 f = (u32)__builtin_amdgcn_s_memrealtime() - tb + d;
  asm volatile("s_mov_b64 exec, 1\n\tds_max_u32 %2, %3\n\tds_add_rtn_u32 %0, %4, %5\n\tds_read_b32 %1, %2\n\ts_mov_b64 exec, -1\n\ts_waitcnt lgkmcnt(0)"
               : "+v"(tk), "=&v"(mx) : "v"(a_max), "v"(f), "v"(a_cnt), "v"(1u) : "memory");
  const u32 lane = threadIdx.x & 63;
  __builtin_amdgcn_raw_buffer_store_b64(u32x2{__builtin_amdgcn_readfirstlane(mx) + tb, tg}, rs,
                                        (lane < 16 && __builtin_amdgcn_readfirstlane(tk) == n - 1) ? off + lane * 8 : OOB, 0, 16);
}

__device__ __forceinline__ void st_g22(__amdgpu_buffer_rsrc_t rs, u32 off, f32x4 v, u32 tg) {
  const u32 a0 = f2u(v[0]) + 0x200u, a1 = f2u(v[1]) + 0x200u, a2 = f2u(v[2]) + 0x200u, a3 = f2u(v[3]) + 0x200u;
  const u32x4 w = {(a0 >> 10) | ((a1 << 12) & 0xffc00000u), (a1 >> 20) | (tg << 12), (a2 >> 10) | ((a3 << 12) & 0xffc00000u), (a3 >> 20) | (tg << 12)};
  __builtin_amdgcn_raw_buffer_store_b128(w, rs, off, 0, 16);
}
__device__ __forceinline__ f32x4 un_g22(u32x4 w) {
  return f32x4{u2f(w[0] << 10), u2f(__builtin_amdgcn_alignbit(w[1], w[0], 22) << 10), u2f(w[2] << 10), u2f(__builtin_amdgcn_alignbit(w[3], w[2], 22) << 10)};
}
template <int N, int SLEEP>
__device__ __forceinline__ void gather22(__amdgpu_buffer_rsrc_t rs, u32 (&offs)[N], u32x4 (&v)[N], u32 tg, u32 a_inc = 0) {
#pragma unroll
  for (int i = 0; i < N; ++i) v[i] = u32x4{0u, 0u, 0u, 0u};
  SPINLOOP for (unsigned spins = 0; spins < SPIN_CAP; ++spins) {
    u32x4 g[N];
#pragma unroll
    for (int i = 0; i < N; ++i) g[i] = ld_gran(rs, offs[i]);
    if (a_inc) asm volatile("s_mov_b64 exec, 1\n\tds_add_u32 %0, %1\n\ts_mov_b64 exec, -1" :: "v"(a_inc), "v"(1u) : "memory");
    bool ok = true;
#pragma unroll
    for (int i = 0; i < N; ++i) {
      const bool hit = (offs[i] != OOB) & (((g[i][1] ^ (tg << 12)) | (g[i][3] ^ (tg << 12))) < 4096u);
#pragma unroll
      for (int k = 0; k < 4; ++k) v[i][k] = hit ? g[i][k] : v[i][k];
      offs[i] = hit ? OOB : offs[i];
      ok &= (offs[i] == OOB);
    }
    if (__all(ok)) break;
    __builtin_amdgcn_s_sleep(SLEEP);
  }
}

__device__ __forceinline__ int sel_lt(int a, int b, int x, int y) {
  int r;
  asm("s_cmp_lt_i32 %1, %2\n\ts_cselect_b32 %0, %3, %4" : "=s"(r) : "s"(a), "s"(b), "s"(x), "s"(y) : "scc");
  return r;
}
__device__ __forceinline__ u32 next_tag(u32 ep) { const u32 t = ep + 1u; return (t & 0xfffffu) ? t : t + 1u; }

__device__ __forceinline__ void lds_arrive(u32 addr, int lane) {
  asm volatile("s_waitcnt lgkmcnt(0)" ::: "memory");
  if (lane == 0) asm volatile("ds_add_u32 %0, %1" :: "v"(addr), "v"(1u) : "memory");
}
__device__ __forceinline__ u32 lds_ticket(u32 addr, int lane) {
  u32 r = 0;
  asm volatile("s_waitcnt lgkmcnt(0)" ::: "memory");
  if (lane == 0) asm volatile("ds_add_rtn_u32 %0, %1, %2\n\ts_waitcnt lgkmcnt(0)" : "=v"(r) : "v"(addr), "v"(1u) : "memory");
  return __builtin_amdgcn_readfirstlane(r);
}
template <int SLEEP>
__device__ __forceinline__ void lds_wait(u32 addr, u32 target) {
  SPINLOOP for (unsigned spins = 0; spins < (1u << 22); ++spins) {
    u32 v;
    asm volatile("ds_read_b32 %0, %1\n\ts_waitcnt lgkmcnt(0)" : "=v"(v) : "v"(addr) : "memory");
    if (v >= target) break;
    __builtin_amdgcn_s_sleep(SLEEP);
  }
}
__device__ __forceinline__ void dma16(const void* gsrc, u32 lds_dst) {
  u32 keep;
  asm volatile("s_mov_b32 %0, m0\n\ts_mov_b32 m0, %2\n\ts_nop 0\n\tglobal_load_lds_dwordx4 %1, off\n\ts_mov_b32 m0, %0"
               : "=&s"(keep) : "v"(gsrc), "s"(lds_dst) : "memory");
}
__device__ __forceinline__ void dma16nt(const void* gsrc, u32 lds_dst) {
  u32 keep;
  asm volatile("s_mov_b32 %0, m0\n\ts_mov_b32 m0, %2\n\ts_nop 0\n\tglobal_load_lds_dwordx4 %1, off nt\n\ts_mov_b32 m0, %0"
               : "=&s"(keep) : "v"(gsrc), "s"(lds_dst) : "memory");
}
__device__ __forceinline__ u32x4 ldasm(const void* p) {
  u32x4 v;
  asm volatile("global_load_dwordx4 %0, %1, off" : "=v"(v) : "v"(p) : "memory");
  return v;
}
__device__ __forceinline__ u32 ldasm_sc1(const void* p) {
  u32 v;
  asm volatile("global_load_dword %0, %1, off sc1" : "=v"(v) : "v"(p) : "memory");
  return v;
}
__device__ __forceinline__ u32x4 ldw(const bf16* p) { return *reinterpret_cast<const u32x4*>(p); }
template <int D2>
__device__ __forceinline__ s16x8 wfrag(u32 addr) {
  const s16x4 lo = __builtin_amdgcn_ds_read_tr16_b64_v4i16((lds_s4p)(uintptr_t)addr);
  const s16x4 hi = __builtin_amdgcn_ds_read_tr16_b64_v4i16((lds_s4p)(uintptr_t)(addr + D2));
  return s16x8{lo[0], lo[1], lo[2], lo[3], hi[0], hi[1], hi[2], hi[3]};
}
#define MFMA(w_, x_, c_) __builtin_amdgcn_mfma_f32_16x16x32_bf16(__builtin_bit_cast(bf16x8v, w_), __builtin_bit_cast(bf16x8v, x_), c_, 0, 0, 0)

struct Desc { const bf16* p; u32 bytes, flags; };
struct Args {
  Desc wq, x, wmod, c;
  unsigned char* ws;
  const bf16 *kc, *vc, *wo, *w1, *b1, *w2, *b2;
  bf16* out;
};

static_assert(offsetof(Args, ws) == 64 && offsetof(Args, out) == 128, "argument offsets");

constexpr int L_W = 0;
constexpr int L_WM2 = 40960;
constexpr int L_XA = 81920;
constexpr int XS = 1312, XH = 704;
constexpr int L_RED = L_XA + 16384;
constexpr int L_MOD = L_RED + 8192;
constexpr int L_XR = L_MOD + 5120;
constexpr int L_PT = L_XR + 5120;
constexpr int L_ACC = L_PT + 2048;
constexpr int L_QS = L_ACC + 4096;
constexpr int L_FI = L_QS + 256;
constexpr int L_ST = L_FI + 256;
constexpr int L_CNT = L_ST + 256;
constexpr int L_END = L_CNT + 128;
static_assert(L_END <= 160 * 1024, "LDS");
constexpr u32 C_Q = L_CNT, C_SC = L_CNT + 4, C_ISS = L_CNT + 8, C_P1 = L_CNT + 20, C_P2 = L_CNT + 24, C_GO = L_CNT + 28, C_W = L_CNT + 12, C_FIN = L_CNT + 32,
              C_KI = L_CNT + 36, C_PP = L_CNT + 40, C_PUB = L_CNT + 56, C_HT = L_CNT + 64, C_MF = L_CNT + 68;

constexpr int NL = 32;
constexpr int PVLAG = P_LAG;
constexpr int NPRE = P_NPRE;
constexpr int NVE = P_NVE;
static_assert(PVLAG <= 16 && (PVLAG & 1) == 0 && NVE <= PVLAG && (NVE & 1) == 0, "row groups");

__global__ void __launch_bounds__(NT, 2) mega_kernel(Args a) {
  __shared__ __attribute__((aligned(1024))) unsigned char lds[L_END];
  float* red = reinterpret_cast<float*>(lds + L_RED);
  u32* xraw = reinterpret_cast<u32*>(lds + L_XR);
  u32* pT = reinterpret_cast<u32*>(lds + L_PT);
  float* accs = reinterpret_cast<float*>(lds + L_ACC);
  u32* qs = reinterpret_cast<u32*>(lds + L_QS);
  float* st = reinterpret_cast<float*>(lds + L_ST);
  u32* cnt = reinterpret_cast<u32*>(lds + L_CNT);

  u32x4 rw, rx;
  unsigned char* wsq;
  const void* kcq;
  asm volatile("s_load_dwordx4 %0, %4, %5\n\ts_load_dwordx4 %1, %4, %5 offset:16\n\ts_load_dwordx2 %2, %4, 64\n\ts_load_dwordx2 %3, %4, 72"
               : "=&s"(rw), "=&s"(rx), "=&s"(wsq), "=&s"(kcq) : "s"(__builtin_amdgcn_kernarg_segment_ptr()), "s"((blockIdx.x & 1) * 32));
  __builtin_amdgcn_sched_barrier(0);
  const int t = threadIdx.x, lane = t & 63;
  const int wave = __builtin_amdgcn_readfirstlane(t >> 6);
  const int u = blockIdx.x;
  const int bh = u >> 1, half = u & 1, b_att = bh >> 5, h_att = bh & 31;
  const int r = lane >> 4, c = lane & 15;
  STAMP_DECL
  STAMPT(0, 0);
  u32 tg;
  const int g = u >> 1, ksp = g & 1, cbp = g >> 1;
  const int s128e = (((((lane & 7) >> 1) ^ ((lane >> 4) & 1)) << 1) | (lane & 1)) * 8;
  const u32 tr128 = (8 * r + (c >> 2)) * 128 + ((((c >> 3) & 1) | ((r & 1) << 1)) * 32) + (c & 3) * 8;
  if (wave < 4) {
    __builtin_amdgcn_s_setprio(3);
    const u32 rb = 8192 - 512 * half;
    const u32 hm = 0u - (u32)half;
    const u32 vo = ((u32)(lane >> 3) * rb + (((lane & 7) ^ ((lane >> 3) & 2)) ^ ((wave & 1) * 4)) * 16) | ((u32)(119 - g) & hm & OOB);
    u32 so = (u32)(ksp * 320 + 8 * wave) * rb + cbp * 128;
    const u32 step = 32 * rb, dstep = 4096, dst = L_W + wave * 1024;
    const u32 oe = wave * 1280 + lane * 16, oo = (r * 1280 + ksp * 640 + (c + 16 * wave) * 16) | ((u32)(39 - c - 16 * wave) & OOB);
    const u32 oa = (oo & hm) | (oe & ~hm), ob = (wave * 1280 + 1024 + c * 16) | (hm & OOB), oc = oe | (hm & OOB), zero = 0;
    u32 ep, keep;
    u32x4 xv[4];
    const u32 pre = ((u32)u << 31) & (u32)(g - 32) & (u32)(1 - wave), po = G_ATT + g * 2048 + (t & 127) * 16;
    const u32x4 emp = {EMPTY, EMPTY, EMPTY, EMPTY};
#define DMA1 "buffer_load_dwordx4 %7, %8, %1 offen nt lds\n\t"
#define DMAN "s_add_u32 m0, m0, %11\n\ts_add_u32 %1, %1, %10\n\t" DMA1
    asm volatile("s_mov_b32 %0, m0\n\ts_mov_b32 m0, %9\n\t"
                 "s_cmp_gt_i32 %18, 1\n\ts_waitcnt lgkmcnt(0)\n\ts_cbranch_scc1 1f\n\t"
                 "global_load_dword %2, %12, %13 sc1\n\t"
                 "buffer_load_dwordx4 %3, %14, %17, 0 offen\n\tbuffer_load_dwordx4 %4, %15, %17, 0 offen\n\t"
                 "buffer_load_dwordx4 %5, %16, %17, 0 offen offset:2560\n\tbuffer_load_dwordx4 %6, %15, %17, 0 offen offset:2560\n"
                 "1:\n\t" DMA1 DMAN DMAN DMAN DMAN DMAN DMAN DMAN DMAN DMAN "s_mov_b32 m0, %0\n\t"
                 "s_cmp_eq_u32 %19, 0\n\ts_cbranch_scc1 2f\n\tglobal_store_dwordx4 %20, %21, %13 sc1\n2:\n\t"
#define B0W "s_barrier\n\ts_memrealtime vcc\n\ts_waitcnt vmcnt(10) lgkmcnt(0)\n\ts_sub_u32 %0, vcc_lo, " STR(AOFF)
                 B0W
                 : "=&s"(keep), "+s"(so), "=&v"(ep), "=&v"(xv[0]), "=&v"(xv[1]), "=&v"(xv[2]), "=&v"(xv[3])
                 : "v"(vo), "s"(rw), "s"(dst), "s"(step), "s"(dstep), "v"(zero), "s"(wsq), "v"(oa), "v"(ob), "v"(oc), "s"(rx), "s"(wave - half),
                   "s"(pre), "v"(po), "v"(emp) : "memory", "scc", "vcc");
#undef DMAN
#undef DMA1
    asm volatile("" : "+s"(wsq));
    const __amdgpu_buffer_rsrc_t rs = __builtin_amdgcn_make_buffer_rsrc(wsq, 0, WS_END, 0x00020000);
    if (half) {
      if (wave < 3) {
        u32x4 sv;
#pragma unroll
        for (int k = 0; k < 4; ++k) {
          const float c0 = lo16(xv[0][k]), c1 = hi16(xv[0][k]);
          sv[k] = cvt2(c0 * frcp(1.f + fexp(-c0)), c1 * frcp(1.f + fexp(-c1)));
        }
        if (c + 16 * wave < 40) *reinterpret_cast<u32x4*>(lds + L_XA + r * XH + (c + 16 * wave) * 16) = sv;
        if (t == 0) { cnt[4] = next_tag(ep); cnt[15] = keep; }
        lds_arrive(C_P1, lane);
        STAMPT(2, 0);
      }
    } else {
      if (wave < 2) {
#pragma unroll
        for (int i = 0; i < 2; ++i) {
          const int b = wave + 2 * i;
          float xf[16];
#pragma unroll
          for (int k = 0; k < 4; ++k) { xf[2 * k] = lo16(xv[2 * i][k]); xf[2 * k + 1] = hi16(xv[2 * i][k]); xf[8 + 2 * k] = lo16(xv[2 * i + 1][k]); xf[8 + 2 * k + 1] = hi16(xv[2 * i + 1][k]); }
          float s = 0.f, s2 = 0.f;
#pragma unroll
          for (int k = 0; k < 8; ++k) { s += xf[k]; s2 += xf[8 + k]; }
          if (lane < 16) s += s2;
          const float mean = wave_sum(s) * (1.f / DM);
          float v = 0.f, v2 = 0.f;
#pragma unroll
          for (int k = 0; k < 8; ++k) { const float d = xf[k] - mean; v += d * d; const float d2 = xf[8 + k] - mean; v2 += d2 * d2; }
          if (lane < 16) v += v2;
          v = wave_sum(v);
          if (lane == 0) { st[24 + 2 * b] = bfr(mean); st[24 + 2 * b + 1] = frcp(bfr(fsqrt(bfr(bfr(v * (1.f / DM)) + 1e-5f)))); }
          *reinterpret_cast<u32x4*>(xraw + b * 320 + lane * 4) = xv[2 * i];
          if (lane < 16) *reinterpret_cast<u32x4*>(xraw + b * 320 + 256 + lane * 4) = xv[2 * i + 1];
        }
        if (t == 0) { cnt[4] = next_tag(ep); cnt[15] = keep; }
        lds_arrive(C_P1, lane);
      }
      lds_wait<1>(C_P1, 2);
      STAMPT(20, 0);
      tg = __builtin_amdgcn_readfirstlane(cnt[4]);
      if (CT0 && u < 8 && t == 192) { const u32 t0 = cnt[15]; st_gran(rs, G_T + u * 16, t0, t0, tg); }
      if (wave < 3) {
        const int b = LAY == 2 ? t & 3 : (t >> 2) & 3, j = LAY == 2 ? t >> 2 : (t >> 4) * 4 + (t & 3);
        u32 offs[8];
        u32x4 gm[8];
#pragma unroll
        for (int i = 0; i < 8; ++i) offs[i] = t < 160 ? G_MOD + GIDX(1024, i & 1, b, ((i >> 1) & 1) * 160 + (i >> 2) * 40 + ksp * 80 + j) : OOB;
        const float mean = st[24 + 2 * b], sd = st[24 + 2 * b + 1];
        float nv[8];
#pragma unroll
        for (int e = 0; e < 2; ++e) {
          const u32x2 xw = *reinterpret_cast<const u32x2*>(xraw + b * 320 + ksp * 160 + e * 80 + 2 * j);
#pragma unroll
          for (int k = 0; k < 4; ++k) {
            const float xf = (k & 1) ? hi16(xw[k >> 1]) : lo16(xw[k >> 1]);
            nv[4 * e + k] = bfr(bfr(xf - mean) * sd);
            asm volatile("" : "+v"(nv[4 * e + k]));
          }
        }
        {
          const u32 tt = __builtin_amdgcn_readfirstlane(cnt[15]) + GT;
          SPINLOOP while ((int)((u32)__builtin_amdgcn_s_memrealtime() - tt) < 0) __builtin_amdgcn_s_sleep(1);
        }
        STAMPT(14, 0);
        gather22<8, 4>(rs, offs, gm, tg, GOPOS == 2 ? C_GO : 0);
        STAMPT(8, 0);
#pragma unroll
        for (int e = 0; e < 2; ++e) {
          const f32x4 sa = un_g22(gm[4 * e]) + un_g22(gm[4 * e + 1]);
          const f32x4 ca = un_g22(gm[4 * e + 2]) + un_g22(gm[4 * e + 3]);
          const u32 s01 = cvt2(sa[0], sa[1]), s23 = cvt2(sa[2], sa[3]), c01 = cvt2(ca[0], ca[1]), c23 = cvt2(ca[2], ca[3]);
          const u32 d01 = cvt2(1.f + lo16(c01), 1.f + hi16(c01)), d23 = cvt2(1.f + lo16(c23), 1.f + hi16(c23));
          const u32 p01 = cvt2(nv[4 * e] * lo16(d01), nv[4 * e + 1] * hi16(d01)), p23 = cvt2(nv[4 * e + 2] * lo16(d23), nv[4 * e + 3] * hi16(d23));
          const float hv[4] = {lo16(p01) + lo16(s01), hi16(p01) + hi16(s01), lo16(p23) + lo16(s23), hi16(p23) + hi16(s23)};
          if (t < 160) *reinterpret_cast<u32x2*>(lds + L_XA + b * XH + (e * 40 + j) * 8) = u32x2{cvt2(hv[0], hv[1]), cvt2(hv[2], hv[3])};
        }
      }
    }
    VM0();
    STAMPT(5, 128);
    lds_arrive(C_P2, lane);
    lds_wait<1>(C_P2, 4);
    STAMPT(7, 0);
    tg = __builtin_amdgcn_readfirstlane(cnt[4]);
    {
      f32x4 acc = {0.f, 0.f, 0.f, 0.f}, acc2 = acc;
#pragma unroll
      for (int s = 0; s < 10; s += 2) {
        acc = MFMA(wfrag<512>(L_W + (tr128 ^ (wave * 32)) + s * 4096), *reinterpret_cast<const u32x4*>(lds + L_XA + (c & 3) * XH + s * 64 + r * 16), acc);
        acc2 = MFMA(wfrag<512>(L_W + (tr128 ^ (wave * 32)) + s * 4096 + 4096), *reinterpret_cast<const u32x4*>(lds + L_XA + (c & 3) * XH + s * 64 + 64 + r * 16), acc2);
      }
      if (c < 4 && u < 240 + 16 - 16 * half) st_g22(rs, G_Q + half * (G_MOD - G_Q) + GIDX(1024, ksp, c, cbp * 16 + wave * 4 + r), acc + acc2, tg);
    }
    if (CT0 && half && u < 8 && t == 192) { const u32 t0 = cnt[15]; st_gran(rs, G_T + u * 16, t0, t0, tg); }
    STAMPT(9, 0);
  } else {
    __builtin_amdgcn_s_setprio(2);
    {
      const u32 co = min((u32)(u >> 3) * 1024u + (u32)((t >> 5) & 7) * 128u + 768u, (u32)CLEN);
      u64 pc;
      asm volatile("s_getpc_b64 %0\n\tglobal_load_dword v128, %1, %0" : "=&s"(pc) : "v"(co) : "memory", "v128");
    }
    if (t < 256 + 32) cnt[lane] = 0;
    const int rot = __builtin_amdgcn_readfirstlane((u >> 3) + ((t >> 6) & 3) * PHW);
    const u32 ko = (u32)(bh * SK + (SK - WIN)) * 256u + (u32)(half * 8 + wave) * (NL * 1024u) + lane * 16;
    STAMPT(30, 448);
#define LK4(a, b, c, d, o) "global_load_dwordx4 " a ", %" #o ", %[k] nt\n\tglobal_load_dwordx4 " b ", %" #o ", %[k] offset:1024 nt\n\t" \
                           "global_load_dwordx4 " c ", %" #o ", %[k] offset:2048 nt\n\tglobal_load_dwordx4 " d ", %" #o ", %[k] offset:3072 nt\n\t"
    asm volatile("s_waitcnt lgkmcnt(0)\n\ts_barrier\n\t"
                 LK4("v[128:131]", "v[132:135]", "v[136:139]", "v[140:143]", 0) LK4("v[144:147]", "v[148:151]", "v[152:155]", "v[156:159]", 1)
                 LK4("v[160:163]", "v[164:167]", "v[168:171]", "v[172:175]", 2)
                 :: "v"(ko + (rot & 7) * 4096), "v"(ko + ((rot + 1) & 7) * 4096),
                   "v"(ko + ((rot + 2) & 7) * 4096),
                   [k] "s"(kcq) : "memory", "v128", "v129", "v130", "v131", "v132", "v133", "v134", "v135", "v136", "v137", "v138", "v139", "v140", "v141", "v142", "v143", "v144", "v145", "v146", "v147", "v148", "v149", "v150", "v151", "v152", "v153", "v154", "v155", "v156", "v157", "v158", "v159", "v160", "v161", "v162", "v163", "v164", "v165", "v166", "v167", "v168", "v169", "v170", "v171", "v172", "v173", "v174", "v175", "v176", "v177", "v178", "v179", "v180", "v181", "v182", "v183", "v184", "v185", "v186", "v187", "v188", "v189", "v190", "v191");
#undef LK4
    STAMPT(31, 448);
  }
  u32 kvoff = (u32)(bh * SK + (SK - WIN)) * 256u + (u32)(half * 8 + wave) * (NL * 1024u) + lane * 16, ph[8];
  const int rot2 = __builtin_amdgcn_readfirstlane((u >> 3) + ((t >> 6) & 3) * PHW);
#pragma unroll
  for (int i = 0; i < 8; ++i) ph[i] = ((i + rot2) & 7) * 4096;
  asm volatile("" : "+v"(kvoff));
  asm volatile("s_waitcnt lgkmcnt(0)" : "+s"(wsq), "+s"(ph[0]), "+s"(ph[1]), "+s"(ph[2]), "+s"(ph[3]), "+s"(ph[4]), "+s"(ph[5]), "+s"(ph[6]), "+s"(ph[7])
               : "s"(rw), "s"(rx), "s"(kcq));
  const __amdgpu_buffer_rsrc_t rs = __builtin_amdgcn_make_buffer_rsrc(wsq, 0, WS_END, 0x00020000);
  int role, ia, jB;
  u32 blkA, om;
#define ROLES() { int g2 = g; asm volatile("" : "+s"(g2)); \
    role = sel_lt(g2, half ? 74 : 122, half ? 0 : sel_lt(g2, 40, 2, 3), half); \
    ia = half ? g2 : g2 - 48; \
    const int iB = g2 - 74, ksA = ia & 7; \
    jB = (iB & ~7) | (int)(((iB < 48 ? 0x75316420u : 0x315420u) >> ((iB & 7) * 4)) & 7); \
    blkA = ((c & 3) * 32 + HEADA(wave & 3)) * 2 + ((c >> 2) & 1); \
    om = G_ML + blkA * 64; \
    asm volatile("" : "+s"(role), "+s"(ia), "+s"(jB), "+v"(blkA), "+v"(om)); }
#define HEADA(hh) ((ksA & 3) + (ksA >> 2) * 16 + (hh) * 4)
  u32x4 gmf[5];
  u32x2 xw;
  {
    u32x4 kv[NL], kh[16];
    const __amdgpu_buffer_rsrc_t rk = __builtin_amdgcn_make_buffer_rsrc((void*)a.kc, 0, 1u << 28, 0x00020000);
    const __amdgpu_buffer_rsrc_t rv = __builtin_amdgcn_make_buffer_rsrc((void*)a.vc, 0, 1u << 28, 0x00020000);
#define LDKV(rsrc, off, i) __builtin_amdgcn_raw_buffer_load_b128(rsrc, (off) + ((i) & 3) * 1024, ph[(i) >> 2], 2)
#define LDS_INC(addr) do { asm volatile("" ::: "memory"); if (lane == 0) asm volatile("ds_add_u32 %0, %1" :: "v"(addr), "v"(1u) : "memory"); } while (0)
#define TR_STEP(x, HALF, CTRL, BIT) _Pragma("unroll") for (int hh = 0; hh < (HALF); ++hh) { \
    const float lo_ = x[hh], hi_ = x[hh + (HALF)]; const float t1_ = lo_ + dpp<CTRL>(lo_), t2_ = hi_ + dpp<CTRL>(hi_); \
    x[hh] = (c & (BIT)) ? t2_ : t1_; }
#define SCORE16(x, src, o) float x[16]; \
    _Pragma("unroll") for (int i = 0; i < 16; ++i) { float d = 0.f; _Pragma("unroll") for (int k = 0; k < 4; ++k) d = dot2(src[(o) + i][k], qpk[k], d); x[i] = d; } \
    TR_STEP(x, 8, XOR8, 8) TR_STEP(x, 4, XOR7, 4) TR_STEP(x, 2, XOR2, 2)
#define SCORE16P(x, src, o, pa_, pb_) float x[16]; \
    _Pragma("unroll") for (int i = 0; i < 16; ++i) { float d = 0.f; _Pragma("unroll") for (int k = 0; k < 4; ++k) d = dot2(src[(o) + i][k], qpk[k], d); x[i] = d; } \
    TR_STEP(x, 8, XOR8, 8) PADX(pa_, pb_, x[0]); TR_STEP(x, 4, XOR7, 4) TR_STEP(x, 2, XOR2, 2)
#define SCALED(v) bfr(bfr(v) * 0.08838834764831845f)
#define PV2(i) _Pragma("unroll") for (int k = 0; k < 4; ++k) { \
      acc[2 * k] = dot2(__builtin_amdgcn_perm(kv[(i) + 1][k], kv[i][k], 0x05040100u), pw[(i) / 2], acc[2 * k]); \
      acc[2 * k + 1] = dot2(__builtin_amdgcn_perm(kv[(i) + 1][k], kv[i][k], 0x07060302u), pw[(i) / 2], acc[2 * k + 1]); }
#define GET_Q() lds_wait<2>(C_Q, 1); u32x4 qpk; \
    asm volatile("ds_read_b128 %0, %2\n\tds_read_b32 %1, %3\n\ts_waitcnt lgkmcnt(0)" : "=&v"(qpk), "=&v"(tg) : "v"((u32)(L_QS + 16 * c)), "v"((u32)(L_CNT + 16)) : "memory"); \
    tg = __builtin_amdgcn_readfirstlane(tg);
#define GET_P() _Pragma("unroll") for (int i4 = 0; i4 < 4; ++i4) { \
      const u32x4 pv = *reinterpret_cast<const u32x4*>(pT + (wave * 4 + r) * 16 + i4 * 4); \
      _Pragma("unroll") for (int j = 0; j < 4; ++j) pw[i4 * 4 + j] = pv[j]; }
    auto gather_q = [&]() __attribute__((always_inline)) {
        u32 offs[1] = {G_Q + GIDX(1024, lane >> 5, b_att, h_att * 32 + (lane & 31))};
        u32x4 gq[1];
        {
          gq[0] = ld_gran(rs, offs[0]);
          const u32x4 gt = ld_gran(rs, lane < 8 ? G_T + lane * 16 : OOB);
          LDS_INC(C_KI);
          {
            const u32 own = cnt[15];
            const bool ok = lane < 8 && gt[1] == tg;
            float d = ok ? __builtin_amdgcn_fmed3f((float)(int)(gt[0] - own + TOFF), -400.f, 400.f) : -500.f;
            d = fmaxf(d, dpp<XOR1>(d)); d = fmaxf(d, dpp<XOR2>(d)); d = fmaxf(d, dpp<XOR7>(d));
            if (lane == 0 && d > -450.f) cnt[15] = own + (u32)(int)d;
          }
          const bool hit = (((gq[0][1] ^ (tg << 12)) | (gq[0][3] ^ (tg << 12))) < 4096u);
          if (!__all(hit)) gather22<1, 4>(rs, offs, gq, tg);
        }
        STAMPW(10);
        f32x4 q4 = un_g22(gq[0]);
#pragma unroll
        for (int k = 0; k < 4; ++k) q4[k] = swap32_add(q4[k], q4[k]);
        if (lane < 32) *reinterpret_cast<u32x2*>(qs + 2 * lane) = u32x2{cvt2(q4[0], q4[1]), cvt2(q4[2], q4[3])};
        lds_arrive(C_Q, lane);
    };
    const int qw = half ? QODD : QEVEN;
    const u32 tau = half ? (g < 74 ? TAU_A : TAU_B) : (g >= 122 ? TAU_X : TAU_E);
#define PACE(n) if ((n) > PB) { const u32 tt_ = t0 + (((n) - PB) * tau >> 4); \
      SPINLOOP while ((int)((u32)__builtin_amdgcn_s_memrealtime() - tt_) < 0) __builtin_amdgcn_s_sleep(1); asm volatile("" ::: "memory"); }
    float acc[8];
#pragma unroll
    for (int k = 0; k < 8; ++k) acc[k] = 0.f;
    u32 pw[16];
    if (__builtin_expect(wave >= 4, 1)) {
      asm volatile("" : "={v[128:131]}"(kv[0]), "={v[132:135]}"(kv[1]), "={v[136:139]}"(kv[2]), "={v[140:143]}"(kv[3]),
                        "={v[144:147]}"(kv[4]), "={v[148:151]}"(kv[5]), "={v[152:155]}"(kv[6]), "={v[156:159]}"(kv[7])
                        , "={v[160:163]}"(kv[8]), "={v[164:167]}"(kv[9]), "={v[168:171]}"(kv[10]), "={v[172:175]}"(kv[11])
                   );
      u32 t0 = 0;
      STAMPT(24, 448);
      const u32 kvoff2 = kvoff ^ 131072u;
#pragma unroll
      for (int i = NK0; i < 48; ++i) {
        if (i == NPRE) { STAMPT(12, 448); STAMPT(13, 256); }
        if (i == NPRE && half == 0) lds_wait<2>(C_GO, GOPOS ? 3 : 4);
        if (PK && (i & 3) == 0) PACE(i)
        if (i < NL) kv[i < NL ? i : 0] = LDKV(rk, kvoff, i & 31); else kh[i < NL ? 0 : i - NL] = LDKV(rk, kvoff2, i & 31);
      }
      if (!QPRE || wave != qw) LDS_INC(C_KI);
      ROLES()
#pragma unroll
      for (int i = 0; i < NK0; i += 4) asm volatile("s_waitcnt vmcnt(%4)" : "+v"(kv[i]), "+v"(kv[i + 1]), "+v"(kv[i + 2]), "+v"(kv[i + 3]) : "i"(48 - NK0) : "memory");
      STAMPT(25, 448);
      if ((QODD >= 4 || QEVEN >= 4) && wave == qw) {
        __builtin_amdgcn_s_setprio(3);
        lds_wait<2>(C_P1, 2 + half);
        tg = __builtin_amdgcn_readfirstlane(cnt[4]);
        gather_q();
        __builtin_amdgcn_s_setprio(2);
      }
      GET_Q()
      SCORE16(xa, kv, 0)
      asm volatile("" : "+v"(xa[0]), "+v"(xa[1]), "+v"(kvoff) :: "memory");
      PAD(e1, l2);
#pragma unroll
      for (int i = 0; i < P_NVA; ++i) kv[i] = LDKV(rv, kvoff, i);
      asm volatile("" ::: "memory");
      SCORE16(xb, kv, 16)
      xa[2] = xb[0]; xa[3] = xb[1];
      TR_STEP(xa, 2, XOR1, 1)
      float s0 = SCALED(xa[0]), s1 = SCALED(xa[1]);
      asm volatile("" : "+v"(s0), "+v"(s1), "+v"(kvoff) :: "memory");
#pragma unroll
      for (int i = P_NVA; i < NVE; ++i) kv[i] = LDKV(rv, kvoff, i);
      asm volatile("" ::: "memory");
      SCORE16P(xh, kh, 0, e2, l3)
      const float s2 = SCALED(xh[0] + dpp<XOR1>(xh[0])), s3 = SCALED(xh[1] + dpp<XOR1>(xh[1]));
      const float m = wave_max(fmaxf(fmaxf(s0, s1), fmaxf(s2, s3)));
      const float p0 = __expf(s0 - m), p1 = __expf(s1 - m), p2 = __expf(s2 - m), p3 = __expf(s3 - m);
      const float l = wave_sum(fmaf(p2 + p3, 0.5f, p0 + p1));
      if (lane == 0) { st[wave] = m; st[8 + wave] = l; }
      pT[(wave * 4 + r) * 16 + (c & 1) * 8 + (c >> 1)] = cvt2(p0, p1);
      pT[((wave - 4) * 4 + r) * 16 + (c >> 1)] = cvt2(p2, p3);
      lds_arrive(C_PP + 4 * (wave - 4), lane);
      __builtin_amdgcn_s_setprio(0);
      STAMPT(27, 448); WSTAMP(1);
      if (CT0) t0 = __builtin_amdgcn_readfirstlane(cnt[15]);
      GET_P()
#pragma unroll
      for (int i = NVE; i < NL; i += 2) {
        if (i == E3POS) PAD(e3, l4);
        if (i == E4POS) PAD(e4, t1);
        PACE(96 + i)
        kv[i] = LDKV(rv, kvoff, i);
        kv[i + 1] = LDKV(rv, kvoff, i + 1);
        asm volatile("" ::: "memory");
        if (i == NL - 2) { LDS_INC(C_ISS); STAMPT(26, 448); }
        if (i >= PVLAG) {
          PV2(i >= PVLAG ? i - PVLAG : 0)
#pragma unroll
          for (int k = 0; k < 8; ++k) asm volatile("" : "+v"(acc[k]));
        }
      }
#pragma unroll
      for (int i = 0; i < 5; ++i) asm("" : "=v"(gmf[i]));
      asm("" : "=v"(xw));
    } else {
      const int ex = g < 74 ? EA : g < 122 ? EB : EC;
      u32 kva = (!half && ex > 2) ? OOB : kvoff, kvb = (!half && ex > 0) ? OOB : kvoff;
      const u32 kxa = (half && ex > 2) ? kvoff - 8 * NL * 1024u : OOB, kxb = (half && ex > 0) ? kvoff - 8 * NL * 1024u : OOB;
      u32x4 kx[4];
      const u32 t0 = __builtin_amdgcn_readfirstlane(cnt[15]);
#pragma unroll
      for (int i = 0; i < 16; ++i) {
        if (PK && (i & 3) == 0) PACE(48 + i)
        kh[i] = LDKV(rk, i < 12 ? kvoff : i < 14 ? kva : kvb, 16 + i);
      }
#pragma unroll
      for (int i = 0; i < 4; ++i) kx[i] = LDKV(rk, i < 2 ? kxa : kxb, 28 + i);
      LDS_INC(C_KI);
      if (wave == 0) {
        if (half == 0 && g >= 80 && g < 120) {
          const int fb = g - 80;
          u32 of2[2];
          u32x4 gm[2];
#pragma unroll
          for (int ks = 0; ks < 2; ++ks) of2[ks] = G_MOD + GIDX(1024, ks, r, 320 + fb * 16 + c);
          gather22<2, 4>(rs, of2, gm, tg);
          const f32x4 mv = un_g22(gm[0]) + un_g22(gm[1]);
          st_gran(rs, G_MF + (((fb / 10) * 4 + r) * 160 + (fb % 10) * 16 + c) * 16, cvt2(mv[0], mv[1]), cvt2(mv[2], mv[3]), tg);
        }
      }
      __builtin_amdgcn_s_setprio(0);
      ROLES()
      STAMPT(2, 0);
#define GO(r_, n, name) "s_cmp_eq_u32 %" #r_ ", " #n "\n\ts_cbranch_scc1 .Lpad_" #name "\n\t"
      asm volatile("s_mov_b64 exec, 0\n\t" GO(0, 0, e1) GO(0, 1, e2) GO(0, 2, e3) GO(0, 3, l1)
                   ".Lpad_role:\n\t" GO(1, 0, a3) GO(1, 1, a2) GO(1, 2, a1) GO(1, 8, b3) GO(1, 9, b2) GO(1, 10, b1) GO(1, 11, b0) GO(1, 16, c1) GO(1, 17, c0)
                   ".Lpad_back:\n\ts_mov_b64 exec, -1" :: "s"(GPAD ? wave : 7), "s"(__builtin_amdgcn_readfirstlane(role * 8 + wave)) : "scc");
#undef GO
      STAMPT(7, 0);
      lds_wait<2>(C_KI, P_GATE);
      if (QHOLD && wave == qw - 4) lds_wait<2>(C_Q, 1);
      asm volatile("" : "+v"(kvoff), "+v"(kva), "+v"(kvb));
#pragma unroll
      for (int i = 0; i < NL; ++i) {
        if (i == L1POS) PAD(l1, e4);
        if (PL && (i & 3) == 0) PACE(64 + PLO + i * PLS / 16)
        kv[i] = LDKV(rv, i < 28 ? kvoff : i < 30 ? kva : kvb, i);
      }
      STAMPT(1, 0);
      GET_Q()
      float xx[4];
#pragma unroll
      for (int i = 0; i < 4; ++i) { float d = 0.f;
#pragma unroll
        for (int k = 0; k < 4; ++k) d = dot2(kx[i][k], qpk[k], d);
        xx[i] = d; }
      asm volatile("" : "+v"(xx[0]), "+v"(xx[1]), "+v"(xx[2]), "+v"(xx[3]) :: "memory");
#pragma unroll
      for (int i = 0; i < 4; ++i) kx[i] = LDKV(rv, i < 2 ? kxa : kxb, 28 + i);
      LDS_INC(C_ISS);
      TR_STEP(xx, 2, XOR8, 8)
#pragma unroll
      for (int i = 0; i < 2; ++i) { xx[i] += dpp<XOR7>(xx[i]); xx[i] += dpp<XOR2>(xx[i]); xx[i] += dpp<XOR1>(xx[i]); }
      SCORE16P(xh, kh, 0, l2, t2)
      const float ninf = -__builtin_inff();
      const bool mine = half || c < 16 - ex, extra = half && c >= 16 - 4 * ex;
      xh[0] += dpp<XOR1>(xh[0]); xh[1] += dpp<XOR1>(xh[1]);
      const float s2 = mine ? SCALED(xh[0]) : ninf, s3 = mine ? SCALED(xh[1]) : ninf;
      const float s4 = extra ? SCALED(xx[0]) : ninf, s5 = extra ? SCALED(xx[1]) : ninf;
      float m = wave_max(fmaxf(fmaxf(s2, s3), fmaxf(s4, s5)));
      lds_wait<2>(C_PP + 4 * wave, 1);
      const float me = st[wave + 4];
      m = fmaxf(m, me);
      const float p2 = __expf(s2 - m), p3 = __expf(s3 - m), p4 = __expf(s4 - m), p5 = __expf(s5 - m);
      const float l = wave_sum(fmaf(p4 + p5, 0.25f, p2 + p3)) * 0.5f;
      const u32 pxo = cvt2(p4, p5), pxt = dppu<XOR8>(pxo);
      const u32 px[2] = {c < 8 ? pxo : pxt, c < 8 ? pxt : pxo};
      if (lane == 0) { st[wave] = m; st[8 + wave] = l; }
      pT[(wave * 4 + r) * 16 + 8 + (c >> 1)] = cvt2(p2, p3);
      STAMPT(4, 0); WSTAMP(1);
      PAD(l3, t3);
      if (lds_ticket(C_SC, lane) == 3) {
        const float mw = st[lane & 7], lw = st[8 + (lane & 7)];
        float M = fmaxf(mw, dpp<XOR1>(mw)); M = fmaxf(M, dpp<XOR2>(M)); M = fmaxf(M, dpp<XOR7>(M));
        const float e = __expf(mw - M);
        float L = lw * e;
        L += dpp<XOR1>(L); L += dpp<XOR2>(L); L += dpp<XOR7>(L);
        if (lane == 0) st_gran(rs, G_ML + u * 64, f2u(M), f2u(L), tg);
        if (lane < 8) st[16 + lane] = e * frcp(L);
      }
      {
        const int cbA = ia >> 3;
        const u32 o0 = role == 0 ? G_MF + (r * 160 + cbA * 16 + c) * 16 : role == 1 ? G_MF + (640 + t) * 16 : OOB, o1 = role == 1 ? o0 : OOB;
#pragma unroll
        for (int i = 0; i < 5; ++i) gmf[i] = __builtin_amdgcn_raw_buffer_load_b128(rs, i ? o1 : o0, i * 4096, 16);
        const __amdgpu_buffer_rsrc_t rb = __builtin_amdgcn_make_buffer_rsrc((void*)(role == 1 ? a.b1 : a.x.p), 0, role == 1 ? FF * 2 : NB * DM * 2, 0x00020000);
        xw = __builtin_amdgcn_raw_buffer_load_b64(rb, role == 1 ? (jB * 32 + (t & 28)) * 2 : role == 0 && (ia & 7) == 0 ? (r * DM + cbA * 64 + c * 4) * 2 : OOB, 0, 0);
      }
      GET_P()
#pragma unroll
      for (int i = 0; i < 16; i += 2) { if (i == L4POS) PADA(l4, a0); PV2(i) }
      const float f = __expf(me - m);
#pragma unroll
      for (int k = 0; k < 8; ++k) acc[k] *= f;
#pragma unroll
      for (int i = 16; i < NL - PVLAG; i += 2) { PV2(i) }
#pragma unroll
      for (int i = 0; i < 4; i += 2)
#pragma unroll
        for (int k = 0; k < 4; ++k) {
          acc[2 * k] = dot2(__builtin_amdgcn_perm(kx[i + 1][k], kx[i][k], 0x05040100u), px[i / 2], acc[2 * k]);
          acc[2 * k + 1] = dot2(__builtin_amdgcn_perm(kx[i + 1][k], kx[i][k], 0x07060302u), px[i / 2], acc[2 * k + 1]);
        }
    }
#pragma unroll
    for (int i = NL - PVLAG; i <= NL - HX; i += 2) { if (i == T1POS) PADA(t1, role); PV2(i) }
    if (!HT) { PV2(NL - HX + 2) } else {
      u32 tk = 0;
      u64 tn;
      asm volatile("s_mov_b64 exec, 1\n\tds_add_rtn_u32 %0, %10, %11\n\ts_mov_b64 exec, -1\n\ts_memrealtime %1"
                   : "+v"(tk), "=s"(tn), "+v"(acc[0]), "+v"(acc[1]), "+v"(acc[2]), "+v"(acc[3]), "+v"(acc[4]), "+v"(acc[5]), "+v"(acc[6]), "+v"(acc[7])
                   : "v"((u32)C_HT), "v"(1u) : "memory");
      PV2(NL - HX + 2)
      asm volatile("s_waitcnt lgkmcnt(0)" : "+v"(tk), "+s"(tn), "+v"(acc[0]), "+v"(acc[1]), "+v"(acc[2]), "+v"(acc[3]), "+v"(acc[4]), "+v"(acc[5]), "+v"(acc[6]), "+v"(acc[7]));
      __builtin_amdgcn_raw_buffer_store_b64(u32x2{(u32)tn + HTD, tg}, rs, tk == 7 ? G_HT + u * 128 : OOB, 0, 16);
      asm volatile("" ::: "memory");
    }
#pragma unroll
    for (int i = NL - HX + 4; i < NL; i += 2) { PV2(i) }
#undef PV2
#undef TR_STEP
#undef SCORE16
#undef SCORE16P
#undef SCALED
#undef GET_Q
#undef GET_P
#undef LDKV
#pragma unroll
    for (int i = 0; i < 4; ++i) acc[i] = swap32_add(acc[i], acc[i + 4]);
#pragma unroll
    for (int i = 0; i < 2; ++i) acc[i] = swap16_add(acc[i], acc[i + 2]);
    *reinterpret_cast<float2*>(accs + wave * 128 + c * 8 + 2 * r) = float2{acc[0], acc[1]};
    STAMPT(6, 0); WSTAMP(2);
  }
  const u32x4 rsv = {(u32)(uintptr_t)wsq, (u32)((uintptr_t)wsq >> 32) & 0xffffu, WS_END, 0x00020000u};
  const int ksA = ia & 7, cbA = ia >> 3;
  u32x4 ml = {0u, 0u, 0u, 0u};
#define MLOOK(W_) asm volatile(LK(0, 1, "0", 0, 2) LKEND : "+v"(ml) : "v"(om), "s"(m2), [rs] "s"(rsv), [w] "i"(W_) : "memory");
  u64 m2 = role == 0 && wave >= 4 ? 0xffull : 0;
  const u32 tkf = lds_ticket(C_FIN, lane);
  const bool lastw = tkf == 7;
  if (!lastw) { MLOOK(63) }
  if (lastw) {
    PAD(t2, role);
    if (lane < 32) {
      f32x4 o = {0.f, 0.f, 0.f, 0.f};
#pragma unroll
      for (int w = 0; w < 8; ++w) o += st[16 + w] * *reinterpret_cast<const f32x4*>(accs + w * 128 + 4 * lane);
      const u32x2 ow = {min(cvt2(o[0], o[1]), EMPTY - 1), min(cvt2(o[2], o[3]), EMPTY - 1)};
      __builtin_amdgcn_raw_buffer_store_b64(ow, rs, G_ATT + u * 256 + lane * 8, 0, 16);
    }
    MLOOK(63)
    LDS_INC(C_PUB);
    STAMPW(11);
  }
  u32 gaw[2];
  float b1v;
  if (role < 2 && wave < 4) {
    if (role == 0) {
      if (!__all(gmf[0][1] == tg && gmf[0][3] == tg)) {
        u32 offs[1] = {G_MF + (r * 160 + cbA * 16 + c) * 16};
        u32x4 gm[1];
        gather<1>(rs, offs, gm, tg);
        gmf[0] = gm[0];
      }
      gaw[0] = gmf[0][0]; gaw[1] = gmf[0][2];
    } else {
      bool ok = true;
#pragma unroll
      for (int i = 0; i < 5; ++i) ok &= (gmf[i][1] == tg) & (gmf[i][3] == tg);
      if (!__all(ok)) {
        u32 offs[5];
#pragma unroll
        for (int i = 0; i < 5; ++i) offs[i] = G_MF + (640 + t + 256 * i) * 16;
        gather<5>(rs, offs, gmf, tg);
      }
#pragma unroll
      for (int i = 0; i < 5; ++i) {
        if (i == 1) PAD(t3, role);
        const int it = t + 256 * i, row = it / 160, gi = it % 160;
        u32 w0 = gmf[i][0], w1 = gmf[i][2];
        if (row >= 4) { w0 = cvt2(1.f + lo16(w0), 1.f + hi16(w0)); w1 = cvt2(1.f + lo16(w1), 1.f + hi16(w1)); }
        *reinterpret_cast<u32x2*>(lds + L_XA + (row < 4 ? row + 8 : row) * XS + gi * 8) = u32x2{w0, w1};
      }
      const u32 bw = (t & 2) ? xw[1] : xw[0];
      b1v = (t & 1) ? hi16(bw) : lo16(bw);
      asm volatile("" : "+v"(b1v));
      lds_arrive(C_MF, lane);
    }
  }
  if (role == 0 && wave < 4) {
    if (half) STAMPT(8, 0);
    lds_wait<2>(C_ISS, 8);
    {
      const bf16* wp = a.wo + (size_t)(HEADA(wave) * 128 + (lane >> 3)) * DM + cbA * 64;
#pragma unroll
      for (int i = 0; i < 16; ++i) {
        if (i == 1) PAD(a0, role);
        if (i == N1A) {
          lds_wait<2>(C_PUB, 1);
          __builtin_amdgcn_raw_buffer_store_b128(u32x4{EMPTY, EMPTY, EMPTY, EMPTY}, rs,
              wave < NREP && lane < 32 ? G_WO + wave * WO_REP + ((cbA * 4 + (lane >> 3)) * 8 + ksA) * 128 + (lane & 7) * 16 : OOB, 0, 16);
          asm volatile("" ::: "memory");
        }
        dma16(wp + (size_t)(8 * i) * DM + (s128e ^ ((i & 1) * 32)), L_W + wave * 16384 + i * 1024);
      }
      LDS_INC(C_ISS);
    }
    STAMPT(16, 0);
  }
#define SELECTOR(sel) u32x4 sel; \
    _Pragma("unroll") for (int d = 0; d < 4; ++d) sel[d] = ((c & 2) == 0 && d == (c >> 2)) ? ((c & 1) ? 0x3f800000u : 0x00003f80u) : 0u;
#define PAIRSUM(sm) (((c & 8) ? sm[1] : sm[0]) + dpp<XOR8>((c & 8) ? sm[0] : sm[1]))
  const u32 tb = __builtin_amdgcn_readfirstlane(cnt[15]);
  if (role == 0) {
    PAD(a1, back);
    u32 a_x = L_XA + ((wave & 3) * 8 + (c & 7)) * 272 + r * 16;
    if (wave < 4) {
      PAD(a2, back);
      float ga4[4] = {lo16(gaw[0]), hi16(gaw[0]), lo16(gaw[1]), hi16(gaw[1])}, x4[4] = {lo16(xw[0]), hi16(xw[0]), lo16(xw[1]), hi16(xw[1])};
      u32 a_red = L_RED + ((wave * 4 + c) * 64 + r * 4) * 4, a_sum = L_RED + lane * 16, a_sc = L_ST + (32 + wave * 8 + (c & 7)) * 4;
      u32 a_out = wave < NREP ? G_WO + wave * WO_REP + ((cbA * 4 + (c >> 2)) * 8 + ksA) * 128 + r * 32 + (c & 3) * 8 : OOB;
      u32 a_w[4];
#pragma unroll
      for (int nt = 0; nt < 4; ++nt) a_w[nt] = L_W + wave * 16384 + (tr128 ^ (nt * 32));
      asm volatile("" : "+v"(ga4[0]), "+v"(ga4[1]), "+v"(ga4[2]), "+v"(ga4[3]), "+v"(x4[0]), "+v"(x4[1]), "+v"(x4[2]), "+v"(x4[3]));
      asm volatile("s_waitcnt vmcnt(%0)" :: "i"(AW) : "memory");
      LDS_INC(C_PP + 4 * wave);
      asm volatile("s_waitcnt vmcnt(4)" : "+v"(a_red), "+v"(a_sum), "+v"(a_sc), "+v"(a_out), "+v"(a_x), "+v"(a_w[0]), "+v"(a_w[1]), "+v"(a_w[2]), "+v"(a_w[3]) :: "memory");
      s16x8 wf[4][4];
#pragma unroll
      for (int s = 0; s < 3; ++s)
#pragma unroll
        for (int nt = 0; nt < 4; ++nt) wf[s][nt] = wfrag<512>(a_w[nt] + s * 4096);
      announce(rs, G_HA + ia * 128, C_GO, C_W, 8, tb, HAW, tg);
      STAMPT(19, 0);
      lds_wait<1>(C_PP + 4 * wave, 3);
      STAMPT(12, 0);
      PAD(a3, back);
      u32x4 av[4];
#pragma unroll
      for (int s = 0; s < 4; ++s) av[s] = *reinterpret_cast<const u32x4*>(lds + a_x + s * 64);
      const float wsc = *reinterpret_cast<const float*>(lds + a_sc);
      asm volatile("s_waitcnt vmcnt(1)" ::: "memory");
      STAMPT(23, 0);
#pragma unroll
      for (int nt = 0; nt < 4; ++nt) wf[3][nt] = wfrag<512>(a_w[nt] + 3 * 4096);
      __builtin_amdgcn_sched_barrier(0);
      f32x4 acc[4];
#pragma unroll
      for (int nt = 0; nt < 4; ++nt) acc[nt] = f32x4{0.f, 0.f, 0.f, 0.f};
#pragma unroll
      for (int s = 0; s < 4; ++s)
#pragma unroll
        for (int nt = 0; nt < 4; ++nt) acc[nt] = MFMA(wf[s][nt], av[s], acc[nt]);
#pragma unroll
      for (int nt = 0; nt < 4; ++nt) { acc[nt] *= wsc; asm("" : "+v"(acc[nt])); }
#pragma unroll
      for (int nt = 0; nt < 4; ++nt)
#pragma unroll
        for (int k = 0; k < 4; ++k) { float v = acc[nt][k] + dpp<SHL4>(acc[nt][k]); asm("" : "+v"(v)); acc[nt][k] = v; }
      asm volatile("" : "+v"(acc[0]), "+v"(acc[1]), "+v"(acc[2]), "+v"(acc[3]));
      if (c < 4) {
#pragma unroll
        for (int nt = 0; nt < 4; ++nt) *reinterpret_cast<f32x4*>(lds + a_red + nt * 64) = acc[nt];
      }
      BAR();
      STAMPT(21, 0);
      f32x4 pp[4];
#pragma unroll
      for (int w = 0; w < 4; ++w) pp[w] = *reinterpret_cast<const f32x4*>(lds + a_sum + w * 1024);
      asm volatile("" : "+v"(pp[0]), "+v"(pp[1]), "+v"(pp[2]), "+v"(pp[3]));
      const f32x4 s = (pp[0] + pp[1]) + (pp[2] + pp[3]);
      const u32x2 o = {min(cvt2(fmaf(s[0], ga4[0], x4[0]), fmaf(s[1], ga4[1], x4[1])), EMPTY - 1), min(cvt2(fmaf(s[2], ga4[2], x4[2]), fmaf(s[3], ga4[3], x4[3])), EMPTY - 1)};
      __builtin_amdgcn_raw_buffer_store_b64(o, rs, a_out, 0, 16);
    } else {
      u32 oa = G_ATT + blkA * 256 + (c >> 3) * 64 + r * 16;
      a_x += (c >> 3) * 64;
      u32x4 av[2];
      u64 m0 = ~0ull, m1 = ~0ull;
      av[0] = av[1] = u32x4{0u, 0u, 0u, 0u};
      asm volatile("s_waitcnt vmcnt(0)" : "+v"(oa), "+v"(a_x), "+v"(ml), "+v"(av[0]), "+v"(av[1]) :: "memory");
      SPINLOOP for (unsigned spins = 0; spins < SPIN_CAP; ++spins) {
        m2 &= __builtin_amdgcn_ballot_w64(ml[1] != tg) | __builtin_amdgcn_ballot_w64(ml[3] != tg);
        if (!m2) break;
        if (!spins) FAIL(4);
        __builtin_amdgcn_s_sleep(ASL);
        MLOOK(0)
      }
      {
        const float M = u2f(ml[0]), L = u2f(ml[2]), Mo = dpp<XOR7>(dpp<0x1B>(M)), Lo = dpp<XOR7>(dpp<0x1B>(L));
        const float Mx = fmaxf(M, Mo), le = L * __expf(M - Mx);
        const float wsc = le * frcp(fmaf(Lo, __expf(Mo - Mx), le));
        if (lane < 8) st[wave * 8 + lane] = wsc;
      }
#define ALOOK(W_) \
        asm volatile(LK(0, 2, "0", 0, 3) LK(1, 2, "0", 128, 4) LKEND \
                     : "+v"(av[0]), "+v"(av[1]) : "v"(oa), "s"(m0), "s"(m1), [rs] "s"(rsv), [w] "i"(W_) : "memory"); \
        m0 = __builtin_amdgcn_ballot_w64(missing(av[0])); \
        m1 = __builtin_amdgcn_ballot_w64(missing(av[1]));
      lds_wait<1>(C_PP + 4 * (wave - 4), 2);
      if (AGT) SPINLOOP for (int k = 0; k < 64; ++k) if ((int)((u32)__builtin_amdgcn_s_memrealtime() - tb) >= (126 * TAU_E >> 4) + AGT) break;
      STAMPT(29, 448);
      ALOOK(0)
      STAMPT(18, 448);
      if (m0 | m1) {
        FAIL(1);
        SPINLOOP for (unsigned spins = 0; spins < SPIN_CAP; ++spins) {
          ALOOK(2 * (PDA - 1))
          if (!(m0 | m1)) break;
          __builtin_amdgcn_s_sleep(ASL);
        }
      }
#undef ALOOK
      asm volatile("" : "+v"(av[0]), "+v"(av[1]));
      *reinterpret_cast<u32x4*>(lds + a_x) = av[0];
      *reinterpret_cast<u32x4*>(lds + a_x + 128) = av[1];
      LDS_INC(C_PP + 4 * (wave - 4));
      STAMPT(22, 448);
      announce(rs, G_HA + ia * 128, C_GO, C_W, 8, tb, HAG, tg);
      BAR();
      asm volatile("" :: "v"(av[0]), "v"(av[1]));
    }
    STAMPT(13, 0);

  } else if (role == 1) {
    PAD(b0, bd);
    const int lb = (c >> 1) & 3, lc = (c & 1) * 8 + 2 * r;
    const int fc = lane & 31, hw = lane >> 5;
    SELECTOR(sel)
    constexpr int L_R2 = L_MOD, L_S2 = L_R2 + 4096;
    const u32 gw = G_WO + (jB % NREP) * WO_REP + wave * 5120;
    u32 go = gw + (c >> 3) * 1024 + r * 128 + (c & 7) * 16, go4 = gw + 4096 + lane * 16;
    u32 gl = G_HA + (((5 * wave + (lane & 8 ? 4 : 0)) >> 2) * 8 + (lane & 7)) * 128;
    u32 a_u = L_XA + wave * 640 + lb * 160 + ((c >> 3) * 16 + lc) * 2, a_u4 = L_XA + wave * 640 + lb * 160 + (64 + lc + (c >> 3)) * 2;
    u32 a_av = L_XA + wave * 640 + (c & 3) * 160 + r * 16, a_ah = a_av + 128 - r * 8;
    u32 a_red = L_R2 + ((wave * 4 + c) * 32 + r * 4) * 4;
    u32 a_st = L_S2 + ((lb * 2 + (c >> 3)) * 8 + wave) * 4;
    u32 a_fin = L_R2 + ((hw * 16 + (wave & 3)) * 32 + fc) * 4;
    u32 a_fst = L_S2 + (wave & 3) * 64;
    u32 a_out = c < 4 ? G_OUT + (wave * 5 * 54 + jB) * 128 + c * 32 + r * 8 : OOB;
    constexpr int L_F2 = L_XR;
    u32 a_fi = BM ? L_F2 + c * 64 + r * 16 : L_FI + (c & 3) * 64 + r * 16, a_fo = (BM ? L_F2 + 768 : L_FI) + ((wave & 3) * 32 + fc) * 2;
    u32 a_ou4 = G_OUT + ((wave * 5 + (c >> 2)) * 54 + jB) * 128 + (c & 3) * 32 + r * 8;
    asm volatile("" : "+v"(a_ou4));
    u32 a_pre = lane < 40 ? G_OUT + ((wave * 5 + (lane >> 3)) * 54 + jB) * 128 + (lane & 7) * 16 : OOB;
    u32x4 emp = {EMPTY, EMPTY, EMPTY, EMPTY};
    asm volatile("" : "+v"(a_pre), "+v"(emp));
#define OPRE() asm volatile("buffer_store_dwordx4 %0, %1, %2, 0 offen sc1\n\ts_nop 1" :: "v"(emp), "v"(a_pre), "s"(rsv) : "memory")
    if (OPOS == 0) OPRE();
    u32 a_w1 = L_W + wave * 5120 + (8 * r + (c >> 2)) * 64 + (r & 1) * 32 + (c & 3) * 8;
    u32 a_wh = L_W + wave * 5120 + 4096 + (4 * r + (c >> 2)) * 64 + (r >> 1) * 32 + (c & 3) * 8;
    u32 a_w2[5];
#pragma unroll
    for (int k = 0; k < 5; ++k) { const int n2 = wave * 5 + k; a_w2[k] = L_WM2 + (n2 >> 2) * 4096 + (tr128 ^ ((n2 & 3) * 32)); }
    asm volatile("" : "+v"(sel), "+v"(go), "+v"(go4), "+v"(gl), "+v"(a_u), "+v"(a_u4), "+v"(a_av), "+v"(a_ah), "+v"(a_red), "+v"(a_st), "+v"(a_fin), "+v"(a_fst), "+v"(a_out));
    asm volatile("" : "+v"(a_fi), "+v"(a_fo), "+v"(a_w1), "+v"(a_wh), "+v"(a_w2[0]), "+v"(a_w2[1]), "+v"(a_w2[2]), "+v"(a_w2[3]), "+v"(a_w2[4]) :: "memory");
    if (BM) *reinterpret_cast<u32*>(lds + L_F2 + t * 4) = 0;
    lds_wait<1>(C_MF, 4);
    float c1f[5];
    {
      const unsigned char* cp = lds + L_XA + (4 + lb) * XS + wave * 160;
#pragma unroll
      for (int i = 0; i < 2; ++i) { const u32 w = *reinterpret_cast<const u32*>(cp + (32 * i + (c >> 3) * 16 + lc) * 2); c1f[2 * i] = lo16(w); c1f[2 * i + 1] = hi16(w); }
      c1f[4] = u2f((u32)*reinterpret_cast<const unsigned short*>(cp + (64 + lc + (c >> 3)) * 2) << 16);
    }
    const unsigned char* pm = lds + L_XA + (4 + (c & 7)) * XS + wave * 160;
    u32x4 mv0 = *reinterpret_cast<const u32x4*>(pm + r * 16), mv1 = *reinterpret_cast<const u32x4*>(pm + 64 + r * 16);
    s16x4 mv2 = *reinterpret_cast<const s16x4*>(pm + 128 + r * 8);
    u32 a_rd = L_RED + ((wave * 8 + c) * 32 + r * 4) * 4;
    asm volatile("" : "+v"(c1f[0]), "+v"(c1f[1]), "+v"(c1f[2]), "+v"(c1f[3]), "+v"(c1f[4]), "+v"(mv0), "+v"(mv1), "+v"(mv2), "+v"(a_rd) :: "memory");
    if (wave < 4) {
      STAMPT(8, 0);
      lds_wait<2>(C_ISS, 8);
      const int h5 = ROT1 * (jB & 1);
      const bf16* wp = a.w1 + (size_t)(wave * 160 + (lane >> 2)) * FF + jB * 32 + ((((((lane >> 1) & 1) ^ (lane >> 5)) << 1) | (lane & 1)) * 8);
      const bf16* wp2 = a.w2 + (size_t)(jB * 32 + wave * 8 + (lane >> 3)) * DM + (s128e ^ ((wave & 1) * 32));
#pragma unroll
      for (int n = 0; n < 20; ++n) {
        if (n == 0) PAD(bd, back);
        if (n == N1B) lds_wait<2>(C_PUB, 1);
        if (n < 5 || (n >= 5 + W2F && n < 10 + W2F)) {
          const int i2 = n < 5 ? n + h5 : n - W2F - h5;
          dma16(wp + (size_t)(16 * i2) * FF, L_W + (wave * 10 + i2) * 1024);
        } else {
          const int k = n < 5 + W2F ? n - 5 : n - 10;
          dma16(wp2 + k * 64, L_WM2 + (k * 4 + wave) * 1024);
        }
      }
      STAMPT(16, 0); STAMPT(5, 128);
    }
    asm volatile("s_waitcnt vmcnt(%0)" :: "i"(10 - W2F) : "memory");
    STAMPT(23, 0);
    PAD(b1, back);
    BAR();
    STAMPT(3, 0);
    if (OPOS == 1) OPRE();
    u32x2 sg;
    if (HEARLY) heads_up_start(rsv, gl, 0xffffull, tg, sg);
    if (HEARLY && OPOS == 3) OPRE();
    s16x8 wf[2][2], wg[5];
    s16x4 wh[2];
#pragma unroll
    for (int nt = 0; nt < 2; ++nt) {
#pragma unroll
      for (int s = 0; s < 2; ++s) wf[s][nt] = wfrag<256>((a_w1 ^ (nt * 32)) + s * 2048);
      wh[nt] = __builtin_amdgcn_ds_read_tr16_b64_v4i16((lds_s4p)(uintptr_t)(a_wh ^ (nt * 32)));
    }
#define W1MMA(acc) \
      f32x4 acc[2]; \
      _Pragma("unroll") for (int nt = 0; nt < 2; ++nt) { const f32x4 z_ = {0.f, 0.f, 0.f, 0.f}; acc[nt] = MFMA(wf[0][nt], av0, z_); } \
      _Pragma("unroll") for (int nt = 0; nt < 2; ++nt) acc[nt] = MFMA(wf[1][nt], av1, acc[nt]); \
      _Pragma("unroll") for (int nt = 0; nt < 2; ++nt) acc[nt] = __builtin_amdgcn_mfma_f32_16x16x16bf16_1k(wh[nt], av2, acc[nt], 0, 0, 0);
    {
      const u32x4 av0 = mv0, av1 = mv1;
      const s16x4 av2 = mv2;
      W1MMA(acc)
#pragma unroll
      for (int nt = 0; nt < 2; ++nt) if (c < 8) *reinterpret_cast<f32x4*>(lds + a_rd + nt * 64) = acc[nt];
    }
    float Tc = 0.f, Ts = 0.f;
#define TSUM() _Pragma("unroll") for (int k = 0; k < 4; ++k) { Tc += red[((hw * 4 + k) * 8 + (wave & 3)) * 32 + fc]; Ts += red[((hw * 4 + k) * 8 + 4 + (wave & 3)) * 32 + fc]; }
    u32x4 pa[5];
    f32x4 s4, s0, s1;
    {
      u64 m[5], old = 0;
#pragma unroll
      for (int i = 0; i < 5; ++i) { m[i] = ~0ull; pa[i] = u32x4{0u, 0u, 0u, 0u}; }
#pragma unroll
      for (int i = 0; i < 5; ++i) asm volatile("" : "+v"(c1f[i]), "+v"(pa[i]));
      if (OPOS == 2) OPRE();
      if (HGT) SPINLOOP for (int k = 0; k < 64; ++k) if ((int)((u32)__builtin_amdgcn_s_memrealtime() - tb) >= (126 * TAU_E >> 4) + HGT) break;
      if (!HEARLY) heads_up_start(rsv, gl, 0xffffull, tg, sg);
      if (!HEARLY && OPOS == 3) OPRE();
      STAMPT(9, 0); STAMPT(30, 448);
      asm volatile("s_waitcnt vmcnt(%0)" :: "i"(OPOS == 3 ? 1 : 0) : "memory");
      STAMPT(20, 0); STAMPT(31, 448);
      if (!BAR2L) BAR();
      STAMPT(29, 0);
#define BLOOK(W_) \
        asm volatile(LK(0, 5, "0", 0, 7) LK(1, 5, "0", 512, 8) LK(2, 5, "0", 2048, 9) LK(3, 5, "0", 2560, 10) LK(4, 6, "0", 0, 11) LKEND \
                     : "+v"(pa[0]), "+v"(pa[1]), "+v"(pa[2]), "+v"(pa[3]), "+v"(pa[4]) \
                     : "v"(go), "v"(go4), "s"(m[0]), "s"(m[1]), "s"(m[2]), "s"(m[3]), "s"(m[4]), [rs] "s"(rsv), [w] "i"(W_) : "memory");
      heads_up<0, OPOS == 3 ? 1 : 0>(rsv, gl, 0xffffull, tg, sg);
      heads_up_time(0xffffull, sg);
      STAMPT(21, 0);
      BLOOK(OPOS == 3 ? 5 : 63)
      announce(rs, G_HB + jB * 128, C_GO, C_W, 8, tb, HCD, tg);
      if (BAR2L) BAR();
#pragma unroll
      for (int k = 0; k < 5; ++k) wg[k] = wfrag<512>(a_w2[k]);
      PAD(b2, back);
      TSUM()
#pragma unroll
      for (int i = 0; i < 5; ++i) asm volatile("" : "+v"(wg[i]));
      asm volatile("" : "+v"(Tc), "+v"(Ts));
      STAMPT(18, 0);
      asm volatile("s_waitcnt vmcnt(1)" : "+v"(pa[0]), "+v"(pa[1]), "+v"(pa[2]), "+v"(pa[3]), "+v"(pa[4]) :: "memory");
      STAMPT(22, 0);
#define BSUM() { const f32x4 z = {0.f, 0.f, 0.f, 0.f}; \
      s4 = MFMA(sel, pa[4], z); s0 = MFMA(sel, pa[0], z); s1 = MFMA(sel, pa[2], z); s0 = MFMA(sel, pa[1], s0); s1 = MFMA(sel, pa[3], s1); }
      SPINLOOP for (unsigned spins = 0; spins < SPIN_CAP; ++spins) {
        BSUM()
        if (!(__builtin_amdgcn_ballot_w64(__builtin_isunordered(s4[0], s4[1])) | __builtin_amdgcn_ballot_w64(__builtin_isunordered(s0[0], s0[1])) |
              __builtin_amdgcn_ballot_w64(__builtin_isunordered(s1[0], s1[1])))) break;
        u64 bad = 0;
#pragma unroll
        for (int i = 0; i < 5; ++i) { m[i] = __builtin_amdgcn_ballot_w64(missing(pa[i])); bad |= m[i]; }
        if (!(bad | old)) break;
        old = bad;
        if (!spins) FAIL(2);
        BLOOK(5 * (PDB - 1))
      }
    }
    STAMPT(14, 0); WSTAMP(3);
    float xf[5];
    {
      float e0 = s4[0], e1 = s4[1];
      asm("" : "+v"(e0), "+v"(e1));
      xf[4] = bfr(((c & 8) ? e1 : e0) + dpp<XOR8>((c & 8) ? e0 : e1));
      *reinterpret_cast<unsigned short*>(lds + a_u4) = (unsigned short)cvt2(xf[4] * c1f[4], 0.f);
      const u32 w0 = cvt2(s0[0], s0[1]), w1 = cvt2(s1[0], s1[1]);
      xf[0] = lo16(w0); xf[1] = hi16(w0); xf[2] = lo16(w1); xf[3] = hi16(w1);
      *reinterpret_cast<u32*>(lds + a_u) = cvt2(xf[0] * c1f[0], xf[1] * c1f[1]);
      *reinterpret_cast<u32*>(lds + a_u + 64) = cvt2(xf[2] * c1f[2], xf[3] * c1f[3]);
    }
    STAMPT(19, 0);
    {
      u32x4 av0 = *reinterpret_cast<const u32x4*>(lds + a_av), av1 = *reinterpret_cast<const u32x4*>(lds + a_av + 64);
      s16x4 av2 = *reinterpret_cast<const s16x4*>(lds + a_ah);
      __builtin_amdgcn_sched_barrier(0);
      const float ps = ((xf[0] + xf[1]) + (xf[2] + xf[3])) + xf[4];
      const float pq = fmaf(xf[0], xf[0], fmaf(xf[1], xf[1], xf[4] * xf[4])) + fmaf(xf[2], xf[2], xf[3] * xf[3]);
      const float t1 = ps + dpp<XOR8>(ps), t2 = pq + dpp<XOR8>(pq);
      float z = (c & 8) ? t2 : t1;
      z += dpp<XOR1>(z);
      asm volatile("" : "+v"(z));
      __builtin_amdgcn_sched_barrier(0);
      W1MMA(acc)
      z = cross_rows_sum(z);
      if ((lane & 49) == 0) *reinterpret_cast<float*>(lds + a_st) = z;
      if (c < 4) {
#pragma unroll
        for (int nt = 0; nt < 2; ++nt) *reinterpret_cast<f32x4*>(lds + a_red + nt * 64) = acc[nt];
      }
    }
#undef W1MMA
    PAD(b3, back);
    BAR();
    STAMPT(24, 0);
    if (wave < 4) {
      f32x4 sv[4];
      float sp[4];
#pragma unroll
      for (int k = 0; k < 4; ++k) sv[k] = *reinterpret_cast<const f32x4*>(lds + a_fst + k * 16);
#pragma unroll
      for (int k = 0; k < 4; ++k) sp[k] = *reinterpret_cast<const float*>(lds + a_fin + k * 512);
      __builtin_amdgcn_sched_barrier(0);
      const f32x4 s1 = sv[0] + sv[1], s2 = sv[2] + sv[3];
      const float mean = ((s1[0] + s1[1]) + (s1[2] + s1[3])) * (1.f / DM);
      const float var = fmaxf(((s2[0] + s2[1]) + (s2[2] + s2[3])) * (1.f / DM) - mean * mean, 0.f);
      const float rstd = __builtin_amdgcn_rsqf(var + 1e-5f);
      const float zp = fmaf(rstd, fmaf(-mean, Tc, (sp[0] + sp[1]) + (sp[2] + sp[3])), Ts);
      const float hh = bfr(bfr(swap32_add(zp, zp)) + b1v);
      const float e2 = __builtin_amdgcn_exp2f(hh * fmaf(hh * hh, 0.1029432397f, 2.302208198f));
      const float f = fmaf(-hh, frcp(e2 + 1.f), hh);
      *reinterpret_cast<unsigned short*>(lds + a_fo) = (unsigned short)cvt2(f, 0.f);
    }
    BAR();
    if (BM) {
      u32x4 av[4];
#pragma unroll
      for (int k = 0; k < 4; ++k) av[k] = *reinterpret_cast<const u32x4*>(lds + a_fi + (3 - k) * 256);
      const f32x4 z = {0.f, 0.f, 0.f, 0.f};
      f32x4 a4 = MFMA(wg[4], av[0], z), a0 = MFMA(wg[0], av[0], z);
#pragma unroll
      for (int k = 1; k < 4; ++k) a0 = MFMA(wg[k], av[k], a0);
      __builtin_amdgcn_sched_barrier(0);
      const u32x2 o4 = {min(cvt2(a4[0], a4[1]), EMPTY - 1), min(cvt2(a4[2], a4[3]), EMPTY - 1)};
      __builtin_amdgcn_raw_buffer_store_b64(o4, rs, a_out, 4 * (54 * 128), 16);
      const u32x2 o0 = {min(cvt2(a0[0], a0[1]), EMPTY - 1), min(cvt2(a0[2], a0[3]), EMPTY - 1)};
      __builtin_amdgcn_raw_buffer_store_b64(o0, rs, a_ou4, 0, 16);
    } else {
      const u32x4 av = *reinterpret_cast<const u32x4*>(lds + a_fi);
      f32x4 acc[5];
#pragma unroll
      for (int k = 0; k < 5; ++k) { const f32x4 z = {0.f, 0.f, 0.f, 0.f}; acc[k] = MFMA(wg[k], av, z); }
      __builtin_amdgcn_sched_barrier(0);
#pragma unroll
      for (int k = 0; k < 5; ++k) {
        const u32x2 o = {min(cvt2(acc[k][0], acc[k][1]), EMPTY - 1), min(cvt2(acc[k][2], acc[k][3]), EMPTY - 1)};
        __builtin_amdgcn_raw_buffer_store_b64(o, rs, a_out, k * (54 * 128), 16);
      }
    }
    STAMPT(15, 0);
    asm volatile("" :: "v"(pa[0]), "v"(pa[1]), "v"(pa[2]), "v"(pa[3]), "v"(pa[4]));

  } else if (role == 2 && wave == 0) {
    PAD(c0, back);
    const int lb = (c >> 1) & 3, col = g * 16 + (c & 1) * 8 + 2 * r + (c >> 3);
    bf16* outp;
    asm volatile("s_load_dwordx2 %0, %1, 128\n\ts_waitcnt lgkmcnt(0)" : "=s"(outp) : "s"(__builtin_amdgcn_kernarg_segment_ptr()));
    const __amdgpu_buffer_rsrc_t ro = __builtin_amdgcn_make_buffer_rsrc(outp, 0, NB * DM * 2, 0x00020000);
#define DUMMY() __builtin_amdgcn_raw_buffer_store_b16((unsigned short)0, ro, (lb * DM + col) * 2, 0, DAUX)
    if (DPOS == 0) DUMMY();
    float b2v = bf2f(a.b2[col]);
    const f32x4 z = {0.f, 0.f, 0.f, 0.f};
    SELECTOR(sel)
    float x1v, gbv;
    {
      u32 offs[1] = {G_MF + ((12 + lb) * 160 + (col >> 2)) * 16};
      u32x4 gm[1];
      gather<1>(rs, offs, gm, tg);
      const u32 gbw = (r & 1) ? gm[0][2] : gm[0][0];
      gbv = (c & 8) ? hi16(gbw) : lo16(gbw);
    }
    u32 oo = lb * DM + col;
    asm volatile("" : "+v"(gbv), "+v"(oo), "+v"(sel), "+v"(b2v));
    u32x4 pa[7];
    u32 go = G_OUT + g * (54 * 128) + lane * 16, gx = G_WO + g * 1024 + lane * 16;
#pragma unroll
    for (int i = 0; i < 7; ++i) { pa[i] = u32x4{0u, 0u, 0u, 0u}; asm volatile("" : "+v"(pa[i])); }
    asm volatile("" : "+v"(go), "+v"(gx));
    u32x2 hv;
    if (CPUB) lds_wait<2>(C_PUB, 1);
    if (DPOS == 1) DUMMY();
    heads_up_start(rsv, G_HA + ((g >> 2) * 8 + lane) * 128, 0xffull, tg, hv);
    heads_up<CSL>(rsv, G_HA + ((g >> 2) * 8 + lane) * 128, 0xffull, tg, hv);
    if (DPOS == 2) DUMMY();
    if (EPE == 1) __builtin_amdgcn_raw_buffer_store_b32(tg, rs, u == 0 && lane == 0 ? WS_EPOCH : OOB, 0, 16);
    hv[0] += CXD;
    heads_up_time(0xffull, hv);
    {
      u32x4 gm[1];
      gather_e<1, 2, 1>(rsv, gx, gm, ~0ull);
      const f32x4 sm = MFMA(sel, gm[0], z);
      x1v = bfr(PAIRSUM(sm));
      asm volatile("" : "+v"(x1v));
    }
    STAMPT(23, 0);
    if (DPOS == 3) DUMMY();
    if (EPE == 2) __builtin_amdgcn_raw_buffer_store_b32(tg, rs, u == 0 && lane == 0 ? WS_EPOCH : OOB, 0, 16);
    PAD(c1, back);
    heads_up_start(rsv, G_HB + lane * 128, (1ull << 54) - 1, tg, hv);
    heads_up<0>(rsv, G_HB + lane * 128, (1ull << 54) - 1, tg, hv);
    heads_up_time((1ull << 54) - 1, hv);
    STAMPT(14, 0);
    f32x4 s0, s1;
    {
      u64 m[7], old = 0;
#pragma unroll
      for (int i = 0; i < 7; ++i) m[i] = i < 6 ? ~0ull : (1ull << 48) - 1;
      look<0>(rsv, go, pa, m);
#define CSUM() { s0 = MFMA(sel, pa[0], z); s1 = MFMA(sel, pa[1], z); \
      _Pragma("unroll") for (int i = 2; i < 7; ++i) { if (i & 1) s1 = MFMA(sel, pa[i], s1); else s0 = MFMA(sel, pa[i], s0); } \
      s0 += s1; }
      SPINLOOP for (unsigned spins = 0; spins < SPIN_CAP; ++spins) {
        CSUM()
        if (!__builtin_amdgcn_ballot_w64(__builtin_isunordered(s0[0], s0[1]))) break;
        u64 bad = 0;
#pragma unroll
        for (int i = 0; i < 7; ++i) { m[i] = __builtin_amdgcn_ballot_w64(missing(pa[i])); bad |= m[i]; }
        if (!(bad | old)) break;
        old = bad;
        if (!spins) FAIL(3);
        look<7 * (PDC - 1)>(rsv, go, pa, m);
      }
    }
    STAMPT(16, 0);
    const float y = bfr(bfr(PAIRSUM(s0)) + b2v);
    if (OUTM & 1) __builtin_amdgcn_raw_buffer_store_b16((unsigned short)cvt2(x1v + bfr(gbv * y), 0.f), ro, oo * 2, 0, FAUX);
    else outp[oo] = __float2bfloat16(x1v + bfr(gbv * y));
    STAMPT(17, 0);
    asm volatile("" :: "v"(pa[0]), "v"(pa[1]), "v"(pa[2]), "v"(pa[3]), "v"(pa[4]), "v"(pa[5]), "v"(pa[6]), "v"(hv));

    if (!EPE && u == 0 && t == 0)
      __hip_atomic_store(reinterpret_cast<u32*>(a.ws + WS_EPOCH), tg, __ATOMIC_RELAXED, __HIP_MEMORY_SCOPE_AGENT);
  }
  else if (EPE == 3 && u == 80 && wave == 0) {
    u32x2 hv;
    lds_wait<2>(C_PUB, 1);
    heads_up_start(rsv, G_HA + lane * 128, 0xffull, tg, hv);
    heads_up<8>(rsv, G_HA + lane * 128, 0xffull, tg, hv);
    __builtin_amdgcn_raw_buffer_store_b32(tg, rs, lane < 32 ? WS_EPOCH + lane * 4 : OOB, 0, 16);
  }
  STAMP_FLUSH;
}

extern "C" void kernel_launch(void* const* d_in, const int* in_sizes, int n_in, void* d_out, int out_size, void* d_ws,
                              size_t ws_size, hipStream_t stream) {
  Args a;
  a.x = Desc{(const bf16*)d_in[0], NB * DM * 2, 0x00020000u};
  a.kc = (const bf16*)d_in[1];
  a.vc = (const bf16*)d_in[2];
  a.wq = Desc{(const bf16*)d_in[3], DM * QD * 2, 0x00020000u};
  a.wo = (const bf16*)d_in[4];
  a.w1 = (const bf16*)d_in[6];
  a.b1 = (const bf16*)d_in[7];
  a.w2 = (const bf16*)d_in[8];
  a.b2 = (const bf16*)d_in[9];
  a.c = Desc{(const bf16*)d_in[10], NB * DM * 2, 0x00020000u};
  a.wmod = Desc{(const bf16*)d_in[11], DM * MODN * 2, 0x00020000u};
  a.out = (bf16*)d_out;
  a.ws = (unsigned char*)d_ws;
  mega_kernel<<<NWG, NT, 0, stream>>>(a);
}
```

```cpp
#define SPINLOOP _Pragma("nounroll")
#define P_LAG 14
#define P_NPRE 32
#define P_NVE 6
#define P_NVA 6
#define QODD 4
#define QEVEN 4
#define MFPOS 0
#define EA 0
#define EB 0
#define EC 4
#define APOLL 0
#define N1E N1A
#define TAU_A 236
#define TAU_B 239
#define TAU_X TAU_A
#define TAU_E 243
#define PK 0
#define PL 1
#define QPRE 1
#define QHOLD 0
#define CT0 1
#define TOFF 55
#define BLIGHT 1
#define BPRE 0
#define BW1 1
#define BMASK 1
#define CSLEEP 1
#define CSL 2
#define CPUB 1
#define CXD 20
#define OPOS 1
#define NREPL 1
#define OUTPRE 1
#define ASL 1
#define AEARLY 1
#define PDC 4
#define PDA 3
#define PDB 3
#define PSB 1
#define HC 1
#define HCD 132
#define HAG 40
#define AW 4
#define BFW -1
#define FULLF 1
#define WARM 1
#define HT 0
#define HX 10
#define HTD 55
#define HSL 1
#define NANT 1
#define HAW 65
#define BM 1
#define HPD 1
#define BT 0
#define BSL 2
#define PB 0
#define GOPOS 2
#define JMAP 1
#define N1A 10
#define N1B 5
#define PT 0
#define PTAUX 16
#define LAY 1
#define OUTM 1
#define EPE 0
#define DPOS 2
#define DAUX 16
#define FAUX 16
#define KOUT 1
#define NK0 12
#define GT 540
#define PLO 0
#define PLS 16
#define ANCH 1
#define AOFF 55
#define AGG 1
#define PHW 0
#define CPF 1
#define CLEN 0x7e00
#define AGT 0
#define GPAD 1
#define MFE 1
#define E3POS 10
#define E4POS 22
#define L1POS 28
#define L4POS 4
#define T1POS 18
#define HEARLY 0
#define BAR2L 0
#define ROT1 5
#define W2F 0
#define HGT 300
#define BGT 320
#define BPREW 0
#define BAN 2
#define HBD 85
#define CGT 0
#define P_GATE 8
#include <hip/hip_runtime.h>
#include <hip/hip_bf16.h>
#include <stdint.h>

using bf16 = __hip_bfloat16;
using u32 = unsigned;
using u64 = unsigned long long;
using u32x2 = __attribute__((ext_vector_type(2))) unsigned;
using u32x4 = __attribute__((ext_vector_type(4))) unsigned;
using f32x4 = __attribute__((ext_vector_type(4))) float;
using bf16x2 = __attribute__((ext_vector_type(2))) __bf16;
typedef __attribute__((ext_vector_type(2))) float f32x2;
typedef short s16x8 __attribute__((ext_vector_type(8)));
typedef short s16x4 __attribute__((ext_vector_type(4)));
typedef __attribute__((address_space(3))) s16x4* lds_s4p;
typedef __attribute__((ext_vector_type(8))) __bf16 bf16x8v;

constexpr int NB = 4, DM = 640, NH = 32, DH = 128, SK = 8192, WIN = 2048, FF = 1728, QD = 4096, MODN = 3840;
constexpr int NT = 512, NWG = 256;

constexpr u32 WS_EPOCH = 0;
constexpr u32 G_T = 128;
constexpr u32 G_Q = 256;
constexpr u32 G_MOD = G_Q + 8 * 1024 * 16;
constexpr u32 G_MF = G_MOD + 8 * 1024 * 16;
constexpr u32 G_HB = G_MF + 16 * 160 * 16;
constexpr u32 G_HA = G_HB + 64 * 128;
constexpr u32 G_HT = G_HA + 80 * 128;
constexpr u32 G_ML = G_HT + 256 * 128;
constexpr u32 EMPTY = 0xffffffffu;
constexpr int NREP = NREPL;
constexpr u32 G_WO = G_ML + 256 * 64;
constexpr u32 WO_REP = 40 * 1024;
constexpr u32 G_ATT = G_WO + NREP * WO_REP;
constexpr u32 G_OUT = G_ATT + 256 * 256;
constexpr u32 WS_END = G_OUT + 40 * 54 * 128;
constexpr u32 OOB = 0xF0000000u;
#define GIDX(n, ks, b, cg) ((((ks) * ((n) / 4) + ((cg) >> 2)) * 16 + (b) * 4 + ((cg) & 3)) * 16)

#define WSTAMP(k) do {} while (0)
#define STAMPW(i) do {} while (0)
#define STAMPT(i, tt) do {} while (0)
#define STAMP_DECL
#define STAMP_FLUSH do {} while (0)
#define FAIL(i) do {} while (0)
#define PAD(name, next) asm volatile(".Lpad_" #name ":\n\ts_cbranch_execz .Lpad_" #next)
#define PADX(name, next, x) asm volatile(".Lpad_" #name ":\n\ts_cbranch_execz .Lpad_" #next : "+v"(x))
#define PADA(name, next) asm volatile(".Lpad_" #name ":\n\ts_cbranch_execz .Lpad_" #next : "+v"(acc[0]), "+v"(acc[1]), "+v"(acc[2]), "+v"(acc[3]), "+v"(acc[4]), "+v"(acc[5]), "+v"(acc[6]), "+v"(acc[7]))
#define STR_(x) #x
#define STR(x) STR_(x)
#define BAR() asm volatile("s_waitcnt lgkmcnt(0)\n\ts_barrier" ::: "memory")
#define VM0() asm volatile("s_waitcnt vmcnt(0)" ::: "memory")

__device__ __forceinline__ float bfr(float x) { return __bfloat162float(__float2bfloat16(x)); }
__device__ __forceinline__ float bf2f(bf16 x) { return __bfloat162float(x); }
__device__ __forceinline__ float lo16(unsigned u) { return __builtin_bit_cast(float, u << 16); }
__device__ __forceinline__ float hi16(unsigned u) { return __builtin_bit_cast(float, u & 0xffff0000u); }
__device__ __forceinline__ u32 f2u(float f) { return __builtin_bit_cast(u32, f); }
__device__ __forceinline__ float u2f(u32 f) { return __builtin_bit_cast(float, f); }
__device__ __forceinline__ u32 cvt2(float a, float b) {
  const f32x2 v = {a, b};
  return __builtin_bit_cast(u32, __builtin_convertvector(v, bf16x2));
}
__device__ __forceinline__ float frcp(float x) { return __builtin_amdgcn_rcpf(x); }
__device__ __forceinline__ float fsqrt(float x) { return __builtin_amdgcn_sqrtf(x); }
__device__ __forceinline__ float fexp(float x) { return __builtin_amdgcn_exp2f(x * 1.4426950408889634f); }
__device__ __forceinline__ float dot2(unsigned k, unsigned q, float acc) {
  return __builtin_amdgcn_fdot2_f32_bf16(__builtin_bit_cast(bf16x2, k), __builtin_bit_cast(bf16x2, q), acc, false);
}
__device__ __forceinline__ float addlo(unsigned w, float acc) { u32 k = 0x00003f80u; asm("" : "+s"(k)); return dot2(w, k, acc); }
__device__ __forceinline__ float addhi(unsigned w, float acc) { return dot2(w, 0x3f800000u, acc); }
template <int CTRL> __device__ __forceinline__ float dpp(float x) {
  return __builtin_bit_cast(float, __builtin_amdgcn_mov_dpp(__builtin_bit_cast(int, x), CTRL, 0xf, 0xf, true));
}
template <int CTRL> __device__ __forceinline__ u32 dppu(u32 x) { return (u32)__builtin_amdgcn_mov_dpp((int)x, CTRL, 0xf, 0xf, true); }
constexpr int XOR1 = 0xB1, XOR2 = 0x4E, XOR7 = 0x141, XOR8 = 0x128, ROR4 = 0x124, ROR2 = 0x122, ROR1 = 0x121, SHL4 = 0x104;
__device__ __forceinline__ float swap32_add(float a, float b) {
  auto s = __builtin_amdgcn_permlane32_swap(f2u(a), f2u(b), false, false);
  return u2f(s[0]) + u2f(s[1]);
}
__device__ __forceinline__ float swap16_add(float a, float b) {
  auto s = __builtin_amdgcn_permlane16_swap(f2u(a), f2u(b), false, false);
  return u2f(s[0]) + u2f(s[1]);
}
__device__ __forceinline__ float cross_rows_sum(float x) { x = swap16_add(x, x); return swap32_add(x, x); }
__device__ __forceinline__ float cross_rows_max(float x) {
  auto s = __builtin_amdgcn_permlane16_swap(f2u(x), f2u(x), false, false);
  x = fmaxf(u2f(s[0]), u2f(s[1]));
  auto s2 = __builtin_amdgcn_permlane32_swap(f2u(x), f2u(x), false, false);
  return fmaxf(u2f(s2[0]), u2f(s2[1]));
}
__device__ __forceinline__ float wave_sum(float x) {
  x += dpp<XOR8>(x); x += dpp<ROR4>(x); x += dpp<ROR2>(x); x += dpp<ROR1>(x);
  return cross_rows_sum(x);
}
__device__ __forceinline__ float wave_max(float x) {
  x = fmaxf(x, dpp<XOR8>(x)); x = fmaxf(x, dpp<ROR4>(x)); x = fmaxf(x, dpp<ROR2>(x)); x = fmaxf(x, dpp<ROR1>(x));
  return cross_rows_max(x);
}

__device__ __forceinline__ void st_gran(__amdgpu_buffer_rsrc_t rs, u32 off, u32 a, u32 b, u32 tg) {
  u32x4 v = {a, tg, b, tg};
  __builtin_amdgcn_raw_buffer_store_b128(v, rs, off, 0, 16);
}
__device__ __forceinline__ u32x4 ld_gran(__amdgpu_buffer_rsrc_t rs, u32 off) {
  return __builtin_amdgcn_raw_buffer_load_b128(rs, off, 0, 16);
}
constexpr unsigned SPIN_CAP = 1u << 18;
template <int N>
__device__ __forceinline__ void gather(__amdgpu_buffer_rsrc_t rs, const u32 (&offs)[N], u32x4 (&g)[N], u32 tg) {
  SPINLOOP for (unsigned spins = 0; spins < SPIN_CAP; ++spins) {
    bool ok = true;
#pragma unroll
    for (int i = 0; i < N; ++i) g[i] = ld_gran(rs, offs[i]);
#pragma unroll
    for (int i = 0; i < N; ++i) ok &= (offs[i] == OOB) | ((g[i][1] == tg) & (g[i][3] == tg));
    if (__all(ok)) break;
    __builtin_amdgcn_s_sleep(1);
  }
}
template <int N>
__device__ __forceinline__ void gather_m(__amdgpu_buffer_rsrc_t rs, u32 (&offs)[N], u32 (&va)[N], u32 (&vb)[N], u32 tg) {
#pragma unroll
  for (int i = 0; i < N; ++i) { va[i] = 0; vb[i] = 0; }
  SPINLOOP for (unsigned spins = 0; spins < SPIN_CAP; ++spins) {
    u32x4 g[N];
#pragma unroll
    for (int i = 0; i < N; ++i) g[i] = ld_gran(rs, offs[i]);
    bool ok = true;
#pragma unroll
    for (int i = 0; i < N; ++i) {
      const bool hit = (offs[i] != OOB) & (g[i][1] == tg) & (g[i][3] == tg);
      va[i] = hit ? g[i][0] : va[i]; vb[i] = hit ? g[i][2] : vb[i];
      offs[i] = hit ? OOB : offs[i];
      ok &= (offs[i] == OOB);
    }
    if (__all(ok)) break;
    __builtin_amdgcn_s_sleep(1);
  }
}

__device__ __forceinline__ bool missing(u32x4 v) { return max(max(v[0], v[1]), max(v[2], v[3])) == EMPTY; }
#define LK(v, o, so, imm, m) "s_mov_b64 exec, %" #m "\n\tbuffer_load_dwordx4 %" #v ", %" #o ", %[rs], " so " offen offset:" #imm " sc1\n\t"
#define LKEND "s_mov_b64 exec, -1\n\ts_waitcnt vmcnt(%[w])"
template <int W> __device__ __forceinline__ void look(u32x4 rs, u32 off, u32x4 (&v)[1], const u64 (&m)[1]) {
  asm volatile(LK(0, 1, "0", 0, 2) LKEND : "+v"(v[0]) : "v"(off), "s"(m[0]), [rs] "s"(rs), [w] "i"(W) : "memory");
}
template <int W> __device__ __forceinline__ void look(u32x4 rs, u32 off, u32x4 (&v)[5], const u64 (&m)[5]) {
  asm volatile(LK(0, 5, "0", 0, 6) LK(1, 5, "0", 1024, 7) LK(2, 5, "0", 2048, 8) LK(3, 5, "0", 3072, 9) LK(4, 5, "%11", 0, 10) LKEND
               : "+v"(v[0]), "+v"(v[1]), "+v"(v[2]), "+v"(v[3]), "+v"(v[4])
               : "v"(off), "s"(m[0]), "s"(m[1]), "s"(m[2]), "s"(m[3]), "s"(m[4]), "s"(4096u), [rs] "s"(rs), [w] "i"(W) : "memory");
}
template <int W> __device__ __forceinline__ void look(u32x4 rs, u32 off, u32x4 (&v)[7], const u64 (&m)[7]) {
  asm volatile(LK(0, 7, "0", 0, 8) LK(1, 7, "0", 1024, 9) LK(2, 7, "0", 2048, 10) LK(3, 7, "0", 3072, 11)
               LK(4, 7, "%15", 0, 12) LK(5, 7, "%15", 1024, 13) LK(6, 7, "%15", 2048, 14) LKEND
               : "+v"(v[0]), "+v"(v[1]), "+v"(v[2]), "+v"(v[3]), "+v"(v[4]), "+v"(v[5]), "+v"(v[6])
               : "v"(off), "s"(m[0]), "s"(m[1]), "s"(m[2]), "s"(m[3]), "s"(m[4]), "s"(m[5]), "s"(m[6]), "s"(4096u), [rs] "s"(rs), [w] "i"(W) : "memory");
}
template <int N, int SLEEP, int D>
__device__ __forceinline__ void gather_e(u32x4 rs, u32 off, u32x4 (&v)[N], u64 last) {
  u64 m[N];
#pragma unroll
  for (int i = 0; i < N; ++i) { m[i] = i < N - 1 ? ~0ull : last; v[i] = u32x4{0u, 0u, 0u, 0u}; }
  look<0>(rs, off, v, m);
  SPINLOOP for (unsigned spins = 0; spins < SPIN_CAP; ++spins) {
    u64 bad = 0;
#pragma unroll
    for (int i = 0; i < N; ++i) { m[i] = __builtin_amdgcn_ballot_w64(missing(v[i])); bad |= m[i]; }
    if (!bad) break;
    if (SLEEP) __builtin_amdgcn_s_sleep(SLEEP);
    look<N * (D - 1)>(rs, off, v, m);
  }
}

#define HLOOK(W_) asm volatile("s_mov_b64 exec, %2\n\tbuffer_load_dwordx2 %0, %1, %3, 0 offen sc1\n\ts_mov_b64 exec, -1\n\ts_waitcnt vmcnt(%4)" \
                               : "+v"(v) : "v"(off), "s"(m), "s"(rs), "i"(W_) : "memory")
__device__ __forceinline__ void heads_up_start(u32x4 rs, u32 off, u64 m, u32 tg, u32x2& v) { v = u32x2{0u, ~tg}; HLOOK(63); }
template <int SLEEP, int W0 = 0>
__device__ __forceinline__ void heads_up(u32x4 rs, u32 off, u64 m, u32 tg, u32x2& v) {
  asm volatile("s_waitcnt vmcnt(%1)" : "+v"(v) : "i"(W0) : "memory");
  SPINLOOP for (unsigned spins = 0; spins < SPIN_CAP; ++spins) {
    m &= __builtin_amdgcn_ballot_w64(v[1] != tg);
    if (!m) break;
    if (SLEEP) __builtin_amdgcn_s_sleep(SLEEP);
    HLOOK(0);
  }
}
__device__ __forceinline__ void heads_up_time(u64 all, u32x2 v) {
  SPINLOOP for (unsigned spins = 0; spins < 64; ++spins)
    if (!(all & __builtin_amdgcn_ballot_w64((int)((u32)__builtin_amdgcn_s_memrealtime() - v[0]) < 0))) break;
}
__device__ __forceinline__ void announce(__amdgpu_buffer_rsrc_t rs, u32 off, u32 a_max, u32 a_cnt, u32 n, u32 tb, u32 d, u32 tg) {
  u32 tk = 0, mx;
  const u32 f = (u32)__builtin_amdgcn_s_memrealtime() - tb + d;
  asm volatile("s_mov_b64 exec, 1\n\tds_max_u32 %2, %3\n\tds_add_rtn_u32 %0, %4, %5\n\tds_read_b32 %1, %2\n\ts_mov_b64 exec, -1\n\ts_waitcnt lgkmcnt(0)"
               : "+v"(tk), "=&v"(mx) : "v"(a_max), "v"(f), "v"(a_cnt), "v"(1u) : "memory");
  const u32 lane = threadIdx.x & 63;
  __builtin_amdgcn_raw_buffer_store_b64(u32x2{__builtin_amdgcn_readfirstlane(mx) + tb, tg}, rs,
                                        (lane < 16 && __builtin_amdgcn_readfirstlane(tk) == n - 1) ? off + lane * 8 : OOB, 0, 16);
}

__device__ __forceinline__ void st_g22(__amdgpu_buffer_rsrc_t rs, u32 off, f32x4 v, u32 tg) {
  const u32 a0 = f2u(v[0]) + 0x200u, a1 = f2u(v[1]) + 0x200u, a2 = f2u(v[2]) + 0x200u, a3 = f2u(v[3]) + 0x200u;
  const u32x4 w = {(a0 >> 10) | ((a1 << 12) & 0xffc00000u), (a1 >> 20) | (tg << 12), (a2 >> 10) | ((a3 << 12) & 0xffc00000u), (a3 >> 20) | (tg << 12)};
  __builtin_amdgcn_raw_buffer_store_b128(w, rs, off, 0, 16);
}
__device__ __forceinline__ f32x4 un_g22(u32x4 w) {
  return f32x4{u2f(w[0] << 10), u2f(__builtin_amdgcn_alignbit(w[1], w[0], 22) << 10), u2f(w[2] << 10), u2f(__builtin_amdgcn_alignbit(w[3], w[2], 22) << 10)};
}
template <int N, int SLEEP>
__device__ __forceinline__ void gather22(__amdgpu_buffer_rsrc_t rs, u32 (&offs)[N], u32x4 (&v)[N], u32 tg, u32 a_inc = 0) {
#pragma unroll
  for (int i = 0; i < N; ++i) v[i] = u32x4{0u, 0u, 0u, 0u};
  SPINLOOP for (unsigned spins = 0; spins < SPIN_CAP; ++spins) {
    u32x4 g[N];
#pragma unroll
    for (int i = 0; i < N; ++i) g[i] = ld_gran(rs, offs[i]);
    if (a_inc) asm volatile("s_mov_b64 exec, 1\n\tds_add_u32 %0, %1\n\ts_mov_b64 exec, -1" :: "v"(a_inc), "v"(1u) : "memory");
    bool ok = true;
#pragma unroll
    for (int i = 0; i < N; ++i) {
      const bool hit = (offs[i] != OOB) & (((g[i][1] ^ (tg << 12)) | (g[i][3] ^ (tg << 12))) < 4096u);
#pragma unroll
      for (int k = 0; k < 4; ++k) v[i][k] = hit ? g[i][k] : v[i][k];
      offs[i] = hit ? OOB : offs[i];
      ok &= (offs[i] == OOB);
    }
    if (__all(ok)) break;
    __builtin_amdgcn_s_sleep(SLEEP);
  }
}

__device__ __forceinline__ int sel_lt(int a, int b, int x, int y) {
  int r;
  asm("s_cmp_lt_i32 %1, %2\n\ts_cselect_b32 %0, %3, %4" : "=s"(r) : "s"(a), "s"(b), "s"(x), "s"(y) : "scc");
  return r;
}
__device__ __forceinline__ u32 next_tag(u32 ep) { const u32 t = ep + 1u; return (t & 0xfffffu) ? t : t + 1u; }

__device__ __forceinline__ void lds_arrive(u32 addr, int lane) {
  asm volatile("s_waitcnt lgkmcnt(0)" ::: "memory");
  if (lane == 0) asm volatile("ds_add_u32 %0, %1" :: "v"(addr), "v"(1u) : "memory");
}
__device__ __forceinline__ u32 lds_ticket(u32 addr, int lane) {
  u32 r = 0;
  asm volatile("s_waitcnt lgkmcnt(0)" ::: "memory");
  if (lane == 0) asm volatile("ds_add_rtn_u32 %0, %1, %2\n\ts_waitcnt lgkmcnt(0)" : "=v"(r) : "v"(addr), "v"(1u) : "memory");
  return __builtin_amdgcn_readfirstlane(r);
}
template <int SLEEP>
__device__ __forceinline__ void lds_wait(u32 addr, u32 target) {
  SPINLOOP for (unsigned spins = 0; spins < (1u << 22); ++spins) {
    u32 v;
    asm volatile("ds_read_b32 %0, %1\n\ts_waitcnt lgkmcnt(0)" : "=v"(v) : "v"(addr) : "memory");
    if (v >= target) break;
    __builtin_amdgcn_s_sleep(SLEEP);
  }
}
__device__ __forceinline__ void dma16(const void* gsrc, u32 lds_dst) {
  u32 keep;
  asm volatile("s_mov_b32 %0, m0\n\ts_mov_b32 m0, %2\n\ts_nop 0\n\tglobal_load_lds_dwordx4 %1, off\n\ts_mov_b32 m0, %0"
               : "=&s"(keep) : "v"(gsrc), "s"(lds_dst) : "memory");
}
__device__ __forceinline__ void dma16nt(const void* gsrc, u32 lds_dst) {
  u32 keep;
  asm volatile("s_mov_b32 %0, m0\n\ts_mov_b32 m0, %2\n\ts_nop 0\n\tglobal_load_lds_dwordx4 %1, off nt\n\ts_mov_b32 m0, %0"
               : "=&s"(keep) : "v"(gsrc), "s"(lds_dst) : "memory");
}
__device__ __forceinline__ u32x4 ldasm(const void* p) {
  u32x4 v;
  asm volatile("global_load_dwordx4 %0, %1, off" : "=v"(v) : "v"(p) : "memory");
  return v;
}
__device__ __forceinline__ u32 ldasm_sc1(const void* p) {
  u32 v;
  asm volatile("global_load_dword %0, %1, off sc1" : "=v"(v) : "v"(p) : "memory");
  return v;
}
__device__ __forceinline__ u32x4 ldw(const bf16* p) { return *reinterpret_cast<const u32x4*>(p); }
template <int D2>
__device__ __forceinline__ s16x8 wfrag(u32 addr) {
  const s16x4 lo = __builtin_amdgcn_ds_read_tr16_b64_v4i16((lds_s4p)(uintptr_t)addr);
  const s16x4 hi = __builtin_amdgcn_ds_read_tr16_b64_v4i16((lds_s4p)(uintptr_t)(addr + D2));
  return s16x8{lo[0], lo[1], lo[2], lo[3], hi[0], hi[1], hi[2], hi[3]};
}
#define MFMA(w_, x_, c_) __builtin_amdgcn_mfma_f32_16x16x32_bf16(__builtin_bit_cast(bf16x8v, w_), __builtin_bit_cast(bf16x8v, x_), c_, 0, 0, 0)

struct Desc { const bf16* p; u32 bytes, flags; };
struct Args {
  Desc wq, x, wmod, c;
  unsigned char* ws;
  const bf16 *kc, *vc, *wo, *w1, *b1, *w2, *b2;
  bf16* out;
};

static_assert(offsetof(Args, ws) == 64 && offsetof(Args, out) == 128, "argument offsets");

constexpr int L_W = 0;
constexpr int L_WM2 = 40960;
constexpr int L_XA = 81920;
constexpr int XS = 1312, XH = 704;
constexpr int L_RED = L_XA + 16384;
constexpr int L_MOD = L_RED + 8192;
constexpr int L_XR = L_MOD + 5120;
constexpr int L_PT = L_XR + 5120;
constexpr int L_ACC = L_PT + 2048;
constexpr int L_QS = L_ACC + 4096;
constexpr int L_FI = L_QS + 256;
constexpr int L_ST = L_FI + 256;
constexpr int L_CNT = L_ST + 256;
constexpr int L_END = L_CNT + 128;
static_assert(L_END <= 160 * 1024, "LDS");
constexpr u32 C_Q = L_CNT, C_SC = L_CNT + 4, C_ISS = L_CNT + 8, C_P1 = L_CNT + 20, C_P2 = L_CNT + 24, C_GO = L_CNT + 28, C_W = L_CNT + 12, C_FIN = L_CNT + 32,
              C_KI = L_CNT + 36, C_PP = L_CNT + 40, C_PUB = L_CNT + 56, C_HT = L_CNT + 64, C_MF = L_CNT + 68;

constexpr int NL = 32;
constexpr int PVLAG = P_LAG;
constexpr int NPRE = P_NPRE;
constexpr int NVE = P_NVE;
static_assert(PVLAG <= 16 && (PVLAG & 1) == 0 && NVE <= PVLAG && (NVE & 1) == 0, "row groups");

__global__ void __launch_bounds__(NT, 2) mega_kernel(Args a) {
  __shared__ __attribute__((aligned(1024))) unsigned char lds[L_END];
  float* red = reinterpret_cast<float*>(lds + L_RED);
  u32* xraw = reinterpret_cast<u32*>(lds + L_XR);
  u32* pT = reinterpret_cast<u32*>(lds + L_PT);
  float* accs = reinterpret_cast<float*>(lds + L_ACC);
  u32* qs = reinterpret_cast<u32*>(lds + L_QS);
  float* st = reinterpret_cast<float*>(lds + L_ST);
  u32* cnt = reinterpret_cast<u32*>(lds + L_CNT);

  u32x4 rw, rx;
  unsigned char* wsq;
  const void* kcq;
  asm volatile("s_load_dwordx4 %0, %4, %5\n\ts_load_dwordx4 %1, %4, %5 offset:16\n\ts_load_dwordx2 %2, %4, 64\n\ts_load_dwordx2 %3, %4, 72"
               : "=&s"(rw), "=&s"(rx), "=&s"(wsq), "=&s"(kcq) : "s"(__builtin_amdgcn_kernarg_segment_ptr()), "s"((blockIdx.x & 1) * 32));
  __builtin_amdgcn_sched_barrier(0);
  const int t = threadIdx.x, lane = t & 63;
  const int wave = __builtin_amdgcn_readfirstlane(t >> 6);
  const int u = blockIdx.x;
  const int bh = u >> 1, half = u & 1, b_att = bh >> 5, h_att = bh & 31;
  const int r = lane >> 4, c = lane & 15;
  STAMP_DECL
  STAMPT(0, 0);
  u32 tg;
  const int g = u >> 1, ksp = g & 1, cbp = g >> 1;
  const int s128e = (((((lane & 7) >> 1) ^ ((lane >> 4) & 1)) << 1) | (lane & 1)) * 8;
  const u32 tr128 = (8 * r + (c >> 2)) * 128 + ((((c >> 3) & 1) | ((r & 1) << 1)) * 32) + (c & 3) * 8;
  if (wave < 4) {
    __builtin_amdgcn_s_setprio(3);
    const u32 rb = 8192 - 512 * half;
    const u32 hm = 0u - (u32)half;
    const u32 vo = ((u32)(lane >> 3) * rb + (((lane & 7) ^ ((lane >> 3) & 2)) ^ ((wave & 1) * 4)) * 16) | ((u32)(119 - g) & hm & OOB);
    u32 so = (u32)(ksp * 320 + 8 * wave) * rb + cbp * 128;
    const u32 step = 32 * rb, dstep = 4096, dst = L_W + wave * 1024;
    const u32 oe = wave * 1280 + lane * 16, oo = (r * 1280 + ksp * 640 + (c + 16 * wave) * 16) | ((u32)(39 - c - 16 * wave) & OOB);
    const u32 oa = (oo & hm) | (oe & ~hm), ob = (wave * 1280 + 1024 + c * 16) | (hm & OOB), oc = oe | (hm & OOB), zero = 0;
    u32 ep, keep;
    u32x4 xv[4];
    static_assert(G_ATT == G_WO + 40960 && G_OUT == G_ATT + 65536, "35 x 3 KB");
    const u32 pre = ((u32)u << 31) & (u32)(g - 35) & (u32)(2 - wave), po = G_WO + g * 3072 + lane * 16;
    const u32x4 emp = {EMPTY, EMPTY, EMPTY, EMPTY};
#define DMA1 "buffer_load_dwordx4 %7, %8, %1 offen nt lds\n\t"
#define DMAN "s_add_u32 m0, m0, %11\n\ts_add_u32 %1, %1, %10\n\t" DMA1
    asm volatile("s_mov_b32 %0, m0\n\ts_mov_b32 m0, %9\n\t"
                 "s_cmp_gt_i32 %18, 1\n\ts_waitcnt lgkmcnt(0)\n\ts_cbranch_scc1 1f\n\t"
                 "global_load_dword %2, %12, %13 sc1\n\t"
                 "buffer_load_dwordx4 %3, %14, %17, 0 offen\n\tbuffer_load_dwordx4 %4, %15, %17, 0 offen\n\t"
                 "buffer_load_dwordx4 %5, %16, %17, 0 offen offset:2560\n\tbuffer_load_dwordx4 %6, %15, %17, 0 offen offset:2560\n"
                 "1:\n\t" DMA1 DMAN DMAN DMAN DMAN DMAN DMAN DMAN DMAN DMAN "s_mov_b32 m0, %0\n\t"
                 "s_cmp_eq_u32 %19, 0\n\ts_cbranch_scc1 2f\n\tglobal_store_dwordx4 %20, %21, %13 sc1\n\t"
                 "global_store_dwordx4 %20, %21, %13 offset:1024 sc1\n\tglobal_store_dwordx4 %20, %21, %13 offset:2048 sc1\n2:\n\t"
#define B0W "s_barrier\n\ts_memrealtime vcc\n\ts_waitcnt vmcnt(10) lgkmcnt(0)\n\ts_sub_u32 %0, vcc_lo, " STR(AOFF)
                 B0W
                 : "=&s"(keep), "+s"(so), "=&v"(ep), "=&v"(xv[0]), "=&v"(xv[1]), "=&v"(xv[2]), "=&v"(xv[3])
                 : "v"(vo), "s"(rw), "s"(dst), "s"(step), "s"(dstep), "v"(zero), "s"(wsq), "v"(oa), "v"(ob), "v"(oc), "s"(rx), "s"(wave - half),
                   "s"(pre), "v"(po), "v"(emp) : "memory", "scc", "vcc");
#undef DMAN
#undef DMA1
    asm volatile("" : "+s"(wsq));
    const __amdgpu_buffer_rsrc_t rs = __builtin_amdgcn_make_buffer_rsrc(wsq, 0, WS_END, 0x00020000);
    if (half) {
      if (wave < 3) {
        u32x4 sv;
#pragma unroll
        for (int k = 0; k < 4; ++k) {
          const float c0 = lo16(xv[0][k]), c1 = hi16(xv[0][k]);
          sv[k] = cvt2(c0 * frcp(1.f + fexp(-c0)), c1 * frcp(1.f + fexp(-c1)));
        }
        if (c + 16 * wave < 40) *reinterpret_cast<u32x4*>(lds + L_XA + r * XH + (c + 16 * wave) * 16) = sv;
        if (t == 0) { cnt[4] = next_tag(ep); cnt[15] = keep; }
        lds_arrive(C_P1, lane);
        STAMPT(2, 0);
      }
    } else {
      if (wave < 2) {
#pragma unroll
        for (int i = 0; i < 2; ++i) {
          const int b = wave + 2 * i;
          float xf[16];
#pragma unroll
          for (int k = 0; k < 4; ++k) { xf[2 * k] = lo16(xv[2 * i][k]); xf[2 * k + 1] = hi16(xv[2 * i][k]); xf[8 + 2 * k] = lo16(xv[2 * i + 1][k]); xf[8 + 2 * k + 1] = hi16(xv[2 * i + 1][k]); }
          float s = 0.f, s2 = 0.f;
#pragma unroll
          for (int k = 0; k < 8; ++k) { s += xf[k]; s2 += xf[8 + k]; }
          if (lane < 16) s += s2;
          const float mean = wave_sum(s) * (1.f / DM);
          float v = 0.f, v2 = 0.f;
#pragma unroll
          for (int k = 0; k < 8; ++k) { const float d = xf[k] - mean; v += d * d; const float d2 = xf[8 + k] - mean; v2 += d2 * d2; }
          if (lane < 16) v += v2;
          v = wave_sum(v);
          if (lane == 0) { st[24 + 2 * b] = bfr(mean); st[24 + 2 * b + 1] = frcp(bfr(fsqrt(bfr(bfr(v * (1.f / DM)) + 1e-5f)))); }
          *reinterpret_cast<u32x4*>(xraw + b * 320 + lane * 4) = xv[2 * i];
          if (lane < 16) *reinterpret_cast<u32x4*>(xraw + b * 320 + 256 + lane * 4) = xv[2 * i + 1];
        }
        if (t == 0) { cnt[4] = next_tag(ep); cnt[15] = keep; }
        lds_arrive(C_P1, lane);
      }
      lds_wait<1>(C_P1, 2);
      STAMPT(20, 0);
      tg = __builtin_amdgcn_readfirstlane(cnt[4]);
      if (CT0 && u < 8 && t == 192) { const u32 t0 = cnt[15]; st_gran(rs, G_T + u * 16, t0, t0, tg); }
      if (wave < 3) {
        const int b = LAY == 2 ? t & 3 : (t >> 2) & 3, j = LAY == 2 ? t >> 2 : (t >> 4) * 4 + (t & 3);
        u32 offs[8];
        u32x4 gm[8];
#pragma unroll
        for (int i = 0; i < 8; ++i) offs[i] = t < 160 ? G_MOD + GIDX(1024, i & 1, b, ((i >> 1) & 1) * 160 + (i >> 2) * 40 + ksp * 80 + j) : OOB;
        const float mean = st[24 + 2 * b], sd = st[24 + 2 * b + 1];
        float nv[8];
#pragma unroll
        for (int e = 0; e < 2; ++e) {
          const u32x2 xw = *reinterpret_cast<const u32x2*>(xraw + b * 320 + ksp * 160 + e * 80 + 2 * j);
#pragma unroll
          for (int k = 0; k < 4; ++k) {
            const float xf = (k & 1) ? hi16(xw[k >> 1]) : lo16(xw[k >> 1]);
            nv[4 * e + k] = bfr(bfr(xf - mean) * sd);
            asm volatile("" : "+v"(nv[4 * e + k]));
          }
        }
        {
          const u32 tt = __builtin_amdgcn_readfirstlane(cnt[15]) + GT;
          SPINLOOP while ((int)((u32)__builtin_amdgcn_s_memrealtime() - tt) < 0) __builtin_amdgcn_s_sleep(1);
        }
        STAMPT(14, 0);
        gather22<8, 4>(rs, offs, gm, tg, GOPOS == 2 ? C_GO : 0);
        STAMPT(8, 0);
#pragma unroll
        for (int e = 0; e < 2; ++e) {
          const f32x4 sa = un_g22(gm[4 * e]) + un_g22(gm[4 * e + 1]);
          const f32x4 ca = un_g22(gm[4 * e + 2]) + un_g22(gm[4 * e + 3]);
          const u32 s01 = cvt2(sa[0], sa[1]), s23 = cvt2(sa[2], sa[3]), c01 = cvt2(ca[0], ca[1]), c23 = cvt2(ca[2], ca[3]);
          const u32 d01 = cvt2(1.f + lo16(c01), 1.f + hi16(c01)), d23 = cvt2(1.f + lo16(c23), 1.f + hi16(c23));
          const u32 p01 = cvt2(nv[4 * e] * lo16(d01), nv[4 * e + 1] * hi16(d01)), p23 = cvt2(nv[4 * e + 2] * lo16(d23), nv[4 * e + 3] * hi16(d23));
          const float hv[4] = {lo16(p01) + lo16(s01), hi16(p01) + hi16(s01), lo16(p23) + lo16(s23), hi16(p23) + hi16(s23)};
          if (t < 160) *reinterpret_cast<u32x2*>(lds + L_XA + b * XH + (e * 40 + j) * 8) = u32x2{cvt2(hv[0], hv[1]), cvt2(hv[2], hv[3])};
        }
      }
    }
    VM0();
    STAMPT(5, 128);
    lds_arrive(C_P2, lane);
    lds_wait<1>(C_P2, 4);
    STAMPT(7, 0);
    tg = __builtin_amdgcn_readfirstlane(cnt[4]);
    {
      f32x4 acc = {0.f, 0.f, 0.f, 0.f}, acc2 = acc;
#pragma unroll
      for (int s = 0; s < 10; s += 2) {
        acc = MFMA(wfrag<512>(L_W + (tr128 ^ (wave * 32)) + s * 4096), *reinterpret_cast<const u32x4*>(lds + L_XA + (c & 3) * XH + s * 64 + r * 16), acc);
        acc2 = MFMA(wfrag<512>(L_W + (tr128 ^ (wave * 32)) + s * 4096 + 4096), *reinterpret_cast<const u32x4*>(lds + L_XA + (c & 3) * XH + s * 64 + 64 + r * 16), acc2);
      }
      if (c < 4 && u < 240 + 16 - 16 * half) st_g22(rs, G_Q + half * (G_MOD - G_Q) + GIDX(1024, ksp, c, cbp * 16 + wave * 4 + r), acc + acc2, tg);
    }
    if (CT0 && half && u < 8 && t == 192) { const u32 t0 = cnt[15]; st_gran(rs, G_T + u * 16, t0, t0, tg); }
    STAMPT(9, 0);
  } else {
    __builtin_amdgcn_s_setprio(2);
    {
      const u32 co = min((u32)(u >> 3) * 1024u + (u32)((t >> 5) & 7) * 128u + 768u, (u32)CLEN);
      u64 pc;
      asm volatile("s_getpc_b64 %0\n\tglobal_load_dword v128, %1, %0" : "=&s"(pc) : "v"(co) : "memory", "v128");
    }
    if (t < 256 + 32) cnt[lane] = 0;
    const int rot = __builtin_amdgcn_readfirstlane((u >> 3) + ((t >> 6) & 3) * PHW);
    const u32 ko = (u32)(bh * SK + (SK - WIN)) * 256u + (u32)(half * 8 + wave) * (NL * 1024u) + lane * 16;
    STAMPT(30, 448);
#define LK4(a, b, c, d, o) "global_load_dwordx4 " a ", %" #o ", %[k] nt\n\tglobal_load_dwordx4 " b ", %" #o ", %[k] offset:1024 nt\n\t" \
                           "global_load_dwordx4 " c ", %" #o ", %[k] offset:2048 nt\n\tglobal_load_dwordx4 " d ", %" #o ", %[k] offset:3072 nt\n\t"
    asm volatile("s_waitcnt lgkmcnt(0)\n\ts_barrier\n\t"
                 LK4("v[128:131]", "v[132:135]", "v[136:139]", "v[140:143]", 0) LK4("v[144:147]", "v[148:151]", "v[152:155]", "v[156:159]", 1)
                 LK4("v[160:163]", "v[164:167]", "v[168:171]", "v[172:175]", 2)
                 :: "v"(ko + (rot & 7) * 4096), "v"(ko + ((rot + 1) & 7) * 4096),
                   "v"(ko + ((rot + 2) & 7) * 4096),
                   [k] "s"(kcq) : "memory", "v128", "v129", "v130", "v131", "v132", "v133", "v134", "v135", "v136", "v137", "v138", "v139", "v140", "v141", "v142", "v143", "v144", "v145", "v146", "v147", "v148", "v149", "v150", "v151", "v152", "v153", "v154", "v155", "v156", "v157", "v158", "v159", "v160", "v161", "v162", "v163", "v164", "v165", "v166", "v167", "v168", "v169", "v170", "v171", "v172", "v173", "v174", "v175", "v176", "v177", "v178", "v179", "v180", "v181", "v182", "v183", "v184", "v185", "v186", "v187", "v188", "v189", "v190", "v191");
#undef LK4
    STAMPT(31, 448);
  }
  u32 kvoff = (u32)(bh * SK + (SK - WIN)) * 256u + (u32)(half * 8 + wave) * (NL * 1024u) + lane * 16, ph[8];
  const int rot2 = __builtin_amdgcn_readfirstlane((u >> 3) + ((t >> 6) & 3) * PHW);
#pragma unroll
  for (int i = 0; i < 8; ++i) ph[i] = ((i + rot2) & 7) * 4096;
  asm volatile("" : "+v"(kvoff));
  asm volatile("s_waitcnt lgkmcnt(0)" : "+s"(wsq), "+s"(ph[0]), "+s"(ph[1]), "+s"(ph[2]), "+s"(ph[3]), "+s"(ph[4]), "+s"(ph[5]), "+s"(ph[6]), "+s"(ph[7])
               : "s"(rw), "s"(rx), "s"(kcq));
  const __amdgpu_buffer_rsrc_t rs = __builtin_amdgcn_make_buffer_rsrc(wsq, 0, WS_END, 0x00020000);
  int role, ia, jB;
  u32 blkA, om;
#define ROLES() { int g2 = g; asm volatile("" : "+s"(g2)); \
    role = sel_lt(g2, half ? 74 : 122, half ? 0 : sel_lt(g2, 40, 2, 3), half); \
    ia = half ? g2 : g2 - 48; \
    const int iB = g2 - 74, ksA = ia & 7; \
    jB = (iB & ~7) | (int)(((iB < 48 ? 0x75316420u : 0x315420u) >> ((iB & 7) * 4)) & 7); \
    blkA = ((c & 3) * 32 + HEADA(wave & 3)) * 2 + ((c >> 2) & 1); \
    om = G_ML + blkA * 64; \
    asm volatile("" : "+s"(role), "+s"(ia), "+s"(jB), "+v"(blkA), "+v"(om)); }
#define HEADA(hh) ((ksA & 3) + (ksA >> 2) * 16 + (hh) * 4)
  u32x4 gmf[5];
  u32x2 xw;
  {
    u32x4 kv[NL], kh[16];
    const __amdgpu_buffer_rsrc_t rk = __builtin_amdgcn_make_buffer_rsrc((void*)a.kc, 0, 1u << 28, 0x00020000);
    const __amdgpu_buffer_rsrc_t rv = __builtin_amdgcn_make_buffer_rsrc((void*)a.vc, 0, 1u << 28, 0x00020000);
#define LDKV(rsrc, off, i) __builtin_amdgcn_raw_buffer_load_b128(rsrc, (off) + ((i) & 3) * 1024, ph[(i) >> 2], 2)
#define LDS_INC(addr) do { asm volatile("" ::: "memory"); if (lane == 0) asm volatile("ds_add_u32 %0, %1" :: "v"(addr), "v"(1u) : "memory"); } while (0)
#define TR_STEP(x, HALF, CTRL, BIT) _Pragma("unroll") for (int hh = 0; hh < (HALF); ++hh) { \
    const float lo_ = x[hh], hi_ = x[hh + (HALF)]; const float t1_ = lo_ + dpp<CTRL>(lo_), t2_ = hi_ + dpp<CTRL>(hi_); \
    x[hh] = (c & (BIT)) ? t2_ : t1_; }
#define SCORE16(x, src, o) float x[16]; \
    _Pragma("unroll") for (int i = 0; i < 16; ++i) { float d = 0.f; _Pragma("unroll") for (int k = 0; k < 4; ++k) d = dot2(src[(o) + i][k], qpk[k], d); x[i] = d; } \
    TR_STEP(x, 8, XOR8, 8) TR_STEP(x, 4, XOR7, 4) TR_STEP(x, 2, XOR2, 2)
#define SCORE16P(x, src, o, pa_, pb_) float x[16]; \
    _Pragma("unroll") for (int i = 0; i < 16; ++i) { float d = 0.f; _Pragma("unroll") for (int k = 0; k < 4; ++k) d = dot2(src[(o) + i][k], qpk[k], d); x[i] = d; } \
    TR_STEP(x, 8, XOR8, 8) PADX(pa_, pb_, x[0]); TR_STEP(x, 4, XOR7, 4) TR_STEP(x, 2, XOR2, 2)
#define SCALED(v) bfr(bfr(v) * 0.08838834764831845f)
#define PV2(i) _Pragma("unroll") for (int k = 0; k < 4; ++k) { \
      acc[2 * k] = dot2(__builtin_amdgcn_perm(kv[(i) + 1][k], kv[i][k], 0x05040100u), pw[(i) / 2], acc[2 * k]); \
      acc[2 * k + 1] = dot2(__builtin_amdgcn_perm(kv[(i) + 1][k], kv[i][k], 0x07060302u), pw[(i) / 2], acc[2 * k + 1]); }
#define GET_Q() lds_wait<2>(C_Q, 1); u32x4 qpk; \
    asm volatile("ds_read_b128 %0, %2\n\tds_read_b32 %1, %3\n\ts_waitcnt lgkmcnt(0)" : "=&v"(qpk), "=&v"(tg) : "v"((u32)(L_QS + 16 * c)), "v"((u32)(L_CNT + 16)) : "memory"); \
    tg = __builtin_amdgcn_readfirstlane(tg);
#define GET_P() _Pragma("unroll") for (int i4 = 0; i4 < 4; ++i4) { \
      const u32x4 pv = *reinterpret_cast<const u32x4*>(pT + (wave * 4 + r) * 16 + i4 * 4); \
      _Pragma("unroll") for (int j = 0; j < 4; ++j) pw[i4 * 4 + j] = pv[j]; }
    auto gather_q = [&]() __attribute__((always_inline)) {
        u32 offs[1] = {G_Q + GIDX(1024, lane >> 5, b_att, h_att * 32 + (lane & 31))};
        u32x4 gq[1];
        {
          gq[0] = ld_gran(rs, offs[0]);
          const u32x4 gt = ld_gran(rs, lane < 8 ? G_T + lane * 16 : OOB);
          LDS_INC(C_KI);
          {
            const u32 own = cnt[15];
            const bool ok = lane < 8 && gt[1] == tg;
            float d = ok ? __builtin_amdgcn_fmed3f((float)(int)(gt[0] - own + TOFF), -400.f, 400.f) : -500.f;
            d = fmaxf(d, dpp<XOR1>(d)); d = fmaxf(d, dpp<XOR2>(d)); d = fmaxf(d, dpp<XOR7>(d));
            if (lane == 0 && d > -450.f) cnt[15] = own + (u32)(int)d;
          }
          const bool hit = (((gq[0][1] ^ (tg << 12)) | (gq[0][3] ^ (tg << 12))) < 4096u);
          if (!__all(hit)) gather22<1, 4>(rs, offs, gq, tg);
        }
        STAMPW(10);
        f32x4 q4 = un_g22(gq[0]);
#pragma unroll
        for (int k = 0; k < 4; ++k) q4[k] = swap32_add(q4[k], q4[k]);
        if (lane < 32) *reinterpret_cast<u32x2*>(qs + 2 * lane) = u32x2{cvt2(q4[0], q4[1]), cvt2(q4[2], q4[3])};
        lds_arrive(C_Q, lane);
    };
    const int qw = half ? QODD : QEVEN;
    const u32 tau = half ? (g < 74 ? TAU_A : TAU_B) : (g >= 122 ? TAU_X : TAU_E);
#define PACE(n) if ((n) > PB) { const u32 tt_ = t0 + (((n) - PB) * tau >> 4); \
      SPINLOOP while ((int)((u32)__builtin_amdgcn_s_memrealtime() - tt_) < 0) __builtin_amdgcn_s_sleep(1); asm volatile("" ::: "memory"); }
    float acc[8];
#pragma unroll
    for (int k = 0; k < 8; ++k) acc[k] = 0.f;
    u32 pw[16];
    if (__builtin_expect(wave >= 4, 1)) {
      asm volatile("" : "={v[128:131]}"(kv[0]), "={v[132:135]}"(kv[1]), "={v[136:139]}"(kv[2]), "={v[140:143]}"(kv[3]),
                        "={v[144:147]}"(kv[4]), "={v[148:151]}"(kv[5]), "={v[152:155]}"(kv[6]), "={v[156:159]}"(kv[7])
                        , "={v[160:163]}"(kv[8]), "={v[164:167]}"(kv[9]), "={v[168:171]}"(kv[10]), "={v[172:175]}"(kv[11])
                   );
      u32 t0 = 0;
      STAMPT(24, 448);
      const u32 kvoff2 = kvoff ^ 131072u;
#pragma unroll
      for (int i = NK0; i < 48; ++i) {
        if (i == NPRE) { STAMPT(12, 448); STAMPT(13, 256); }
        if (i == NPRE && half == 0) lds_wait<2>(C_GO, GOPOS ? 3 : 4);
        if (PK && (i & 3) == 0) PACE(i)
        if (i < NL) kv[i < NL ? i : 0] = LDKV(rk, kvoff, i & 31); else kh[i < NL ? 0 : i - NL] = LDKV(rk, kvoff2, i & 31);
      }
      if (!QPRE || wave != qw) LDS_INC(C_KI);
      ROLES()
#pragma unroll
      for (int i = 0; i < NK0; i += 4) asm volatile("s_waitcnt vmcnt(%4)" : "+v"(kv[i]), "+v"(kv[i + 1]), "+v"(kv[i + 2]), "+v"(kv[i + 3]) : "i"(48 - NK0) : "memory");
      STAMPT(25, 448);
      if ((QODD >= 4 || QEVEN >= 4) && wave == qw) {
        __builtin_amdgcn_s_setprio(3);
        lds_wait<2>(C_P1, 2 + half);
        tg = __builtin_amdgcn_readfirstlane(cnt[4]);
        gather_q();
        __builtin_amdgcn_s_setprio(2);
      }
      GET_Q()
      SCORE16(xa, kv, 0)
      asm volatile("" : "+v"(xa[0]), "+v"(xa[1]), "+v"(kvoff) :: "memory");
      PAD(e1, l2);
#pragma unroll
      for (int i = 0; i < P_NVA; ++i) kv[i] = LDKV(rv, kvoff, i);
      asm volatile("" ::: "memory");
      SCORE16(xb, kv, 16)
      xa[2] = xb[0]; xa[3] = xb[1];
      TR_STEP(xa, 2, XOR1, 1)
      float s0 = SCALED(xa[0]), s1 = SCALED(xa[1]);
      asm volatile("" : "+v"(s0), "+v"(s1), "+v"(kvoff) :: "memory");
#pragma unroll
      for (int i = P_NVA; i < NVE; ++i) kv[i] = LDKV(rv, kvoff, i);
      asm volatile("" ::: "memory");
      SCORE16P(xh, kh, 0, e2, l3)
      const float s2 = SCALED(xh[0] + dpp<XOR1>(xh[0])), s3 = SCALED(xh[1] + dpp<XOR1>(xh[1]));
      const float m = wave_max(fmaxf(fmaxf(s0, s1), fmaxf(s2, s3)));
      const float p0 = __expf(s0 - m), p1 = __expf(s1 - m), p2 = __expf(s2 - m), p3 = __expf(s3 - m);
      const float l = wave_sum(fmaf(p2 + p3, 0.5f, p0 + p1));
      if (lane == 0) { st[wave] = m; st[8 + wave] = l; }
      pT[(wave * 4 + r) * 16 + (c & 1) * 8 + (c >> 1)] = cvt2(p0, p1);
      pT[((wave - 4) * 4 + r) * 16 + (c >> 1)] = cvt2(p2, p3);
      lds_arrive(C_PP + 4 * (wave - 4), lane);
      __builtin_amdgcn_s_setprio(0);
      STAMPT(27, 448); WSTAMP(1);
      if (CT0) t0 = __builtin_amdgcn_readfirstlane(cnt[15]);
      GET_P()
#pragma unroll
      for (int i = NVE; i < NL; i += 2) {
        if (i == E3POS) PAD(e3, l4);
        if (i == E4POS) PAD(e4, t1);
        PACE(96 + i)
        kv[i] = LDKV(rv, kvoff, i);
        kv[i + 1] = LDKV(rv, kvoff, i + 1);
        asm volatile("" ::: "memory");
        if (i == NL - 2) { LDS_INC(C_ISS); STAMPT(26, 448); }
        if (i >= PVLAG) {
          PV2(i >= PVLAG ? i - PVLAG : 0)
#pragma unroll
          for (int k = 0; k < 8; ++k) asm volatile("" : "+v"(acc[k]));
        }
      }
#pragma unroll
      for (int i = 0; i < 5; ++i) asm("" : "=v"(gmf[i]));
      asm("" : "=v"(xw));
    } else {
      const int ex = g < 74 ? EA : g < 122 ? EB : EC;
      u32 kva = (!half && ex > 2) ? OOB : kvoff, kvb = (!half && ex > 0) ? OOB : kvoff;
      const u32 kxa = (half && ex > 2) ? kvoff - 8 * NL * 1024u : OOB, kxb = (half && ex > 0) ? kvoff - 8 * NL * 1024u : OOB;
      u32x4 kx[4];
      const u32 t0 = __builtin_amdgcn_readfirstlane(cnt[15]);
#pragma unroll
      for (int i = 0; i < 16; ++i) {
        if (PK && (i & 3) == 0) PACE(48 + i)
        kh[i] = LDKV(rk, i < 12 ? kvoff : i < 14 ? kva : kvb, 16 + i);
      }
#pragma unroll
      for (int i = 0; i < 4; ++i) kx[i] = LDKV(rk, i < 2 ? kxa : kxb, 28 + i);
      LDS_INC(C_KI);
      if (wave == 0) {
        if (half == 0 && g >= 80 && g < 120) {
          const int fb = g - 80;
          u32 of2[2];
          u32x4 gm[2];
#pragma unroll
          for (int ks = 0; ks < 2; ++ks) of2[ks] = G_MOD + GIDX(1024, ks, r, 320 + fb * 16 + c);
          gather22<2, 4>(rs, of2, gm, tg);
          const f32x4 mv = un_g22(gm[0]) + un_g22(gm[1]);
          st_gran(rs, G_MF + (((fb / 10) * 4 + r) * 160 + (fb % 10) * 16 + c) * 16, cvt2(mv[0], mv[1]), cvt2(mv[2], mv[3]), tg);
        }
      }
      __builtin_amdgcn_s_setprio(0);
      ROLES()
      STAMPT(2, 0);
#define GO(r_, n, name) "s_cmp_eq_u32 %" #r_ ", " #n "\n\ts_cbranch_scc1 .Lpad_" #name "\n\t"
      asm volatile("s_mov_b64 exec, 0\n\t" GO(0, 0, e1) GO(0, 1, e2) GO(0, 2, e3) GO(0, 3, l1)
                   ".Lpad_role:\n\t" GO(1, 0, a3) GO(1, 1, a2) GO(1, 2, a1) GO(1, 8, b3) GO(1, 9, b2) GO(1, 10, b1) GO(1, 11, b0) GO(1, 16, c1) GO(1, 17, c0)
                   ".Lpad_back:\n\ts_mov_b64 exec, -1" :: "s"(GPAD ? wave : 7), "s"(__builtin_amdgcn_readfirstlane(role * 8 + wave)) : "scc");
#undef GO
      STAMPT(7, 0);
      lds_wait<2>(C_KI, P_GATE);
      if (QHOLD && wave == qw - 4) lds_wait<2>(C_Q, 1);
      asm volatile("" : "+v"(kvoff), "+v"(kva), "+v"(kvb));
#pragma unroll
      for (int i = 0; i < NL; ++i) {
        if (i == L1POS) PAD(l1, e4);
        if (PL && (i & 3) == 0) PACE(64 + PLO + i * PLS / 16)
        kv[i] = LDKV(rv, i < 28 ? kvoff : i < 30 ? kva : kvb, i);
      }
      STAMPT(1, 0);
      GET_Q()
      float xx[4];
#pragma unroll
      for (int i = 0; i < 4; ++i) { float d = 0.f;
#pragma unroll
        for (int k = 0; k < 4; ++k) d = dot2(kx[i][k], qpk[k], d);
        xx[i] = d; }
      asm volatile("" : "+v"(xx[0]), "+v"(xx[1]), "+v"(xx[2]), "+v"(xx[3]) :: "memory");
#pragma unroll
      for (int i = 0; i < 4; ++i) kx[i] = LDKV(rv, i < 2 ? kxa : kxb, 28 + i);
      LDS_INC(C_ISS);
      TR_STEP(xx, 2, XOR8, 8)
#pragma unroll
      for (int i = 0; i < 2; ++i) { xx[i] += dpp<XOR7>(xx[i]); xx[i] += dpp<XOR2>(xx[i]); xx[i] += dpp<XOR1>(xx[i]); }
      SCORE16P(xh, kh, 0, l2, t2)
      const float ninf = -__builtin_inff();
      const bool mine = half || c < 16 - ex, extra = half && c >= 16 - 4 * ex;
      xh[0] += dpp<XOR1>(xh[0]); xh[1] += dpp<XOR1>(xh[1]);
      const float s2 = mine ? SCALED(xh[0]) : ninf, s3 = mine ? SCALED(xh[1]) : ninf;
      const float s4 = extra ? SCALED(xx[0]) : ninf, s5 = extra ? SCALED(xx[1]) : ninf;
      float m = wave_max(fmaxf(fmaxf(s2, s3), fmaxf(s4, s5)));
      lds_wait<2>(C_PP + 4 * wave, 1);
      const float me = st[wave + 4];
      m = fmaxf(m, me);
      const float p2 = __expf(s2 - m), p3 = __expf(s3 - m), p4 = __expf(s4 - m), p5 = __expf(s5 - m);
      const float l = wave_sum(fmaf(p4 + p5, 0.25f, p2 + p3)) * 0.5f;
      const u32 pxo = cvt2(p4, p5), pxt = dppu<XOR8>(pxo);
      const u32 px[2] = {c < 8 ? pxo : pxt, c < 8 ? pxt : pxo};
      if (lane == 0) { st[wave] = m; st[8 + wave] = l; }
      pT[(wave * 4 + r) * 16 + 8 + (c >> 1)] = cvt2(p2, p3);
      STAMPT(4, 0); WSTAMP(1);
      PAD(l3, t3);
      if (lds_ticket(C_SC, lane) == 3) {
        const float mw = st[lane & 7], lw = st[8 + (lane & 7)];
        float M = fmaxf(mw, dpp<XOR1>(mw)); M = fmaxf(M, dpp<XOR2>(M)); M = fmaxf(M, dpp<XOR7>(M));
        const float e = __expf(mw - M);
        float L = lw * e;
        L += dpp<XOR1>(L); L += dpp<XOR2>(L); L += dpp<XOR7>(L);
        if (lane == 0) st_gran(rs, G_ML + u * 64, f2u(M), f2u(L), tg);
        if (lane < 8) st[16 + lane] = e * frcp(L);
      }
      {
        const int cbA = ia >> 3;
        const u32 o0 = role == 0 ? G_MF + (r * 160 + cbA * 16 + c) * 16 : role == 1 ? G_MF + (640 + t) * 16 : OOB, o1 = role == 1 ? o0 : OOB;
#pragma unroll
        for (int i = 0; i < 5; ++i) gmf[i] = __builtin_amdgcn_raw_buffer_load_b128(rs, i ? o1 : o0, i * 4096, 16);
        const __amdgpu_buffer_rsrc_t rb = __builtin_amdgcn_make_buffer_rsrc((void*)(role == 1 ? a.b1 : a.x.p), 0, role == 1 ? FF * 2 : NB * DM * 2, 0x00020000);
        xw = __builtin_amdgcn_raw_buffer_load_b64(rb, role == 1 ? (jB * 32 + (t & 28)) * 2 : role == 0 && (ia & 7) == 0 ? (r * DM + cbA * 64 + c * 4) * 2 : OOB, 0, 0);
      }
      GET_P()
#pragma unroll
      for (int i = 0; i < 16; i += 2) { if (i == L4POS) PADA(l4, a0); PV2(i) }
      const float f = __expf(me - m);
#pragma unroll
      for (int k = 0; k < 8; ++k) acc[k] *= f;
#pragma unroll
      for (int i = 16; i < NL - PVLAG; i += 2) { PV2(i) }
#pragma unroll
      for (int i = 0; i < 4; i += 2)
#pragma unroll
        for (int k = 0; k < 4; ++k) {
          acc[2 * k] = dot2(__builtin_amdgcn_perm(kx[i + 1][k], kx[i][k], 0x05040100u), px[i / 2], acc[2 * k]);
          acc[2 * k + 1] = dot2(__builtin_amdgcn_perm(kx[i + 1][k], kx[i][k], 0x07060302u), px[i / 2], acc[2 * k + 1]);
        }
    }
#pragma unroll
    for (int i = NL - PVLAG; i <= NL - HX; i += 2) { if (i == T1POS) PADA(t1, role); PV2(i) }
    if (!HT) { PV2(NL - HX + 2) } else {
      u32 tk = 0;
      u64 tn;
      asm volatile("s_mov_b64 exec, 1\n\tds_add_rtn_u32 %0, %10, %11\n\ts_mov_b64 exec, -1\n\ts_memrealtime %1"
                   : "+v"(tk), "=s"(tn), "+v"(acc[0]), "+v"(acc[1]), "+v"(acc[2]), "+v"(acc[3]), "+v"(acc[4]), "+v"(acc[5]), "+v"(acc[6]), "+v"(acc[7])
                   : "v"((u32)C_HT), "v"(1u) : "memory");
      PV2(NL - HX + 2)
      asm volatile("s_waitcnt lgkmcnt(0)" : "+v"(tk), "+s"(tn), "+v"(acc[0]), "+v"(acc[1]), "+v"(acc[2]), "+v"(acc[3]), "+v"(acc[4]), "+v"(acc[5]), "+v"(acc[6]), "+v"(acc[7]));
      __builtin_amdgcn_raw_buffer_store_b64(u32x2{(u32)tn + HTD, tg}, rs, tk == 7 ? G_HT + u * 128 : OOB, 0, 16);
      asm volatile("" ::: "memory");
    }
#pragma unroll
    for (int i = NL - HX + 4; i < NL; i += 2) { PV2(i) }
#undef PV2
#undef TR_STEP
#undef SCORE16
#undef SCORE16P
#undef SCALED
#undef GET_Q
#undef GET_P
#undef LDKV
#pragma unroll
    for (int i = 0; i < 4; ++i) acc[i] = swap32_add(acc[i], acc[i + 4]);
#pragma unroll
    for (int i = 0; i < 2; ++i) acc[i] = swap16_add(acc[i], acc[i + 2]);
    *reinterpret_cast<float2*>(accs + wave * 128 + c * 8 + 2 * r) = float2{acc[0], acc[1]};
    STAMPT(6, 0); WSTAMP(2);
  }
  const u32x4 rsv = {(u32)(uintptr_t)wsq, (u32)((uintptr_t)wsq >> 32) & 0xffffu, WS_END, 0x00020000u};
  const int ksA = ia & 7, cbA = ia >> 3;
  u32x4 ml = {0u, 0u, 0u, 0u};
#define MLOOK(W_) asm volatile(LK(0, 1, "0", 0, 2) LKEND : "+v"(ml) : "v"(om), "s"(m2), [rs] "s"(rsv), [w] "i"(W_) : "memory");
  u64 m2 = role == 0 && wave >= 4 ? 0xffull : 0;
  const u32 tkf = lds_ticket(C_FIN, lane);
  const bool lastw = tkf == 7;
  if (!lastw) { MLOOK(63) }
  if (lastw) {
    PAD(t2, role);
    if (lane < 32) {
      f32x4 o = {0.f, 0.f, 0.f, 0.f};
#pragma unroll
      for (int w = 0; w < 8; ++w) o += st[16 + w] * *reinterpret_cast<const f32x4*>(accs + w * 128 + 4 * lane);
      const u32x2 ow = {min(cvt2(o[0], o[1]), EMPTY - 1), min(cvt2(o[2], o[3]), EMPTY - 1)};
      __builtin_amdgcn_raw_buffer_store_b64(ow, rs, G_ATT + u * 256 + lane * 8, 0, 16);
    }
    MLOOK(63)
    LDS_INC(C_PUB);
    STAMPW(11);
  }
  u32 gaw[2];
  float b1v;
  if (role < 2 && wave < 4) {
    if (role == 0) {
      if (!__all(gmf[0][1] == tg && gmf[0][3] == tg)) {
        u32 offs[1] = {G_MF + (r * 160 + cbA * 16 + c) * 16};
        u32x4 gm[1];
        gather<1>(rs, offs, gm, tg);
        gmf[0] = gm[0];
      }
      gaw[0] = gmf[0][0]; gaw[1] = gmf[0][2];
    } else {
      bool ok = true;
#pragma unroll
      for (int i = 0; i < 5; ++i) ok &= (gmf[i][1] == tg) & (gmf[i][3] == tg);
      if (!__all(ok)) {
        u32 offs[5];
#pragma unroll
        for (int i = 0; i < 5; ++i) offs[i] = G_MF + (640 + t + 256 * i) * 16;
        gather<5>(rs, offs, gmf, tg);
      }
#pragma unroll
      for (int i = 0; i < 5; ++i) {
        if (i == 1) PAD(t3, role);
        const int it = t + 256 * i, row = it / 160, gi = it % 160;
        u32 w0 = gmf[i][0], w1 = gmf[i][2];
        if (row >= 4) { w0 = cvt2(1.f + lo16(w0), 1.f + hi16(w0)); w1 = cvt2(1.f + lo16(w1), 1.f + hi16(w1)); }
        *reinterpret_cast<u32x2*>(lds + L_XA + (row < 4 ? row + 8 : row) * XS + gi * 8) = u32x2{w0, w1};
      }
      const u32 bw = (t & 2) ? xw[1] : xw[0];
      b1v = (t & 1) ? hi16(bw) : lo16(bw);
      asm volatile("" : "+v"(b1v));
      lds_arrive(C_MF, lane);
    }
  }
  if (role == 0 && wave < 4) {
    if (half) STAMPT(8, 0);
    lds_wait<2>(C_ISS, 8);
    {
      const bf16* wp = a.wo + (size_t)(HEADA(wave) * 128 + (lane >> 3)) * DM + cbA * 64;
#pragma unroll
      for (int i = 0; i < 16; ++i) {
        if (i == 1) PAD(a0, role);
        if (i == N1A) {
          lds_wait<2>(C_PUB, 1);
          asm volatile("" ::: "memory");
        }
        dma16(wp + (size_t)(8 * i) * DM + (s128e ^ ((i & 1) * 32)), L_W + wave * 16384 + i * 1024);
      }
      LDS_INC(C_ISS);
    }
    STAMPT(16, 0);
  }
#define SELECTOR(sel) u32x4 sel; \
    _Pragma("unroll") for (int d = 0; d < 4; ++d) sel[d] = ((c & 2) == 0 && d == (c >> 2)) ? ((c & 1) ? 0x3f800000u : 0x00003f80u) : 0u;
#define PAIRSUM(sm) (((c & 8) ? sm[1] : sm[0]) + dpp<XOR8>((c & 8) ? sm[0] : sm[1]))
  const u32 tb = __builtin_amdgcn_readfirstlane(cnt[15]);
  if (role == 0) {
    PAD(a1, back);
    if (wave < 4) {
      float ga4[4] = {lo16(gaw[0]), hi16(gaw[0]), lo16(gaw[1]), hi16(gaw[1])}, x4[4] = {lo16(xw[0]), hi16(xw[0]), lo16(xw[1]), hi16(xw[1])};
      u32 a_sum = L_RED + lane * 16;
      u32 a_out = wave < NREP ? G_WO + wave * WO_REP + ((cbA * 4 + (c >> 2)) * 8 + ksA) * 128 + r * 32 + (c & 3) * 8 : OOB;
      asm volatile("" : "+v"(ga4[0]), "+v"(ga4[1]), "+v"(ga4[2]), "+v"(ga4[3]), "+v"(x4[0]), "+v"(x4[1]), "+v"(x4[2]), "+v"(x4[3]));
      asm volatile("s_waitcnt vmcnt(4)" : "+v"(a_sum), "+v"(a_out) :: "memory");
      LDS_INC(C_PP + 4 * wave);
      STAMPT(19, 0);
      u32 a_fl = lane < 16 ? G_HA + ia * 128 + lane * 8 : OOB;
      PAD(a2, back);
      asm volatile("s_waitcnt vmcnt(0)" : "+v"(a_fl) :: "memory");
      LDS_INC(C_PP + 4 * wave);
      STAMPT(23, 0);
      if (wave == 0) {
        lds_wait<0>(C_W, 4);
        STAMPT(12, 0);
        __builtin_amdgcn_raw_buffer_store_b64(u32x2{(u32)__builtin_amdgcn_s_memrealtime() + HAG, tg}, rs, a_fl, 0, 16);
      }
      BAR();
      STAMPT(21, 0);
      f32x4 pp[4];
#pragma unroll
      for (int w = 0; w < 4; ++w) pp[w] = *reinterpret_cast<const f32x4*>(lds + a_sum + w * 1024);
      asm volatile("" : "+v"(pp[0]), "+v"(pp[1]), "+v"(pp[2]), "+v"(pp[3]));
      const f32x4 s = (pp[0] + pp[1]) + (pp[2] + pp[3]);
      const u32x2 o = {min(cvt2(fmaf(s[0], ga4[0], x4[0]), fmaf(s[1], ga4[1], x4[1])), EMPTY - 1), min(cvt2(fmaf(s[2], ga4[2], x4[2]), fmaf(s[3], ga4[3], x4[3])), EMPTY - 1)};
      __builtin_amdgcn_raw_buffer_store_b64(o, rs, a_out, 0, 16);
    } else {
      u32 oa = G_ATT + blkA * 256 + (c >> 3) * 64 + r * 16;
      u32 a_red = L_RED + (((wave & 3) * 4 + c) * 64 + r * 4) * 4;
      u32 a_w[4];
#pragma unroll
      for (int nt = 0; nt < 4; ++nt) a_w[nt] = L_W + (wave & 3) * 16384 + (tr128 ^ (nt * 32));
      u32x4 av[2];
      u64 m0 = ~0ull, m1 = ~0ull;
      av[0] = av[1] = u32x4{0u, 0u, 0u, 0u};
      asm volatile("s_waitcnt vmcnt(0)" : "+v"(oa), "+v"(ml), "+v"(av[0]), "+v"(av[1]), "+v"(a_red), "+v"(a_w[0]), "+v"(a_w[1]), "+v"(a_w[2]), "+v"(a_w[3]) :: "memory");
      SPINLOOP for (unsigned spins = 0; spins < SPIN_CAP; ++spins) {
        m2 &= __builtin_amdgcn_ballot_w64(ml[1] != tg) | __builtin_amdgcn_ballot_w64(ml[3] != tg);
        if (!m2) break;
        if (!spins) FAIL(4);
        __builtin_amdgcn_s_sleep(ASL);
        MLOOK(0)
      }
      float wsc;
      {
        const float M = u2f(ml[0]), L = u2f(ml[2]), Mo = dpp<XOR7>(dpp<0x1B>(M)), Lo = dpp<XOR7>(dpp<0x1B>(L));
        const float Mx = fmaxf(M, Mo), le = L * __expf(M - Mx);
        wsc = le * frcp(fmaf(Lo, __expf(Mo - Mx), le));
        wsc = u2f((u32)__builtin_amdgcn_ds_bpermute((c & 7) * 4, (int)f2u(wsc)));
        asm volatile("" : "+v"(wsc));
      }
#define ALOOK(W_) \
        asm volatile(LK(0, 2, "0", 0, 3) LK(1, 2, "0", 128, 4) LKEND \
                     : "+v"(av[0]), "+v"(av[1]) : "v"(oa), "s"(m0), "s"(m1), [rs] "s"(rsv), [w] "i"(W_) : "memory");
#define ACHK() \
        m0 = __builtin_amdgcn_ballot_w64(missing(av[0])); \
        m1 = __builtin_amdgcn_ballot_w64(missing(av[1]));
      lds_wait<1>(C_PP + 4 * (wave - 4), 2);
      if (AGT) SPINLOOP for (int k = 0; k < 64; ++k) if ((int)((u32)__builtin_amdgcn_s_memrealtime() - tb) >= (126 * TAU_E >> 4) + AGT) break;
      STAMPT(29, 448);
      ALOOK(63)
      s16x8 wf[4][4];
#pragma unroll
      for (int s = 0; s < 3; ++s)
#pragma unroll
        for (int nt = 0; nt < 4; ++nt) wf[s][nt] = wfrag<512>(a_w[nt] + s * 4096);
      lds_wait<1>(C_PP + 4 * (wave - 4), 3);
#pragma unroll
      for (int nt = 0; nt < 4; ++nt) wf[3][nt] = wfrag<512>(a_w[nt] + 3 * 4096);
      u32 a_cw = C_W, one = 1;
      u64 mw = 0x000f000f000f000full;
      asm volatile("" : "+v"(wf[3][0]), "+v"(wf[3][1]), "+v"(wf[3][2]), "+v"(wf[3][3]), "+v"(a_cw), "+v"(one), "+s"(mw));
      PAD(a3, back);
      asm volatile("s_waitcnt vmcnt(0)" : "+v"(av[0]), "+v"(av[1]) :: "memory");
      ACHK()
      STAMPT(18, 448);
      if (m0 | m1) {
        FAIL(1);
        SPINLOOP for (unsigned spins = 0; spins < SPIN_CAP; ++spins) {
          ALOOK(2 * (PDA - 1))
          ACHK()
          if (!(m0 | m1)) break;
          __builtin_amdgcn_s_sleep(ASL);
        }
      }
#undef ALOOK
#undef ACHK
      asm volatile("" : "+v"(av[0]), "+v"(av[1]));
      f32x4 acc[4], acb[4];
      const f32x4 z = {0.f, 0.f, 0.f, 0.f};
#define SB() __builtin_amdgcn_sched_barrier(0)
#define PIN(x) asm volatile("" : "+v"(x)); SB();
#define M1(n) { acc[n] = MFMA(wf[0][n], av[0], z); PIN(acc[n]) acb[n] = MFMA(wf[1][n], av[0], z); PIN(acb[n]) }
#define M2A(n) { acb[n] = MFMA(wf[3][n], av[1], acb[n]); PIN(acb[n]) }
#define M2B(n) { acc[n] = MFMA(wf[2][n], av[1], acc[n]); PIN(acc[n]) }
#define D8(n, k) { float v = acc[n][k] + dpp<XOR8>(acb[n][k]); PIN(v) acc[n][k] = v; v = acc[n][k + 1] + dpp<XOR8>(acb[n][k + 1]); PIN(v) acc[n][k + 1] = v; }
#define MW(n) { acc[n] *= wsc; PIN(acc[n]) }
#define D4(n) { _Pragma("unroll") for (int k = 0; k < 4; ++k) { float v = acc[n][k] + dpp<SHL4>(acc[n][k]); PIN(v) acc[n][k] = v; } }
#define WR(n) asm volatile("s_mov_b64 exec, %3\n\tds_write_b128 %0, %1 offset:" STR(n * 64) "\n\tds_write_b128 %0, %2 offset:" STR(n * 64 + 64) "\n\ts_mov_b64 exec, -1" \
                           :: "v"(a_red), "v"(acc[n]), "v"(acc[n + 1]), "s"(mw) : "memory")
      SB();
      M1(0)
      asm volatile("s_mov_b64 exec, 1\n\tds_add_u32 %0, %1\n\ts_mov_b64 exec, -1" :: "v"(a_cw), "v"(one) : "memory");
      SB();
      M1(1) M2A(0) M2B(0) M1(2) M2A(1) M2B(1) M1(3)
      M2A(2) D8(0, 0) M2B(2) D8(0, 2) M2A(3) D8(1, 0) M2B(3) D8(1, 2)
      MW(0) MW(1) D4(0) D4(1) D8(2, 0) D8(2, 2)
      WR(0);
      SB();
      D8(3, 0) D8(3, 2) MW(2) MW(3) D4(2) D4(3)
      WR(2);
#undef M1
#undef M2A
#undef M2B
#undef D8
#undef MW
#undef D4
#undef WR
      STAMPT(22, 448);
      BAR();
      asm volatile("" :: "v"(av[0]), "v"(av[1]));
    }
    STAMPT(13, 0);

  } else if (role == 1) {
    PAD(b0, bd);
    const int lb = (c >> 1) & 3, lc = (c & 1) * 8 + 2 * r;
    const int fc = lane & 31, hw = lane >> 5;
    SELECTOR(sel)
    constexpr int L_R2 = L_MOD, L_S2 = L_R2 + 4096;
    const u32 gw = G_WO + (jB % NREP) * WO_REP + wave * 5120;
    u32 go = gw + (c >> 3) * 1024 + r * 128 + (c & 7) * 16, go4 = gw + 4096 + lane * 16;
    u32 a_u = L_XA + wave * 640 + lb * 160 + ((c >> 3) * 16 + lc) * 2, a_u4 = L_XA + wave * 640 + lb * 160 + (64 + lc + (c >> 3)) * 2;
    u32 a_av = L_XA + wave * 640 + (c & 3) * 160 + r * 16, a_ah = a_av + 128 - r * 8;
    u32 a_red = L_R2 + ((wave * 4 + c) * 32 + r * 4) * 4;
    u32 a_st = L_S2 + ((lb * 2 + (c >> 3)) * 8 + wave) * 4;
    u32 a_fin = L_R2 + ((hw * 16 + (wave & 3)) * 32 + fc) * 4;
    u32 a_fst = L_S2 + (wave & 3) * 64;
    u32 a_out = c < 4 ? G_OUT + (wave * 5 * 54 + jB) * 128 + c * 32 + r * 8 : OOB;
    constexpr int L_F2 = L_XR;
    u32 a_fi = BM ? L_F2 + c * 64 + r * 16 : L_FI + (c & 3) * 64 + r * 16, a_fo = (BM ? L_F2 + 768 : L_FI) + ((wave & 3) * 32 + fc) * 2;
    u32 a_ou4 = G_OUT + ((wave * 5 + (c >> 2)) * 54 + jB) * 128 + (c & 3) * 32 + r * 8;
    asm volatile("" : "+v"(a_ou4));
    u32 a_pre = lane < 40 ? G_OUT + ((wave * 5 + (lane >> 3)) * 54 + jB) * 128 + (lane & 7) * 16 : OOB;
    u32x4 emp = {EMPTY, EMPTY, EMPTY, EMPTY};
    asm volatile("" : "+v"(a_pre), "+v"(emp));
#define OPRE() asm volatile("buffer_store_dwordx4 %0, %1, %2, 0 offen sc1\n\ts_nop 1" :: "v"(emp), "v"(a_pre), "s"(rsv) : "memory")
    if (OPOS == 0) OPRE();
    u32 a_w1 = L_W + wave * 5120 + (8 * r + (c >> 2)) * 64 + (r & 1) * 32 + (c & 3) * 8;
    u32 a_wh = L_W + wave * 5120 + 4096 + (4 * r + (c >> 2)) * 64 + (r >> 1) * 32 + (c & 3) * 8;
    u32 a_w2[5];
#pragma unroll
    for (int k = 0; k < 5; ++k) { const int n2 = wave * 5 + k; a_w2[k] = L_WM2 + (n2 >> 2) * 4096 + (tr128 ^ ((n2 & 3) * 32)); }
    asm volatile("" : "+v"(sel), "+v"(go), "+v"(go4), "+v"(a_u), "+v"(a_u4), "+v"(a_av), "+v"(a_ah), "+v"(a_red), "+v"(a_st), "+v"(a_fin), "+v"(a_fst), "+v"(a_out));
    asm volatile("" : "+v"(a_fi), "+v"(a_fo), "+v"(a_w1), "+v"(a_wh), "+v"(a_w2[0]), "+v"(a_w2[1]), "+v"(a_w2[2]), "+v"(a_w2[3]), "+v"(a_w2[4]) :: "memory");
    if (BM) *reinterpret_cast<u32*>(lds + L_F2 + t * 4) = 0;
    lds_wait<1>(C_MF, 4);
    float c1f[5];
    {
      const unsigned char* cp = lds + L_XA + (4 + lb) * XS + wave * 160;
#pragma unroll
      for (int i = 0; i < 2; ++i) { const u32 w = *reinterpret_cast<const u32*>(cp + (32 * i + (c >> 3) * 16 + lc) * 2); c1f[2 * i] = lo16(w); c1f[2 * i + 1] = hi16(w); }
      c1f[4] = u2f((u32)*reinterpret_cast<const unsigned short*>(cp + (64 + lc + (c >> 3)) * 2) << 16);
    }
    const unsigned char* pm = lds + L_XA + (4 + (c & 7)) * XS + wave * 160;
    u32x4 mv0 = *reinterpret_cast<const u32x4*>(pm + r * 16), mv1 = *reinterpret_cast<const u32x4*>(pm + 64 + r * 16);
    s16x4 mv2 = *reinterpret_cast<const s16x4*>(pm + 128 + r * 8);
    u32 a_rd = L_RED + ((wave * 8 + c) * 32 + r * 4) * 4;
    asm volatile("" : "+v"(c1f[0]), "+v"(c1f[1]), "+v"(c1f[2]), "+v"(c1f[3]), "+v"(c1f[4]), "+v"(mv0), "+v"(mv1), "+v"(mv2), "+v"(a_rd) :: "memory");
    if (wave < 4) {
      STAMPT(8, 0);
      lds_wait<2>(C_ISS, 8);
      const int h5 = ROT1 * (jB & 1);
      const bf16* wp = a.w1 + (size_t)(wave * 160 + (lane >> 2)) * FF + jB * 32 + ((((((lane >> 1) & 1) ^ (lane >> 5)) << 1) | (lane & 1)) * 8);
      const bf16* wp2 = a.w2 + (size_t)(jB * 32 + wave * 8 + (lane >> 3)) * DM + (s128e ^ ((wave & 1) * 32));
#pragma unroll
      for (int n = 0; n < 20; ++n) {
        if (n == 0) PAD(bd, back);
        if (n == N1B) lds_wait<2>(C_PUB, 1);
        if (n < 5 || (n >= 5 + W2F && n < 10 + W2F)) {
          const int i2 = n < 5 ? n + h5 : n - W2F - h5;
          dma16(wp + (size_t)(16 * i2) * FF, L_W + (wave * 10 + i2) * 1024);
        } else {
          const int k = n < 5 + W2F ? n - 5 : n - 10;
          dma16(wp2 + k * 64, L_WM2 + (k * 4 + wave) * 1024);
        }
      }
      STAMPT(16, 0); STAMPT(5, 128);
    }
    asm volatile("s_waitcnt vmcnt(%0)" :: "i"(10 - W2F) : "memory");
    STAMPT(23, 0);
    PAD(b1, back);
    BAR();
    STAMPT(3, 0);
    if (OPOS == 1) OPRE();
    s16x8 wf[2][2], wg[5];
    s16x4 wh[2];
#pragma unroll
    for (int nt = 0; nt < 2; ++nt) {
#pragma unroll
      for (int s = 0; s < 2; ++s) wf[s][nt] = wfrag<256>((a_w1 ^ (nt * 32)) + s * 2048);
      wh[nt] = __builtin_amdgcn_ds_read_tr16_b64_v4i16((lds_s4p)(uintptr_t)(a_wh ^ (nt * 32)));
    }
#define W1MMA(acc) \
      f32x4 acc[2]; \
      _Pragma("unroll") for (int nt = 0; nt < 2; ++nt) { const f32x4 z_ = {0.f, 0.f, 0.f, 0.f}; acc[nt] = MFMA(wf[0][nt], av0, z_); } \
      _Pragma("unroll") for (int nt = 0; nt < 2; ++nt) acc[nt] = MFMA(wf[1][nt], av1, acc[nt]); \
      _Pragma("unroll") for (int nt = 0; nt < 2; ++nt) acc[nt] = __builtin_amdgcn_mfma_f32_16x16x16bf16_1k(wh[nt], av2, acc[nt], 0, 0, 0);
    {
      const u32x4 av0 = mv0, av1 = mv1;
      const s16x4 av2 = mv2;
      W1MMA(acc)
#pragma unroll
      for (int nt = 0; nt < 2; ++nt) if (c < 8) *reinterpret_cast<f32x4*>(lds + a_rd + nt * 64) = acc[nt];
    }
    float Tc = 0.f, Ts = 0.f;
#define TSUM() _Pragma("unroll") for (int k = 0; k < 4; ++k) { Tc += red[((hw * 4 + k) * 8 + (wave & 3)) * 32 + fc]; Ts += red[((hw * 4 + k) * 8 + 4 + (wave & 3)) * 32 + fc]; }
    u32x4 pa[5];
    f32x4 s4, s0, s1;
    u32 a_fl = lane < 16 ? G_HB + jB * 128 + lane * 8 : OOB, tk = C_W;
    u32x2 fv = {1u, tg};
    {
      u64 m[5], old = 0;
#pragma unroll
      for (int i = 0; i < 5; ++i) { m[i] = ~0ull; pa[i] = u32x4{0u, 0u, 0u, 0u}; }
#pragma unroll
      for (int i = 0; i < 5; ++i) asm volatile("" : "+v"(c1f[i]), "+v"(pa[i]));
#define BLOOK(W_) \
        asm volatile(LK(0, 5, "0", 0, 7) LK(1, 5, "0", 512, 8) LK(2, 5, "0", 2048, 9) LK(3, 5, "0", 2560, 10) LK(4, 6, "0", 0, 11) LKEND \
                     : "+v"(pa[0]), "+v"(pa[1]), "+v"(pa[2]), "+v"(pa[3]), "+v"(pa[4]) \
                     : "v"(go), "v"(go4), "s"(m[0]), "s"(m[1]), "s"(m[2]), "s"(m[3]), "s"(m[4]), [rs] "s"(rsv), [w] "i"(W_) : "memory");
      STAMPT(9, 0); STAMPT(30, 448);
      if (BPREW) {
        VM0();
        STAMPT(20, 0); STAMPT(31, 448);
        BAR();
        STAMPT(29, 0);
      }
      SPINLOOP for (int k = 0; k < 256; ++k) if ((int)((u32)__builtin_amdgcn_s_memrealtime() - tb) >= (126 * TAU_E >> 4) + BGT) break;
      STAMPT(21, 0);
      BLOOK(5)
      if (!BPREW) { STAMPT(20, 0); STAMPT(31, 448); }
      if (!BAN) announce(rs, G_HB + jB * 128, C_GO, C_W, 8, tb, HCD, tg);
      if (!BPREW) { BAR(); STAMPT(29, 0); }
#pragma unroll
      for (int k = 0; k < 5; ++k) wg[k] = wfrag<512>(a_w2[k]);
      PAD(b2, back);
      TSUM()
#pragma unroll
      for (int i = 0; i < 5; ++i) asm volatile("" : "+v"(wg[i]));
      asm volatile("" : "+v"(Tc), "+v"(Ts));
      STAMPT(18, 0);
      asm volatile("s_waitcnt vmcnt(%8)" : "+v"(pa[0]), "+v"(pa[1]), "+v"(pa[2]), "+v"(pa[3]), "+v"(pa[4]), "+v"(a_fl), "+v"(fv), "+v"(tk) : "i"(BAN ? 0 : 1) : "memory");
      STAMPT(22, 0);
#define BSUM() { const f32x4 z = {0.f, 0.f, 0.f, 0.f}; \
      s0 = MFMA(sel, pa[0], z); PIN(s0) s1 = MFMA(sel, pa[2], z); PIN(s1) s0 = MFMA(sel, pa[1], s0); PIN(s0) s1 = MFMA(sel, pa[3], s1); PIN(s1) s4 = MFMA(sel, pa[4], z); PIN(s4) \
      if (BAN) asm volatile("s_memrealtime s[98:99]" ::: "s98", "s99"); SB(); }
#define BNAN() (__builtin_amdgcn_ballot_w64(__builtin_isunordered(s0[0], s0[1])) | __builtin_amdgcn_ballot_w64(__builtin_isunordered(s1[0], s1[1])) | \
                __builtin_amdgcn_ballot_w64(__builtin_isunordered(s4[0], s4[1])))
      BSUM()
      if (BNAN()) {
        FAIL(2);
        SPINLOOP for (unsigned spins = 0; spins < SPIN_CAP; ++spins) {
          u64 bad = 0;
#pragma unroll
          for (int i = 0; i < 5; ++i) { m[i] = __builtin_amdgcn_ballot_w64(missing(pa[i])); bad |= m[i]; }
          if (!(bad | old)) break;
          old = bad;
          BLOOK(5 * (PDB - 1))
          BSUM()
          if (!BNAN()) break;
        }
      }
    }
#define BFLAG(W_) u32 tl; \
        asm volatile("s_waitcnt lgkmcnt(%3)\n\ts_add_u32 %0, s98, %2" : "=s"(tl), "+v"(tk) : "i"(HBD), "i"(W_) : "scc"); \
        fv[0] = tl; \
        asm volatile("v_readfirstlane_b32 %0, %1\n\ts_cmp_eq_u32 %0, 7\n\ts_cbranch_scc0 1f\n\tbuffer_store_dwordx2 %2, %3, %4, 0 offen sc1\n1:" \
                     : "=&s"(tl) : "v"(tk), "v"(fv), "v"(a_fl), "s"(rsv) : "scc"); \
        SB();
    if (BAN >= 2) asm volatile("s_mov_b64 exec, 1\n\tds_add_rtn_u32 %0, %0, %1\n\ts_mov_b64 exec, -1" : "+v"(tk) : "v"(fv[0]));
    SB();
    STAMPT(14, 0); WSTAMP(3);
    float xf[5];
    {
      const u32 w0 = cvt2(s0[0], s0[1]), w1 = cvt2(s1[0], s1[1]);
      xf[0] = lo16(w0); xf[1] = hi16(w0); xf[2] = lo16(w1); xf[3] = hi16(w1);
      *reinterpret_cast<u32*>(lds + a_u) = cvt2(xf[0] * c1f[0], xf[1] * c1f[1]);
      *reinterpret_cast<u32*>(lds + a_u + 64) = cvt2(xf[2] * c1f[2], xf[3] * c1f[3]);
    }
    STAMPT(19, 0);
    {
      u32x4 av0 = *reinterpret_cast<const u32x4*>(lds + a_av), av1 = *reinterpret_cast<const u32x4*>(lds + a_av + 64);
      SB();
      if (BAN == 2) { BFLAG(3) }
      float e0 = s4[0], e1 = s4[1];
      asm volatile("" : "+v"(e0), "+v"(e1));
      xf[4] = bfr(((c & 8) ? e1 : e0) + dpp<XOR8>((c & 8) ? e0 : e1));
      *reinterpret_cast<unsigned short*>(lds + a_u4) = (unsigned short)cvt2(xf[4] * c1f[4], 0.f);
      s16x4 av2 = *reinterpret_cast<const s16x4*>(lds + a_ah);
      SB();
      if (BAN == 1) asm volatile("s_mov_b64 exec, 1\n\tds_add_rtn_u32 %0, %0, %1\n\ts_mov_b64 exec, -1" : "+v"(tk) : "v"(fv[0]));
      SB();
      if (BAN == 3) { BFLAG(5) }
      const float ps = ((xf[0] + xf[1]) + (xf[2] + xf[3])) + xf[4];
      const float pq = fmaf(xf[0], xf[0], fmaf(xf[1], xf[1], xf[4] * xf[4])) + fmaf(xf[2], xf[2], xf[3] * xf[3]);
      const float t1 = ps + dpp<XOR8>(ps), t2 = pq + dpp<XOR8>(pq);
      float z = (c & 8) ? t2 : t1;
      z += dpp<XOR1>(z);
      asm volatile("" : "+v"(z));
      __builtin_amdgcn_sched_barrier(0);
      W1MMA(acc)
      if (BAN == 1) {
        asm volatile("" : "+v"(acc[0]), "+v"(acc[1]));
        BFLAG(0)
      }
      z = cross_rows_sum(z);
      if ((lane & 49) == 0) *reinterpret_cast<float*>(lds + a_st) = z;
      if (c < 4) {
#pragma unroll
        for (int nt = 0; nt < 2; ++nt) *reinterpret_cast<f32x4*>(lds + a_red + nt * 64) = acc[nt];
      }
    }
#undef W1MMA
    PAD(b3, back);
    BAR();
    STAMPT(24, 0);
    if (wave < 4) {
      f32x4 sv[4];
      float sp[4];
#pragma unroll
      for (int k = 0; k < 4; ++k) sv[k] = *reinterpret_cast<const f32x4*>(lds + a_fst + k * 16);
#pragma unroll
      for (int k = 0; k < 4; ++k) sp[k] = *reinterpret_cast<const float*>(lds + a_fin + k * 512);
      __builtin_amdgcn_sched_barrier(0);
      const f32x4 s1 = sv[0] + sv[1], s2 = sv[2] + sv[3];
      const float mean = ((s1[0] + s1[1]) + (s1[2] + s1[3])) * (1.f / DM);
      const float var = fmaxf(((s2[0] + s2[1]) + (s2[2] + s2[3])) * (1.f / DM) - mean * mean, 0.f);
      const float rstd = __builtin_amdgcn_rsqf(var + 1e-5f);
      const float zp = fmaf(rstd, fmaf(-mean, Tc, (sp[0] + sp[1]) + (sp[2] + sp[3])), Ts);
      const float hh = bfr(bfr(swap32_add(zp, zp)) + b1v);
      const float e2 = __builtin_amdgcn_exp2f(hh * fmaf(hh * hh, 0.1029432397f, 2.302208198f));
      const float f = fmaf(-hh, frcp(e2 + 1.f), hh);
      *reinterpret_cast<unsigned short*>(lds + a_fo) = (unsigned short)cvt2(f, 0.f);
    }
    BAR();
    if (BM) {
      u32x4 av[4];
#pragma unroll
      for (int k = 0; k < 4; ++k) av[k] = *reinterpret_cast<const u32x4*>(lds + a_fi + (3 - k) * 256);
      SB();
      const f32x4 z = {0.f, 0.f, 0.f, 0.f};
      f32x4 a4 = MFMA(wg[4], av[0], z);
      PIN(a4)
      f32x4 a0 = MFMA(wg[0], av[0], z);
      PIN(a0)
#pragma unroll
      for (int k = 1; k < 4; ++k) { a0 = MFMA(wg[k], av[k], a0); PIN(a0) }
      __builtin_amdgcn_sched_barrier(0);
      const u32x2 o4 = {min(cvt2(a4[0], a4[1]), EMPTY - 1), min(cvt2(a4[2], a4[3]), EMPTY - 1)};
      __builtin_amdgcn_raw_buffer_store_b64(o4, rs, a_out, 4 * (54 * 128), 16);
      const u32x2 o0 = {min(cvt2(a0[0], a0[1]), EMPTY - 1), min(cvt2(a0[2], a0[3]), EMPTY - 1)};
      __builtin_amdgcn_raw_buffer_store_b64(o0, rs, a_ou4, 0, 16);
    } else {
      const u32x4 av = *reinterpret_cast<const u32x4*>(lds + a_fi);
      f32x4 acc[5];
#pragma unroll
      for (int k = 0; k < 5; ++k) { const f32x4 z = {0.f, 0.f, 0.f, 0.f}; acc[k] = MFMA(wg[k], av, z); }
      __builtin_amdgcn_sched_barrier(0);
#pragma unroll
      for (int k = 0; k < 5; ++k) {
        const u32x2 o = {min(cvt2(acc[k][0], acc[k][1]), EMPTY - 1), min(cvt2(acc[k][2], acc[k][3]), EMPTY - 1)};
        __builtin_amdgcn_raw_buffer_store_b64(o, rs, a_out, k * (54 * 128), 16);
      }
    }
    STAMPT(15, 0);
    asm volatile("" :: "v"(pa[0]), "v"(pa[1]), "v"(pa[2]), "v"(pa[3]), "v"(pa[4]));

  } else if (role == 2 && wave == 0) {
    PAD(c0, back);
    const int lb = (c >> 1) & 3, col = g * 16 + (c & 1) * 8 + 2 * r + (c >> 3);
    bf16* outp;
    asm volatile("s_load_dwordx2 %0, %1, 128\n\ts_waitcnt lgkmcnt(0)" : "=s"(outp) : "s"(__builtin_amdgcn_kernarg_segment_ptr()));
    const __amdgpu_buffer_rsrc_t ro = __builtin_amdgcn_make_buffer_rsrc(outp, 0, NB * DM * 2, 0x00020000);
#define DUMMY() __builtin_amdgcn_raw_buffer_store_b16((unsigned short)0, ro, (lb * DM + col) * 2, 0, DAUX)
    if (DPOS == 0) DUMMY();
    float b2v = bf2f(a.b2[col]);
    const f32x4 z = {0.f, 0.f, 0.f, 0.f};
    SELECTOR(sel)
    float x1v, gbv;
    {
      u32 offs[1] = {G_MF + ((12 + lb) * 160 + (col >> 2)) * 16};
      u32x4 gm[1];
      gather<1>(rs, offs, gm, tg);
      const u32 gbw = (r & 1) ? gm[0][2] : gm[0][0];
      gbv = (c & 8) ? hi16(gbw) : lo16(gbw);
    }
    u32 oo = lb * DM + col;
    asm volatile("" : "+v"(gbv), "+v"(oo), "+v"(sel), "+v"(b2v));
    u32x4 pa[7];
    u32 go = G_OUT + g * (54 * 128) + lane * 16, gx = G_WO + g * 1024 + lane * 16;
#pragma unroll
    for (int i = 0; i < 7; ++i) { pa[i] = u32x4{0u, 0u, 0u, 0u}; asm volatile("" : "+v"(pa[i])); }
    asm volatile("" : "+v"(go), "+v"(gx));
    u32x2 hv;
    if (CPUB) lds_wait<2>(C_PUB, 1);
    if (DPOS == 1) DUMMY();
    heads_up_start(rsv, G_HA + ((g >> 2) * 8 + lane) * 128, 0xffull, tg, hv);
    heads_up<CSL>(rsv, G_HA + ((g >> 2) * 8 + lane) * 128, 0xffull, tg, hv);
    if (DPOS == 2) DUMMY();
    if (EPE == 1) __builtin_amdgcn_raw_buffer_store_b32(tg, rs, u == 0 && lane == 0 ? WS_EPOCH : OOB, 0, 16);
    hv[0] += CXD;
    heads_up_time(0xffull, hv);
    {
      u32x4 gm[1];
      gather_e<1, 2, 1>(rsv, gx, gm, ~0ull);
      const f32x4 sm = MFMA(sel, gm[0], z);
      x1v = bfr(PAIRSUM(sm));
      asm volatile("" : "+v"(x1v));
    }
    STAMPT(23, 0);
    if (DPOS == 3) DUMMY();
    if (EPE == 2) __builtin_amdgcn_raw_buffer_store_b32(tg, rs, u == 0 && lane == 0 ? WS_EPOCH : OOB, 0, 16);
    PAD(c1, back);
    if (CGT) SPINLOOP for (int k = 0; k < 256; ++k) if ((int)((u32)__builtin_amdgcn_s_memrealtime() - tb) >= (126 * TAU_E >> 4) + CGT) break;
    heads_up_start(rsv, G_HB + lane * 128, (1ull << 54) - 1, tg, hv);
    heads_up<0>(rsv, G_HB + lane * 128, (1ull << 54) - 1, tg, hv);
    heads_up_time((1ull << 54) - 1, hv);
    STAMPT(14, 0);
    f32x4 s0, s1;
    {
      u64 m[7], old = 0;
#pragma unroll
      for (int i = 0; i < 7; ++i) m[i] = i < 6 ? ~0ull : (1ull << 48) - 1;
      look<0>(rsv, go, pa, m);
#define CSUM() { s0 = MFMA(sel, pa[0], z); s1 = MFMA(sel, pa[1], z); \
      _Pragma("unroll") for (int i = 2; i < 7; ++i) { if (i & 1) s1 = MFMA(sel, pa[i], s1); else s0 = MFMA(sel, pa[i], s0); } \
      s0 += s1; }
      SPINLOOP for (unsigned spins = 0; spins < SPIN_CAP; ++spins) {
        CSUM()
        if (!__builtin_amdgcn_ballot_w64(__builtin_isunordered(s0[0], s0[1]))) break;
        u64 bad = 0;
#pragma unroll
        for (int i = 0; i < 7; ++i) { m[i] = __builtin_amdgcn_ballot_w64(missing(pa[i])); bad |= m[i]; }
        if (!(bad | old)) break;
        old = bad;
        if (!spins) FAIL(3);
        look<7 * (PDC - 1)>(rsv, go, pa, m);
      }
    }
    STAMPT(16, 0);
    const float y = bfr(bfr(PAIRSUM(s0)) + b2v);
    if (OUTM & 1) __builtin_amdgcn_raw_buffer_store_b16((unsigned short)cvt2(x1v + bfr(gbv * y), 0.f), ro, oo * 2, 0, FAUX);
    else outp[oo] = __float2bfloat16(x1v + bfr(gbv * y));
    STAMPT(17, 0);
    asm volatile("" :: "v"(pa[0]), "v"(pa[1]), "v"(pa[2]), "v"(pa[3]), "v"(pa[4]), "v"(pa[5]), "v"(pa[6]), "v"(hv));

    if (!EPE && u == 0 && t == 0)
      __hip_atomic_store(reinterpret_cast<u32*>(a.ws + WS_EPOCH), tg, __ATOMIC_RELAXED, __HIP_MEMORY_SCOPE_AGENT);
  }
  else if (EPE == 3 && u == 80 && wave == 0) {
    u32x2 hv;
    lds_wait<2>(C_PUB, 1);
    heads_up_start(rsv, G_HA + lane * 128, 0xffull, tg, hv);
    heads_up<8>(rsv, G_HA + lane * 128, 0xffull, tg, hv);
    __builtin_amdgcn_raw_buffer_store_b32(tg, rs, lane < 32 ? WS_EPOCH + lane * 4 : OOB, 0, 16);
  }
  STAMP_FLUSH;
}

extern "C" void kernel_launch(void* const* d_in, const int* in_sizes, int n_in, void* d_out, int out_size, void* d_ws,
                              size_t ws_size, hipStream_t stream) {
  Args a;
  a.x = Desc{(const bf16*)d_in[0], NB * DM * 2, 0x00020000u};
  a.kc = (const bf16*)d_in[1];
  a.vc = (const bf16*)d_in[2];
  a.wq = Desc{(const bf16*)d_in[3], DM * QD * 2, 0x00020000u};
  a.wo = (const bf16*)d_in[4];
  a.w1 = (const bf16*)d_in[6];
  a.b1 = (const bf16*)d_in[7];
  a.w2 = (const bf16*)d_in[8];
  a.b2 = (const bf16*)d_in[9];
  a.c = Desc{(const bf16*)d_in[10], NB * DM * 2, 0x00020000u};
  a.wmod = Desc{(const bf16*)d_in[11], DM * MODN * 2, 0x00020000u};
  a.out = (bf16*)d_out;
  a.ws = (unsigned char*)d_ws;
  mega_kernel<<<NWG, NT, 0, stream>>>(a);
}
```

```cpp
#define SPINLOOP _Pragma("nounroll")
#define P_LAG 14
#define P_NPRE 32
#define P_NVE 6
#define P_NVA 6
#define QODD 4
#define QEVEN 4
#define MFPOS 0
#define EA 0
#define EB 0
#define EC 4
#define APOLL 0
#define N1E N1A
#define TAU_A 236
#define TAU_B 239
#define TAU_X TAU_A
#define TAU_E 244
#define PK 0
#define PL 1
#define QPRE 1
#define QHOLD 0
#define CT0 1
#define TOFF 55
#define BLIGHT 1
#define BPRE 0
#define BW1 1
#define BMASK 1
#define CSLEEP 1
#define CSL 2
#define CPUB 1
#define CXD 20
#define OPOS 1
#define NREPL 1
#define OUTPRE 1
#define ASL 1
#define AEARLY 1
#define PDC 4
#define PDA 3
#define PDB 3
#define PSB 1
#define HC 1
#define HCD 132
#define HAG 40
#define AW 4
#define BFW -1
#define FULLF 1
#define WARM 1
#define HT 0
#define HX 14
#define HTD 55
#define HSL 1
#define NANT 1
#define HAW 65
#define BM 1
#define HPD 1
#define BT 0
#define BSL 2
#define PB 0
#define GOPOS 2
#define JMAP 1
#define N1A 10
#define N1B 5
#define PT 0
#define PTAUX 16
#define LAY 1
#define OUTM 1
#define EPE 0
#define DPOS 2
#define DAUX 16
#define FAUX 16
#define KOUT 1
#define NK0 12
#define GT 540
#define PLO 5
#define PLS 17
#define ANCH 1
#define AOFF 55
#define AGG 1
#define PHW 0
#define CPF 1
#define CLEN 0x7e00
#define AGT 0
#define GPAD 1
#define MFE 1
#define E3POS 10
#define E4POS 22
#define L1POS 28
#define L4POS 4
#define T1POS 18
#define HEARLY 0
#define BAR2L 0
#define ROT1 5
#define W2F 0
#define HGT 300
#define BGT 304
#define BPREW 0
#define BAN 2
#define HBD 85
#define CGT 0
#define MLP 30
#define BHOLD 0
#define TAU_L 239
#define DL0 42
#define EVT 310
#define FXA 1
#define RPRI 0
#define EVW 40
#define KP 0
#define KTO 270
#define KTE 0
#define SLS 0
#define SLF 30
#define SLM 50
#define DLS DLA
#define BFIRST 1
#define EVL 1
#define DLA 53
#define DLB 30
#define CST 1
#define POFF 0
#define NCL 4
#define CLS (NCL == 8 ? u & 7 : NCL == 4 ? (u >> 1) & 3 : (u >> 3) & (NCL - 1))
#define P_GATE 8
#include <hip/hip_runtime.h>
#include <hip/hip_bf16.h>
#include <stdint.h>

using bf16 = __hip_bfloat16;
using u32 = unsigned;
using u64 = unsigned long long;
using u32x2 = __attribute__((ext_vector_type(2))) unsigned;
using u32x4 = __attribute__((ext_vector_type(4))) unsigned;
using f32x4 = __attribute__((ext_vector_type(4))) float;
using bf16x2 = __attribute__((ext_vector_type(2))) __bf16;
typedef __attribute__((ext_vector_type(2))) float f32x2;
typedef short s16x8 __attribute__((ext_vector_type(8)));
typedef short s16x4 __attribute__((ext_vector_type(4)));
typedef __attribute__((address_space(3))) s16x4* lds_s4p;
typedef __attribute__((ext_vector_type(8))) __bf16 bf16x8v;

constexpr int NB = 4, DM = 640, NH = 32, DH = 128, SK = 8192, WIN = 2048, FF = 1728, QD = 4096, MODN = 3840;
constexpr int NT = 512, NWG = 256;

constexpr u32 WS_EPOCH = 0;
constexpr u32 G_T = 128;
constexpr u32 G_Q = 256;
constexpr u32 G_MOD = G_Q + 8 * 1024 * 16;
constexpr u32 G_MF = G_MOD + 8 * 1024 * 16;
constexpr u32 G_HB = G_MF + 16 * 160 * 16;
constexpr u32 G_HA = G_HB + 64 * 128;
constexpr u32 G_HT = G_HA + 80 * 128;
constexpr u32 G_ML = G_HT + 256 * 128;
constexpr u32 EMPTY = 0xffffffffu;
constexpr int NREP = NREPL;
constexpr u32 G_WO = G_ML + 256 * 64;
constexpr u32 WO_REP = 40 * 1024;
constexpr u32 G_ATT = G_WO + NREP * WO_REP;
constexpr u32 G_TK = G_ATT + 256 * 256;
constexpr u32 G_OUT = G_TK + 1024;
constexpr u32 WS_END = G_OUT + 40 * 54 * 128;
constexpr u32 OOB = 0xF0000000u;
#define GIDX(n, ks, b, cg) ((((ks) * ((n) / 4) + ((cg) >> 2)) * 16 + (b) * 4 + ((cg) & 3)) * 16)

#define WSTAMP(k) do {} while (0)
#define STAMPW(i) do {} while (0)
#define STAMPT(i, tt) do {} while (0)
#define STAMP_DECL
#define STAMP_FLUSH do {} while (0)
#define FAIL(i) do {} while (0)
#define PAD(name, next) asm volatile(".Lpad_" #name ":\n\ts_cbranch_execz .Lpad_" #next)
#define PADX(name, next, x) asm volatile(".Lpad_" #name ":\n\ts_cbranch_execz .Lpad_" #next : "+v"(x))
#define PADA(name, next) asm volatile(".Lpad_" #name ":\n\ts_cbranch_execz .Lpad_" #next : "+v"(acc[0]), "+v"(acc[1]), "+v"(acc[2]), "+v"(acc[3]), "+v"(acc[4]), "+v"(acc[5]), "+v"(acc[6]), "+v"(acc[7]))
#define STR_(x) #x
#define STR(x) STR_(x)
#define BAR() asm volatile("s_waitcnt lgkmcnt(0)\n\ts_barrier" ::: "memory")
#define VM0() asm volatile("s_waitcnt vmcnt(0)" ::: "memory")

__device__ __forceinline__ float bfr(float x) { return __bfloat162float(__float2bfloat16(x)); }
__device__ __forceinline__ float bf2f(bf16 x) { return __bfloat162float(x); }
__device__ __forceinline__ float lo16(unsigned u) { return __builtin_bit_cast(float, u << 16); }
__device__ __forceinline__ float hi16(unsigned u) { return __builtin_bit_cast(float, u & 0xffff0000u); }
__device__ __forceinline__ u32 f2u(float f) { return __builtin_bit_cast(u32, f); }
__device__ __forceinline__ float u2f(u32 f) { return __builtin_bit_cast(float, f); }
__device__ __forceinline__ u32 cvt2(float a, float b) {
  const f32x2 v = {a, b};
  return __builtin_bit_cast(u32, __builtin_convertvector(v, bf16x2));
}
__device__ __forceinline__ float frcp(float x) { return __builtin_amdgcn_rcpf(x); }
__device__ __forceinline__ float fsqrt(float x) { return __builtin_amdgcn_sqrtf(x); }
__device__ __forceinline__ float fexp(float x) { return __builtin_amdgcn_exp2f(x * 1.4426950408889634f); }
__device__ __forceinline__ float dot2(unsigned k, unsigned q, float acc) {
  return __builtin_amdgcn_fdot2_f32_bf16(__builtin_bit_cast(bf16x2, k), __builtin_bit_cast(bf16x2, q), acc, false);
}
__device__ __forceinline__ float addlo(unsigned w, float acc) { u32 k = 0x00003f80u; asm("" : "+s"(k)); return dot2(w, k, acc); }
__device__ __forceinline__ float addhi(unsigned w, float acc) { return dot2(w, 0x3f800000u, acc); }
template <int CTRL> __device__ __forceinline__ float dpp(float x) {
  return __builtin_bit_cast(float, __builtin_amdgcn_mov_dpp(__builtin_bit_cast(int, x), CTRL, 0xf, 0xf, true));
}
template <int CTRL> __device__ __forceinline__ u32 dppu(u32 x) { return (u32)__builtin_amdgcn_mov_dpp((int)x, CTRL, 0xf, 0xf, true); }
constexpr int XOR1 = 0xB1, XOR2 = 0x4E, XOR7 = 0x141, XOR8 = 0x128, ROR4 = 0x124, ROR2 = 0x122, ROR1 = 0x121, SHL4 = 0x104;
__device__ __forceinline__ float swap32_add(float a, float b) {
  auto s = __builtin_amdgcn_permlane32_swap(f2u(a), f2u(b), false, false);
  return u2f(s[0]) + u2f(s[1]);
}
__device__ __forceinline__ float swap16_add(float a, float b) {
  auto s = __builtin_amdgcn_permlane16_swap(f2u(a), f2u(b), false, false);
  return u2f(s[0]) + u2f(s[1]);
}
__device__ __forceinline__ float cross_rows_sum(float x) { x = swap16_add(x, x); return swap32_add(x, x); }
__device__ __forceinline__ float cross_rows_max(float x) {
  auto s = __builtin_amdgcn_permlane16_swap(f2u(x), f2u(x), false, false);
  x = fmaxf(u2f(s[0]), u2f(s[1]));
  auto s2 = __builtin_amdgcn_permlane32_swap(f2u(x), f2u(x), false, false);
  return fmaxf(u2f(s2[0]), u2f(s2[1]));
}
__device__ __forceinline__ float wave_sum(float x) {
  x += dpp<XOR8>(x); x += dpp<ROR4>(x); x += dpp<ROR2>(x); x += dpp<ROR1>(x);
  return cross_rows_sum(x);
}
__device__ __forceinline__ float wave_max(float x) {
  x = fmaxf(x, dpp<XOR8>(x)); x = fmaxf(x, dpp<ROR4>(x)); x = fmaxf(x, dpp<ROR2>(x)); x = fmaxf(x, dpp<ROR1>(x));
  return cross_rows_max(x);
}

__device__ __forceinline__ void st_gran(__amdgpu_buffer_rsrc_t rs, u32 off, u32 a, u32 b, u32 tg) {
  u32x4 v = {a, tg, b, tg};
  __builtin_amdgcn_raw_buffer_store_b128(v, rs, off, 0, 16);
}
__device__ __forceinline__ u32x4 ld_gran(__amdgpu_buffer_rsrc_t rs, u32 off) {
  return __builtin_amdgcn_raw_buffer_load_b128(rs, off, 0, 16);
}
constexpr unsigned SPIN_CAP = 1u << 18;
template <int N>
__device__ __forceinline__ void gather(__amdgpu_buffer_rsrc_t rs, const u32 (&offs)[N], u32x4 (&g)[N], u32 tg) {
  SPINLOOP for (unsigned spins = 0; spins < SPIN_CAP; ++spins) {
    bool ok = true;
#pragma unroll
    for (int i = 0; i < N; ++i) g[i] = ld_gran(rs, offs[i]);
#pragma unroll
    for (int i = 0; i < N; ++i) ok &= (offs[i] == OOB) | ((g[i][1] == tg) & (g[i][3] == tg));
    if (__all(ok)) break;
    __builtin_amdgcn_s_sleep(1);
  }
}
template <int N>
__device__ __forceinline__ void gather_m(__amdgpu_buffer_rsrc_t rs, u32 (&offs)[N], u32 (&va)[N], u32 (&vb)[N], u32 tg) {
#pragma unroll
  for (int i = 0; i < N; ++i) { va[i] = 0; vb[i] = 0; }
  SPINLOOP for (unsigned spins = 0; spins < SPIN_CAP; ++spins) {
    u32x4 g[N];
#pragma unroll
    for (int i = 0; i < N; ++i) g[i] = ld_gran(rs, offs[i]);
    bool ok = true;
#pragma unroll
    for (int i = 0; i < N; ++i) {
      const bool hit = (offs[i] != OOB) & (g[i][1] == tg) & (g[i][3] == tg);
      va[i] = hit ? g[i][0] : va[i]; vb[i] = hit ? g[i][2] : vb[i];
      offs[i] = hit ? OOB : offs[i];
      ok &= (offs[i] == OOB);
    }
    if (__all(ok)) break;
    __builtin_amdgcn_s_sleep(1);
  }
}

__device__ __forceinline__ bool missing(u32x4 v) { return max(max(v[0], v[1]), max(v[2], v[3])) == EMPTY; }
#define LK(v, o, so, imm, m) "s_mov_b64 exec, %" #m "\n\tbuffer_load_dwordx4 %" #v ", %" #o ", %[rs], " so " offen offset:" #imm " sc1\n\t"
#define LKEND "s_mov_b64 exec, -1\n\ts_waitcnt vmcnt(%[w])"
template <int W> __device__ __forceinline__ void look(u32x4 rs, u32 off, u32x4 (&v)[1], const u64 (&m)[1]) {
  asm volatile(LK(0, 1, "0", 0, 2) LKEND : "+v"(v[0]) : "v"(off), "s"(m[0]), [rs] "s"(rs), [w] "i"(W) : "memory");
}
template <int W> __device__ __forceinline__ void look(u32x4 rs, u32 off, u32x4 (&v)[5], const u64 (&m)[5]) {
  asm volatile(LK(0, 5, "0", 0, 6) LK(1, 5, "0", 1024, 7) LK(2, 5, "0", 2048, 8) LK(3, 5, "0", 3072, 9) LK(4, 5, "%11", 0, 10) LKEND
               : "+v"(v[0]), "+v"(v[1]), "+v"(v[2]), "+v"(v[3]), "+v"(v[4])
               : "v"(off), "s"(m[0]), "s"(m[1]), "s"(m[2]), "s"(m[3]), "s"(m[4]), "s"(4096u), [rs] "s"(rs), [w] "i"(W) : "memory");
}
template <int W> __device__ __forceinline__ void look(u32x4 rs, u32 off, u32x4 (&v)[7], const u64 (&m)[7]) {
  asm volatile(LK(0, 7, "0", 0, 8) LK(1, 7, "0", 1024, 9) LK(2, 7, "0", 2048, 10) LK(3, 7, "0", 3072, 11)
               LK(4, 7, "%15", 0, 12) LK(5, 7, "%15", 1024, 13) LK(6, 7, "%15", 2048, 14) LKEND
               : "+v"(v[0]), "+v"(v[1]), "+v"(v[2]), "+v"(v[3]), "+v"(v[4]), "+v"(v[5]), "+v"(v[6])
               : "v"(off), "s"(m[0]), "s"(m[1]), "s"(m[2]), "s"(m[3]), "s"(m[4]), "s"(m[5]), "s"(m[6]), "s"(4096u), [rs] "s"(rs), [w] "i"(W) : "memory");
}
template <int N, int SLEEP, int D>
__device__ __forceinline__ void gather_e(u32x4 rs, u32 off, u32x4 (&v)[N], u64 last) {
  u64 m[N];
#pragma unroll
  for (int i = 0; i < N; ++i) { m[i] = i < N - 1 ? ~0ull : last; v[i] = u32x4{0u, 0u, 0u, 0u}; }
  look<0>(rs, off, v, m);
  SPINLOOP for (unsigned spins = 0; spins < SPIN_CAP; ++spins) {
    u64 bad = 0;
#pragma unroll
    for (int i = 0; i < N; ++i) { m[i] = __builtin_amdgcn_ballot_w64(missing(v[i])); bad |= m[i]; }
    if (!bad) break;
    if (SLEEP) __builtin_amdgcn_s_sleep(SLEEP);
    look<N * (D - 1)>(rs, off, v, m);
  }
}

#define HLOOK(W_) asm volatile("s_mov_b64 exec, %2\n\tbuffer_load_dwordx2 %0, %1, %3, 0 offen sc1\n\ts_mov_b64 exec, -1\n\ts_waitcnt vmcnt(%4)" \
                               : "+v"(v) : "v"(off), "s"(m), "s"(rs), "i"(W_) : "memory")
__device__ __forceinline__ void heads_up_start(u32x4 rs, u32 off, u64 m, u32 tg, u32x2& v) { v = u32x2{0u, ~tg}; HLOOK(63); }
template <int SLEEP, int W0 = 0>
__device__ __forceinline__ void heads_up(u32x4 rs, u32 off, u64 m, u32 tg, u32x2& v) {
  asm volatile("s_waitcnt vmcnt(%1)" : "+v"(v) : "i"(W0) : "memory");
  SPINLOOP for (unsigned spins = 0; spins < SPIN_CAP; ++spins) {
    m &= __builtin_amdgcn_ballot_w64(v[1] != tg);
    if (!m) break;
    if (SLEEP) __builtin_amdgcn_s_sleep(SLEEP);
    HLOOK(0);
  }
}
__device__ __forceinline__ void heads_up_time(u64 all, u32x2 v) {
  SPINLOOP for (unsigned spins = 0; spins < 64; ++spins)
    if (!(all & __builtin_amdgcn_ballot_w64((int)((u32)__builtin_amdgcn_s_memrealtime() - v[0]) < 0))) break;
}
__device__ __forceinline__ void announce(__amdgpu_buffer_rsrc_t rs, u32 off, u32 a_max, u32 a_cnt, u32 n, u32 tb, u32 d, u32 tg) {
  u32 tk = 0, mx;
  const u32 f = (u32)__builtin_amdgcn_s_memrealtime() - tb + d;
  asm volatile("s_mov_b64 exec, 1\n\tds_max_u32 %2, %3\n\tds_add_rtn_u32 %0, %4, %5\n\tds_read_b32 %1, %2\n\ts_mov_b64 exec, -1\n\ts_waitcnt lgkmcnt(0)"
               : "+v"(tk), "=&v"(mx) : "v"(a_max), "v"(f), "v"(a_cnt), "v"(1u) : "memory");
  const u32 lane = threadIdx.x & 63;
  __builtin_amdgcn_raw_buffer_store_b64(u32x2{__builtin_amdgcn_readfirstlane(mx) + tb, tg}, rs,
                                        (lane < 16 && __builtin_amdgcn_readfirstlane(tk) == n - 1) ? off + lane * 8 : OOB, 0, 16);
}

__device__ __forceinline__ void st_g22(__amdgpu_buffer_rsrc_t rs, u32 off, f32x4 v, u32 tg) {
  const u32 a0 = f2u(v[0]) + 0x200u, a1 = f2u(v[1]) + 0x200u, a2 = f2u(v[2]) + 0x200u, a3 = f2u(v[3]) + 0x200u;
  const u32x4 w = {(a0 >> 10) | ((a1 << 12) & 0xffc00000u), (a1 >> 20) | (tg << 12), (a2 >> 10) | ((a3 << 12) & 0xffc00000u), (a3 >> 20) | (tg << 12)};
  __builtin_amdgcn_raw_buffer_store_b128(w, rs, off, 0, 16);
}
__device__ __forceinline__ f32x4 un_g22(u32x4 w) {
  return f32x4{u2f(w[0] << 10), u2f(__builtin_amdgcn_alignbit(w[1], w[0], 22) << 10), u2f(w[2] << 10), u2f(__builtin_amdgcn_alignbit(w[3], w[2], 22) << 10)};
}
template <int N, int SLEEP>
__device__ __forceinline__ void gather22(__amdgpu_buffer_rsrc_t rs, u32 (&offs)[N], u32x4 (&v)[N], u32 tg, u32 a_inc = 0) {
#pragma unroll
  for (int i = 0; i < N; ++i) v[i] = u32x4{0u, 0u, 0u, 0u};
  SPINLOOP for (unsigned spins = 0; spins < SPIN_CAP; ++spins) {
    u32x4 g[N];
#pragma unroll
    for (int i = 0; i < N; ++i) g[i] = ld_gran(rs, offs[i]);
    if (a_inc) asm volatile("s_mov_b64 exec, 1\n\tds_add_u32 %0, %1\n\ts_mov_b64 exec, -1" :: "v"(a_inc), "v"(1u) : "memory");
    bool ok = true;
#pragma unroll
    for (int i = 0; i < N; ++i) {
      const bool hit = (offs[i] != OOB) & (((g[i][1] ^ (tg << 12)) | (g[i][3] ^ (tg << 12))) < 4096u);
#pragma unroll
      for (int k = 0; k < 4; ++k) v[i][k] = hit ? g[i][k] : v[i][k];
      offs[i] = hit ? OOB : offs[i];
      ok &= (offs[i] == OOB);
    }
    if (__all(ok)) break;
    __builtin_amdgcn_s_sleep(SLEEP);
  }
}

__device__ __forceinline__ int sel_lt(int a, int b, int x, int y) {
  int r;
  asm("s_cmp_lt_i32 %1, %2\n\ts_cselect_b32 %0, %3, %4" : "=s"(r) : "s"(a), "s"(b), "s"(x), "s"(y) : "scc");
  return r;
}
__device__ __forceinline__ u32 next_tag(u32 ep) { const u32 t = ep + 1u; return (t & 0xfffffu) ? t : t + 1u; }

__device__ __forceinline__ void lds_arrive(u32 addr, int lane) {
  asm volatile("s_waitcnt lgkmcnt(0)" ::: "memory");
  if (lane == 0) asm volatile("ds_add_u32 %0, %1" :: "v"(addr), "v"(1u) : "memory");
}
__device__ __forceinline__ u32 lds_ticket(u32 addr, int lane) {
  u32 r = 0;
  asm volatile("s_waitcnt lgkmcnt(0)" ::: "memory");
  if (lane == 0) asm volatile("ds_add_rtn_u32 %0, %1, %2\n\ts_waitcnt lgkmcnt(0)" : "=v"(r) : "v"(addr), "v"(1u) : "memory");
  return __builtin_amdgcn_readfirstlane(r);
}
template <int SLEEP>
__device__ __forceinline__ void lds_wait(u32 addr, u32 target) {
  SPINLOOP for (unsigned spins = 0; spins < (1u << 22); ++spins) {
    u32 v;
    asm volatile("ds_read_b32 %0, %1\n\ts_waitcnt lgkmcnt(0)" : "=v"(v) : "v"(addr) : "memory");
    if (v >= target) break;
    __builtin_amdgcn_s_sleep(SLEEP);
  }
}
__device__ __forceinline__ void dma16(const void* gsrc, u32 lds_dst) {
  u32 keep;
  asm volatile("s_mov_b32 %0, m0\n\ts_mov_b32 m0, %2\n\ts_nop 0\n\tglobal_load_lds_dwordx4 %1, off\n\ts_mov_b32 m0, %0"
               : "=&s"(keep) : "v"(gsrc), "s"(lds_dst) : "memory");
}
__device__ __forceinline__ void dma16nt(const void* gsrc, u32 lds_dst) {
  u32 keep;
  asm volatile("s_mov_b32 %0, m0\n\ts_mov_b32 m0, %2\n\ts_nop 0\n\tglobal_load_lds_dwordx4 %1, off nt\n\ts_mov_b32 m0, %0"
               : "=&s"(keep) : "v"(gsrc), "s"(lds_dst) : "memory");
}
__device__ __forceinline__ u32x4 ldasm(const void* p) {
  u32x4 v;
  asm volatile("global_load_dwordx4 %0, %1, off" : "=v"(v) : "v"(p) : "memory");
  return v;
}
__device__ __forceinline__ u32 ldasm_sc1(const void* p) {
  u32 v;
  asm volatile("global_load_dword %0, %1, off sc1" : "=v"(v) : "v"(p) : "memory");
  return v;
}
__device__ __forceinline__ u32x4 ldw(const bf16* p) { return *reinterpret_cast<const u32x4*>(p); }
template <int D2>
__device__ __forceinline__ s16x8 wfrag(u32 addr) {
  const s16x4 lo = __builtin_amdgcn_ds_read_tr16_b64_v4i16((lds_s4p)(uintptr_t)addr);
  const s16x4 hi = __builtin_amdgcn_ds_read_tr16_b64_v4i16((lds_s4p)(uintptr_t)(addr + D2));
  return s16x8{lo[0], lo[1], lo[2], lo[3], hi[0], hi[1], hi[2], hi[3]};
}
#define MFMA(w_, x_, c_) __builtin_amdgcn_mfma_f32_16x16x32_bf16(__builtin_bit_cast(bf16x8v, w_), __builtin_bit_cast(bf16x8v, x_), c_, 0, 0, 0)

struct Desc { const bf16* p; u32 bytes, flags; };
struct Args {
  Desc wq, x, wmod, c;
  unsigned char* ws;
  const bf16 *kc, *vc, *wo, *w1, *b1, *w2, *b2;
  bf16* out;
};

static_assert(offsetof(Args, ws) == 64 && offsetof(Args, out) == 128, "argument offsets");

constexpr int L_W = 0;
constexpr int L_WM2 = 40960;
constexpr int L_XA = 81920;
constexpr int XS = 1312, XH = 704;
constexpr int L_RED = L_XA + 16384;
constexpr int L_MOD = L_RED + 8192;
constexpr int L_XR = L_MOD + 5120;
constexpr int L_PT = L_XR + 5120;
constexpr int L_ACC = L_PT + 2048;
constexpr int L_QS = L_ACC + 4096;
constexpr int L_FI = L_QS + 256;
constexpr int L_ST = L_FI + 256;
constexpr int L_CNT = L_ST + 256;
constexpr int L_END = L_CNT + 128;
static_assert(L_END <= 160 * 1024, "LDS");
constexpr u32 C_Q = L_CNT, C_SC = L_CNT + 4, C_ISS = L_CNT + 8, C_P1 = L_CNT + 20, C_P2 = L_CNT + 24, C_GO = L_CNT + 28, C_W = L_CNT + 12, C_FIN = L_CNT + 32,
              C_KI = L_CNT + 36, C_PP = L_CNT + 40, C_PUB = L_CNT + 56, C_HT = L_CNT + 64, C_MF = L_CNT + 68, C_RL = L_CNT + 72, C_DL = L_CNT + 76;

constexpr int NL = 32;
constexpr int PVLAG = P_LAG;
constexpr int NPRE = P_NPRE;
constexpr int NVE = P_NVE;
static_assert(PVLAG <= 16 && (PVLAG & 1) == 0 && NVE <= PVLAG && (NVE & 1) == 0, "row groups");

__global__ void __launch_bounds__(NT, 2) mega_kernel(Args a) {
  __shared__ __attribute__((aligned(1024))) unsigned char lds[L_END];
  float* red = reinterpret_cast<float*>(lds + L_RED);
  u32* xraw = reinterpret_cast<u32*>(lds + L_XR);
  u32* pT = reinterpret_cast<u32*>(lds + L_PT);
  float* accs = reinterpret_cast<float*>(lds + L_ACC);
  u32* qs = reinterpret_cast<u32*>(lds + L_QS);
  float* st = reinterpret_cast<float*>(lds + L_ST);
  u32* cnt = reinterpret_cast<u32*>(lds + L_CNT);

  u32x4 rw, rx;
  unsigned char* wsq;
  const void* kcq;
  asm volatile("s_load_dwordx4 %0, %4, %5\n\ts_load_dwordx4 %1, %4, %5 offset:16\n\ts_load_dwordx2 %2, %4, 64\n\ts_load_dwordx2 %3, %4, 72"
               : "=&s"(rw), "=&s"(rx), "=&s"(wsq), "=&s"(kcq) : "s"(__builtin_amdgcn_kernarg_segment_ptr()), "s"((blockIdx.x & 1) * 32));
  __builtin_amdgcn_sched_barrier(0);
  const int t = threadIdx.x, lane = t & 63;
  const int wave = __builtin_amdgcn_readfirstlane(t >> 6);
  const int u = blockIdx.x;
  const int bh = u >> 1, half = u & 1, b_att = bh >> 5, h_att = bh & 31;
  const int r = lane >> 4, c = lane & 15;
  STAMP_DECL
  STAMPT(0, 0);
  u32 tg;
  const int g = u >> 1, ksp = g & 1, cbp = g >> 1;
#define fav (half || (g >= 120 && g < 126))
  const int s128e = (((((lane & 7) >> 1) ^ ((lane >> 4) & 1)) << 1) | (lane & 1)) * 8;
  const u32 tr128 = (8 * r + (c >> 2)) * 128 + ((((c >> 3) & 1) | ((r & 1) << 1)) * 32) + (c & 3) * 8;
  if (wave < 4) {
    __builtin_amdgcn_s_setprio(3);
    const u32 rb = 8192 - 512 * half;
    const u32 hm = 0u - (u32)half;
    const u32 vo = ((u32)(lane >> 3) * rb + (((lane & 7) ^ ((lane >> 3) & 2)) ^ ((wave & 1) * 4)) * 16) | ((u32)(119 - g) & hm & OOB);
    u32 so = (u32)(ksp * 320 + 8 * wave) * rb + cbp * 128;
    const u32 step = 32 * rb, dstep = 4096, dst = L_W + wave * 1024;
    const u32 oe = wave * 1280 + lane * 16, oo = (r * 1280 + ksp * 640 + (c + 16 * wave) * 16) | ((u32)(39 - c - 16 * wave) & OOB);
    const u32 oa = (oo & hm) | (oe & ~hm), ob = (wave * 1280 + 1024 + c * 16) | (hm & OOB), oc = oe | (hm & OOB), zero = 0;
    u32 ep, keep;
    u32x4 xv[4];
    static_assert(G_ATT == G_WO + 40960 && G_OUT == G_WO + 35 * 3072, "35 x 3 KB");
    const u32 pre = ((u32)u << 31) & (u32)(g - 35) & (u32)(2 - wave), po = G_WO + g * 3072 + lane * 16;
    const u32x4 emp = {EMPTY, EMPTY, EMPTY, EMPTY};
#define DMA1 "buffer_load_dwordx4 %7, %8, %1 offen nt lds\n\t"
#define DMAN "s_add_u32 m0, m0, %11\n\ts_add_u32 %1, %1, %10\n\t" DMA1
    asm volatile("s_mov_b32 %0, m0\n\ts_mov_b32 m0, %9\n\t"
                 "s_cmp_gt_i32 %18, 1\n\ts_waitcnt lgkmcnt(0)\n\ts_cbranch_scc1 1f\n\t"
                 "global_load_dword %2, %12, %13 sc1\n\t"
                 "buffer_load_dwordx4 %3, %14, %17, 0 offen\n\tbuffer_load_dwordx4 %4, %15, %17, 0 offen\n\t"
                 "buffer_load_dwordx4 %5, %16, %17, 0 offen offset:2560\n\tbuffer_load_dwordx4 %6, %15, %17, 0 offen offset:2560\n"
                 "1:\n\t" DMA1 DMAN DMAN DMAN DMAN DMAN DMAN DMAN DMAN DMAN "s_mov_b32 m0, %0\n\t"
                 "s_cmp_eq_u32 %19, 0\n\ts_cbranch_scc1 2f\n\tglobal_store_dwordx4 %20, %21, %13 sc1\n\t"
                 "global_store_dwordx4 %20, %21, %13 offset:1024 sc1\n\tglobal_store_dwordx4 %20, %21, %13 offset:2048 sc1\n2:\n\t"
#define B0W "s_barrier\n\ts_memrealtime vcc\n\ts_waitcnt vmcnt(10) lgkmcnt(0)\n\ts_sub_u32 %0, vcc_lo, " STR(AOFF)
                 B0W
                 : "=&s"(keep), "+s"(so), "=&v"(ep), "=&v"(xv[0]), "=&v"(xv[1]), "=&v"(xv[2]), "=&v"(xv[3])
                 : "v"(vo), "s"(rw), "s"(dst), "s"(step), "s"(dstep), "v"(zero), "s"(wsq), "v"(oa), "v"(ob), "v"(oc), "s"(rx), "s"(wave - half),
                   "s"(pre), "v"(po), "v"(emp) : "memory", "scc", "vcc");
#undef DMAN
#undef DMA1
    asm volatile("" : "+s"(wsq));
    const __amdgpu_buffer_rsrc_t rs = __builtin_amdgcn_make_buffer_rsrc(wsq, 0, WS_END, 0x00020000);
    if (half) {
      if (wave < 3) {
        u32x4 sv;
#pragma unroll
        for (int k = 0; k < 4; ++k) {
          const float c0 = lo16(xv[0][k]), c1 = hi16(xv[0][k]);
          sv[k] = cvt2(c0 * frcp(1.f + fexp(-c0)), c1 * frcp(1.f + fexp(-c1)));
        }
        if (c + 16 * wave < 40) *reinterpret_cast<u32x4*>(lds + L_XA + r * XH + (c + 16 * wave) * 16) = sv;
        if (t == 0) { cnt[4] = next_tag(ep); cnt[15] = keep; }
        lds_arrive(C_P1, lane);
        STAMPT(2, 0);
      }
    } else {
      if (wave < 2) {
#pragma unroll
        for (int i = 0; i < 2; ++i) {
          const int b = wave + 2 * i;
          float xf[16];
#pragma unroll
          for (int k = 0; k < 4; ++k) { xf[2 * k] = lo16(xv[2 * i][k]); xf[2 * k + 1] = hi16(xv[2 * i][k]); xf[8 + 2 * k] = lo16(xv[2 * i + 1][k]); xf[8 + 2 * k + 1] = hi16(xv[2 * i + 1][k]); }
          float s = 0.f, s2 = 0.f;
#pragma unroll
          for (int k = 0; k < 8; ++k) { s += xf[k]; s2 += xf[8 + k]; }
          if (lane < 16) s += s2;
          const float mean = wave_sum(s) * (1.f / DM);
          float v = 0.f, v2 = 0.f;
#pragma unroll
          for (int k = 0; k < 8; ++k) { const float d = xf[k] - mean; v += d * d; const float d2 = xf[8 + k] - mean; v2 += d2 * d2; }
          if (lane < 16) v += v2;
          v = wave_sum(v);
          if (lane == 0) { st[24 + 2 * b] = bfr(mean); st[24 + 2 * b + 1] = frcp(bfr(fsqrt(bfr(bfr(v * (1.f / DM)) + 1e-5f)))); }
          *reinterpret_cast<u32x4*>(xraw + b * 320 + lane * 4) = xv[2 * i];
          if (lane < 16) *reinterpret_cast<u32x4*>(xraw + b * 320 + 256 + lane * 4) = xv[2 * i + 1];
        }
        if (t == 0) { cnt[4] = next_tag(ep); cnt[15] = keep; }
        lds_arrive(C_P1, lane);
      }
      lds_wait<1>(C_P1, 2);
      STAMPT(20, 0);
      tg = __builtin_amdgcn_readfirstlane(cnt[4]);
      if (CT0 && u < 8 && t == 192) { const u32 t0 = cnt[15]; st_gran(rs, G_T + u * 16, t0, t0, tg); }
      if (wave < 3) {
        const int b = LAY == 2 ? t & 3 : (t >> 2) & 3, j = LAY == 2 ? t >> 2 : (t >> 4) * 4 + (t & 3);
        u32 offs[8];
        u32x4 gm[8];
#pragma unroll
        for (int i = 0; i < 8; ++i) offs[i] = t < 160 ? G_MOD + GIDX(1024, i & 1, b, ((i >> 1) & 1) * 160 + (i >> 2) * 40 + ksp * 80 + j) : OOB;
        const float mean = st[24 + 2 * b], sd = st[24 + 2 * b + 1];
        float nv[8];
#pragma unroll
        for (int e = 0; e < 2; ++e) {
          const u32x2 xw = *reinterpret_cast<const u32x2*>(xraw + b * 320 + ksp * 160 + e * 80 + 2 * j);
#pragma unroll
          for (int k = 0; k < 4; ++k) {
            const float xf = (k & 1) ? hi16(xw[k >> 1]) : lo16(xw[k >> 1]);
            nv[4 * e + k] = bfr(bfr(xf - mean) * sd);
            asm volatile("" : "+v"(nv[4 * e + k]));
          }
        }
        {
          const u32 tt = __builtin_amdgcn_readfirstlane(cnt[15]) + GT;
          SPINLOOP while ((int)((u32)__builtin_amdgcn_s_memrealtime() - tt) < 0) __builtin_amdgcn_s_sleep(1);
        }
        STAMPT(14, 0);
        gather22<8, 4>(rs, offs, gm, tg, GOPOS == 2 ? C_GO : 0);
        STAMPT(8, 0);
#pragma unroll
        for (int e = 0; e < 2; ++e) {
          const f32x4 sa = un_g22(gm[4 * e]) + un_g22(gm[4 * e + 1]);
          const f32x4 ca = un_g22(gm[4 * e + 2]) + un_g22(gm[4 * e + 3]);
          const u32 s01 = cvt2(sa[0], sa[1]), s23 = cvt2(sa[2], sa[3]), c01 = cvt2(ca[0], ca[1]), c23 = cvt2(ca[2], ca[3]);
          const u32 d01 = cvt2(1.f + lo16(c01), 1.f + hi16(c01)), d23 = cvt2(1.f + lo16(c23), 1.f + hi16(c23));
          const u32 p01 = cvt2(nv[4 * e] * lo16(d01), nv[4 * e + 1] * hi16(d01)), p23 = cvt2(nv[4 * e + 2] * lo16(d23), nv[4 * e + 3] * hi16(d23));
          const float hv[4] = {lo16(p01) + lo16(s01), hi16(p01) + hi16(s01), lo16(p23) + lo16(s23), hi16(p23) + hi16(s23)};
          if (t < 160) *reinterpret_cast<u32x2*>(lds + L_XA + b * XH + (e * 40 + j) * 8) = u32x2{cvt2(hv[0], hv[1]), cvt2(hv[2], hv[3])};
        }
      }
    }
    VM0();
    STAMPT(5, 128);
    lds_arrive(C_P2, lane);
    lds_wait<1>(C_P2, 4);
    STAMPT(7, 0);
    tg = __builtin_amdgcn_readfirstlane(cnt[4]);
    {
      f32x4 acc = {0.f, 0.f, 0.f, 0.f}, acc2 = acc;
#pragma unroll
      for (int s = 0; s < 10; s += 2) {
        acc = MFMA(wfrag<512>(L_W + (tr128 ^ (wave * 32)) + s * 4096), *reinterpret_cast<const u32x4*>(lds + L_XA + (c & 3) * XH + s * 64 + r * 16), acc);
        acc2 = MFMA(wfrag<512>(L_W + (tr128 ^ (wave * 32)) + s * 4096 + 4096), *reinterpret_cast<const u32x4*>(lds + L_XA + (c & 3) * XH + s * 64 + 64 + r * 16), acc2);
      }
      if (c < 4 && u < 240 + 16 - 16 * half) st_g22(rs, G_Q + half * (G_MOD - G_Q) + GIDX(1024, ksp, c, cbp * 16 + wave * 4 + r), acc + acc2, tg);
    }
    if (CT0 && half && u < 8 && t == 192) { const u32 t0 = cnt[15]; st_gran(rs, G_T + u * 16, t0, t0, tg); }
    STAMPT(9, 0);
  } else {
    __builtin_amdgcn_s_setprio(2);
    {
      const u32 co = min((u32)(u >> 3) * 1024u + (u32)((t >> 5) & 7) * 128u + 768u, (u32)CLEN);
      u64 pc;
      asm volatile("s_getpc_b64 %0\n\tglobal_load_dword v128, %1, %0" : "=&s"(pc) : "v"(co) : "memory", "v128");
    }
    if (t < 256 + 32) cnt[lane] = 0;
    const int rot = __builtin_amdgcn_readfirstlane((u >> 3) + ((t >> 6) & 3) * PHW);
    const u32 ko = (u32)(bh * SK + (SK - WIN)) * 256u + (u32)(half * 8 + wave) * (NL * 1024u) + lane * 16;
    STAMPT(30, 448);
#define LK4(a, b, c, d, o) "global_load_dwordx4 " a ", %" #o ", %[k] nt\n\tglobal_load_dwordx4 " b ", %" #o ", %[k] offset:1024 nt\n\t" \
                           "global_load_dwordx4 " c ", %" #o ", %[k] offset:2048 nt\n\tglobal_load_dwordx4 " d ", %" #o ", %[k] offset:3072 nt\n\t"
    asm volatile("s_waitcnt lgkmcnt(0)\n\ts_barrier\n\t"
                 LK4("v[128:131]", "v[132:135]", "v[136:139]", "v[140:143]", 0) LK4("v[144:147]", "v[148:151]", "v[152:155]", "v[156:159]", 1)
                 LK4("v[160:163]", "v[164:167]", "v[168:171]", "v[172:175]", 2)
                 :: "v"(ko + (rot & 7) * 4096), "v"(ko + ((rot + 1) & 7) * 4096),
                   "v"(ko + ((rot + 2) & 7) * 4096),
                   [k] "s"(kcq) : "memory", "v128", "v129", "v130", "v131", "v132", "v133", "v134", "v135", "v136", "v137", "v138", "v139", "v140", "v141", "v142", "v143", "v144", "v145", "v146", "v147", "v148", "v149", "v150", "v151", "v152", "v153", "v154", "v155", "v156", "v157", "v158", "v159", "v160", "v161", "v162", "v163", "v164", "v165", "v166", "v167", "v168", "v169", "v170", "v171", "v172", "v173", "v174", "v175", "v176", "v177", "v178", "v179", "v180", "v181", "v182", "v183", "v184", "v185", "v186", "v187", "v188", "v189", "v190", "v191");
#undef LK4
    STAMPT(31, 448);
  }
  u32 kvoff = (u32)(bh * SK + (SK - WIN)) * 256u + (u32)(half * 8 + wave) * (NL * 1024u) + lane * 16, ph[8];
  const int rot2 = __builtin_amdgcn_readfirstlane((u >> 3) + ((t >> 6) & 3) * PHW);
#pragma unroll
  for (int i = 0; i < 8; ++i) ph[i] = ((i + rot2) & 7) * 4096;
  asm volatile("" : "+v"(kvoff));
  asm volatile("s_waitcnt lgkmcnt(0)" : "+s"(wsq), "+s"(ph[0]), "+s"(ph[1]), "+s"(ph[2]), "+s"(ph[3]), "+s"(ph[4]), "+s"(ph[5]), "+s"(ph[6]), "+s"(ph[7])
               : "s"(rw), "s"(rx), "s"(kcq));
  const __amdgpu_buffer_rsrc_t rs = __builtin_amdgcn_make_buffer_rsrc(wsq, 0, WS_END, 0x00020000);
  u32 rw2;
#define BLKA(ia_) (((c & 3) * 32 + (((ia_) & 3) + (((ia_) >> 2) & 1) * 16 + (wave & 3) * 4)) * 2 + ((c >> 2) & 1))
#define RD_ROLE() { u32 v_; asm volatile("ds_read_b32 %0, %1\n\ts_waitcnt lgkmcnt(0)" : "=v"(v_) : "v"((u32)C_RL) : "memory"); rw2 = __builtin_amdgcn_readfirstlane(v_); }
#define WAIT_ROLE() SPINLOOP for (unsigned spins = 0; spins < (1u << 22); ++spins) { RD_ROLE() if ((int)rw2 < 0) break; __builtin_amdgcn_s_sleep(2); }
#define HEADA(hh) ((ksA & 3) + (ksA >> 2) * 16 + (hh) * 4)
  u32x4 gmf[5], ml;
  u32 tkf, a_pub;
  f32x4 psc, pw4[4];
  u32x2 xw;
  {
    u32x4 kv[NL], kh[16];
    const __amdgpu_buffer_rsrc_t rk = __builtin_amdgcn_make_buffer_rsrc((void*)a.kc, 0, 1u << 28, 0x00020000);
    const __amdgpu_buffer_rsrc_t rv = __builtin_amdgcn_make_buffer_rsrc((void*)a.vc, 0, 1u << 28, 0x00020000);
#define LDKV(rsrc, off, i) __builtin_amdgcn_raw_buffer_load_b128(rsrc, (off) + ((i) & 3) * 1024, ph[(i) >> 2], 2)
#define LDS_INC(addr) do { asm volatile("" ::: "memory"); if (lane == 0) asm volatile("ds_add_u32 %0, %1" :: "v"(addr), "v"(1u) : "memory"); } while (0)
#define TR_STEP(x, HALF, CTRL, BIT) _Pragma("unroll") for (int hh = 0; hh < (HALF); ++hh) { \
    const float lo_ = x[hh], hi_ = x[hh + (HALF)]; const float t1_ = lo_ + dpp<CTRL>(lo_), t2_ = hi_ + dpp<CTRL>(hi_); \
    x[hh] = (c & (BIT)) ? t2_ : t1_; }
#define SCORE16(x, src, o) float x[16]; \
    _Pragma("unroll") for (int i = 0; i < 16; ++i) { float d = 0.f; _Pragma("unroll") for (int k = 0; k < 4; ++k) d = dot2(src[(o) + i][k], qpk[k], d); x[i] = d; } \
    TR_STEP(x, 8, XOR8, 8) TR_STEP(x, 4, XOR7, 4) TR_STEP(x, 2, XOR2, 2)
#define SCORE16P(x, src, o, pa_, pb_) float x[16]; \
    _Pragma("unroll") for (int i = 0; i < 16; ++i) { float d = 0.f; _Pragma("unroll") for (int k = 0; k < 4; ++k) d = dot2(src[(o) + i][k], qpk[k], d); x[i] = d; } \
    TR_STEP(x, 8, XOR8, 8) PADX(pa_, pb_, x[0]); TR_STEP(x, 4, XOR7, 4) TR_STEP(x, 2, XOR2, 2)
#define SCALED(v) bfr(bfr(v) * 0.08838834764831845f)
#define PV2(i) _Pragma("unroll") for (int k = 0; k < 4; ++k) { \
      acc[2 * k] = dot2(__builtin_amdgcn_perm(kv[(i) + 1][k], kv[i][k], 0x05040100u), pw[(i) / 2], acc[2 * k]); \
      acc[2 * k + 1] = dot2(__builtin_amdgcn_perm(kv[(i) + 1][k], kv[i][k], 0x07060302u), pw[(i) / 2], acc[2 * k + 1]); }
#define GET_Q() lds_wait<2>(C_Q, 1); u32x4 qpk; \
    asm volatile("ds_read_b128 %0, %2\n\tds_read_b32 %1, %3\n\ts_waitcnt lgkmcnt(0)" : "=&v"(qpk), "=&v"(tg) : "v"((u32)(L_QS + 16 * c)), "v"((u32)(L_CNT + 16)) : "memory"); \
    tg = __builtin_amdgcn_readfirstlane(tg);
#define GET_P() _Pragma("unroll") for (int i4 = 0; i4 < 4; ++i4) { \
      const u32x4 pv = *reinterpret_cast<const u32x4*>(pT + (wave * 4 + r) * 16 + i4 * 4); \
      _Pragma("unroll") for (int j = 0; j < 4; ++j) pw[i4 * 4 + j] = pv[j]; }
    auto gather_q = [&]() __attribute__((always_inline)) {
        u32 offs[1] = {G_Q + GIDX(1024, lane >> 5, b_att, h_att * 32 + (lane & 31))};
        u32x4 gq[1];
        {
          gq[0] = ld_gran(rs, offs[0]);
          const u32x4 gt = ld_gran(rs, lane < 8 ? G_T + lane * 16 : OOB);
          LDS_INC(C_KI);
          {
            const u32 own = cnt[15];
            const bool ok = lane < 8 && gt[1] == tg;
            float d = ok ? __builtin_amdgcn_fmed3f((float)(int)(gt[0] - own + TOFF), -400.f, 400.f) : -500.f;
            d = fmaxf(d, dpp<XOR1>(d)); d = fmaxf(d, dpp<XOR2>(d)); d = fmaxf(d, dpp<XOR7>(d));
            if (lane == 0 && d > -450.f) cnt[15] = own + (u32)(int)d;
          }
          const bool hit = (((gq[0][1] ^ (tg << 12)) | (gq[0][3] ^ (tg << 12))) < 4096u);
          if (!__all(hit)) gather22<1, 4>(rs, offs, gq, tg);
        }
        STAMPW(10);
        f32x4 q4 = un_g22(gq[0]);
#pragma unroll
        for (int k = 0; k < 4; ++k) q4[k] = swap32_add(q4[k], q4[k]);
        if (lane < 32) *reinterpret_cast<u32x2*>(qs + 2 * lane) = u32x2{cvt2(q4[0], q4[1]), cvt2(q4[2], q4[3])};
        lds_arrive(C_Q, lane);
    };
    const int qw = half ? QODD : QEVEN;
    const u32 tau = fav ? TAU_L : TAU_E;
#define PACE(n) if ((n) > PB) { const u32 tt_ = t0 + (((n) - PB) * tau >> 4); \
      SPINLOOP while ((int)((u32)__builtin_amdgcn_s_memrealtime() - tt_) < 0) __builtin_amdgcn_s_sleep(1); asm volatile("" ::: "memory"); }
#define PACE2(n) { u32 v_, d_; \
      asm volatile("1:\n\tds_read_b32 %0, %2\n\ts_memrealtime vcc\n\ts_waitcnt lgkmcnt(0)\n\tv_readfirstlane_b32 %1, %0\n\ts_add_u32 %1, %1, vcc_lo\n\t" \
                   "s_sub_u32 %1, %1, %3\n\ts_cmp_ge_i32 %1, %4\n\ts_cbranch_scc1 2f\n\ts_sleep 1\n\ts_branch 1b\n2:" \
                   : "=&v"(v_), "=&s"(d_) : "v"(a_dl), "s"(t0), "i"(((n) * TAU_E >> 4) - POFF) : "memory", "scc", "vcc"); }
    float acc[8];
#pragma unroll
    for (int k = 0; k < 8; ++k) acc[k] = 0.f;
    u32 pw[16];
    if (__builtin_expect(wave >= 4, 1)) {
      asm volatile("" : "={v[128:131]}"(kv[0]), "={v[132:135]}"(kv[1]), "={v[136:139]}"(kv[2]), "={v[140:143]}"(kv[3]),
                        "={v[144:147]}"(kv[4]), "={v[148:151]}"(kv[5]), "={v[152:155]}"(kv[6]), "={v[156:159]}"(kv[7])
                        , "={v[160:163]}"(kv[8]), "={v[164:167]}"(kv[9]), "={v[168:171]}"(kv[10]), "={v[172:175]}"(kv[11])
                   );
      u32 t0 = 0;
      const u32 tk0 = KP ? (u32)__builtin_amdgcn_s_memrealtime() : 0u, ktau = half ? KTO : KTE;
      STAMPT(24, 448);
      const u32 kvoff2 = kvoff ^ 131072u;
#pragma unroll
      for (int i = NK0; i < 48; ++i) {
        if (i == NPRE) { STAMPT(12, 448); STAMPT(13, 256); }
        if (i == NPRE && half == 0) lds_wait<2>(C_GO, GOPOS ? 3 : 4);
        if (KP && i >= KP && (i & 3) == 0) { const u32 tt_ = tk0 + (i * ktau >> 4);
          SPINLOOP while ((int)((u32)__builtin_amdgcn_s_memrealtime() - tt_) < 0) __builtin_amdgcn_s_sleep(1); asm volatile("" ::: "memory"); }
        if (i < NL) kv[i < NL ? i : 0] = LDKV(rk, kvoff, i & 31); else kh[i < NL ? 0 : i - NL] = LDKV(rk, kvoff2, i & 31);
      }
      if (!QPRE || wave != qw) LDS_INC(C_KI);
#pragma unroll
      for (int i = 0; i < NK0; i += 4) asm volatile("s_waitcnt vmcnt(%4)" : "+v"(kv[i]), "+v"(kv[i + 1]), "+v"(kv[i + 2]), "+v"(kv[i + 3]) : "i"(48 - NK0) : "memory");
      STAMPT(25, 448);
      if ((QODD >= 4 || QEVEN >= 4) && wave == qw) {
        __builtin_amdgcn_s_setprio(3);
        lds_wait<2>(C_P1, 2 + half);
        tg = __builtin_amdgcn_readfirstlane(cnt[4]);
        gather_q();
        __builtin_amdgcn_s_setprio(2);
      }
      GET_Q()
      SCORE16(xa, kv, 0)
      asm volatile("" : "+v"(xa[0]), "+v"(xa[1]), "+v"(kvoff) :: "memory");
      PAD(e1, l2);
#pragma unroll
      for (int i = 0; i < P_NVA; ++i) kv[i] = LDKV(rv, kvoff, i);
      asm volatile("" ::: "memory");
      SCORE16(xb, kv, 16)
      xa[2] = xb[0]; xa[3] = xb[1];
      TR_STEP(xa, 2, XOR1, 1)
      float s0 = SCALED(xa[0]), s1 = SCALED(xa[1]);
      asm volatile("" : "+v"(s0), "+v"(s1), "+v"(kvoff) :: "memory");
#pragma unroll
      for (int i = P_NVA; i < NVE; ++i) kv[i] = LDKV(rv, kvoff, i);
      asm volatile("" ::: "memory");
      SCORE16P(xh, kh, 0, e2, l3)
      const float s2 = SCALED(xh[0] + dpp<XOR1>(xh[0])), s3 = SCALED(xh[1] + dpp<XOR1>(xh[1]));
      const float m = wave_max(fmaxf(fmaxf(s0, s1), fmaxf(s2, s3)));
      const float p0 = __expf(s0 - m), p1 = __expf(s1 - m), p2 = __expf(s2 - m), p3 = __expf(s3 - m);
      const float l = wave_sum(fmaf(p2 + p3, 0.5f, p0 + p1));
      if (lane == 0) { st[wave] = m; st[8 + wave] = l; }
      pT[(wave * 4 + r) * 16 + (c & 1) * 8 + (c >> 1)] = cvt2(p0, p1);
      pT[((wave - 4) * 4 + r) * 16 + (c >> 1)] = cvt2(p2, p3);
      lds_arrive(C_PP + 4 * (wave - 4), lane);
      __builtin_amdgcn_s_setprio(0);
      STAMPT(27, 448); WSTAMP(1);
      if (CT0) t0 = __builtin_amdgcn_readfirstlane(cnt[15]);
      GET_P()
      u32 a_dl = C_DL;
      asm volatile("" : "+v"(a_dl));
#pragma unroll
      for (int i = NVE; i < NL; i += 2) {
        if (i == E3POS) PAD(e3, l4);
        if (i == E4POS) PAD(e4, t1);
        u32 om0;
        if (i == MLP) { RD_ROLE() om0 = (rw2 & 0x80000003u) == 0x80000000u && lane < 8 ? G_ML + BLKA(rw2 >> 8) * 64 : OOB; asm volatile("" : "+v"(om0)); }
        PACE2(96 + i)
        if (i == MLP) ml = ld_gran(rs, om0);
        kv[i] = LDKV(rv, kvoff, i);
        kv[i + 1] = LDKV(rv, kvoff, i + 1);
        asm volatile("" ::: "memory");
        if (i == NL - 2) { LDS_INC(C_ISS); STAMPT(26, 448); }
        if (i >= PVLAG) {
          PV2(i >= PVLAG ? i - PVLAG : 0)
#pragma unroll
          for (int k = 0; k < 8; ++k) asm volatile("" : "+v"(acc[k]));
        }
      }
    } else {
      const int ex = (g >= 120 && g < 126) ? EC : 0;
      u32 kva = (!half && ex > 2) ? OOB : kvoff, kvb = (!half && ex > 0) ? OOB : kvoff;
      const u32 kxa = (half && ex > 2) ? kvoff - 8 * NL * 1024u : OOB, kxb = (half && ex > 0) ? kvoff - 8 * NL * 1024u : OOB;
      u32x4 kx[4];
      const u32 t0 = __builtin_amdgcn_readfirstlane(cnt[15]);
#pragma unroll
      for (int i = 0; i < 16; ++i) {
        if (PK && (i & 3) == 0) PACE(48 + i)
        kh[i] = LDKV(rk, i < 12 ? kvoff : i < 14 ? kva : kvb, 16 + i);
      }
#pragma unroll
      for (int i = 0; i < 4; ++i) kx[i] = LDKV(rk, i < 2 ? kxa : kxb, 28 + i);
      LDS_INC(C_KI);
      if (wave == 0) {
        if (half == 0 && g >= 80 && g < 120) {
          const int fb = g - 80;
          u32 of2[2];
          u32x4 gm[2];
#pragma unroll
          for (int ks = 0; ks < 2; ++ks) of2[ks] = G_MOD + GIDX(1024, ks, r, 320 + fb * 16 + c);
          gather22<2, 4>(rs, of2, gm, tg);
          const f32x4 mv = un_g22(gm[0]) + un_g22(gm[1]);
          st_gran(rs, G_MF + (((fb / 10) * 4 + r) * 160 + (fb % 10) * 16 + c) * 16, cvt2(mv[0], mv[1]), cvt2(mv[2], mv[3]), tg);
        }
      }
      __builtin_amdgcn_s_setprio(0);
      if (t == 0 && fav) cnt[19] = DL0;
      STAMPT(2, 0);
#define GO(r_, n, name) "s_cmp_eq_u32 %" #r_ ", " #n "\n\ts_cbranch_scc1 .Lpad_" #name "\n\t"
      asm volatile("s_mov_b64 exec, 0\n\t" GO(0, 0, e1) GO(0, 1, e2) GO(0, 2, e3) GO(0, 3, l1)
                   ".Lpad_role:\n\t" GO(0, 0, c1) GO(0, 1, c0) GO(0, 2, b3) GO(0, 3, b0)
                   ".Lpad_back:\n\ts_mov_b64 exec, -1" :: "s"(wave) : "scc");
#undef GO
      STAMPT(7, 0);
      lds_wait<2>(C_KI, P_GATE);
      if (QHOLD && wave == qw - 4) lds_wait<2>(C_Q, 1);
      asm volatile("" : "+v"(kvoff), "+v"(kva), "+v"(kvb));
#pragma unroll
      for (int i = 0; i < NL; ++i) {
        if (i == L1POS) PAD(l1, e4);
        if (PL && (i & 3) == 0) PACE(64 + PLO + i * PLS / 16)
        kv[i] = LDKV(rv, i < 28 ? kvoff : i < 30 ? kva : kvb, i);
      }
      STAMPT(1, 0);
      GET_Q()
      const bool fixc = half == 0 && (g < 40 || (FXA && fav));
      const u32 tka = t == 0 ? G_TK + CLS * 128 : OOB;
      u32 tkv = __builtin_amdgcn_raw_ptr_buffer_atomic_add_i32(1, rs, half ? tka : OOB, 0, 16);
      asm volatile("" ::: "memory");
      float xx[4];
#pragma unroll
      for (int i = 0; i < 4; ++i) { float d = 0.f;
#pragma unroll
        for (int k = 0; k < 4; ++k) d = dot2(kx[i][k], qpk[k], d);
        xx[i] = d; }
      asm volatile("" : "+v"(xx[0]), "+v"(xx[1]), "+v"(xx[2]), "+v"(xx[3]) :: "memory");
#pragma unroll
      for (int i = 0; i < 4; ++i) kx[i] = LDKV(rv, i < 2 ? kxa : kxb, 28 + i);
      LDS_INC(C_ISS);
      TR_STEP(xx, 2, XOR8, 8)
#pragma unroll
      for (int i = 0; i < 2; ++i) { xx[i] += dpp<XOR7>(xx[i]); xx[i] += dpp<XOR2>(xx[i]); xx[i] += dpp<XOR1>(xx[i]); }
      SCORE16P(xh, kh, 0, l2, t2)
      const float ninf = -__builtin_inff();
      const bool mine = half || c < 16 - ex, extra = half && c >= 16 - 4 * ex;
      xh[0] += dpp<XOR1>(xh[0]); xh[1] += dpp<XOR1>(xh[1]);
      const float s2 = mine ? SCALED(xh[0]) : ninf, s3 = mine ? SCALED(xh[1]) : ninf;
      const float s4 = extra ? SCALED(xx[0]) : ninf, s5 = extra ? SCALED(xx[1]) : ninf;
      float m = wave_max(fmaxf(fmaxf(s2, s3), fmaxf(s4, s5)));
      lds_wait<2>(C_PP + 4 * wave, 1);
      const float me = st[wave + 4];
      m = fmaxf(m, me);
      const float p2 = __expf(s2 - m), p3 = __expf(s3 - m), p4 = __expf(s4 - m), p5 = __expf(s5 - m);
      const float l = wave_sum(fmaf(p4 + p5, 0.25f, p2 + p3)) * 0.5f;
      const u32 pxo = cvt2(p4, p5), pxt = dppu<XOR8>(pxo);
      const u32 px[2] = {c < 8 ? pxo : pxt, c < 8 ? pxt : pxo};
      if (lane == 0) { st[wave] = m; st[8 + wave] = l; }
      pT[(wave * 4 + r) * 16 + 8 + (c >> 1)] = cvt2(p2, p3);
      STAMPT(4, 0); WSTAMP(1);
      PAD(l3, t3);
      if (lds_ticket(C_SC, lane) == 3) {
        const float mw = st[lane & 7], lw = st[8 + (lane & 7)];
        float M = fmaxf(mw, dpp<XOR1>(mw)); M = fmaxf(M, dpp<XOR2>(M)); M = fmaxf(M, dpp<XOR7>(M));
        const float e = __expf(mw - M);
        float L = lw * e;
        L += dpp<XOR1>(L); L += dpp<XOR2>(L); L += dpp<XOR7>(L);
        if (lane == 0) st_gran(rs, G_ML + u * 64, f2u(M), f2u(L), tg);
        if (lane < 8) st[16 + lane] = e * frcp(L);
      }
      bool sub = false;
      if (wave == 0 && !fav) {
        const u32 tt = __builtin_amdgcn_readfirstlane(cnt[15]) + (126 * TAU_E >> 4) - EVT;
        sub = (int)((u32)__builtin_amdgcn_s_memrealtime() - tt) < EVW;
        SPINLOOP while ((int)((u32)__builtin_amdgcn_s_memrealtime() - tt) < 0) __builtin_amdgcn_s_sleep(1);
      }
      asm volatile("" ::: "memory");
      const u32 tkw = __builtin_amdgcn_raw_ptr_buffer_atomic_add_i32(fixc ? 0 : 0x10001, rs, sub && (SLS || !fixc) ? tka : OOB, 0, 16);
      asm volatile("" ::: "memory");
      asm("" : "=v"(ml));
      GET_P()
#pragma unroll
      for (int i = 0; i < 16; i += 2) { if (i == L4POS) PADA(l4, a0); PV2(i) }
      const float f = __expf(me - m);
#pragma unroll
      for (int k = 0; k < 8; ++k) acc[k] *= f;
#pragma unroll
      for (int i = 16; i < NL - PVLAG; i += 2) { PV2(i) }
#pragma unroll
      for (int i = 0; i < 4; i += 2)
#pragma unroll
        for (int k = 0; k < 4; ++k) {
          acc[2 * k] = dot2(__builtin_amdgcn_perm(kx[i + 1][k], kx[i][k], 0x05040100u), px[i / 2], acc[2 * k]);
          acc[2 * k + 1] = dot2(__builtin_amdgcn_perm(kx[i + 1][k], kx[i][k], 0x07060302u), px[i / 2], acc[2 * k + 1]);
        }
      asm volatile("" : "+v"(acc[0]), "+v"(acc[1]), "+v"(acc[2]), "+v"(acc[3]), "+v"(acc[4]), "+v"(acc[5]), "+v"(acc[6]), "+v"(acc[7]) :: "memory");
      rw2 = 0;
      if (wave == 0) {
        constexpr int NT2 = FXA ? 128 : 134;
        const u32 tks = __builtin_amdgcn_readfirstlane(tkv + tkw + 1u);
        const int n0 = (tks & 0xffff) * NCL + CLS;
        const int slow = sub ? -min(max(SLF - (int)((tks & 0xffff) - (tks >> 16)), 0) * SLS, SLM) : 0;
        const int n = fixc ? (g < 40 ? 134 + g : g + 8) : (fav || sub) ? sel_lt(n0, NT2, n0, 174) : 174;
        constexpr int N0 = 54;
        const int rl = sel_lt(n, N0, 1, sel_lt(n, 134, 0, sel_lt(n, 174, 2, 3)));
        int un = n - sel_lt(n, N0, 0, sel_lt(n, 134, N0, 134));
        if (NCL == 8 && rl == 1 && un < 48) un = (un & 48) | (un & 7) << 1 | (un >> 3 & 1);
        if (NCL == 4 && rl == 1 && un < 48) un = (un & 56) | (un & 3) << 1 | (un >> 2 & 1);
        if (lane == 0) *reinterpret_cast<u32x2*>(lds + C_RL) = u32x2{0x80000000u | (u32)(un & 255) << 8 | (u32)rl, (u32)sel_lt(n, N0, DLB, sel_lt(n, 134, fav ? DLA : DLS, slow))};
        STAMPT(28, 0);
      }
    }
#pragma unroll
    for (int i = NL - PVLAG; i <= NL - HX; i += 2) { if (i == T1POS) PADA(t1, role); PV2(i) }
    u32 a_aw = L_ACC + (wave * 128 + c * 8 + 2 * r) * 4, a_as = L_ST + 64 + (lane >> 5) * 16, a_ar = L_ACC + (lane >> 5) * 2048 + (lane & 31) * 8, one = 1;
    tkf = C_FIN;
    a_pub = G_ATT + u * 256 + lane * 4;
    asm volatile("" : "+v"(acc[0]), "+v"(acc[1]), "+v"(acc[2]), "+v"(acc[3]), "+v"(acc[4]), "+v"(acc[5]), "+v"(acc[6]), "+v"(acc[7]),
                      "+v"(a_aw), "+v"(a_as), "+v"(a_ar), "+v"(one), "+v"(tkf), "+v"(a_pub));
    if (!HT) { PV2(NL - HX + 2) } else {
      u32 tk = 0;
      u64 tn;
      asm volatile("s_mov_b64 exec, 1\n\tds_add_rtn_u32 %0, %10, %11\n\ts_mov_b64 exec, -1\n\ts_memrealtime %1"
                   : "+v"(tk), "=s"(tn), "+v"(acc[0]), "+v"(acc[1]), "+v"(acc[2]), "+v"(acc[3]), "+v"(acc[4]), "+v"(acc[5]), "+v"(acc[6]), "+v"(acc[7])
                   : "v"((u32)C_HT), "v"(1u) : "memory");
      PV2(NL - HX + 2)
      asm volatile("s_waitcnt lgkmcnt(0)" : "+v"(tk), "+s"(tn), "+v"(acc[0]), "+v"(acc[1]), "+v"(acc[2]), "+v"(acc[3]), "+v"(acc[4]), "+v"(acc[5]), "+v"(acc[6]), "+v"(acc[7]));
      __builtin_amdgcn_raw_buffer_store_b64(u32x2{(u32)tn + HTD, tg}, rs, tk == 7 ? G_HT + u * 128 : OOB, 0, 16);
      asm volatile("" ::: "memory");
    }
#pragma unroll
    for (int i = NL - HX + 4; i < NL - 2; i += 2) { PV2(i) }
    PV2(NL - 2)
#undef PV2
#undef TR_STEP
#undef SCORE16
#undef SCORE16P
#undef SCALED
#undef GET_Q
#undef GET_P
#undef LDKV
#pragma unroll
    for (int i = 0; i < 4; ++i) acc[i] = swap32_add(acc[i], acc[i + 4]);
#pragma unroll
    for (int i = 0; i < 2; ++i) acc[i] = swap16_add(acc[i], acc[i + 2]);
    asm volatile("ds_write_b64 %6, %7\n\ts_mov_b64 exec, 1\n\tds_add_rtn_u32 %0, %0, %8\n\ts_mov_b64 exec, -1\n\tds_read_b128 %1, %9\n\t"
                 "ds_read2_b64 %2, %10 offset1:32\n\tds_read2_b64 %3, %10 offset0:64 offset1:96\n\t"
                 "ds_read2_b64 %4, %10 offset0:128 offset1:160\n\tds_read2_b64 %5, %10 offset0:192 offset1:224\n\ts_waitcnt lgkmcnt(0)"
                 : "+v"(tkf), "=&v"(psc), "=&v"(pw4[0]), "=&v"(pw4[1]), "=&v"(pw4[2]), "=&v"(pw4[3])
                 : "v"(a_aw), "v"(f32x2{acc[0], acc[1]}), "v"(one), "v"(a_as), "v"(a_ar) : "memory");
    STAMPT(6, 0); WSTAMP(2);
  }
  const u32x4 rsv = {(u32)(uintptr_t)wsq, (u32)((uintptr_t)wsq >> 32) & 0xffffu, WS_END, 0x00020000u};
#define MLOOK(W_) asm volatile(LK(0, 1, "0", 0, 2) LKEND : "+v"(ml) : "v"(om), "s"(m2), [rs] "s"(rsv), [w] "i"(W_) : "memory");
  if (__builtin_amdgcn_readfirstlane(tkf) == 7) {
    PAD(t2, role);
    const f32x4 o = (psc[0] * pw4[0] + psc[1] * pw4[1]) + (psc[2] * pw4[2] + psc[3] * pw4[3]);
    __builtin_amdgcn_raw_buffer_store_b32(min(cvt2(swap32_add(o[0], o[2]), swap32_add(o[1], o[3])), EMPTY - 1), rs, a_pub, 0, 16);
    LDS_INC(C_PUB);
    STAMPW(11);
  }
  if ((int)rw2 >= 0) WAIT_ROLE()
  const int role = rw2 & 3, ia = (rw2 >> 8) & 255, jB = ia;
  const int ksA = ia & 7, cbA = ia >> 3;
  const u32 blkA = BLKA(ia), om = G_ML + blkA * 64;
  float b1v;
  u32x4 gaq;
  if (role == 0 && wave < 4) {
    if (RPRI) __builtin_amdgcn_s_setprio(RPRI);
    {
      const u32x4 rxv = {(u32)(uintptr_t)a.x.p, (u32)((uintptr_t)a.x.p >> 32) & 0xffffu, NB * DM * 2, 0x00020000u};
      asm volatile("buffer_load_dwordx4 %0, %2, %4, 0 offen sc1\n\tbuffer_load_dwordx2 %1, %3, %5, 0 offen" : "=&v"(gaq), "=&v"(xw)
                   : "v"(G_MF + (r * 160 + cbA * 16 + c) * 16), "v"(ksA == 0 ? (r * DM + cbA * 64 + c * 4) * 2 : OOB), "s"(rsv), "s"(rxv) : "memory");
    }
    if (half) STAMPT(8, 0);
    lds_wait<2>(C_ISS, 8);
    {
      const bf16* wp = a.wo + (size_t)(HEADA(wave) * 128 + (lane >> 3)) * DM + cbA * 64;
#pragma unroll
      for (int i = 0; i < 16; ++i) {
        if (i == 1) PAD(a0, role);
        if (i == N1A) {
          lds_wait<2>(C_PUB, 1);
          asm volatile("" ::: "memory");
        }
        dma16(wp + (size_t)(8 * i) * DM + (s128e ^ ((i & 1) * 32)), L_W + wave * 16384 + i * 1024);
      }
      LDS_INC(C_ISS);
    }
    STAMPT(16, 0);
  }
#define SELECTOR(sel) u32x4 sel; \
    _Pragma("unroll") for (int d = 0; d < 4; ++d) sel[d] = ((c & 2) == 0 && d == (c >> 2)) ? ((c & 1) ? 0x3f800000u : 0x00003f80u) : 0u;
#define PAIRSUM(sm) (((c & 8) ? sm[1] : sm[0]) + dpp<XOR8>((c & 8) ? sm[0] : sm[1]))
  const u32 tb = __builtin_amdgcn_readfirstlane(cnt[15]);
  if (role == 0) {
    PAD(a1, back);
    if (wave < 4) {
      u32 a_sum = L_RED + lane * 16;
      u32 a_out = wave < NREP ? G_WO + wave * WO_REP + ((cbA * 4 + (c >> 2)) * 8 + ksA) * 128 + r * 32 + (c & 3) * 8 : OOB;
      asm volatile("s_waitcnt vmcnt(4)" : "+v"(a_sum), "+v"(a_out), "+v"(gaq), "+v"(xw) :: "memory");
      LDS_INC(C_PP + 4 * wave);
      if (!__all(gaq[1] == tg && gaq[3] == tg)) {
        u32 offs[1] = {G_MF + (r * 160 + cbA * 16 + c) * 16};
        u32x4 gm[1];
        gather<1>(rs, offs, gm, tg);
        gaq = gm[0];
      }
      float ga4[4] = {lo16(gaq[0]), hi16(gaq[0]), lo16(gaq[2]), hi16(gaq[2])}, x4[4] = {lo16(xw[0]), hi16(xw[0]), lo16(xw[1]), hi16(xw[1])};
      asm volatile("" : "+v"(ga4[0]), "+v"(ga4[1]), "+v"(ga4[2]), "+v"(ga4[3]), "+v"(x4[0]), "+v"(x4[1]), "+v"(x4[2]), "+v"(x4[3]));
      STAMPT(19, 0);
      u32 a_fl = lane < 16 ? G_HA + ia * 128 + lane * 8 : OOB;
      PAD(a2, back);
      asm volatile("s_waitcnt vmcnt(0)" : "+v"(a_fl) :: "memory");
      LDS_INC(C_PP + 4 * wave);
      STAMPT(23, 0);
      if (wave == 0) {
        lds_wait<0>(C_W, 4);
        STAMPT(12, 0);
        __builtin_amdgcn_raw_buffer_store_b64(u32x2{(u32)__builtin_amdgcn_s_memrealtime() + HAG, tg}, rs, a_fl, 0, 16);
      }
      BAR();
      STAMPT(21, 0);
      f32x4 pp[4];
#pragma unroll
      for (int w = 0; w < 4; ++w) pp[w] = *reinterpret_cast<const f32x4*>(lds + a_sum + w * 1024);
      asm volatile("" : "+v"(pp[0]), "+v"(pp[1]), "+v"(pp[2]), "+v"(pp[3]));
      const f32x4 s = (pp[0] + pp[1]) + (pp[2] + pp[3]);
      const u32x2 o = {min(cvt2(fmaf(s[0], ga4[0], x4[0]), fmaf(s[1], ga4[1], x4[1])), EMPTY - 1), min(cvt2(fmaf(s[2], ga4[2], x4[2]), fmaf(s[3], ga4[3], x4[3])), EMPTY - 1)};
      __builtin_amdgcn_raw_buffer_store_b64(o, rs, a_out, 0, 16);
    } else {
      u32 oa = G_ATT + blkA * 256 + (c >> 3) * 64 + r * 16;
      u32 a_red = L_RED + (((wave & 3) * 4 + c) * 64 + r * 4) * 4;
      u32 a_w[4];
#pragma unroll
      for (int nt = 0; nt < 4; ++nt) a_w[nt] = L_W + (wave & 3) * 16384 + (tr128 ^ (nt * 32));
      u32x4 av[2];
      u64 m0 = ~0ull, m1 = ~0ull, m2 = 0xffull;
      av[0] = av[1] = u32x4{0u, 0u, 0u, 0u};
      asm volatile("" : "+v"(oa), "+v"(ml), "+v"(av[0]), "+v"(av[1]), "+v"(a_red), "+v"(a_w[0]), "+v"(a_w[1]), "+v"(a_w[2]), "+v"(a_w[3]) :: "memory");
      SPINLOOP for (unsigned spins = 0; spins < SPIN_CAP; ++spins) {
        m2 &= __builtin_amdgcn_ballot_w64(ml[1] != tg) | __builtin_amdgcn_ballot_w64(ml[3] != tg);
        if (!m2) break;
        if (!spins) FAIL(4);
        __builtin_amdgcn_s_sleep(ASL);
        MLOOK(0)
      }
      float wsc;
      {
        const float M = u2f(ml[0]), L = u2f(ml[2]), Mo = dpp<XOR7>(dpp<0x1B>(M)), Lo = dpp<XOR7>(dpp<0x1B>(L));
        const float Mx = fmaxf(M, Mo), le = L * __expf(M - Mx);
        wsc = le * frcp(fmaf(Lo, __expf(Mo - Mx), le));
        wsc = u2f((u32)__builtin_amdgcn_ds_bpermute((c & 7) * 4, (int)f2u(wsc)));
        asm volatile("" : "+v"(wsc));
      }
#define ALOOK(W_) \
        asm volatile(LK(0, 2, "0", 0, 3) LK(1, 2, "0", 128, 4) LKEND \
                     : "+v"(av[0]), "+v"(av[1]) : "v"(oa), "s"(m0), "s"(m1), [rs] "s"(rsv), [w] "i"(W_) : "memory");
#define ACHK() \
        m0 = __builtin_amdgcn_ballot_w64(missing(av[0])); \
        m1 = __builtin_amdgcn_ballot_w64(missing(av[1]));
      lds_wait<1>(C_PP + 4 * (wave - 4), 2);
      if (AGT) SPINLOOP for (int k = 0; k < 64; ++k) if ((int)((u32)__builtin_amdgcn_s_memrealtime() - tb) >= (126 * TAU_E >> 4) + AGT) break;
      STAMPT(29, 448);
      ALOOK(63)
      s16x8 wf[4][4];
#pragma unroll
      for (int s = 0; s < 3; ++s)
#pragma unroll
        for (int nt = 0; nt < 4; ++nt) wf[s][nt] = wfrag<512>(a_w[nt] + s * 4096);
      lds_wait<1>(C_PP + 4 * (wave - 4), 3);
#pragma unroll
      for (int nt = 0; nt < 4; ++nt) wf[3][nt] = wfrag<512>(a_w[nt] + 3 * 4096);
      u32 a_cw = C_W, one = 1;
      u64 mw = 0x000f000f000f000full;
      asm volatile("" : "+v"(wf[3][0]), "+v"(wf[3][1]), "+v"(wf[3][2]), "+v"(wf[3][3]), "+v"(a_cw), "+v"(one), "+s"(mw));
      PAD(a3, b4);
      asm volatile("s_waitcnt vmcnt(0)" : "+v"(av[0]), "+v"(av[1]) :: "memory");
      ACHK()
      STAMPT(18, 448);
      if (m0 | m1) {
        FAIL(1);
        SPINLOOP for (unsigned spins = 0; spins < SPIN_CAP; ++spins) {
          ALOOK(2 * (PDA - 1))
          ACHK()
          if (!(m0 | m1)) break;
          __builtin_amdgcn_s_sleep(ASL);
        }
      }
#undef ALOOK
#undef ACHK
      asm volatile("" : "+v"(av[0]), "+v"(av[1]));
      f32x4 acc[4], acb[4];
      const f32x4 z = {0.f, 0.f, 0.f, 0.f};
#define SB() __builtin_amdgcn_sched_barrier(0)
#define PIN(x) asm volatile("" : "+v"(x)); SB();
#define M1(n) { acc[n] = MFMA(wf[0][n], av[0], z); PIN(acc[n]) acb[n] = MFMA(wf[1][n], av[0], z); PIN(acb[n]) }
#define M2A(n) { acb[n] = MFMA(wf[3][n], av[1], acb[n]); PIN(acb[n]) }
#define M2B(n) { acc[n] = MFMA(wf[2][n], av[1], acc[n]); PIN(acc[n]) }
#define D8(n, k) { float v = acc[n][k] + dpp<XOR8>(acb[n][k]); PIN(v) acc[n][k] = v; v = acc[n][k + 1] + dpp<XOR8>(acb[n][k + 1]); PIN(v) acc[n][k + 1] = v; }
#define MW(n) { acc[n] *= wsc; PIN(acc[n]) }
#define D4(n) { _Pragma("unroll") for (int k = 0; k < 4; ++k) { float v = acc[n][k] + dpp<SHL4>(acc[n][k]); PIN(v) acc[n][k] = v; } }
#define WR(n) asm volatile("s_mov_b64 exec, %3\n\tds_write_b128 %0, %1 offset:" STR(n * 64) "\n\tds_write_b128 %0, %2 offset:" STR(n * 64 + 64) "\n\ts_mov_b64 exec, -1" \
                           :: "v"(a_red), "v"(acc[n]), "v"(acc[n + 1]), "s"(mw) : "memory")
      SB();
      M1(0)
      asm volatile("s_mov_b64 exec, 1\n\tds_add_u32 %0, %1\n\ts_mov_b64 exec, -1" :: "v"(a_cw), "v"(one) : "memory");
      SB();
      M1(1) M2A(0) M2B(0) M1(2) M2A(1) M2B(1) M1(3)
      M2A(2) D8(0, 0) M2B(2) D8(0, 2) M2A(3) D8(1, 0) M2B(3) D8(1, 2)
      MW(0) MW(1) D4(0) D4(1) D8(2, 0) D8(2, 2)
      WR(0);
      SB();
      D8(3, 0) D8(3, 2) MW(2) MW(3) D4(2) D4(3)
      WR(2);
#undef M1
#undef M2A
#undef M2B
#undef D8
#undef MW
#undef D4
#undef WR
      STAMPT(22, 448);
      BAR();
      asm volatile("" :: "v"(av[0]), "v"(av[1]));
    }
    STAMPT(13, 0);

  } else if (role == 1) {
    PAD(b0, bd);
    u32x2 bw2;
    asm("" : "=v"(bw2));
    if (wave < 4) {
      if (RPRI) __builtin_amdgcn_s_setprio(RPRI);
#pragma unroll
      for (int i = 0; i < 5; ++i) gmf[i] = __builtin_amdgcn_raw_buffer_load_b128(rs, G_MF + (640 + t) * 16, i * 4096, 16);
      const __amdgpu_buffer_rsrc_t rb = __builtin_amdgcn_make_buffer_rsrc((void*)a.b1, 0, FF * 2, 0x00020000);
      bw2 = __builtin_amdgcn_raw_buffer_load_b64(rb, (jB * 32 + (t & 28)) * 2, 0, 0);
      asm volatile("" ::: "memory");
    }
    const int lb = (c >> 1) & 3, lc = (c & 1) * 8 + 2 * r;
    const int fc = lane & 31, hw = lane >> 5;
    SELECTOR(sel)
    constexpr int L_R2 = L_MOD, L_S2 = L_R2 + 4096;
    const u32 gw = G_WO + (jB % NREP) * WO_REP + wave * 5120;
    u32 go = gw + (c >> 3) * 1024 + r * 128 + (c & 7) * 16, go4 = gw + 4096 + lane * 16;
    u32 a_u = L_XA + wave * 640 + lb * 160 + ((c >> 3) * 16 + lc) * 2, a_u4 = L_XA + wave * 640 + lb * 160 + (64 + lc + (c >> 3)) * 2;
    u32 a_av = L_XA + wave * 640 + (c & 3) * 160 + r * 16, a_ah = a_av + 128 - r * 8;
    u32 a_red = L_R2 + ((wave * 4 + c) * 32 + r * 4) * 4;
    u32 a_st = L_S2 + ((lb * 2 + (c >> 3)) * 8 + wave) * 4;
    u32 a_fin = L_R2 + ((hw * 16 + (wave & 3)) * 32 + fc) * 4;
    u32 a_fst = L_S2 + (wave & 3) * 64;
    u32 a_out = c < 4 ? G_OUT + (wave * 5 * 54 + jB) * 128 + c * 32 + r * 8 : OOB;
    constexpr int L_F2 = L_XR;
    u32 a_fi = BM ? L_F2 + c * 64 + r * 16 : L_FI + (c & 3) * 64 + r * 16, a_fo = (BM ? L_F2 + 768 : L_FI) + ((wave & 3) * 32 + fc) * 2;
    u32 a_ou4 = G_OUT + ((wave * 5 + (c >> 2)) * 54 + jB) * 128 + (c & 3) * 32 + r * 8;
    asm volatile("" : "+v"(a_ou4));
    u32 a_pre = lane < 40 ? G_OUT + ((wave * 5 + (lane >> 3)) * 54 + jB) * 128 + (lane & 7) * 16 : OOB;
    u32x4 emp = {EMPTY, EMPTY, EMPTY, EMPTY};
    asm volatile("" : "+v"(a_pre), "+v"(emp));
#define OPRE() asm volatile("buffer_store_dwordx4 %0, %1, %2, 0 offen sc1\n\ts_nop 1" :: "v"(emp), "v"(a_pre), "s"(rsv) : "memory")
    if (OPOS == 0) OPRE();
    u32 a_w1 = L_W + wave * 5120 + (8 * r + (c >> 2)) * 64 + (r & 1) * 32 + (c & 3) * 8;
    u32 a_wh = L_W + wave * 5120 + 4096 + (4 * r + (c >> 2)) * 64 + (r >> 1) * 32 + (c & 3) * 8;
    u32 a_w2[5];
#pragma unroll
    for (int k = 0; k < 5; ++k) { const int n2 = wave * 5 + k; a_w2[k] = L_WM2 + (n2 >> 2) * 4096 + (tr128 ^ ((n2 & 3) * 32)); }
    asm volatile("" : "+v"(sel), "+v"(go), "+v"(go4), "+v"(a_u), "+v"(a_u4), "+v"(a_av), "+v"(a_ah), "+v"(a_red), "+v"(a_st), "+v"(a_fin), "+v"(a_fst), "+v"(a_out));
    asm volatile("" : "+v"(a_fi), "+v"(a_fo), "+v"(a_w1), "+v"(a_wh), "+v"(a_w2[0]), "+v"(a_w2[1]), "+v"(a_w2[2]), "+v"(a_w2[3]), "+v"(a_w2[4]) :: "memory");
    if (BM) *reinterpret_cast<u32*>(lds + L_F2 + t * 4) = 0;
    PAD(b4, back);
    asm volatile("" : "+v"(bw2));
    if (wave < 4) {
      STAMPT(7, 0);
      bool ok = true;
#pragma unroll
      for (int i = 0; i < 5; ++i) ok &= (gmf[i][1] == tg) & (gmf[i][3] == tg);
      if (!__all(ok)) {
        u32 offs[5];
#pragma unroll
        for (int i = 0; i < 5; ++i) offs[i] = G_MF + (640 + t + 256 * i) * 16;
        gather<5>(rs, offs, gmf, tg);
      }
#pragma unroll
      for (int i = 0; i < 5; ++i) {
        if (i == 2) PAD(t3, role);
        const int it = t + 256 * i, row = it / 160, gi = it % 160;
        u32 w0 = gmf[i][0], w1 = gmf[i][2];
        if (row >= 4) { w0 = cvt2(1.f + lo16(w0), 1.f + hi16(w0)); w1 = cvt2(1.f + lo16(w1), 1.f + hi16(w1)); }
        *reinterpret_cast<u32x2*>(lds + L_XA + (row < 4 ? row + 8 : row) * XS + gi * 8) = u32x2{w0, w1};
      }
      const u32 bw = (t & 2) ? bw2[1] : bw2[0];
      b1v = (t & 1) ? hi16(bw) : lo16(bw);
      asm volatile("" : "+v"(b1v));
      lds_arrive(C_MF, lane);
    }
    lds_wait<1>(C_MF, 4);
    float c1f[5];
    {
      const unsigned char* cp = lds + L_XA + (4 + lb) * XS + wave * 160;
#pragma unroll
      for (int i = 0; i < 2; ++i) { const u32 w = *reinterpret_cast<const u32*>(cp + (32 * i + (c >> 3) * 16 + lc) * 2); c1f[2 * i] = lo16(w); c1f[2 * i + 1] = hi16(w); }
      c1f[4] = u2f((u32)*reinterpret_cast<const unsigned short*>(cp + (64 + lc + (c >> 3)) * 2) << 16);
    }
    const unsigned char* pm = lds + L_XA + (4 + (c & 7)) * XS + wave * 160;
    u32x4 mv0 = *reinterpret_cast<const u32x4*>(pm + r * 16), mv1 = *reinterpret_cast<const u32x4*>(pm + 64 + r * 16);
    s16x4 mv2 = *reinterpret_cast<const s16x4*>(pm + 128 + r * 8);
    u32 a_rd = L_RED + ((wave * 8 + c) * 32 + r * 4) * 4;
    asm volatile("" : "+v"(c1f[0]), "+v"(c1f[1]), "+v"(c1f[2]), "+v"(c1f[3]), "+v"(c1f[4]), "+v"(mv0), "+v"(mv1), "+v"(mv2), "+v"(a_rd) :: "memory");
    if (wave < 4) {
      STAMPT(8, 0);
      lds_wait<2>(C_ISS, 8);
      if (BHOLD) SPINLOOP for (int k = 0; k < 256; ++k) { if ((int)((u32)__builtin_amdgcn_s_memrealtime() - tb) >= (126 * TAU_E >> 4) + BHOLD) break; __builtin_amdgcn_s_sleep(2); }
      const int h5 = ROT1 * (jB & 1);
      const bf16* wp = a.w1 + (size_t)(wave * 160 + (lane >> 2)) * FF + jB * 32 + ((((((lane >> 1) & 1) ^ (lane >> 5)) << 1) | (lane & 1)) * 8);
      const bf16* wp2 = a.w2 + (size_t)(jB * 32 + wave * 8 + (lane >> 3)) * DM + (s128e ^ ((wave & 1) * 32));
#pragma unroll
      for (int n = 0; n < 20; ++n) {
        if (n == 0) PAD(bd, back);
        if (n == N1B) lds_wait<2>(C_PUB, 1);
        if (n < 5 || (n >= 5 + W2F && n < 10 + W2F)) {
          const int i2 = n < 5 ? n + h5 : n - W2F - h5;
          dma16(wp + (size_t)(16 * i2) * FF, L_W + (wave * 10 + i2) * 1024);
        } else {
          const int k = n < 5 + W2F ? n - 5 : n - 10;
          dma16(wp2 + k * 64, L_WM2 + (k * 4 + wave) * 1024);
        }
      }
      STAMPT(16, 0); STAMPT(5, 128);
    }
    asm volatile("s_waitcnt vmcnt(%0)" :: "i"(10 - W2F) : "memory");
    STAMPT(23, 0);
    PAD(b1, a1);
    BAR();
    STAMPT(3, 0);
    if (OPOS == 1) OPRE();
    s16x8 wf[2][2], wg[5];
    s16x4 wh[2];
#pragma unroll
    for (int nt = 0; nt < 2; ++nt) {
#pragma unroll
      for (int s = 0; s < 2; ++s) wf[s][nt] = wfrag<256>((a_w1 ^ (nt * 32)) + s * 2048);
      wh[nt] = __builtin_amdgcn_ds_read_tr16_b64_v4i16((lds_s4p)(uintptr_t)(a_wh ^ (nt * 32)));
    }
#define W1MMA(acc) \
      f32x4 acc[2]; \
      _Pragma("unroll") for (int nt = 0; nt < 2; ++nt) { const f32x4 z_ = {0.f, 0.f, 0.f, 0.f}; acc[nt] = MFMA(wf[0][nt], av0, z_); } \
      _Pragma("unroll") for (int nt = 0; nt < 2; ++nt) acc[nt] = MFMA(wf[1][nt], av1, acc[nt]); \
      _Pragma("unroll") for (int nt = 0; nt < 2; ++nt) acc[nt] = __builtin_amdgcn_mfma_f32_16x16x16bf16_1k(wh[nt], av2, acc[nt], 0, 0, 0);
    {
      const u32x4 av0 = mv0, av1 = mv1;
      const s16x4 av2 = mv2;
      W1MMA(acc)
#pragma unroll
      for (int nt = 0; nt < 2; ++nt) if (c < 8) *reinterpret_cast<f32x4*>(lds + a_rd + nt * 64) = acc[nt];
    }
    float Tc = 0.f, Ts = 0.f;
#define TSUM() _Pragma("unroll") for (int k = 0; k < 4; ++k) { Tc += red[((hw * 4 + k) * 8 + (wave & 3)) * 32 + fc]; Ts += red[((hw * 4 + k) * 8 + 4 + (wave & 3)) * 32 + fc]; }
    u32x4 pa[5];
    f32x4 s4, s0, s1;
    u32 a_fl = lane < 16 ? G_HB + jB * 128 + lane * 8 : OOB, tk = C_W;
    u32x2 fv = {1u, tg};
    {
      u64 m[5], old = 0;
#pragma unroll
      for (int i = 0; i < 5; ++i) { m[i] = ~0ull; pa[i] = u32x4{0u, 0u, 0u, 0u}; }
#pragma unroll
      for (int i = 0; i < 5; ++i) asm volatile("" : "+v"(c1f[i]), "+v"(pa[i]));
#define BLOOK(W_) \
        asm volatile(LK(0, 5, "0", 0, 7) LK(1, 5, "0", 512, 8) LK(2, 5, "0", 2048, 9) LK(3, 5, "0", 2560, 10) LK(4, 6, "0", 0, 11) LKEND \
                     : "+v"(pa[0]), "+v"(pa[1]), "+v"(pa[2]), "+v"(pa[3]), "+v"(pa[4]) \
                     : "v"(go), "v"(go4), "s"(m[0]), "s"(m[1]), "s"(m[2]), "s"(m[3]), "s"(m[4]), [rs] "s"(rsv), [w] "i"(W_) : "memory");
      STAMPT(9, 0); STAMPT(30, 448);
      if (BPREW) {
        VM0();
        STAMPT(20, 0); STAMPT(31, 448);
        BAR();
        STAMPT(29, 0);
      }
      SPINLOOP for (int k = 0; k < 256; ++k) if ((int)((u32)__builtin_amdgcn_s_memrealtime() - tb) >= (126 * TAU_E >> 4) + BGT) break;
      STAMPT(21, 0);
      BLOOK(5)
      if (!BPREW) { STAMPT(20, 0); STAMPT(31, 448); }
      if (!BAN) announce(rs, G_HB + jB * 128, C_GO, C_W, 8, tb, HCD, tg);
      if (!BPREW) { BAR(); STAMPT(29, 0); }
#pragma unroll
      for (int k = 0; k < 5; ++k) wg[k] = wfrag<512>(a_w2[k]);
      PAD(b2, a2);
      TSUM()
#pragma unroll
      for (int i = 0; i < 5; ++i) asm volatile("" : "+v"(wg[i]));
      asm volatile("" : "+v"(Tc), "+v"(Ts));
      STAMPT(18, 0);
      asm volatile("s_waitcnt vmcnt(%8)" : "+v"(pa[0]), "+v"(pa[1]), "+v"(pa[2]), "+v"(pa[3]), "+v"(pa[4]), "+v"(a_fl), "+v"(fv), "+v"(tk) : "i"(BAN ? 0 : 1) : "memory");
      STAMPT(22, 0);
#define BSUM() { const f32x4 z = {0.f, 0.f, 0.f, 0.f}; \
      s0 = MFMA(sel, pa[0], z); PIN(s0) s1 = MFMA(sel, pa[2], z); PIN(s1) s0 = MFMA(sel, pa[1], s0); PIN(s0) s1 = MFMA(sel, pa[3], s1); PIN(s1) s4 = MFMA(sel, pa[4], z); PIN(s4) \
      if (BAN) asm volatile("s_memrealtime s[98:99]" ::: "s98", "s99"); SB(); }
#define BNAN() (__builtin_amdgcn_ballot_w64(__builtin_isunordered(s0[0], s0[1])) | __builtin_amdgcn_ballot_w64(__builtin_isunordered(s1[0], s1[1])) | \
                __builtin_amdgcn_ballot_w64(__builtin_isunordered(s4[0], s4[1])))
      BSUM()
      if (BNAN()) {
        FAIL(2);
        SPINLOOP for (unsigned spins = 0; spins < SPIN_CAP; ++spins) {
          u64 bad = 0;
#pragma unroll
          for (int i = 0; i < 5; ++i) { m[i] = __builtin_amdgcn_ballot_w64(missing(pa[i])); bad |= m[i]; }
          if (!(bad | old)) break;
          old = bad;
          BLOOK(5 * (PDB - 1))
          BSUM()
          if (!BNAN()) break;
        }
      }
    }
#define BFLAG(W_) u32 tl; \
        asm volatile("s_waitcnt lgkmcnt(%3)\n\ts_add_u32 %0, s98, %2" : "=s"(tl), "+v"(tk) : "i"(HBD), "i"(W_) : "scc"); \
        fv[0] = tl; \
        asm volatile("v_readfirstlane_b32 %0, %1\n\ts_cmp_eq_u32 %0, 7\n\ts_cbranch_scc0 1f\n\tbuffer_store_dwordx2 %2, %3, %4, 0 offen sc1\n1:" \
                     : "=&s"(tl) : "v"(tk), "v"(fv), "v"(a_fl), "s"(rsv) : "scc"); \
        SB();
    if (BAN >= 2) asm volatile("s_mov_b64 exec, 1\n\tds_add_rtn_u32 %0, %0, %1\n\ts_mov_b64 exec, -1" : "+v"(tk) : "v"(fv[0]));
    SB();
    STAMPT(14, 0); WSTAMP(3);
    float xf[5];
    {
      const u32 w0 = cvt2(s0[0], s0[1]), w1 = cvt2(s1[0], s1[1]);
      xf[0] = lo16(w0); xf[1] = hi16(w0); xf[2] = lo16(w1); xf[3] = hi16(w1);
      *reinterpret_cast<u32*>(lds + a_u) = cvt2(xf[0] * c1f[0], xf[1] * c1f[1]);
      *reinterpret_cast<u32*>(lds + a_u + 64) = cvt2(xf[2] * c1f[2], xf[3] * c1f[3]);
    }
    STAMPT(19, 0);
    {
      u32x4 av0 = *reinterpret_cast<const u32x4*>(lds + a_av), av1 = *reinterpret_cast<const u32x4*>(lds + a_av + 64);
      SB();
      if (BAN == 2) { BFLAG(3) }
      float e0 = s4[0], e1 = s4[1];
      asm volatile("" : "+v"(e0), "+v"(e1));
      xf[4] = bfr(((c & 8) ? e1 : e0) + dpp<XOR8>((c & 8) ? e0 : e1));
      *reinterpret_cast<unsigned short*>(lds + a_u4) = (unsigned short)cvt2(xf[4] * c1f[4], 0.f);
      s16x4 av2 = *reinterpret_cast<const s16x4*>(lds + a_ah);
      SB();
      if (BAN == 1) asm volatile("s_mov_b64 exec, 1\n\tds_add_rtn_u32 %0, %0, %1\n\ts_mov_b64 exec, -1" : "+v"(tk) : "v"(fv[0]));
      SB();
      if (BAN == 3) { BFLAG(5) }
      const float ps = ((xf[0] + xf[1]) + (xf[2] + xf[3])) + xf[4];
      const float pq = fmaf(xf[0], xf[0], fmaf(xf[1], xf[1], xf[4] * xf[4])) + fmaf(xf[2], xf[2], xf[3] * xf[3]);
      const float t1 = ps + dpp<XOR8>(ps), t2 = pq + dpp<XOR8>(pq);
      float z = (c & 8) ? t2 : t1;
      z += dpp<XOR1>(z);
      asm volatile("" : "+v"(z));
      __builtin_amdgcn_sched_barrier(0);
      W1MMA(acc)
      if (BAN == 1) {
        asm volatile("" : "+v"(acc[0]), "+v"(acc[1]));
        BFLAG(0)
      }
      z = cross_rows_sum(z);
      if ((lane & 49) == 0) *reinterpret_cast<float*>(lds + a_st) = z;
      if (c < 4) {
#pragma unroll
        for (int nt = 0; nt < 2; ++nt) *reinterpret_cast<f32x4*>(lds + a_red + nt * 64) = acc[nt];
      }
    }
#undef W1MMA
    PAD(b3, a3);
    BAR();
    STAMPT(24, 0);
    if (wave < 4) {
      f32x4 sv[4];
      float sp[4];
#pragma unroll
      for (int k = 0; k < 4; ++k) sv[k] = *reinterpret_cast<const f32x4*>(lds + a_fst + k * 16);
#pragma unroll
      for (int k = 0; k < 4; ++k) sp[k] = *reinterpret_cast<const float*>(lds + a_fin + k * 512);
      __builtin_amdgcn_sched_barrier(0);
      const f32x4 s1 = sv[0] + sv[1], s2 = sv[2] + sv[3];
      const float mean = ((s1[0] + s1[1]) + (s1[2] + s1[3])) * (1.f / DM);
      const float var = fmaxf(((s2[0] + s2[1]) + (s2[2] + s2[3])) * (1.f / DM) - mean * mean, 0.f);
      const float rstd = __builtin_amdgcn_rsqf(var + 1e-5f);
      const float zp = fmaf(rstd, fmaf(-mean, Tc, (sp[0] + sp[1]) + (sp[2] + sp[3])), Ts);
      const float hh = bfr(bfr(swap32_add(zp, zp)) + b1v);
      const float e2 = __builtin_amdgcn_exp2f(hh * fmaf(hh * hh, 0.1029432397f, 2.302208198f));
      const float f = fmaf(-hh, frcp(e2 + 1.f), hh);
      *reinterpret_cast<unsigned short*>(lds + a_fo) = (unsigned short)cvt2(f, 0.f);
    }
    BAR();
    if (BM) {
      u32x4 av[4];
#pragma unroll
      for (int k = 0; k < 4; ++k) av[k] = *reinterpret_cast<const u32x4*>(lds + a_fi + (3 - k) * 256);
      SB();
      const f32x4 z = {0.f, 0.f, 0.f, 0.f};
      f32x4 a4 = MFMA(wg[4], av[0], z);
      PIN(a4)
      f32x4 a0 = MFMA(wg[0], av[0], z);
      PIN(a0)
#pragma unroll
      for (int k = 1; k < 4; ++k) { a0 = MFMA(wg[k], av[k], a0); PIN(a0) }
      __builtin_amdgcn_sched_barrier(0);
      const u32x2 o4 = {min(cvt2(a4[0], a4[1]), EMPTY - 1), min(cvt2(a4[2], a4[3]), EMPTY - 1)};
      __builtin_amdgcn_raw_buffer_store_b64(o4, rs, a_out, 4 * (54 * 128), 16);
      const u32x2 o0 = {min(cvt2(a0[0], a0[1]), EMPTY - 1), min(cvt2(a0[2], a0[3]), EMPTY - 1)};
      __builtin_amdgcn_raw_buffer_store_b64(o0, rs, a_ou4, 0, 16);
    } else {
      const u32x4 av = *reinterpret_cast<const u32x4*>(lds + a_fi);
      f32x4 acc[5];
#pragma unroll
      for (int k = 0; k < 5; ++k) { const f32x4 z = {0.f, 0.f, 0.f, 0.f}; acc[k] = MFMA(wg[k], av, z); }
      __builtin_amdgcn_sched_barrier(0);
#pragma unroll
      for (int k = 0; k < 5; ++k) {
        const u32x2 o = {min(cvt2(acc[k][0], acc[k][1]), EMPTY - 1), min(cvt2(acc[k][2], acc[k][3]), EMPTY - 1)};
        __builtin_amdgcn_raw_buffer_store_b64(o, rs, a_out, k * (54 * 128), 16);
      }
    }
    STAMPT(15, 0);
    asm volatile("" :: "v"(pa[0]), "v"(pa[1]), "v"(pa[2]), "v"(pa[3]), "v"(pa[4]));

  } else if (role == 2 && wave == 0) {
    PAD(c0, b1);
    const int lb = (c >> 1) & 3, col = ia * 16 + (c & 1) * 8 + 2 * r + (c >> 3);
    bf16* outp;
    asm volatile("s_load_dwordx2 %0, %1, 128\n\ts_waitcnt lgkmcnt(0)" : "=s"(outp) : "s"(__builtin_amdgcn_kernarg_segment_ptr()));
    const __amdgpu_buffer_rsrc_t ro = __builtin_amdgcn_make_buffer_rsrc(outp, 0, NB * DM * 2, 0x00020000);
#define DUMMY() __builtin_amdgcn_raw_buffer_store_b16((unsigned short)0, ro, (lb * DM + col) * 2, 0, DAUX)
    if (DPOS == 0) DUMMY();
    float b2v = bf2f(a.b2[col]);
    const f32x4 z = {0.f, 0.f, 0.f, 0.f};
    SELECTOR(sel)
    float x1v, gbv;
    {
      u32 offs[1] = {G_MF + ((12 + lb) * 160 + (col >> 2)) * 16};
      u32x4 gm[1];
      gather<1>(rs, offs, gm, tg);
      const u32 gbw = (r & 1) ? gm[0][2] : gm[0][0];
      gbv = (c & 8) ? hi16(gbw) : lo16(gbw);
    }
    u32 oo = lb * DM + col;
    asm volatile("" : "+v"(gbv), "+v"(oo), "+v"(sel), "+v"(b2v));
    u32x4 pa[7];
    u32 go = G_OUT + ia * (54 * 128) + lane * 16, gx = G_WO + ia * 1024 + lane * 16;
#pragma unroll
    for (int i = 0; i < 7; ++i) { pa[i] = u32x4{0u, 0u, 0u, 0u}; asm volatile("" : "+v"(pa[i])); }
    asm volatile("" : "+v"(go), "+v"(gx));
    u32x2 hv;
    if (CPUB) lds_wait<2>(C_PUB, 1);
    if (DPOS == 1) DUMMY();
    heads_up_start(rsv, G_HA + ((ia >> 2) * 8 + lane) * 128, 0xffull, tg, hv);
    heads_up<CSL>(rsv, G_HA + ((ia >> 2) * 8 + lane) * 128, 0xffull, tg, hv);
    if (DPOS == 2) DUMMY();
    if (EPE == 1) __builtin_amdgcn_raw_buffer_store_b32(tg, rs, ia == 0 && lane == 0 ? WS_EPOCH : OOB, 0, 16);
    hv[0] += CXD;
    heads_up_time(0xffull, hv);
    {
      u32x4 gm[1];
      gather_e<1, 2, 1>(rsv, gx, gm, ~0ull);
      const f32x4 sm = MFMA(sel, gm[0], z);
      x1v = bfr(PAIRSUM(sm));
      asm volatile("" : "+v"(x1v));
    }
    STAMPT(23, 0);
    if (DPOS == 3) DUMMY();
    if (EPE == 2) __builtin_amdgcn_raw_buffer_store_b32(tg, rs, ia == 0 && lane == 0 ? WS_EPOCH : OOB, 0, 16);
    PAD(c1, b2);
    if (CGT) SPINLOOP for (int k = 0; k < 256; ++k) if ((int)((u32)__builtin_amdgcn_s_memrealtime() - tb) >= (126 * TAU_E >> 4) + CGT) break;
    heads_up_start(rsv, G_HB + lane * 128, (1ull << 54) - 1, tg, hv);
    heads_up<0>(rsv, G_HB + lane * 128, (1ull << 54) - 1, tg, hv);
    heads_up_time((1ull << 54) - 1, hv);
    STAMPT(14, 0);
    f32x4 s0, s1;
    {
      u64 m[7], old = 0;
#pragma unroll
      for (int i = 0; i < 7; ++i) m[i] = i < 6 ? ~0ull : (1ull << 48) - 1;
      look<63>(rsv, go, pa, m);
#define CW(i, W_) asm volatile("s_waitcnt vmcnt(%1)" : "+v"(pa[i]) : "i"(W_ ? 6 - (i) : 63) : "memory");
#define CSUM(W_) { CW(0, W_) s0 = MFMA(sel, pa[0], z); PIN(s0) CW(1, W_) s1 = MFMA(sel, pa[1], z); PIN(s1) \
      CW(2, W_) s0 = MFMA(sel, pa[2], s0); PIN(s0) CW(3, W_) s1 = MFMA(sel, pa[3], s1); PIN(s1) CW(4, W_) s0 = MFMA(sel, pa[4], s0); PIN(s0) \
      CW(5, W_) s1 = MFMA(sel, pa[5], s1); PIN(s1) CW(6, W_) s0 = MFMA(sel, pa[6], s0); PIN(s0) s0 += s1; }
      CSUM(1)
      if (__builtin_amdgcn_ballot_w64(__builtin_isunordered(s0[0], s0[1]))) {
        FAIL(3);
        SPINLOOP for (unsigned spins = 0; spins < SPIN_CAP; ++spins) {
          u64 bad = 0;
#pragma unroll
          for (int i = 0; i < 7; ++i) { m[i] = __builtin_amdgcn_ballot_w64(missing(pa[i])); bad |= m[i]; }
          if (!(bad | old)) break;
          old = bad;
          look<7 * (PDC - 1)>(rsv, go, pa, m);
          CSUM(0)
          if (!__builtin_amdgcn_ballot_w64(__builtin_isunordered(s0[0], s0[1]))) break;
        }
      }
    }
    STAMPT(16, 0);
    const float y = bfr(bfr(PAIRSUM(s0)) + b2v);
    if (OUTM & 1) __builtin_amdgcn_raw_buffer_store_b16((unsigned short)cvt2(x1v + bfr(gbv * y), 0.f), ro, oo * 2, 0, FAUX);
    else outp[oo] = __float2bfloat16(x1v + bfr(gbv * y));
    STAMPT(17, 0);
    asm volatile("" :: "v"(pa[0]), "v"(pa[1]), "v"(pa[2]), "v"(pa[3]), "v"(pa[4]), "v"(pa[5]), "v"(pa[6]), "v"(hv));

    if (!EPE && ia == 0 && t == 0)
      __hip_atomic_store(reinterpret_cast<u32*>(a.ws + WS_EPOCH), tg, __ATOMIC_RELAXED, __HIP_MEMORY_SCOPE_AGENT);
  }
  STAMP_FLUSH;
}

extern "C" void kernel_launch(void* const* d_in, const int* in_sizes, int n_in, void* d_out, int out_size, void* d_ws,
                              size_t ws_size, hipStream_t stream) {
  Args a;
  a.x = Desc{(const bf16*)d_in[0], NB * DM * 2, 0x00020000u};
  a.kc = (const bf16*)d_in[1];
  a.vc = (const bf16*)d_in[2];
  a.wq = Desc{(const bf16*)d_in[3], DM * QD * 2, 0x00020000u};
  a.wo = (const bf16*)d_in[4];
  a.w1 = (const bf16*)d_in[6];
  a.b1 = (const bf16*)d_in[7];
  a.w2 = (const bf16*)d_in[8];
  a.b2 = (const bf16*)d_in[9];
  a.c = Desc{(const bf16*)d_in[10], NB * DM * 2, 0x00020000u};
  a.wmod = Desc{(const bf16*)d_in[11], DM * MODN * 2, 0x00020000u};
  a.out = (bf16*)d_out;
  a.ws = (unsigned char*)d_ws;
  mega_kernel<<<NWG, NT, 0, stream>>>(a);
}
```

```cpp
#define SPINLOOP _Pragma("nounroll")
#define P_LAG 14
#define P_NPRE 32
#define P_NVE 6
#define P_NVA 6
#define QODD 4
#define QEVEN 4
#define MFPOS 0
#define EA 0
#define EB 0
#define EC 4
#define APOLL 0
#define N1E N1A
#define TAU_A 236
#define TAU_B 239
#define TAU_X TAU_A
#define TAU_E 244
#define PK 0
#define PL 1
#define QPRE 1
#define QHOLD 0
#define CT0 1
#define TOFF 55
#define BLIGHT 1
#define BPRE 0
#define BW1 1
#define BMASK 1
#define CSLEEP 1
#define CSL 2
#define CPUB 1
#define CXD 20
#define OPOS 1
#define NREPL 2
#define OUTPRE 1
#define ASL 1
#define AEARLY 1
#define PDC 4
#define PDA 3
#define PDB 3
#define PSB 1
#define HC 1
#define HCD 132
#define HAG 40
#define AW 4
#define BFW -1
#define FULLF 1
#define WARM 1
#define HT 0
#define HX 14
#define HTD 55
#define HSL 1
#define NANT 1
#define HAW 65
#define BM 1
#define HPD 1
#define BT 0
#define BSL 2
#define PB 0
#define GOPOS 2
#define JMAP 1
#define N1A 10
#define N1B 5
#define PT 0
#define PTAUX 16
#define LAY 1
#define OUTM 1
#define EPE 0
#define DPOS 2
#define DAUX 16
#define FAUX 16
#define KOUT 1
#define NK0 12
#define GT 540
#define PLO 5
#define PLS 17
#define ANCH 1
#define AOFF 55
#define AGG 1
#define PHW 0
#define CPF 1
#define CLEN 0x8080
#define AGT 0
#define GPAD 1
#define MFE 1
#define E3POS 10
#define E4POS 22
#define L1POS 28
#define L4POS 4
#define T1POS 18
#define HEARLY 0
#define BAR2L 0
#define ROT1 5
#define W2F 0
#define HGT 300
#define BGT 304
#define BPREW 0
#define BAN 2
#define HBD 85
#define CGT 0
#define MLP 30
#define BHOLD 0
#define TAU_L 239
#define DL0 42
#define EVT 310
#define FXA 1
#define RPRI 0
#define EVW 40
#define KP 0
#define KTO 270
#define KTE 0
#define SLS 0
#define SLF 30
#define SLM 50
#define BRT 1
#define BXD 0
#define PDB2 1
#define DLS DLA
#define BFIRST 1
#define EVL 1
#define DLA 53
#define DLB 30
#define CST 1
#define POFF 0
#define NCL 4
#define CLS (NCL == 8 ? u & 7 : NCL == 4 ? (u >> 1) & 3 : (u >> 3) & (NCL - 1))
#define P_GATE 8
#include <hip/hip_runtime.h>
#include <hip/hip_bf16.h>
#include <stdint.h>

using bf16 = __hip_bfloat16;
using u32 = unsigned;
using u64 = unsigned long long;
using u32x2 = __attribute__((ext_vector_type(2))) unsigned;
using u32x4 = __attribute__((ext_vector_type(4))) unsigned;
using f32x4 = __attribute__((ext_vector_type(4))) float;
using bf16x2 = __attribute__((ext_vector_type(2))) __bf16;
typedef __attribute__((ext_vector_type(2))) float f32x2;
typedef short s16x8 __attribute__((ext_vector_type(8)));
typedef short s16x4 __attribute__((ext_vector_type(4)));
typedef __attribute__((address_space(3))) s16x4* lds_s4p;
typedef __attribute__((ext_vector_type(8))) __bf16 bf16x8v;

constexpr int NB = 4, DM = 640, NH = 32, DH = 128, SK = 8192, WIN = 2048, FF = 1728, QD = 4096, MODN = 3840;
constexpr int NT = 512, NWG = 256;

constexpr u32 WS_EPOCH = 0;
constexpr u32 G_T = 128;
constexpr u32 G_Q = 256;
constexpr u32 G_MOD = G_Q + 8 * 1024 * 16;
constexpr u32 G_MF = G_MOD + 8 * 1024 * 16;
constexpr u32 G_HB = G_MF + 16 * 160 * 16;
constexpr u32 G_HA = G_HB + 64 * 128;
constexpr u32 G_HT = G_HA + 80 * 128;
constexpr u32 G_ML = G_HT + 256 * 128;
constexpr u32 EMPTY = 0xffffffffu;
constexpr int NREP = NREPL;
constexpr u32 G_WO = G_ML + 256 * 64;
constexpr u32 WO_REP = 40 * 1024;
constexpr u32 G_ATT = G_WO + NREP * WO_REP;
constexpr u32 G_TK = G_ATT + 256 * 256;
constexpr u32 G_OUT = G_WO + 37 * 4096;
constexpr u32 WS_END = G_OUT + 40 * 54 * 128;
constexpr u32 OOB = 0xF0000000u;
#define GIDX(n, ks, b, cg) ((((ks) * ((n) / 4) + ((cg) >> 2)) * 16 + (b) * 4 + ((cg) & 3)) * 16)

#define WSTAMP(k) do {} while (0)
#define STAMPW(i) do {} while (0)
#define STAMPT(i, tt) do {} while (0)
#define STAMP_DECL
#define STAMP_FLUSH do {} while (0)
#define FAIL(i) do {} while (0)
#define PAD(name, next) asm volatile(".Lpad_" #name ":\n\ts_cbranch_execz .Lpad_" #next)
#define PADX(name, next, x) asm volatile(".Lpad_" #name ":\n\ts_cbranch_execz .Lpad_" #next : "+v"(x))
#define PADA(name, next) asm volatile(".Lpad_" #name ":\n\ts_cbranch_execz .Lpad_" #next : "+v"(acc[0]), "+v"(acc[1]), "+v"(acc[2]), "+v"(acc[3]), "+v"(acc[4]), "+v"(acc[5]), "+v"(acc[6]), "+v"(acc[7]))
#define STR_(x) #x
#define STR(x) STR_(x)
#define BAR() asm volatile("s_waitcnt lgkmcnt(0)\n\ts_barrier" ::: "memory")
#define VM0() asm volatile("s_waitcnt vmcnt(0)" ::: "memory")

__device__ __forceinline__ float bfr(float x) { return __bfloat162float(__float2bfloat16(x)); }
__device__ __forceinline__ float bf2f(bf16 x) { return __bfloat162float(x); }
__device__ __forceinline__ float lo16(unsigned u) { return __builtin_bit_cast(float, u << 16); }
__device__ __forceinline__ float hi16(unsigned u) { return __builtin_bit_cast(float, u & 0xffff0000u); }
__device__ __forceinline__ u32 f2u(float f) { return __builtin_bit_cast(u32, f); }
__device__ __forceinline__ float u2f(u32 f) { return __builtin_bit_cast(float, f); }
__device__ __forceinline__ u32 cvt2(float a, float b) {
  const f32x2 v = {a, b};
  return __builtin_bit_cast(u32, __builtin_convertvector(v, bf16x2));
}
__device__ __forceinline__ float frcp(float x) { return __builtin_amdgcn_rcpf(x); }
__device__ __forceinline__ float fsqrt(float x) { return __builtin_amdgcn_sqrtf(x); }
__device__ __forceinline__ float fexp(float x) { return __builtin_amdgcn_exp2f(x * 1.4426950408889634f); }
__device__ __forceinline__ float dot2(unsigned k, unsigned q, float acc) {
  return __builtin_amdgcn_fdot2_f32_bf16(__builtin_bit_cast(bf16x2, k), __builtin_bit_cast(bf16x2, q), acc, false);
}
__device__ __forceinline__ float addlo(unsigned w, float acc) { u32 k = 0x00003f80u; asm("" : "+s"(k)); return dot2(w, k, acc); }
__device__ __forceinline__ float addhi(unsigned w, float acc) { return dot2(w, 0x3f800000u, acc); }
template <int CTRL> __device__ __forceinline__ float dpp(float x) {
  return __builtin_bit_cast(float, __builtin_amdgcn_mov_dpp(__builtin_bit_cast(int, x), CTRL, 0xf, 0xf, true));
}
template <int CTRL> __device__ __forceinline__ u32 dppu(u32 x) { return (u32)__builtin_amdgcn_mov_dpp((int)x, CTRL, 0xf, 0xf, true); }
constexpr int XOR1 = 0xB1, XOR2 = 0x4E, XOR7 = 0x141, XOR8 = 0x128, ROR4 = 0x124, ROR2 = 0x122, ROR1 = 0x121, SHL4 = 0x104;
__device__ __forceinline__ float swap32_add(float a, float b) {
  auto s = __builtin_amdgcn_permlane32_swap(f2u(a), f2u(b), false, false);
  return u2f(s[0]) + u2f(s[1]);
}
__device__ __forceinline__ float swap16_add(float a, float b) {
  auto s = __builtin_amdgcn_permlane16_swap(f2u(a), f2u(b), false, false);
  return u2f(s[0]) + u2f(s[1]);
}
__device__ __forceinline__ float cross_rows_sum(float x) { x = swap16_add(x, x); return swap32_add(x, x); }
__device__ __forceinline__ float cross_rows_max(float x) {
  auto s = __builtin_amdgcn_permlane16_swap(f2u(x), f2u(x), false, false);
  x = fmaxf(u2f(s[0]), u2f(s[1]));
  auto s2 = __builtin_amdgcn_permlane32_swap(f2u(x), f2u(x), false, false);
  return fmaxf(u2f(s2[0]), u2f(s2[1]));
}
__device__ __forceinline__ float wave_sum(float x) {
  x += dpp<XOR8>(x); x += dpp<ROR4>(x); x += dpp<ROR2>(x); x += dpp<ROR1>(x);
  return cross_rows_sum(x);
}
__device__ __forceinline__ float wave_max(float x) {
  x = fmaxf(x, dpp<XOR8>(x)); x = fmaxf(x, dpp<ROR4>(x)); x = fmaxf(x, dpp<ROR2>(x)); x = fmaxf(x, dpp<ROR1>(x));
  return cross_rows_max(x);
}

__device__ __forceinline__ void st_gran(__amdgpu_buffer_rsrc_t rs, u32 off, u32 a, u32 b, u32 tg) {
  u32x4 v = {a, tg, b, tg};
  __builtin_amdgcn_raw_buffer_store_b128(v, rs, off, 0, 16);
}
__device__ __forceinline__ u32x4 ld_gran(__amdgpu_buffer_rsrc_t rs, u32 off) {
  return __builtin_amdgcn_raw_buffer_load_b128(rs, off, 0, 16);
}
constexpr unsigned SPIN_CAP = 1u << 18;
template <int N>
__device__ __forceinline__ void gather(__amdgpu_buffer_rsrc_t rs, const u32 (&offs)[N], u32x4 (&g)[N], u32 tg) {
  SPINLOOP for (unsigned spins = 0; spins < SPIN_CAP; ++spins) {
    bool ok = true;
#pragma unroll
    for (int i = 0; i < N; ++i) g[i] = ld_gran(rs, offs[i]);
#pragma unroll
    for (int i = 0; i < N; ++i) ok &= (offs[i] == OOB) | ((g[i][1] == tg) & (g[i][3] == tg));
    if (__all(ok)) break;
    __builtin_amdgcn_s_sleep(1);
  }
}
template <int N>
__device__ __forceinline__ void gather_m(__amdgpu_buffer_rsrc_t rs, u32 (&offs)[N], u32 (&va)[N], u32 (&vb)[N], u32 tg) {
#pragma unroll
  for (int i = 0; i < N; ++i) { va[i] = 0; vb[i] = 0; }
  SPINLOOP for (unsigned spins = 0; spins < SPIN_CAP; ++spins) {
    u32x4 g[N];
#pragma unroll
    for (int i = 0; i < N; ++i) g[i] = ld_gran(rs, offs[i]);
    bool ok = true;
#pragma unroll
    for (int i = 0; i < N; ++i) {
      const bool hit = (offs[i] != OOB) & (g[i][1] == tg) & (g[i][3] == tg);
      va[i] = hit ? g[i][0] : va[i]; vb[i] = hit ? g[i][2] : vb[i];
      offs[i] = hit ? OOB : offs[i];
      ok &= (offs[i] == OOB);
    }
    if (__all(ok)) break;
    __builtin_amdgcn_s_sleep(1);
  }
}

__device__ __forceinline__ bool missing(u32x4 v) { return max(max(v[0], v[1]), max(v[2], v[3])) == EMPTY; }
#define LK(v, o, so, imm, m) "s_mov_b64 exec, %" #m "\n\tbuffer_load_dwordx4 %" #v ", %" #o ", %[rs], " so " offen offset:" #imm " sc1\n\t"
#define LKEND "s_mov_b64 exec, -1\n\ts_waitcnt vmcnt(%[w])"
template <int W> __device__ __forceinline__ void look(u32x4 rs, u32 off, u32x4 (&v)[1], const u64 (&m)[1]) {
  asm volatile(LK(0, 1, "0", 0, 2) LKEND : "+v"(v[0]) : "v"(off), "s"(m[0]), [rs] "s"(rs), [w] "i"(W) : "memory");
}
template <int W> __device__ __forceinline__ void look(u32x4 rs, u32 off, u32x4 (&v)[5], const u64 (&m)[5]) {
  asm volatile(LK(0, 5, "0", 0, 6) LK(1, 5, "0", 1024, 7) LK(2, 5, "0", 2048, 8) LK(3, 5, "0", 3072, 9) LK(4, 5, "%11", 0, 10) LKEND
               : "+v"(v[0]), "+v"(v[1]), "+v"(v[2]), "+v"(v[3]), "+v"(v[4])
               : "v"(off), "s"(m[0]), "s"(m[1]), "s"(m[2]), "s"(m[3]), "s"(m[4]), "s"(4096u), [rs] "s"(rs), [w] "i"(W) : "memory");
}
template <int W> __device__ __forceinline__ void look(u32x4 rs, u32 off, u32x4 (&v)[7], const u64 (&m)[7]) {
  asm volatile(LK(0, 7, "0", 0, 8) LK(1, 7, "0", 1024, 9) LK(2, 7, "0", 2048, 10) LK(3, 7, "0", 3072, 11)
               LK(4, 7, "%15", 0, 12) LK(5, 7, "%15", 1024, 13) LK(6, 7, "%15", 2048, 14) LKEND
               : "+v"(v[0]), "+v"(v[1]), "+v"(v[2]), "+v"(v[3]), "+v"(v[4]), "+v"(v[5]), "+v"(v[6])
               : "v"(off), "s"(m[0]), "s"(m[1]), "s"(m[2]), "s"(m[3]), "s"(m[4]), "s"(m[5]), "s"(m[6]), "s"(4096u), [rs] "s"(rs), [w] "i"(W) : "memory");
}
template <int N, int SLEEP, int D>
__device__ __forceinline__ void gather_e(u32x4 rs, u32 off, u32x4 (&v)[N], u64 last) {
  u64 m[N];
#pragma unroll
  for (int i = 0; i < N; ++i) { m[i] = i < N - 1 ? ~0ull : last; v[i] = u32x4{0u, 0u, 0u, 0u}; }
  look<0>(rs, off, v, m);
  SPINLOOP for (unsigned spins = 0; spins < SPIN_CAP; ++spins) {
    u64 bad = 0;
#pragma unroll
    for (int i = 0; i < N; ++i) { m[i] = __builtin_amdgcn_ballot_w64(missing(v[i])); bad |= m[i]; }
    if (!bad) break;
    if (SLEEP) __builtin_amdgcn_s_sleep(SLEEP);
    look<N * (D - 1)>(rs, off, v, m);
  }
}

#define HLOOK(W_) asm volatile("s_mov_b64 exec, %2\n\tbuffer_load_dwordx2 %0, %1, %3, 0 offen sc1\n\ts_mov_b64 exec, -1\n\ts_waitcnt vmcnt(%4)" \
                               : "+v"(v) : "v"(off), "s"(m), "s"(rs), "i"(W_) : "memory")
__device__ __forceinline__ void heads_up_start(u32x4 rs, u32 off, u64 m, u32 tg, u32x2& v) { v = u32x2{0u, ~tg}; HLOOK(63); }
template <int SLEEP, int W0 = 0>
__device__ __forceinline__ void heads_up(u32x4 rs, u32 off, u64 m, u32 tg, u32x2& v) {
  asm volatile("s_waitcnt vmcnt(%1)" : "+v"(v) : "i"(W0) : "memory");
  SPINLOOP for (unsigned spins = 0; spins < SPIN_CAP; ++spins) {
    m &= __builtin_amdgcn_ballot_w64(v[1] != tg);
    if (!m) break;
    if (SLEEP) __builtin_amdgcn_s_sleep(SLEEP);
    HLOOK(0);
  }
}
__device__ __forceinline__ void heads_up_time(u64 all, u32x2 v) {
  SPINLOOP for (unsigned spins = 0; spins < 64; ++spins)
    if (!(all & __builtin_amdgcn_ballot_w64((int)((u32)__builtin_amdgcn_s_memrealtime() - v[0]) < 0))) break;
}
__device__ __forceinline__ void announce(__amdgpu_buffer_rsrc_t rs, u32 off, u32 a_max, u32 a_cnt, u32 n, u32 tb, u32 d, u32 tg) {
  u32 tk = 0, mx;
  const u32 f = (u32)__builtin_amdgcn_s_memrealtime() - tb + d;
  asm volatile("s_mov_b64 exec, 1\n\tds_max_u32 %2, %3\n\tds_add_rtn_u32 %0, %4, %5\n\tds_read_b32 %1, %2\n\ts_mov_b64 exec, -1\n\ts_waitcnt lgkmcnt(0)"
               : "+v"(tk), "=&v"(mx) : "v"(a_max), "v"(f), "v"(a_cnt), "v"(1u) : "memory");
  const u32 lane = threadIdx.x & 63;
  __builtin_amdgcn_raw_buffer_store_b64(u32x2{__builtin_amdgcn_readfirstlane(mx) + tb, tg}, rs,
                                        (lane < 16 && __builtin_amdgcn_readfirstlane(tk) == n - 1) ? off + lane * 8 : OOB, 0, 16);
}

__device__ __forceinline__ void st_g22(__amdgpu_buffer_rsrc_t rs, u32 off, f32x4 v, u32 tg) {
  const u32 a0 = f2u(v[0]) + 0x200u, a1 = f2u(v[1]) + 0x200u, a2 = f2u(v[2]) + 0x200u, a3 = f2u(v[3]) + 0x200u;
  const u32x4 w = {(a0 >> 10) | ((a1 << 12) & 0xffc00000u), (a1 >> 20) | (tg << 12), (a2 >> 10) | ((a3 << 12) & 0xffc00000u), (a3 >> 20) | (tg << 12)};
  __builtin_amdgcn_raw_buffer_store_b128(w, rs, off, 0, 16);
}
__device__ __forceinline__ f32x4 un_g22(u32x4 w) {
  return f32x4{u2f(w[0] << 10), u2f(__builtin_amdgcn_alignbit(w[1], w[0], 22) << 10), u2f(w[2] << 10), u2f(__builtin_amdgcn_alignbit(w[3], w[2], 22) << 10)};
}
template <int N, int SLEEP>
__device__ __forceinline__ void gather22(__amdgpu_buffer_rsrc_t rs, u32 (&offs)[N], u32x4 (&v)[N], u32 tg, u32 a_inc = 0) {
#pragma unroll
  for (int i = 0; i < N; ++i) v[i] = u32x4{0u, 0u, 0u, 0u};
  SPINLOOP for (unsigned spins = 0; spins < SPIN_CAP; ++spins) {
    u32x4 g[N];
#pragma unroll
    for (int i = 0; i < N; ++i) g[i] = ld_gran(rs, offs[i]);
    if (a_inc) asm volatile("s_mov_b64 exec, 1\n\tds_add_u32 %0, %1\n\ts_mov_b64 exec, -1" :: "v"(a_inc), "v"(1u) : "memory");
    bool ok = true;
#pragma unroll
    for (int i = 0; i < N; ++i) {
      const bool hit = (offs[i] != OOB) & (((g[i][1] ^ (tg << 12)) | (g[i][3] ^ (tg << 12))) < 4096u);
#pragma unroll
      for (int k = 0; k < 4; ++k) v[i][k] = hit ? g[i][k] : v[i][k];
      offs[i] = hit ? OOB : offs[i];
      ok &= (offs[i] == OOB);
    }
    if (__all(ok)) break;
    __builtin_amdgcn_s_sleep(SLEEP);
  }
}

__device__ __forceinline__ int sel_lt(int a, int b, int x, int y) {
  int r;
  asm("s_cmp_lt_i32 %1, %2\n\ts_cselect_b32 %0, %3, %4" : "=s"(r) : "s"(a), "s"(b), "s"(x), "s"(y) : "scc");
  return r;
}
__device__ __forceinline__ u32 next_tag(u32 ep) { const u32 t = ep + 1u; return (t & 0xfffffu) ? t : t + 1u; }

__device__ __forceinline__ void lds_arrive(u32 addr, int lane) {
  asm volatile("s_waitcnt lgkmcnt(0)" ::: "memory");
  if (lane == 0) asm volatile("ds_add_u32 %0, %1" :: "v"(addr), "v"(1u) : "memory");
}
__device__ __forceinline__ u32 lds_ticket(u32 addr, int lane) {
  u32 r = 0;
  asm volatile("s_waitcnt lgkmcnt(0)" ::: "memory");
  if (lane == 0) asm volatile("ds_add_rtn_u32 %0, %1, %2\n\ts_waitcnt lgkmcnt(0)" : "=v"(r) : "v"(addr), "v"(1u) : "memory");
  return __builtin_amdgcn_readfirstlane(r);
}
template <int SLEEP>
__device__ __forceinline__ void lds_wait(u32 addr, u32 target) {
  SPINLOOP for (unsigned spins = 0; spins < (1u << 22); ++spins) {
    u32 v;
    asm volatile("ds_read_b32 %0, %1\n\ts_waitcnt lgkmcnt(0)" : "=v"(v) : "v"(addr) : "memory");
    if (v >= target) break;
    __builtin_amdgcn_s_sleep(SLEEP);
  }
}
__device__ __forceinline__ void dma16(const void* gsrc, u32 lds_dst) {
  u32 keep;
  asm volatile("s_mov_b32 %0, m0\n\ts_mov_b32 m0, %2\n\ts_nop 0\n\tglobal_load_lds_dwordx4 %1, off\n\ts_mov_b32 m0, %0"
               : "=&s"(keep) : "v"(gsrc), "s"(lds_dst) : "memory");
}
__device__ __forceinline__ void dma16nt(const void* gsrc, u32 lds_dst) {
  u32 keep;
  asm volatile("s_mov_b32 %0, m0\n\ts_mov_b32 m0, %2\n\ts_nop 0\n\tglobal_load_lds_dwordx4 %1, off nt\n\ts_mov_b32 m0, %0"
               : "=&s"(keep) : "v"(gsrc), "s"(lds_dst) : "memory");
}
__device__ __forceinline__ u32x4 ldasm(const void* p) {
  u32x4 v;
  asm volatile("global_load_dwordx4 %0, %1, off" : "=v"(v) : "v"(p) : "memory");
  return v;
}
__device__ __forceinline__ u32 ldasm_sc1(const void* p) {
  u32 v;
  asm volatile("global_load_dword %0, %1, off sc1" : "=v"(v) : "v"(p) : "memory");
  return v;
}
__device__ __forceinline__ u32x4 ldw(const bf16* p) { return *reinterpret_cast<const u32x4*>(p); }
template <int D2>
__device__ __forceinline__ s16x8 wfrag(u32 addr) {
  const s16x4 lo = __builtin_amdgcn_ds_read_tr16_b64_v4i16((lds_s4p)(uintptr_t)addr);
  const s16x4 hi = __builtin_amdgcn_ds_read_tr16_b64_v4i16((lds_s4p)(uintptr_t)(addr + D2));
  return s16x8{lo[0], lo[1], lo[2], lo[3], hi[0], hi[1], hi[2], hi[3]};
}
#define MFMA(w_, x_, c_) __builtin_amdgcn_mfma_f32_16x16x32_bf16(__builtin_bit_cast(bf16x8v, w_), __builtin_bit_cast(bf16x8v, x_), c_, 0, 0, 0)

struct Desc { const bf16* p; u32 bytes, flags; };
struct Args {
  Desc wq, x, wmod, c;
  unsigned char* ws;
  const bf16 *kc, *vc, *wo, *w1, *b1, *w2, *b2;
  bf16* out;
};

static_assert(offsetof(Args, ws) == 64 && offsetof(Args, out) == 128, "argument offsets");

constexpr int L_W = 0;
constexpr int L_WM2 = 40960;
constexpr int L_XA = 81920;
constexpr int XS = 1312, XH = 704;
constexpr int L_RED = L_XA + 16384;
constexpr int L_MOD = L_RED + 8192;
constexpr int L_XR = L_MOD + 5120;
constexpr int L_PT = L_XR + 5120;
constexpr int L_ACC = L_PT + 2048;
constexpr int L_QS = L_ACC + 4096;
constexpr int L_FI = L_QS + 256;
constexpr int L_ST = L_FI + 256;
constexpr int L_CNT = L_ST + 256;
constexpr int L_END = L_CNT + 128;
static_assert(L_END <= 160 * 1024, "LDS");
constexpr u32 C_Q = L_CNT, C_SC = L_CNT + 4, C_ISS = L_CNT + 8, C_P1 = L_CNT + 20, C_P2 = L_CNT + 24, C_GO = L_CNT + 28, C_W = L_CNT + 12, C_FIN = L_CNT + 32,
              C_KI = L_CNT + 36, C_PP = L_CNT + 40, C_PUB = L_CNT + 56, C_HT = L_CNT + 64, C_MF = L_CNT + 68, C_RL = L_CNT + 72, C_DL = L_CNT + 76;

constexpr int NL = 32;
constexpr int PVLAG = P_LAG;
constexpr int NPRE = P_NPRE;
constexpr int NVE = P_NVE;
static_assert(PVLAG <= 16 && (PVLAG & 1) == 0 && NVE <= PVLAG && (NVE & 1) == 0, "row groups");

__global__ void __launch_bounds__(NT, 2) mega_kernel(Args a) {
  __shared__ __attribute__((aligned(1024))) unsigned char lds[L_END];
  float* red = reinterpret_cast<float*>(lds + L_RED);
  u32* xraw = reinterpret_cast<u32*>(lds + L_XR);
  u32* pT = reinterpret_cast<u32*>(lds + L_PT);
  float* accs = reinterpret_cast<float*>(lds + L_ACC);
  u32* qs = reinterpret_cast<u32*>(lds + L_QS);
  float* st = reinterpret_cast<float*>(lds + L_ST);
  u32* cnt = reinterpret_cast<u32*>(lds + L_CNT);

  u32x4 rw, rx;
  unsigned char* wsq;
  const void* kcq;
  asm volatile("s_load_dwordx4 %0, %4, %5\n\ts_load_dwordx4 %1, %4, %5 offset:16\n\ts_load_dwordx2 %2, %4, 64\n\ts_load_dwordx2 %3, %4, 72"
               : "=&s"(rw), "=&s"(rx), "=&s"(wsq), "=&s"(kcq) : "s"(__builtin_amdgcn_kernarg_segment_ptr()), "s"((blockIdx.x & 1) * 32));
  __builtin_amdgcn_sched_barrier(0);
  const int t = threadIdx.x, lane = t & 63;
  const int wave = __builtin_amdgcn_readfirstlane(t >> 6);
  const int u = blockIdx.x;
  const int bh = u >> 1, half = u & 1, b_att = bh >> 5, h_att = bh & 31;
  const int r = lane >> 4, c = lane & 15;
  STAMP_DECL
  STAMPT(0, 0);
  u32 tg;
  const int g = u >> 1, ksp = g & 1, cbp = g >> 1;
#define fav (half || (g >= 120 && g < 126))
  const int s128e = (((((lane & 7) >> 1) ^ ((lane >> 4) & 1)) << 1) | (lane & 1)) * 8;
  const u32 tr128 = (8 * r + (c >> 2)) * 128 + ((((c >> 3) & 1) | ((r & 1) << 1)) * 32) + (c & 3) * 8;
  if (wave < 4) {
    __builtin_amdgcn_s_setprio(3);
    const u32 rb = 8192 - 512 * half;
    const u32 hm = 0u - (u32)half;
    const u32 vo = ((u32)(lane >> 3) * rb + (((lane & 7) ^ ((lane >> 3) & 2)) ^ ((wave & 1) * 4)) * 16) | ((u32)(119 - g) & hm & OOB);
    u32 so = (u32)(ksp * 320 + 8 * wave) * rb + cbp * 128;
    const u32 step = 32 * rb, dstep = 4096, dst = L_W + wave * 1024;
    const u32 oe = wave * 1280 + lane * 16, oo = (r * 1280 + ksp * 640 + (c + 16 * wave) * 16) | ((u32)(39 - c - 16 * wave) & OOB);
    const u32 oa = (oo & hm) | (oe & ~hm), ob = (wave * 1280 + 1024 + c * 16) | (hm & OOB), oc = oe | (hm & OOB), zero = 0;
    u32 ep, keep;
    u32x4 xv[4];
    static_assert(G_TK + 1024 <= G_OUT, "37 x 4 KB");
    const u32 pre = ((u32)u << 31) & (u32)(g - 37) & (u32)(2 - wave), po = G_WO + g * 4096 + lane * 16;
    const u32x4 emp = {EMPTY, EMPTY, EMPTY, EMPTY};
#define DMA1 "buffer_load_dwordx4 %7, %8, %1 offen nt lds\n\t"
#define DMAN "s_add_u32 m0, m0, %11\n\ts_add_u32 %1, %1, %10\n\t" DMA1
    asm volatile("s_mov_b32 %0, m0\n\ts_mov_b32 m0, %9\n\t"
                 "s_cmp_gt_i32 %18, 1\n\ts_waitcnt lgkmcnt(0)\n\ts_cbranch_scc1 1f\n\t"
                 "global_load_dword %2, %12, %13 sc1\n\t"
                 "buffer_load_dwordx4 %3, %14, %17, 0 offen\n\tbuffer_load_dwordx4 %4, %15, %17, 0 offen\n\t"
                 "buffer_load_dwordx4 %5, %16, %17, 0 offen offset:2560\n\tbuffer_load_dwordx4 %6, %15, %17, 0 offen offset:2560\n"
                 "1:\n\t" DMA1 DMAN DMAN DMAN DMAN DMAN DMAN DMAN DMAN DMAN "s_mov_b32 m0, %0\n\t"
                 "s_cmp_eq_u32 %19, 0\n\ts_cbranch_scc1 2f\n\tglobal_store_dwordx4 %20, %21, %13 sc1\n\t"
                 "global_store_dwordx4 %20, %21, %13 offset:1024 sc1\n\tglobal_store_dwordx4 %20, %21, %13 offset:2048 sc1\n\t"
                 "global_store_dwordx4 %20, %21, %13 offset:3072 sc1\n2:\n\t"
#define B0W "s_barrier\n\ts_memrealtime vcc\n\ts_waitcnt vmcnt(10) lgkmcnt(0)\n\ts_sub_u32 %0, vcc_lo, " STR(AOFF)
                 B0W
                 : "=&s"(keep), "+s"(so), "=&v"(ep), "=&v"(xv[0]), "=&v"(xv[1]), "=&v"(xv[2]), "=&v"(xv[3])
                 : "v"(vo), "s"(rw), "s"(dst), "s"(step), "s"(dstep), "v"(zero), "s"(wsq), "v"(oa), "v"(ob), "v"(oc), "s"(rx), "s"(wave - half),
                   "s"(pre), "v"(po), "v"(emp) : "memory", "scc", "vcc");
#undef DMAN
#undef DMA1
    asm volatile("" : "+s"(wsq));
    const __amdgpu_buffer_rsrc_t rs = __builtin_amdgcn_make_buffer_rsrc(wsq, 0, WS_END, 0x00020000);
    if (half) {
      if (wave < 3) {
        u32x4 sv;
#pragma unroll
        for (int k = 0; k < 4; ++k) {
          const float c0 = lo16(xv[0][k]), c1 = hi16(xv[0][k]);
          sv[k] = cvt2(c0 * frcp(1.f + fexp(-c0)), c1 * frcp(1.f + fexp(-c1)));
        }
        if (c + 16 * wave < 40) *reinterpret_cast<u32x4*>(lds + L_XA + r * XH + (c + 16 * wave) * 16) = sv;
        if (t == 0) { cnt[4] = next_tag(ep); cnt[15] = keep; }
        lds_arrive(C_P1, lane);
        STAMPT(2, 0);
      }
    } else {
      if (wave < 2) {
#pragma unroll
        for (int i = 0; i < 2; ++i) {
          const int b = wave + 2 * i;
          float xf[16];
#pragma unroll
          for (int k = 0; k < 4; ++k) { xf[2 * k] = lo16(xv[2 * i][k]); xf[2 * k + 1] = hi16(xv[2 * i][k]); xf[8 + 2 * k] = lo16(xv[2 * i + 1][k]); xf[8 + 2 * k + 1] = hi16(xv[2 * i + 1][k]); }
          float s = 0.f, s2 = 0.f;
#pragma unroll
          for (int k = 0; k < 8; ++k) { s += xf[k]; s2 += xf[8 + k]; }
          if (lane < 16) s += s2;
          const float mean = wave_sum(s) * (1.f / DM);
          float v = 0.f, v2 = 0.f;
#pragma unroll
          for (int k = 0; k < 8; ++k) { const float d = xf[k] - mean; v += d * d; const float d2 = xf[8 + k] - mean; v2 += d2 * d2; }
          if (lane < 16) v += v2;
          v = wave_sum(v);
          if (lane == 0) { st[24 + 2 * b] = bfr(mean); st[24 + 2 * b + 1] = frcp(bfr(fsqrt(bfr(bfr(v * (1.f / DM)) + 1e-5f)))); }
          *reinterpret_cast<u32x4*>(xraw + b * 320 + lane * 4) = xv[2 * i];
          if (lane < 16) *reinterpret_cast<u32x4*>(xraw + b * 320 + 256 + lane * 4) = xv[2 * i + 1];
        }
        if (t == 0) { cnt[4] = next_tag(ep); cnt[15] = keep; }
        lds_arrive(C_P1, lane);
      }
      lds_wait<1>(C_P1, 2);
      STAMPT(20, 0);
      tg = __builtin_amdgcn_readfirstlane(cnt[4]);
      if (CT0 && u < 8 && t == 192) { const u32 t0 = cnt[15]; st_gran(rs, G_T + u * 16, t0, t0, tg); }
      if (wave < 3) {
        const int b = LAY == 2 ? t & 3 : (t >> 2) & 3, j = LAY == 2 ? t >> 2 : (t >> 4) * 4 + (t & 3);
        u32 offs[8];
        u32x4 gm[8];
#pragma unroll
        for (int i = 0; i < 8; ++i) offs[i] = t < 160 ? G_MOD + GIDX(1024, i & 1, b, ((i >> 1) & 1) * 160 + (i >> 2) * 40 + ksp * 80 + j) : OOB;
        const float mean = st[24 + 2 * b], sd = st[24 + 2 * b + 1];
        float nv[8];
#pragma unroll
        for (int e = 0; e < 2; ++e) {
          const u32x2 xw = *reinterpret_cast<const u32x2*>(xraw + b * 320 + ksp * 160 + e * 80 + 2 * j);
#pragma unroll
          for (int k = 0; k < 4; ++k) {
            const float xf = (k & 1) ? hi16(xw[k >> 1]) : lo16(xw[k >> 1]);
            nv[4 * e + k] = bfr(bfr(xf - mean) * sd);
            asm volatile("" : "+v"(nv[4 * e + k]));
          }
        }
        {
          const u32 tt = __builtin_amdgcn_readfirstlane(cnt[15]) + GT;
          SPINLOOP while ((int)((u32)__builtin_amdgcn_s_memrealtime() - tt) < 0) __builtin_amdgcn_s_sleep(1);
        }
        STAMPT(14, 0);
        gather22<8, 4>(rs, offs, gm, tg, GOPOS == 2 ? C_GO : 0);
        STAMPT(8, 0);
#pragma unroll
        for (int e = 0; e < 2; ++e) {
          const f32x4 sa = un_g22(gm[4 * e]) + un_g22(gm[4 * e + 1]);
          const f32x4 ca = un_g22(gm[4 * e + 2]) + un_g22(gm[4 * e + 3]);
          const u32 s01 = cvt2(sa[0], sa[1]), s23 = cvt2(sa[2], sa[3]), c01 = cvt2(ca[0], ca[1]), c23 = cvt2(ca[2], ca[3]);
          const u32 d01 = cvt2(1.f + lo16(c01), 1.f + hi16(c01)), d23 = cvt2(1.f + lo16(c23), 1.f + hi16(c23));
          const u32 p01 = cvt2(nv[4 * e] * lo16(d01), nv[4 * e + 1] * hi16(d01)), p23 = cvt2(nv[4 * e + 2] * lo16(d23), nv[4 * e + 3] * hi16(d23));
          const float hv[4] = {lo16(p01) + lo16(s01), hi16(p01) + hi16(s01), lo16(p23) + lo16(s23), hi16(p23) + hi16(s23)};
          if (t < 160) *reinterpret_cast<u32x2*>(lds + L_XA + b * XH + (e * 40 + j) * 8) = u32x2{cvt2(hv[0], hv[1]), cvt2(hv[2], hv[3])};
        }
      }
    }
    VM0();
    STAMPT(5, 128);
    lds_arrive(C_P2, lane);
    lds_wait<1>(C_P2, 4);
    STAMPT(7, 0);
    tg = __builtin_amdgcn_readfirstlane(cnt[4]);
    {
      f32x4 acc = {0.f, 0.f, 0.f, 0.f}, acc2 = acc;
#pragma unroll
      for (int s = 0; s < 10; s += 2) {
        acc = MFMA(wfrag<512>(L_W + (tr128 ^ (wave * 32)) + s * 4096), *reinterpret_cast<const u32x4*>(lds + L_XA + (c & 3) * XH + s * 64 + r * 16), acc);
        acc2 = MFMA(wfrag<512>(L_W + (tr128 ^ (wave * 32)) + s * 4096 + 4096), *reinterpret_cast<const u32x4*>(lds + L_XA + (c & 3) * XH + s * 64 + 64 + r * 16), acc2);
      }
      if (c < 4 && u < 240 + 16 - 16 * half) st_g22(rs, G_Q + half * (G_MOD - G_Q) + GIDX(1024, ksp, c, cbp * 16 + wave * 4 + r), acc + acc2, tg);
    }
    if (CT0 && half && u < 8 && t == 192) { const u32 t0 = cnt[15]; st_gran(rs, G_T + u * 16, t0, t0, tg); }
    STAMPT(9, 0);
  } else {
    __builtin_amdgcn_s_setprio(2);
    {
      const u32 co = min((u32)(u >> 3) * 1024u + (u32)((t >> 5) & 7) * 128u + 768u, (u32)CLEN);
      u64 pc;
      asm volatile("s_getpc_b64 %0\n\tglobal_load_dword v128, %1, %0" : "=&s"(pc) : "v"(co) : "memory", "v128");
    }
    if (t < 256 + 32) cnt[lane] = 0;
    const int rot = __builtin_amdgcn_readfirstlane((u >> 3) + ((t >> 6) & 3) * PHW);
    const u32 ko = (u32)(bh * SK + (SK - WIN)) * 256u + (u32)(half * 8 + wave) * (NL * 1024u) + lane * 16;
    STAMPT(30, 448);
#define LK4(a, b, c, d, o) "global_load_dwordx4 " a ", %" #o ", %[k] nt\n\tglobal_load_dwordx4 " b ", %" #o ", %[k] offset:1024 nt\n\t" \
                           "global_load_dwordx4 " c ", %" #o ", %[k] offset:2048 nt\n\tglobal_load_dwordx4 " d ", %" #o ", %[k] offset:3072 nt\n\t"
    asm volatile("s_waitcnt lgkmcnt(0)\n\ts_barrier\n\t"
                 LK4("v[128:131]", "v[132:135]", "v[136:139]", "v[140:143]", 0) LK4("v[144:147]", "v[148:151]", "v[152:155]", "v[156:159]", 1)
                 LK4("v[160:163]", "v[164:167]", "v[168:171]", "v[172:175]", 2)
                 :: "v"(ko + (rot & 7) * 4096), "v"(ko + ((rot + 1) & 7) * 4096),
                   "v"(ko + ((rot + 2) & 7) * 4096),
                   [k] "s"(kcq) : "memory", "v128", "v129", "v130", "v131", "v132", "v133", "v134", "v135", "v136", "v137", "v138", "v139", "v140", "v141", "v142", "v143", "v144", "v145", "v146", "v147", "v148", "v149", "v150", "v151", "v152", "v153", "v154", "v155", "v156", "v157", "v158", "v159", "v160", "v161", "v162", "v163", "v164", "v165", "v166", "v167", "v168", "v169", "v170", "v171", "v172", "v173", "v174", "v175", "v176", "v177", "v178", "v179", "v180", "v181", "v182", "v183", "v184", "v185", "v186", "v187", "v188", "v189", "v190", "v191");
#undef LK4
    STAMPT(31, 448);
  }
  u32 kvoff = (u32)(bh * SK + (SK - WIN)) * 256u + (u32)(half * 8 + wave) * (NL * 1024u) + lane * 16, ph[8];
  const int rot2 = __builtin_amdgcn_readfirstlane((u >> 3) + ((t >> 6) & 3) * PHW);
#pragma unroll
  for (int i = 0; i < 8; ++i) ph[i] = ((i + rot2) & 7) * 4096;
  asm volatile("" : "+v"(kvoff));
  asm volatile("s_waitcnt lgkmcnt(0)" : "+s"(wsq), "+s"(ph[0]), "+s"(ph[1]), "+s"(ph[2]), "+s"(ph[3]), "+s"(ph[4]), "+s"(ph[5]), "+s"(ph[6]), "+s"(ph[7])
               : "s"(rw), "s"(rx), "s"(kcq));
  const __amdgpu_buffer_rsrc_t rs = __builtin_amdgcn_make_buffer_rsrc(wsq, 0, WS_END, 0x00020000);
  u32 rw2;
#define BLKA(ia_) (((c & 3) * 32 + (((ia_) & 3) + (((ia_) >> 2) & 1) * 16 + (wave & 3) * 4)) * 2 + ((c >> 2) & 1))
#define RD_ROLE() { u32 v_; asm volatile("ds_read_b32 %0, %1\n\ts_waitcnt lgkmcnt(0)" : "=v"(v_) : "v"((u32)C_RL) : "memory"); rw2 = __builtin_amdgcn_readfirstlane(v_); }
#define WAIT_ROLE() SPINLOOP for (unsigned spins = 0; spins < (1u << 22); ++spins) { RD_ROLE() if ((int)rw2 < 0) break; __builtin_amdgcn_s_sleep(2); }
#define HEADA(hh) ((ksA & 3) + (ksA >> 2) * 16 + (hh) * 4)
  u32x4 gmf[5], ml;
  u32 tkf, a_pub;
  f32x4 psc, pw4[4];
  u32x2 xw;
  {
    u32x4 kv[NL], kh[16];
    const __amdgpu_buffer_rsrc_t rk = __builtin_amdgcn_make_buffer_rsrc((void*)a.kc, 0, 1u << 28, 0x00020000);
    const __amdgpu_buffer_rsrc_t rv = __builtin_amdgcn_make_buffer_rsrc((void*)a.vc, 0, 1u << 28, 0x00020000);
#define LDKV(rsrc, off, i) __builtin_amdgcn_raw_buffer_load_b128(rsrc, (off) + ((i) & 3) * 1024, ph[(i) >> 2], 2)
#define LDS_INC(addr) do { asm volatile("" ::: "memory"); if (lane == 0) asm volatile("ds_add_u32 %0, %1" :: "v"(addr), "v"(1u) : "memory"); } while (0)
#define TR_STEP(x, HALF, CTRL, BIT) _Pragma("unroll") for (int hh = 0; hh < (HALF); ++hh) { \
    const float lo_ = x[hh], hi_ = x[hh + (HALF)]; const float t1_ = lo_ + dpp<CTRL>(lo_), t2_ = hi_ + dpp<CTRL>(hi_); \
    x[hh] = (c & (BIT)) ? t2_ : t1_; }
#define SCORE16(x, src, o) float x[16]; \
    _Pragma("unroll") for (int i = 0; i < 16; ++i) { float d = 0.f; _Pragma("unroll") for (int k = 0; k < 4; ++k) d = dot2(src[(o) + i][k], qpk[k], d); x[i] = d; } \
    TR_STEP(x, 8, XOR8, 8) TR_STEP(x, 4, XOR7, 4) TR_STEP(x, 2, XOR2, 2)
#define SCORE16P(x, src, o, pa_, pb_) float x[16]; \
    _Pragma("unroll") for (int i = 0; i < 16; ++i) { float d = 0.f; _Pragma("unroll") for (int k = 0; k < 4; ++k) d = dot2(src[(o) + i][k], qpk[k], d); x[i] = d; } \
    TR_STEP(x, 8, XOR8, 8) PADX(pa_, pb_, x[0]); TR_STEP(x, 4, XOR7, 4) TR_STEP(x, 2, XOR2, 2)
#define SCALED(v) bfr(bfr(v) * 0.08838834764831845f)
#define PV2(i) _Pragma("unroll") for (int k = 0; k < 4; ++k) { \
      acc[2 * k] = dot2(__builtin_amdgcn_perm(kv[(i) + 1][k], kv[i][k], 0x05040100u), pw[(i) / 2], acc[2 * k]); \
      acc[2 * k + 1] = dot2(__builtin_amdgcn_perm(kv[(i) + 1][k], kv[i][k], 0x07060302u), pw[(i) / 2], acc[2 * k + 1]); }
#define GET_Q() lds_wait<2>(C_Q, 1); u32x4 qpk; \
    asm volatile("ds_read_b128 %0, %2\n\tds_read_b32 %1, %3\n\ts_waitcnt lgkmcnt(0)" : "=&v"(qpk), "=&v"(tg) : "v"((u32)(L_QS + 16 * c)), "v"((u32)(L_CNT + 16)) : "memory"); \
    tg = __builtin_amdgcn_readfirstlane(tg);
#define GET_P() _Pragma("unroll") for (int i4 = 0; i4 < 4; ++i4) { \
      const u32x4 pv = *reinterpret_cast<const u32x4*>(pT + (wave * 4 + r) * 16 + i4 * 4); \
      _Pragma("unroll") for (int j = 0; j < 4; ++j) pw[i4 * 4 + j] = pv[j]; }
    auto gather_q = [&]() __attribute__((always_inline)) {
        u32 offs[1] = {G_Q + GIDX(1024, lane >> 5, b_att, h_att * 32 + (lane & 31))};
        u32x4 gq[1];
        {
          gq[0] = ld_gran(rs, offs[0]);
          const u32x4 gt = ld_gran(rs, lane < 8 ? G_T + lane * 16 : OOB);
          LDS_INC(C_KI);
          {
            const u32 own = cnt[15];
            const bool ok = lane < 8 && gt[1] == tg;
            float d = ok ? __builtin_amdgcn_fmed3f((float)(int)(gt[0] - own + TOFF), -400.f, 400.f) : -500.f;
            d = fmaxf(d, dpp<XOR1>(d)); d = fmaxf(d, dpp<XOR2>(d)); d = fmaxf(d, dpp<XOR7>(d));
            if (lane == 0 && d > -450.f) cnt[15] = own + (u32)(int)d;
          }
          const bool hit = (((gq[0][1] ^ (tg << 12)) | (gq[0][3] ^ (tg << 12))) < 4096u);
          if (!__all(hit)) gather22<1, 4>(rs, offs, gq, tg);
        }
        STAMPW(10);
        f32x4 q4 = un_g22(gq[0]);
#pragma unroll
        for (int k = 0; k < 4; ++k) q4[k] = swap32_add(q4[k], q4[k]);
        if (lane < 32) *reinterpret_cast<u32x2*>(qs + 2 * lane) = u32x2{cvt2(q4[0], q4[1]), cvt2(q4[2], q4[3])};
        lds_arrive(C_Q, lane);
    };
    const int qw = half ? QODD : QEVEN;
    const u32 tau = fav ? TAU_L : TAU_E;
#define PACE(n) if ((n) > PB) { const u32 tt_ = t0 + (((n) - PB) * tau >> 4); \
      SPINLOOP while ((int)((u32)__builtin_amdgcn_s_memrealtime() - tt_) < 0) __builtin_amdgcn_s_sleep(1); asm volatile("" ::: "memory"); }
#define PACE2(n) { u32 v_, d_; \
      asm volatile("1:\n\tds_read_b32 %0, %2\n\ts_memrealtime vcc\n\ts_waitcnt lgkmcnt(0)\n\tv_readfirstlane_b32 %1, %0\n\ts_add_u32 %1, %1, vcc_lo\n\t" \
                   "s_sub_u32 %1, %1, %3\n\ts_cmp_ge_i32 %1, %4\n\ts_cbranch_scc1 2f\n\ts_sleep 1\n\ts_branch 1b\n2:" \
                   : "=&v"(v_), "=&s"(d_) : "v"(a_dl), "s"(t0), "i"(((n) * TAU_E >> 4) - POFF) : "memory", "scc", "vcc"); }
    float acc[8];
#pragma unroll
    for (int k = 0; k < 8; ++k) acc[k] = 0.f;
    u32 pw[16];
    if (__builtin_expect(wave >= 4, 1)) {
      asm volatile("" : "={v[128:131]}"(kv[0]), "={v[132:135]}"(kv[1]), "={v[136:139]}"(kv[2]), "={v[140:143]}"(kv[3]),
                        "={v[144:147]}"(kv[4]), "={v[148:151]}"(kv[5]), "={v[152:155]}"(kv[6]), "={v[156:159]}"(kv[7])
                        , "={v[160:163]}"(kv[8]), "={v[164:167]}"(kv[9]), "={v[168:171]}"(kv[10]), "={v[172:175]}"(kv[11])
                   );
      u32 t0 = 0;
      const u32 tk0 = KP ? (u32)__builtin_amdgcn_s_memrealtime() : 0u, ktau = half ? KTO : KTE;
      STAMPT(24, 448);
      const u32 kvoff2 = kvoff ^ 131072u;
#pragma unroll
      for (int i = NK0; i < 48; ++i) {
        if (i == NPRE) { STAMPT(12, 448); STAMPT(13, 256); }
        if (i == NPRE && half == 0) lds_wait<2>(C_GO, GOPOS ? 3 : 4);
        if (KP && i >= KP && (i & 3) == 0) { const u32 tt_ = tk0 + (i * ktau >> 4);
          SPINLOOP while ((int)((u32)__builtin_amdgcn_s_memrealtime() - tt_) < 0) __builtin_amdgcn_s_sleep(1); asm volatile("" ::: "memory"); }
        if (i < NL) kv[i < NL ? i : 0] = LDKV(rk, kvoff, i & 31); else kh[i < NL ? 0 : i - NL] = LDKV(rk, kvoff2, i & 31);
      }
      if (!QPRE || wave != qw) LDS_INC(C_KI);
#pragma unroll
      for (int i = 0; i < NK0; i += 4) asm volatile("s_waitcnt vmcnt(%4)" : "+v"(kv[i]), "+v"(kv[i + 1]), "+v"(kv[i + 2]), "+v"(kv[i + 3]) : "i"(48 - NK0) : "memory");
      STAMPT(25, 448);
      if ((QODD >= 4 || QEVEN >= 4) && wave == qw) {
        __builtin_amdgcn_s_setprio(3);
        lds_wait<2>(C_P1, 2 + half);
        tg = __builtin_amdgcn_readfirstlane(cnt[4]);
        gather_q();
        __builtin_amdgcn_s_setprio(2);
      }
      GET_Q()
      SCORE16(xa, kv, 0)
      asm volatile("" : "+v"(xa[0]), "+v"(xa[1]), "+v"(kvoff) :: "memory");
      PAD(e1, l2);
#pragma unroll
      for (int i = 0; i < P_NVA; ++i) kv[i] = LDKV(rv, kvoff, i);
      asm volatile("" ::: "memory");
      SCORE16(xb, kv, 16)
      xa[2] = xb[0]; xa[3] = xb[1];
      TR_STEP(xa, 2, XOR1, 1)
      float s0 = SCALED(xa[0]), s1 = SCALED(xa[1]);
      asm volatile("" : "+v"(s0), "+v"(s1), "+v"(kvoff) :: "memory");
#pragma unroll
      for (int i = P_NVA; i < NVE; ++i) kv[i] = LDKV(rv, kvoff, i);
      asm volatile("" ::: "memory");
      SCORE16P(xh, kh, 0, e2, l3)
      const float s2 = SCALED(xh[0] + dpp<XOR1>(xh[0])), s3 = SCALED(xh[1] + dpp<XOR1>(xh[1]));
      const float m = wave_max(fmaxf(fmaxf(s0, s1), fmaxf(s2, s3)));
      const float p0 = __expf(s0 - m), p1 = __expf(s1 - m), p2 = __expf(s2 - m), p3 = __expf(s3 - m);
      const float l = wave_sum(fmaf(p2 + p3, 0.5f, p0 + p1));
      if (lane == 0) { st[wave] = m; st[8 + wave] = l; }
      pT[(wave * 4 + r) * 16 + (c & 1) * 8 + (c >> 1)] = cvt2(p0, p1);
      pT[((wave - 4) * 4 + r) * 16 + (c >> 1)] = cvt2(p2, p3);
      lds_arrive(C_PP + 4 * (wave - 4), lane);
      __builtin_amdgcn_s_setprio(0);
      STAMPT(27, 448); WSTAMP(1);
      if (CT0) t0 = __builtin_amdgcn_readfirstlane(cnt[15]);
      GET_P()
      u32 a_dl = C_DL;
      asm volatile("" : "+v"(a_dl));
#pragma unroll
      for (int i = NVE; i < NL; i += 2) {
        if (i == E3POS) PAD(e3, l4);
        if (i == E4POS) PAD(e4, t1);
        u32 om0;
        if (i == MLP) { RD_ROLE() om0 = (rw2 & 0x80000003u) == 0x80000000u && lane < 8 ? G_ML + BLKA(rw2 >> 8) * 64 : OOB; asm volatile("" : "+v"(om0)); }
        PACE2(96 + i)
        if (i == MLP) ml = ld_gran(rs, om0);
        kv[i] = LDKV(rv, kvoff, i);
        kv[i + 1] = LDKV(rv, kvoff, i + 1);
        asm volatile("" ::: "memory");
        if (i == NL - 2) { LDS_INC(C_ISS); STAMPT(26, 448); }
        if (i >= PVLAG) {
          PV2(i >= PVLAG ? i - PVLAG : 0)
#pragma unroll
          for (int k = 0; k < 8; ++k) asm volatile("" : "+v"(acc[k]));
        }
      }
    } else {
      const int ex = (g >= 120 && g < 126) ? EC : 0;
      u32 kva = (!half && ex > 2) ? OOB : kvoff, kvb = (!half && ex > 0) ? OOB : kvoff;
      const u32 kxa = (half && ex > 2) ? kvoff - 8 * NL * 1024u : OOB, kxb = (half && ex > 0) ? kvoff - 8 * NL * 1024u : OOB;
      u32x4 kx[4];
      const u32 t0 = __builtin_amdgcn_readfirstlane(cnt[15]);
#pragma unroll
      for (int i = 0; i < 16; ++i) {
        if (PK && (i & 3) == 0) PACE(48 + i)
        kh[i] = LDKV(rk, i < 12 ? kvoff : i < 14 ? kva : kvb, 16 + i);
      }
#pragma unroll
      for (int i = 0; i < 4; ++i) kx[i] = LDKV(rk, i < 2 ? kxa : kxb, 28 + i);
      LDS_INC(C_KI);
      if (wave == 0) {
        if (half == 0 && g >= 80 && g < 120) {
          const int fb = g - 80;
          u32 of2[2];
          u32x4 gm[2];
#pragma unroll
          for (int ks = 0; ks < 2; ++ks) of2[ks] = G_MOD + GIDX(1024, ks, r, 320 + fb * 16 + c);
          gather22<2, 4>(rs, of2, gm, tg);
          const f32x4 mv = un_g22(gm[0]) + un_g22(gm[1]);
          st_gran(rs, G_MF + (((fb / 10) * 4 + r) * 160 + (fb % 10) * 16 + c) * 16, cvt2(mv[0], mv[1]), cvt2(mv[2], mv[3]), tg);
        }
      }
      __builtin_amdgcn_s_setprio(0);
      if (t == 0 && fav) cnt[19] = DL0;
      STAMPT(2, 0);
#define GO(r_, n, name) "s_cmp_eq_u32 %" #r_ ", " #n "\n\ts_cbranch_scc1 .Lpad_" #name "\n\t"
      asm volatile("s_mov_b64 exec, 0\n\t" GO(0, 0, e1) GO(0, 1, e2) GO(0, 2, e3) GO(0, 3, l1)
                   ".Lpad_role:\n\t" GO(0, 0, c1) GO(0, 1, c0) GO(0, 2, b3) GO(0, 3, b0)
                   ".Lpad_back:\n\ts_mov_b64 exec, -1" :: "s"(wave) : "scc");
#undef GO
      STAMPT(7, 0);
      lds_wait<2>(C_KI, P_GATE);
      if (QHOLD && wave == qw - 4) lds_wait<2>(C_Q, 1);
      asm volatile("" : "+v"(kvoff), "+v"(kva), "+v"(kvb));
#pragma unroll
      for (int i = 0; i < NL; ++i) {
        if (i == L1POS) PAD(l1, e4);
        if (PL && (i & 3) == 0) PACE(64 + PLO + i * PLS / 16)
        kv[i] = LDKV(rv, i < 28 ? kvoff : i < 30 ? kva : kvb, i);
      }
      STAMPT(1, 0);
      GET_Q()
      const bool fixc = half == 0 && (g < 40 || (FXA && fav));
      const u32 tka = t == 0 ? G_TK + CLS * 128 : OOB;
      u32 tkv = __builtin_amdgcn_raw_ptr_buffer_atomic_add_i32(1, rs, half ? tka : OOB, 0, 16);
      asm volatile("" ::: "memory");
      float xx[4];
#pragma unroll
      for (int i = 0; i < 4; ++i) { float d = 0.f;
#pragma unroll
        for (int k = 0; k < 4; ++k) d = dot2(kx[i][k], qpk[k], d);
        xx[i] = d; }
      asm volatile("" : "+v"(xx[0]), "+v"(xx[1]), "+v"(xx[2]), "+v"(xx[3]) :: "memory");
#pragma unroll
      for (int i = 0; i < 4; ++i) kx[i] = LDKV(rv, i < 2 ? kxa : kxb, 28 + i);
      LDS_INC(C_ISS);
      TR_STEP(xx, 2, XOR8, 8)
#pragma unroll
      for (int i = 0; i < 2; ++i) { xx[i] += dpp<XOR7>(xx[i]); xx[i] += dpp<XOR2>(xx[i]); xx[i] += dpp<XOR1>(xx[i]); }
      SCORE16P(xh, kh, 0, l2, t2)
      const float ninf = -__builtin_inff();
      const bool mine = half || c < 16 - ex, extra = half && c >= 16 - 4 * ex;
      xh[0] += dpp<XOR1>(xh[0]); xh[1] += dpp<XOR1>(xh[1]);
      const float s2 = mine ? SCALED(xh[0]) : ninf, s3 = mine ? SCALED(xh[1]) : ninf;
      const float s4 = extra ? SCALED(xx[0]) : ninf, s5 = extra ? SCALED(xx[1]) : ninf;
      float m = wave_max(fmaxf(fmaxf(s2, s3), fmaxf(s4, s5)));
      lds_wait<2>(C_PP + 4 * wave, 1);
      const float me = st[wave + 4];
      m = fmaxf(m, me);
      const float p2 = __expf(s2 - m), p3 = __expf(s3 - m), p4 = __expf(s4 - m), p5 = __expf(s5 - m);
      const float l = wave_sum(fmaf(p4 + p5, 0.25f, p2 + p3)) * 0.5f;
      const u32 pxo = cvt2(p4, p5), pxt = dppu<XOR8>(pxo);
      const u32 px[2] = {c < 8 ? pxo : pxt, c < 8 ? pxt : pxo};
      if (lane == 0) { st[wave] = m; st[8 + wave] = l; }
      pT[(wave * 4 + r) * 16 + 8 + (c >> 1)] = cvt2(p2, p3);
      STAMPT(4, 0); WSTAMP(1);
      PAD(l3, t3);
      if (lds_ticket(C_SC, lane) == 3) {
        const float mw = st[lane & 7], lw = st[8 + (lane & 7)];
        float M = fmaxf(mw, dpp<XOR1>(mw)); M = fmaxf(M, dpp<XOR2>(M)); M = fmaxf(M, dpp<XOR7>(M));
        const float e = __expf(mw - M);
        float L = lw * e;
        L += dpp<XOR1>(L); L += dpp<XOR2>(L); L += dpp<XOR7>(L);
        if (lane == 0) st_gran(rs, G_ML + u * 64, f2u(M), f2u(L), tg);
        if (lane < 8) st[16 + lane] = e * frcp(L);
      }
      bool sub = false;
      if (wave == 0 && !fav) {
        const u32 tt = __builtin_amdgcn_readfirstlane(cnt[15]) + (126 * TAU_E >> 4) - EVT;
        sub = (int)((u32)__builtin_amdgcn_s_memrealtime() - tt) < EVW;
        SPINLOOP while ((int)((u32)__builtin_amdgcn_s_memrealtime() - tt) < 0) __builtin_amdgcn_s_sleep(1);
      }
      asm volatile("" ::: "memory");
      const u32 tkw = __builtin_amdgcn_raw_ptr_buffer_atomic_add_i32(fixc ? 0 : 0x10001, rs, sub && (SLS || !fixc) ? tka : OOB, 0, 16);
      asm volatile("" ::: "memory");
      asm("" : "=v"(ml));
      GET_P()
#pragma unroll
      for (int i = 0; i < 16; i += 2) { if (i == L4POS) PADA(l4, a0); PV2(i) }
      const float f = __expf(me - m);
#pragma unroll
      for (int k = 0; k < 8; ++k) acc[k] *= f;
#pragma unroll
      for (int i = 16; i < NL - PVLAG; i += 2) { PV2(i) }
#pragma unroll
      for (int i = 0; i < 4; i += 2)
#pragma unroll
        for (int k = 0; k < 4; ++k) {
          acc[2 * k] = dot2(__builtin_amdgcn_perm(kx[i + 1][k], kx[i][k], 0x05040100u), px[i / 2], acc[2 * k]);
          acc[2 * k + 1] = dot2(__builtin_amdgcn_perm(kx[i + 1][k], kx[i][k], 0x07060302u), px[i / 2], acc[2 * k + 1]);
        }
      asm volatile("" : "+v"(acc[0]), "+v"(acc[1]), "+v"(acc[2]), "+v"(acc[3]), "+v"(acc[4]), "+v"(acc[5]), "+v"(acc[6]), "+v"(acc[7]) :: "memory");
      rw2 = 0;
      if (wave == 0) {
        constexpr int NT2 = FXA ? 128 : 134;
        const u32 tks = __builtin_amdgcn_readfirstlane(tkv + tkw + 1u);
        const int n0 = (tks & 0xffff) * NCL + CLS;
        const int slow = sub ? -min(max(SLF - (int)((tks & 0xffff) - (tks >> 16)), 0) * SLS, SLM) : 0;
        const int n = fixc ? (g < 40 ? 134 + g : g + 8) : (fav || sub) ? sel_lt(n0, NT2, n0, 174) : 174;
        constexpr int N0 = 54;
        const int rl = sel_lt(n, N0, 1, sel_lt(n, 134, 0, sel_lt(n, 174, 2, 3)));
        int un = n - sel_lt(n, N0, 0, sel_lt(n, 134, N0, 134));
        if (NCL == 8 && rl == 1 && un < 48) un = (un & 48) | (un & 7) << 1 | (un >> 3 & 1);
        if (NCL == 4 && rl == 1 && un < 48) un = (un & 56) | (un & 3) << 1 | (un >> 2 & 1);
        if (lane == 0) *reinterpret_cast<u32x2*>(lds + C_RL) = u32x2{0x80000000u | (u32)(un & 255) << 8 | (u32)rl, (u32)sel_lt(n, N0, DLB, sel_lt(n, 134, fav ? DLA : DLS, slow))};
        STAMPT(28, 0);
      }
    }
#pragma unroll
    for (int i = NL - PVLAG; i <= NL - HX; i += 2) { if (i == T1POS) PADA(t1, role); PV2(i) }
    u32 a_aw = L_ACC + (wave * 128 + c * 8 + 2 * r) * 4, a_as = L_ST + 64 + (lane >> 5) * 16, a_ar = L_ACC + (lane >> 5) * 2048 + (lane & 31) * 8, one = 1;
    tkf = C_FIN;
    a_pub = G_ATT + u * 256 + lane * 4;
    asm volatile("" : "+v"(acc[0]), "+v"(acc[1]), "+v"(acc[2]), "+v"(acc[3]), "+v"(acc[4]), "+v"(acc[5]), "+v"(acc[6]), "+v"(acc[7]),
                      "+v"(a_aw), "+v"(a_as), "+v"(a_ar), "+v"(one), "+v"(tkf), "+v"(a_pub));
    if (!HT) { PV2(NL - HX + 2) } else {
      u32 tk = 0;
      u64 tn;
      asm volatile("s_mov_b64 exec, 1\n\tds_add_rtn_u32 %0, %10, %11\n\ts_mov_b64 exec, -1\n\ts_memrealtime %1"
                   : "+v"(tk), "=s"(tn), "+v"(acc[0]), "+v"(acc[1]), "+v"(acc[2]), "+v"(acc[3]), "+v"(acc[4]), "+v"(acc[5]), "+v"(acc[6]), "+v"(acc[7])
                   : "v"((u32)C_HT), "v"(1u) : "memory");
      PV2(NL - HX + 2)
      asm volatile("s_waitcnt lgkmcnt(0)" : "+v"(tk), "+s"(tn), "+v"(acc[0]), "+v"(acc[1]), "+v"(acc[2]), "+v"(acc[3]), "+v"(acc[4]), "+v"(acc[5]), "+v"(acc[6]), "+v"(acc[7]));
      __builtin_amdgcn_raw_buffer_store_b64(u32x2{(u32)tn + HTD, tg}, rs, tk == 7 ? G_HT + u * 128 : OOB, 0, 16);
      asm volatile("" ::: "memory");
    }
#pragma unroll
    for (int i = NL - HX + 4; i < NL - 2; i += 2) { PV2(i) }
    PV2(NL - 2)
#undef PV2
#undef TR_STEP
#undef SCORE16
#undef SCORE16P
#undef SCALED
#undef GET_Q
#undef GET_P
#undef LDKV
#pragma unroll
    for (int i = 0; i < 4; ++i) acc[i] = swap32_add(acc[i], acc[i + 4]);
#pragma unroll
    for (int i = 0; i < 2; ++i) acc[i] = swap16_add(acc[i], acc[i + 2]);
    asm volatile("ds_write_b64 %6, %7\n\ts_mov_b64 exec, 1\n\tds_add_rtn_u32 %0, %0, %8\n\ts_mov_b64 exec, -1\n\tds_read_b128 %1, %9\n\t"
                 "ds_read2_b64 %2, %10 offset1:32\n\tds_read2_b64 %3, %10 offset0:64 offset1:96\n\t"
                 "ds_read2_b64 %4, %10 offset0:128 offset1:160\n\tds_read2_b64 %5, %10 offset0:192 offset1:224\n\ts_waitcnt lgkmcnt(0)"
                 : "+v"(tkf), "=&v"(psc), "=&v"(pw4[0]), "=&v"(pw4[1]), "=&v"(pw4[2]), "=&v"(pw4[3])
                 : "v"(a_aw), "v"(f32x2{acc[0], acc[1]}), "v"(one), "v"(a_as), "v"(a_ar) : "memory");
    STAMPT(6, 0); WSTAMP(2);
  }
  const u32x4 rsv = {(u32)(uintptr_t)wsq, (u32)((uintptr_t)wsq >> 32) & 0xffffu, WS_END, 0x00020000u};
#define MLOOK(W_) asm volatile(LK(0, 1, "0", 0, 2) LKEND : "+v"(ml) : "v"(om), "s"(m2), [rs] "s"(rsv), [w] "i"(W_) : "memory");
  if (__builtin_amdgcn_readfirstlane(tkf) == 7) {
    PAD(t2, role);
    const f32x4 o = (psc[0] * pw4[0] + psc[1] * pw4[1]) + (psc[2] * pw4[2] + psc[3] * pw4[3]);
    __builtin_amdgcn_raw_buffer_store_b32(min(cvt2(swap32_add(o[0], o[2]), swap32_add(o[1], o[3])), EMPTY - 1), rs, a_pub, 0, 16);
    LDS_INC(C_PUB);
    STAMPW(11);
  }
  if ((int)rw2 >= 0) WAIT_ROLE()
  const int role = rw2 & 3, ia = (rw2 >> 8) & 255, jB = ia;
  const int ksA = ia & 7, cbA = ia >> 3;
  const u32 blkA = BLKA(ia), om = G_ML + blkA * 64;
  float b1v;
  u32x4 gaq;
  if (role == 0 && wave < 4) {
    if (RPRI) __builtin_amdgcn_s_setprio(RPRI);
    {
      const u32x4 rxv = {(u32)(uintptr_t)a.x.p, (u32)((uintptr_t)a.x.p >> 32) & 0xffffu, NB * DM * 2, 0x00020000u};
      asm volatile("buffer_load_dwordx4 %0, %2, %4, 0 offen sc1\n\tbuffer_load_dwordx2 %1, %3, %5, 0 offen" : "=&v"(gaq), "=&v"(xw)
                   : "v"(G_MF + (r * 160 + cbA * 16 + c) * 16), "v"(ksA == 0 ? (r * DM + cbA * 64 + c * 4) * 2 : OOB), "s"(rsv), "s"(rxv) : "memory");
    }
    if (half) STAMPT(8, 0);
    lds_wait<2>(C_ISS, 8);
    {
      const bf16* wp = a.wo + (size_t)(HEADA(wave) * 128 + (lane >> 3)) * DM + cbA * 64;
#pragma unroll
      for (int i = 0; i < 16; ++i) {
        if (i == 1) PAD(a0, role);
        if (i == N1A) {
          lds_wait<2>(C_PUB, 1);
          asm volatile("" ::: "memory");
        }
        dma16(wp + (size_t)(8 * i) * DM + (s128e ^ ((i & 1) * 32)), L_W + wave * 16384 + i * 1024);
      }
      LDS_INC(C_ISS);
    }
    STAMPT(16, 0);
  }
#define SELECTOR(sel) u32x4 sel; \
    _Pragma("unroll") for (int d = 0; d < 4; ++d) sel[d] = ((c & 2) == 0 && d == (c >> 2)) ? ((c & 1) ? 0x3f800000u : 0x00003f80u) : 0u;
#define PAIRSUM(sm) (((c & 8) ? sm[1] : sm[0]) + dpp<XOR8>((c & 8) ? sm[0] : sm[1]))
  const u32 tb = __builtin_amdgcn_readfirstlane(cnt[15]);
  if (role == 0) {
    PAD(a1, back);
    if (wave < 4) {
      u32 a_sum = L_RED + lane * 16;
      u32 a_out = wave < NREP ? G_WO + wave * WO_REP + ((cbA * 4 + (c >> 2)) * 8 + ksA) * 128 + r * 32 + (c & 3) * 8 : OOB;
      asm volatile("s_waitcnt vmcnt(4)" : "+v"(a_sum), "+v"(a_out), "+v"(gaq), "+v"(xw) :: "memory");
      LDS_INC(C_PP + 4 * wave);
      if (!__all(gaq[1] == tg && gaq[3] == tg)) {
        u32 offs[1] = {G_MF + (r * 160 + cbA * 16 + c) * 16};
        u32x4 gm[1];
        gather<1>(rs, offs, gm, tg);
        gaq = gm[0];
      }
      float ga4[4] = {lo16(gaq[0]), hi16(gaq[0]), lo16(gaq[2]), hi16(gaq[2])}, x4[4] = {lo16(xw[0]), hi16(xw[0]), lo16(xw[1]), hi16(xw[1])};
      asm volatile("" : "+v"(ga4[0]), "+v"(ga4[1]), "+v"(ga4[2]), "+v"(ga4[3]), "+v"(x4[0]), "+v"(x4[1]), "+v"(x4[2]), "+v"(x4[3]));
      STAMPT(19, 0);
      u32 a_fl = lane < 16 ? G_HA + ia * 128 + lane * 8 : OOB;
      PAD(a2, back);
      asm volatile("s_waitcnt vmcnt(0)" : "+v"(a_fl) :: "memory");
      LDS_INC(C_PP + 4 * wave);
      STAMPT(23, 0);
      if (wave == 0) {
        lds_wait<0>(C_W, 4);
        STAMPT(12, 0);
        __builtin_amdgcn_raw_buffer_store_b64(u32x2{(u32)__builtin_amdgcn_s_memrealtime() + HAG, tg}, rs, a_fl, 0, 16);
      }
      BAR();
      STAMPT(21, 0);
      f32x4 pp[4];
#pragma unroll
      for (int w = 0; w < 4; ++w) pp[w] = *reinterpret_cast<const f32x4*>(lds + a_sum + w * 1024);
      asm volatile("" : "+v"(pp[0]), "+v"(pp[1]), "+v"(pp[2]), "+v"(pp[3]));
      const f32x4 s = (pp[0] + pp[1]) + (pp[2] + pp[3]);
      const u32x2 o = {min(cvt2(fmaf(s[0], ga4[0], x4[0]), fmaf(s[1], ga4[1], x4[1])), EMPTY - 1), min(cvt2(fmaf(s[2], ga4[2], x4[2]), fmaf(s[3], ga4[3], x4[3])), EMPTY - 1)};
      __builtin_amdgcn_raw_buffer_store_b64(o, rs, a_out, 0, 16);
    } else {
      u32 oa = G_ATT + blkA * 256 + (c >> 3) * 64 + r * 16;
      u32 a_red = L_RED + (((wave & 3) * 4 + c) * 64 + r * 4) * 4;
      u32 a_w[4];
#pragma unroll
      for (int nt = 0; nt < 4; ++nt) a_w[nt] = L_W + (wave & 3) * 16384 + (tr128 ^ (nt * 32));
      u32x4 av[2];
      u64 m0 = ~0ull, m1 = ~0ull, m2 = 0xffull;
      av[0] = av[1] = u32x4{0u, 0u, 0u, 0u};
      asm volatile("" : "+v"(oa), "+v"(ml), "+v"(av[0]), "+v"(av[1]), "+v"(a_red), "+v"(a_w[0]), "+v"(a_w[1]), "+v"(a_w[2]), "+v"(a_w[3]) :: "memory");
      SPINLOOP for (unsigned spins = 0; spins < SPIN_CAP; ++spins) {
        m2 &= __builtin_amdgcn_ballot_w64(ml[1] != tg) | __builtin_amdgcn_ballot_w64(ml[3] != tg);
        if (!m2) break;
        if (!spins) FAIL(4);
        __builtin_amdgcn_s_sleep(ASL);
        MLOOK(0)
      }
      float wsc;
      {
        const float M = u2f(ml[0]), L = u2f(ml[2]), Mo = dpp<XOR7>(dpp<0x1B>(M)), Lo = dpp<XOR7>(dpp<0x1B>(L));
        const float Mx = fmaxf(M, Mo), le = L * __expf(M - Mx);
        wsc = le * frcp(fmaf(Lo, __expf(Mo - Mx), le));
        wsc = u2f((u32)__builtin_amdgcn_ds_bpermute((c & 7) * 4, (int)f2u(wsc)));
        asm volatile("" : "+v"(wsc));
      }
#define ALOOK(W_) \
        asm volatile(LK(0, 2, "0", 0, 3) LK(1, 2, "0", 128, 4) LKEND \
                     : "+v"(av[0]), "+v"(av[1]) : "v"(oa), "s"(m0), "s"(m1), [rs] "s"(rsv), [w] "i"(W_) : "memory");
#define ACHK() \
        m0 = __builtin_amdgcn_ballot_w64(missing(av[0])); \
        m1 = __builtin_amdgcn_ballot_w64(missing(av[1]));
      lds_wait<1>(C_PP + 4 * (wave - 4), 2);
      if (AGT) SPINLOOP for (int k = 0; k < 64; ++k) if ((int)((u32)__builtin_amdgcn_s_memrealtime() - tb) >= (126 * TAU_E >> 4) + AGT) break;
      STAMPT(29, 448);
      ALOOK(63)
      s16x8 wf[4][4];
#pragma unroll
      for (int s = 0; s < 3; ++s)
#pragma unroll
        for (int nt = 0; nt < 4; ++nt) wf[s][nt] = wfrag<512>(a_w[nt] + s * 4096);
      lds_wait<1>(C_PP + 4 * (wave - 4), 3);
#pragma unroll
      for (int nt = 0; nt < 4; ++nt) wf[3][nt] = wfrag<512>(a_w[nt] + 3 * 4096);
      u32 a_cw = C_W, one = 1;
      u64 mw = 0x000f000f000f000full;
      asm volatile("" : "+v"(wf[3][0]), "+v"(wf[3][1]), "+v"(wf[3][2]), "+v"(wf[3][3]), "+v"(a_cw), "+v"(one), "+s"(mw));
      PAD(a3, b4);
      asm volatile("s_waitcnt vmcnt(0)" : "+v"(av[0]), "+v"(av[1]) :: "memory");
      ACHK()
      STAMPT(18, 448);
      if (m0 | m1) {
        FAIL(1);
        SPINLOOP for (unsigned spins = 0; spins < SPIN_CAP; ++spins) {
          ALOOK(2 * (PDA - 1))
          ACHK()
          if (!(m0 | m1)) break;
          __builtin_amdgcn_s_sleep(ASL);
        }
      }
#undef ALOOK
#undef ACHK
      asm volatile("" : "+v"(av[0]), "+v"(av[1]));
      f32x4 acc[4], acb[4];
      const f32x4 z = {0.f, 0.f, 0.f, 0.f};
#define SB() __builtin_amdgcn_sched_barrier(0)
#define PIN(x) asm volatile("" : "+v"(x)); SB();
#define M1(n) { acc[n] = MFMA(wf[0][n], av[0], z); PIN(acc[n]) acb[n] = MFMA(wf[1][n], av[0], z); PIN(acb[n]) }
#define M2A(n) { acb[n] = MFMA(wf[3][n], av[1], acb[n]); PIN(acb[n]) }
#define M2B(n) { acc[n] = MFMA(wf[2][n], av[1], acc[n]); PIN(acc[n]) }
#define D8(n, k) { float v = acc[n][k] + dpp<XOR8>(acb[n][k]); PIN(v) acc[n][k] = v; v = acc[n][k + 1] + dpp<XOR8>(acb[n][k + 1]); PIN(v) acc[n][k + 1] = v; }
#define MW(n) { acc[n] *= wsc; PIN(acc[n]) }
#define D4(n) { _Pragma("unroll") for (int k = 0; k < 4; ++k) { float v = acc[n][k] + dpp<SHL4>(acc[n][k]); PIN(v) acc[n][k] = v; } }
#define WR(n) asm volatile("s_mov_b64 exec, %3\n\tds_write_b128 %0, %1 offset:" STR(n * 64) "\n\tds_write_b128 %0, %2 offset:" STR(n * 64 + 64) "\n\ts_mov_b64 exec, -1" \
                           :: "v"(a_red), "v"(acc[n]), "v"(acc[n + 1]), "s"(mw) : "memory")
      SB();
      M1(0)
      asm volatile("s_mov_b64 exec, 1\n\tds_add_u32 %0, %1\n\ts_mov_b64 exec, -1" :: "v"(a_cw), "v"(one) : "memory");
      SB();
      M1(1) M2A(0) M2B(0) M1(2) M2A(1) M2B(1) M1(3)
      M2A(2) D8(0, 0) M2B(2) D8(0, 2) M2A(3) D8(1, 0) M2B(3) D8(1, 2)
      MW(0) MW(1) D4(0) D4(1) D8(2, 0) D8(2, 2)
      WR(0);
      SB();
      D8(3, 0) D8(3, 2) MW(2) MW(3) D4(2) D4(3)
      WR(2);
#undef M1
#undef M2A
#undef M2B
#undef D8
#undef MW
#undef D4
#undef WR
      STAMPT(22, 448);
      BAR();
      asm volatile("" :: "v"(av[0]), "v"(av[1]));
    }
    STAMPT(13, 0);

  } else if (role == 1) {
    PAD(b0, bd);
    u32x2 bw2;
    asm("" : "=v"(bw2));
    if (wave < 4) {
      if (RPRI) __builtin_amdgcn_s_setprio(RPRI);
#pragma unroll
      for (int i = 0; i < 5; ++i) gmf[i] = __builtin_amdgcn_raw_buffer_load_b128(rs, G_MF + (640 + t) * 16, i * 4096, 16);
      const __amdgpu_buffer_rsrc_t rb = __builtin_amdgcn_make_buffer_rsrc((void*)a.b1, 0, FF * 2, 0x00020000);
      bw2 = __builtin_amdgcn_raw_buffer_load_b64(rb, (jB * 32 + (t & 28)) * 2, 0, 0);
      asm volatile("" ::: "memory");
    }
    const int lb = (c >> 1) & 3, lc = (c & 1) * 8 + 2 * r;
    const int fc = lane & 31, hw = lane >> 5;
    SELECTOR(sel)
    constexpr int L_R2 = L_MOD, L_S2 = L_R2 + 4096;
    const u32 gw = G_WO + wave * 5120;
    u32 go = gw + (c >> 3) * 1024 + r * 128 + (c & 7) * 16, go4 = gw + 4096 + lane * 16;
    u32 a_u = L_XA + wave * 640 + lb * 160 + ((c >> 3) * 16 + lc) * 2, a_u4 = L_XA + wave * 640 + lb * 160 + (64 + lc + (c >> 3)) * 2;
    u32 a_av = L_XA + wave * 640 + (c & 3) * 160 + r * 16, a_ah = a_av + 128 - r * 8;
    u32 a_red = L_R2 + ((wave * 4 + c) * 32 + r * 4) * 4;
    u32 a_st = L_S2 + ((lb * 2 + (c >> 3)) * 8 + wave) * 4;
    u32 a_fin = L_R2 + ((hw * 16 + (wave & 3)) * 32 + fc) * 4;
    u32 a_fst = L_S2 + (wave & 3) * 64;
    u32 a_out = c < 4 ? G_OUT + (wave * 5 * 54 + jB) * 128 + c * 32 + r * 8 : OOB;
    constexpr int L_F2 = L_XR;
    u32 a_fi = BM ? L_F2 + c * 64 + r * 16 : L_FI + (c & 3) * 64 + r * 16, a_fo = (BM ? L_F2 + 768 : L_FI) + ((wave & 3) * 32 + fc) * 2;
    u32 a_ou4 = G_OUT + ((wave * 5 + (c >> 2)) * 54 + jB) * 128 + (c & 3) * 32 + r * 8;
    asm volatile("" : "+v"(a_ou4));
    u32 a_pre = lane < 40 ? G_OUT + ((wave * 5 + (lane >> 3)) * 54 + jB) * 128 + (lane & 7) * 16 : OOB;
    u32x4 emp = {EMPTY, EMPTY, EMPTY, EMPTY};
    asm volatile("" : "+v"(a_pre), "+v"(emp));
#define OPRE() asm volatile("buffer_store_dwordx4 %0, %1, %2, 0 offen sc1\n\ts_nop 1" :: "v"(emp), "v"(a_pre), "s"(rsv) : "memory")
    if (OPOS == 0) OPRE();
    u32 a_w1 = L_W + wave * 5120 + (8 * r + (c >> 2)) * 64 + (r & 1) * 32 + (c & 3) * 8;
    u32 a_wh = L_W + wave * 5120 + 4096 + (4 * r + (c >> 2)) * 64 + (r >> 1) * 32 + (c & 3) * 8;
    u32 a_w2[5];
#pragma unroll
    for (int k = 0; k < 5; ++k) { const int n2 = wave * 5 + k; a_w2[k] = L_WM2 + (n2 >> 2) * 4096 + (tr128 ^ ((n2 & 3) * 32)); }
    asm volatile("" : "+v"(sel), "+v"(go), "+v"(go4), "+v"(a_u), "+v"(a_u4), "+v"(a_av), "+v"(a_ah), "+v"(a_red), "+v"(a_st), "+v"(a_fin), "+v"(a_fst), "+v"(a_out));
    asm volatile("" : "+v"(a_fi), "+v"(a_fo), "+v"(a_w1), "+v"(a_wh), "+v"(a_w2[0]), "+v"(a_w2[1]), "+v"(a_w2[2]), "+v"(a_w2[3]), "+v"(a_w2[4]) :: "memory");
    if (BM) *reinterpret_cast<u32*>(lds + L_F2 + t * 4) = 0;
    PAD(b4, back);
    asm volatile("" : "+v"(bw2));
    if (wave < 4) {
      STAMPT(7, 0);
      bool ok = true;
#pragma unroll
      for (int i = 0; i < 5; ++i) ok &= (gmf[i][1] == tg) & (gmf[i][3] == tg);
      if (!__all(ok)) {
        u32 offs[5];
#pragma unroll
        for (int i = 0; i < 5; ++i) offs[i] = G_MF + (640 + t + 256 * i) * 16;
        gather<5>(rs, offs, gmf, tg);
      }
#pragma unroll
      for (int i = 0; i < 5; ++i) {
        if (i == 2) PAD(t3, role);
        const int it = t + 256 * i, row = it / 160, gi = it % 160;
        u32 w0 = gmf[i][0], w1 = gmf[i][2];
        if (row >= 4) { w0 = cvt2(1.f + lo16(w0), 1.f + hi16(w0)); w1 = cvt2(1.f + lo16(w1), 1.f + hi16(w1)); }
        *reinterpret_cast<u32x2*>(lds + L_XA + (row < 4 ? row + 8 : row) * XS + gi * 8) = u32x2{w0, w1};
      }
      const u32 bw = (t & 2) ? bw2[1] : bw2[0];
      b1v = (t & 1) ? hi16(bw) : lo16(bw);
      asm volatile("" : "+v"(b1v));
      lds_arrive(C_MF, lane);
    }
    lds_wait<1>(C_MF, 4);
    float c1f[5];
    {
      const unsigned char* cp = lds + L_XA + (4 + lb) * XS + wave * 160;
#pragma unroll
      for (int i = 0; i < 2; ++i) { const u32 w = *reinterpret_cast<const u32*>(cp + (32 * i + (c >> 3) * 16 + lc) * 2); c1f[2 * i] = lo16(w); c1f[2 * i + 1] = hi16(w); }
      c1f[4] = u2f((u32)*reinterpret_cast<const unsigned short*>(cp + (64 + lc + (c >> 3)) * 2) << 16);
    }
    const unsigned char* pm = lds + L_XA + (4 + (c & 7)) * XS + wave * 160;
    u32x4 mv0 = *reinterpret_cast<const u32x4*>(pm + r * 16), mv1 = *reinterpret_cast<const u32x4*>(pm + 64 + r * 16);
    s16x4 mv2 = *reinterpret_cast<const s16x4*>(pm + 128 + r * 8);
    u32 a_rd = L_RED + ((wave * 8 + c) * 32 + r * 4) * 4;
    asm volatile("" : "+v"(c1f[0]), "+v"(c1f[1]), "+v"(c1f[2]), "+v"(c1f[3]), "+v"(c1f[4]), "+v"(mv0), "+v"(mv1), "+v"(mv2), "+v"(a_rd) :: "memory");
    if (wave < 4) {
      STAMPT(8, 0);
      lds_wait<2>(C_ISS, 8);
      if (BHOLD) SPINLOOP for (int k = 0; k < 256; ++k) { if ((int)((u32)__builtin_amdgcn_s_memrealtime() - tb) >= (126 * TAU_E >> 4) + BHOLD) break; __builtin_amdgcn_s_sleep(2); }
      const int h5 = ROT1 * (jB & 1);
      const bf16* wp = a.w1 + (size_t)(wave * 160 + (lane >> 2)) * FF + jB * 32 + ((((((lane >> 1) & 1) ^ (lane >> 5)) << 1) | (lane & 1)) * 8);
      const bf16* wp2 = a.w2 + (size_t)(jB * 32 + wave * 8 + (lane >> 3)) * DM + (s128e ^ ((wave & 1) * 32));
#pragma unroll
      for (int n = 0; n < 20; ++n) {
        if (n == 0) PAD(bd, back);
        if (n == N1B) lds_wait<2>(C_PUB, 1);
        if (n < 5 || (n >= 5 + W2F && n < 10 + W2F)) {
          const int i2 = n < 5 ? n + h5 : n - W2F - h5;
          dma16(wp + (size_t)(16 * i2) * FF, L_W + (wave * 10 + i2) * 1024);
        } else {
          const int k = n < 5 + W2F ? n - 5 : n - 10;
          dma16(wp2 + k * 64, L_WM2 + (k * 4 + wave) * 1024);
        }
      }
      STAMPT(16, 0); STAMPT(5, 128);
    }
    asm volatile("s_waitcnt vmcnt(%0)" :: "i"(10 - W2F) : "memory");
    STAMPT(23, 0);
    PAD(b1, a1);
    BAR();
    STAMPT(3, 0);
    if (OPOS == 1) OPRE();
    s16x8 wf[2][2], wg[5];
    s16x4 wh[2];
#pragma unroll
    for (int nt = 0; nt < 2; ++nt) {
#pragma unroll
      for (int s = 0; s < 2; ++s) wf[s][nt] = wfrag<256>((a_w1 ^ (nt * 32)) + s * 2048);
      wh[nt] = __builtin_amdgcn_ds_read_tr16_b64_v4i16((lds_s4p)(uintptr_t)(a_wh ^ (nt * 32)));
    }
#define W1MMA(acc) \
      f32x4 acc[2]; \
      _Pragma("unroll") for (int nt = 0; nt < 2; ++nt) { const f32x4 z_ = {0.f, 0.f, 0.f, 0.f}; acc[nt] = MFMA(wf[0][nt], av0, z_); } \
      _Pragma("unroll") for (int nt = 0; nt < 2; ++nt) acc[nt] = MFMA(wf[1][nt], av1, acc[nt]); \
      _Pragma("unroll") for (int nt = 0; nt < 2; ++nt) acc[nt] = __builtin_amdgcn_mfma_f32_16x16x16bf16_1k(wh[nt], av2, acc[nt], 0, 0, 0);
    {
      const u32x4 av0 = mv0, av1 = mv1;
      const s16x4 av2 = mv2;
      W1MMA(acc)
#pragma unroll
      for (int nt = 0; nt < 2; ++nt) if (c < 8) *reinterpret_cast<f32x4*>(lds + a_rd + nt * 64) = acc[nt];
    }
    float Tc = 0.f, Ts = 0.f;
#define TSUM() _Pragma("unroll") for (int k = 0; k < 4; ++k) { Tc += red[((hw * 4 + k) * 8 + (wave & 3)) * 32 + fc]; Ts += red[((hw * 4 + k) * 8 + 4 + (wave & 3)) * 32 + fc]; }
    u32x4 pa[5];
    f32x4 s4, s0, s1;
    u32 a_fl = lane < 16 ? G_HB + jB * 128 + lane * 8 : OOB, tk = C_W;
    u32x2 fv = {1u, tg};
    {
      u64 m[5], old = 0;
#pragma unroll
      for (int i = 0; i < 5; ++i) { m[i] = ~0ull; pa[i] = u32x4{0u, 0u, 0u, 0u}; }
#pragma unroll
      for (int i = 0; i < 5; ++i) asm volatile("" : "+v"(c1f[i]), "+v"(pa[i]));
#define BLOOK(W_) \
        asm volatile(LK(0, 5, "0", 0, 7) LK(1, 5, "0", 512, 8) LK(2, 5, "0", 2048, 9) LK(3, 5, "0", 2560, 10) LK(4, 6, "0", 0, 11) LKEND \
                     : "+v"(pa[0]), "+v"(pa[1]), "+v"(pa[2]), "+v"(pa[3]), "+v"(pa[4]) \
                     : "v"(go), "v"(go4), "s"(m[0]), "s"(m[1]), "s"(m[2]), "s"(m[3]), "s"(m[4]), [rs] "s"(rsv), [w] "i"(W_) : "memory");
      STAMPT(9, 0); STAMPT(30, 448);
      if (BPREW) {
        VM0();
        STAMPT(20, 0); STAMPT(31, 448);
        BAR();
        STAMPT(29, 0);
      }
      SPINLOOP for (int k = 0; k < 256; ++k) if ((int)((u32)__builtin_amdgcn_s_memrealtime() - tb) >= (126 * TAU_E >> 4) + BGT) break;
      STAMPT(21, 0);
      BLOOK(5)
      if (!BPREW) { STAMPT(20, 0); STAMPT(31, 448); }
      if (!BAN) announce(rs, G_HB + jB * 128, C_GO, C_W, 8, tb, HCD, tg);
      if (!BPREW) { BAR(); STAMPT(29, 0); }
#pragma unroll
      for (int k = 0; k < 5; ++k) wg[k] = wfrag<512>(a_w2[k]);
      TSUM()
#pragma unroll
      for (int i = 0; i < 5; ++i) asm volatile("" : "+v"(wg[i]));
      asm volatile("" : "+v"(Tc), "+v"(Ts));
      PAD(b2, a2);
      STAMPT(18, 0);
      asm volatile("s_waitcnt vmcnt(%8)" : "+v"(pa[0]), "+v"(pa[1]), "+v"(pa[2]), "+v"(pa[3]), "+v"(pa[4]), "+v"(a_fl), "+v"(fv), "+v"(tk) : "i"(BAN ? 0 : 1) : "memory");
      STAMPT(22, 0);
#define BSUM() { const f32x4 z = {0.f, 0.f, 0.f, 0.f}; \
      s0 = MFMA(sel, pa[0], z); PIN(s0) s1 = MFMA(sel, pa[2], z); PIN(s1) s0 = MFMA(sel, pa[1], s0); PIN(s0) s1 = MFMA(sel, pa[3], s1); PIN(s1) s4 = MFMA(sel, pa[4], z); PIN(s4) \
      if (BAN) asm volatile("s_memrealtime s[98:99]" ::: "s98", "s99"); SB(); }
#define BNAN() (__builtin_amdgcn_ballot_w64(__builtin_isunordered(s0[0], s0[1])) | __builtin_amdgcn_ballot_w64(__builtin_isunordered(s1[0], s1[1])) | \
                __builtin_amdgcn_ballot_w64(__builtin_isunordered(s4[0], s4[1])))
      BSUM()
      if (BNAN()) {
        FAIL(2);
        bool again = true;
        if (BRT) {
          u32x2 hv;
          const u32 fo = G_HA + ((wave * 5 >> 2) * 8 + lane) * 128;
#pragma unroll
          for (int i = 0; i < 5; ++i) m[i] = __builtin_amdgcn_ballot_w64(missing(pa[i]));
          BLOOK(63)
          heads_up_start(rsv, fo, 0xffffull, tg, hv);
          asm volatile("s_waitcnt vmcnt(1)" : "+v"(pa[0]), "+v"(pa[1]), "+v"(pa[2]), "+v"(pa[3]), "+v"(pa[4]) :: "memory");
          BSUM()
          again = BNAN();
          if (again) {
            FAIL(5);
            heads_up<BSL>(rsv, fo, 0xffffull, tg, hv);
            hv[0] += BXD;
            go += WO_REP; go4 += WO_REP;
            heads_up_time(0xffffull, hv);
          }
          asm volatile("s_waitcnt vmcnt(0)" : "+v"(hv) :: "memory");
        }
        if (again) SPINLOOP for (unsigned spins = 0; spins < SPIN_CAP; ++spins) {
          u64 bad = 0;
#pragma unroll
          for (int i = 0; i < 5; ++i) { m[i] = __builtin_amdgcn_ballot_w64(missing(pa[i])); bad |= m[i]; }
          if (!(bad | old)) break;
          old = bad;
          BLOOK(5 * (PDB2 - 1))
          BSUM()
          if (!BNAN()) break;
        }
      }
    }
#define BFLAG(W_) u32 tl; \
        asm volatile("s_waitcnt lgkmcnt(%3)\n\ts_add_u32 %0, s98, %2" : "=s"(tl), "+v"(tk) : "i"(HBD), "i"(W_) : "scc"); \
        fv[0] = tl; \
        asm volatile("v_readfirstlane_b32 %0, %1\n\ts_cmp_eq_u32 %0, 7\n\ts_cbranch_scc0 1f\n\tbuffer_store_dwordx2 %2, %3, %4, 0 offen sc1\n1:" \
                     : "=&s"(tl) : "v"(tk), "v"(fv), "v"(a_fl), "s"(rsv) : "scc"); \
        SB();
    if (BAN >= 2) asm volatile("s_mov_b64 exec, 1\n\tds_add_rtn_u32 %0, %0, %1\n\ts_mov_b64 exec, -1" : "+v"(tk) : "v"(fv[0]));
    PAD(b3, a3);
    SB();
    STAMPT(14, 0); WSTAMP(3);
    float xf[5];
    {
      const u32 w0 = cvt2(s0[0], s0[1]), w1 = cvt2(s1[0], s1[1]);
      xf[0] = lo16(w0); xf[1] = hi16(w0); xf[2] = lo16(w1); xf[3] = hi16(w1);
      *reinterpret_cast<u32*>(lds + a_u) = cvt2(xf[0] * c1f[0], xf[1] * c1f[1]);
      *reinterpret_cast<u32*>(lds + a_u + 64) = cvt2(xf[2] * c1f[2], xf[3] * c1f[3]);
    }
    STAMPT(19, 0);
    {
      u32x4 av0 = *reinterpret_cast<const u32x4*>(lds + a_av), av1 = *reinterpret_cast<const u32x4*>(lds + a_av + 64);
      SB();
      if (BAN == 2) { BFLAG(3) }
      float e0 = s4[0], e1 = s4[1];
      asm volatile("" : "+v"(e0), "+v"(e1));
      xf[4] = bfr(((c & 8) ? e1 : e0) + dpp<XOR8>((c & 8) ? e0 : e1));
      *reinterpret_cast<unsigned short*>(lds + a_u4) = (unsigned short)cvt2(xf[4] * c1f[4], 0.f);
      s16x4 av2 = *reinterpret_cast<const s16x4*>(lds + a_ah);
      SB();
      if (BAN == 1) asm volatile("s_mov_b64 exec, 1\n\tds_add_rtn_u32 %0, %0, %1\n\ts_mov_b64 exec, -1" : "+v"(tk) : "v"(fv[0]));
      SB();
      if (BAN == 3) { BFLAG(5) }
      const float ps = ((xf[0] + xf[1]) + (xf[2] + xf[3])) + xf[4];
      const float pq = fmaf(xf[0], xf[0], fmaf(xf[1], xf[1], xf[4] * xf[4])) + fmaf(xf[2], xf[2], xf[3] * xf[3]);
      const float t1 = ps + dpp<XOR8>(ps), t2 = pq + dpp<XOR8>(pq);
      float z = (c & 8) ? t2 : t1;
      z += dpp<XOR1>(z);
      asm volatile("" : "+v"(z));
      __builtin_amdgcn_sched_barrier(0);
      W1MMA(acc)
      if (BAN == 1) {
        asm volatile("" : "+v"(acc[0]), "+v"(acc[1]));
        BFLAG(0)
      }
      z = cross_rows_sum(z);
      if ((lane & 49) == 0) *reinterpret_cast<float*>(lds + a_st) = z;
      if (c < 4) {
#pragma unroll
        for (int nt = 0; nt < 2; ++nt) *reinterpret_cast<f32x4*>(lds + a_red + nt * 64) = acc[nt];
      }
    }
#undef W1MMA
    BAR();
    STAMPT(24, 0);
    if (wave < 4) {
      f32x4 sv[4];
      float sp[4];
#pragma unroll
      for (int k = 0; k < 4; ++k) sv[k] = *reinterpret_cast<const f32x4*>(lds + a_fst + k * 16);
#pragma unroll
      for (int k = 0; k < 4; ++k) sp[k] = *reinterpret_cast<const float*>(lds + a_fin + k * 512);
      __builtin_amdgcn_sched_barrier(0);
      const f32x4 s1 = sv[0] + sv[1], s2 = sv[2] + sv[3];
      const float mean = ((s1[0] + s1[1]) + (s1[2] + s1[3])) * (1.f / DM);
      const float var = fmaxf(((s2[0] + s2[1]) + (s2[2] + s2[3])) * (1.f / DM) - mean * mean, 0.f);
      const float rstd = __builtin_amdgcn_rsqf(var + 1e-5f);
      const float zp = fmaf(rstd, fmaf(-mean, Tc, (sp[0] + sp[1]) + (sp[2] + sp[3])), Ts);
      const float hh = bfr(bfr(swap32_add(zp, zp)) + b1v);
      const float e2 = __builtin_amdgcn_exp2f(hh * fmaf(hh * hh, 0.1029432397f, 2.302208198f));
      const float f = fmaf(-hh, frcp(e2 + 1.f), hh);
      *reinterpret_cast<unsigned short*>(lds + a_fo) = (unsigned short)cvt2(f, 0.f);
    }
    BAR();
    if (BM) {
      u32x4 av[4];
#pragma unroll
      for (int k = 0; k < 4; ++k) av[k] = *reinterpret_cast<const u32x4*>(lds + a_fi + (3 - k) * 256);
      SB();
      const f32x4 z = {0.f, 0.f, 0.f, 0.f};
      f32x4 a4 = MFMA(wg[4], av[0], z);
      PIN(a4)
      f32x4 a0 = MFMA(wg[0], av[0], z);
      PIN(a0)
#pragma unroll
      for (int k = 1; k < 4; ++k) { a0 = MFMA(wg[k], av[k], a0); PIN(a0) }
      __builtin_amdgcn_sched_barrier(0);
      const u32x2 o4 = {min(cvt2(a4[0], a4[1]), EMPTY - 1), min(cvt2(a4[2], a4[3]), EMPTY - 1)};
      __builtin_amdgcn_raw_buffer_store_b64(o4, rs, a_out, 4 * (54 * 128), 16);
      const u32x2 o0 = {min(cvt2(a0[0], a0[1]), EMPTY - 1), min(cvt2(a0[2], a0[3]), EMPTY - 1)};
      __builtin_amdgcn_raw_buffer_store_b64(o0, rs, a_ou4, 0, 16);
    } else {
      const u32x4 av = *reinterpret_cast<const u32x4*>(lds + a_fi);
      f32x4 acc[5];
#pragma unroll
      for (int k = 0; k < 5; ++k) { const f32x4 z = {0.f, 0.f, 0.f, 0.f}; acc[k] = MFMA(wg[k], av, z); }
      __builtin_amdgcn_sched_barrier(0);
#pragma unroll
      for (int k = 0; k < 5; ++k) {
        const u32x2 o = {min(cvt2(acc[k][0], acc[k][1]), EMPTY - 1), min(cvt2(acc[k][2], acc[k][3]), EMPTY - 1)};
        __builtin_amdgcn_raw_buffer_store_b64(o, rs, a_out, k * (54 * 128), 16);
      }
    }
    STAMPT(15, 0);
    asm volatile("" :: "v"(pa[0]), "v"(pa[1]), "v"(pa[2]), "v"(pa[3]), "v"(pa[4]));

  } else if (role == 2 && wave == 0) {
    PAD(c0, b1);
    const int lb = (c >> 1) & 3, col = ia * 16 + (c & 1) * 8 + 2 * r + (c >> 3);
    bf16* outp;
    asm volatile("s_load_dwordx2 %0, %1, 128\n\ts_waitcnt lgkmcnt(0)" : "=s"(outp) : "s"(__builtin_amdgcn_kernarg_segment_ptr()));
    const __amdgpu_buffer_rsrc_t ro = __builtin_amdgcn_make_buffer_rsrc(outp, 0, NB * DM * 2, 0x00020000);
#define DUMMY() __builtin_amdgcn_raw_buffer_store_b16((unsigned short)0, ro, (lb * DM + col) * 2, 0, DAUX)
    if (DPOS == 0) DUMMY();
    float b2v = bf2f(a.b2[col]);
    const f32x4 z = {0.f, 0.f, 0.f, 0.f};
    SELECTOR(sel)
    float x1v, gbv;
    {
      u32 offs[1] = {G_MF + ((12 + lb) * 160 + (col >> 2)) * 16};
      u32x4 gm[1];
      gather<1>(rs, offs, gm, tg);
      const u32 gbw = (r & 1) ? gm[0][2] : gm[0][0];
      gbv = (c & 8) ? hi16(gbw) : lo16(gbw);
    }
    u32 oo = lb * DM + col;
    asm volatile("" : "+v"(gbv), "+v"(oo), "+v"(sel), "+v"(b2v));
    u32x4 pa[7];
    u32 go = G_OUT + ia * (54 * 128) + lane * 16, gx = G_WO + ia * 1024 + lane * 16;
#pragma unroll
    for (int i = 0; i < 7; ++i) { pa[i] = u32x4{0u, 0u, 0u, 0u}; asm volatile("" : "+v"(pa[i])); }
    asm volatile("" : "+v"(go), "+v"(gx));
    u32x2 hv;
    if (CPUB) lds_wait<2>(C_PUB, 1);
    if (DPOS == 1) DUMMY();
    heads_up_start(rsv, G_HA + ((ia >> 2) * 8 + lane) * 128, 0xffull, tg, hv);
    heads_up<CSL>(rsv, G_HA + ((ia >> 2) * 8 + lane) * 128, 0xffull, tg, hv);
    if (DPOS == 2) DUMMY();
    if (EPE == 1) __builtin_amdgcn_raw_buffer_store_b32(tg, rs, ia == 0 && lane == 0 ? WS_EPOCH : OOB, 0, 16);
    hv[0] += CXD;
    heads_up_time(0xffull, hv);
    {
      u32x4 gm[1];
      gather_e<1, 2, 1>(rsv, gx, gm, ~0ull);
      const f32x4 sm = MFMA(sel, gm[0], z);
      x1v = bfr(PAIRSUM(sm));
      asm volatile("" : "+v"(x1v));
    }
    STAMPT(23, 0);
    if (DPOS == 3) DUMMY();
    if (EPE == 2) __builtin_amdgcn_raw_buffer_store_b32(tg, rs, ia == 0 && lane == 0 ? WS_EPOCH : OOB, 0, 16);
    PAD(c1, b2);
    if (CGT) SPINLOOP for (int k = 0; k < 256; ++k) if ((int)((u32)__builtin_amdgcn_s_memrealtime() - tb) >= (126 * TAU_E >> 4) + CGT) break;
    heads_up_start(rsv, G_HB + lane * 128, (1ull << 54) - 1, tg, hv);
    heads_up<0>(rsv, G_HB + lane * 128, (1ull << 54) - 1, tg, hv);
    heads_up_time((1ull << 54) - 1, hv);
    STAMPT(14, 0);
    f32x4 s0, s1;
    {
      u64 m[7], old = 0;
#pragma unroll
      for (int i = 0; i < 7; ++i) m[i] = i < 6 ? ~0ull : (1ull << 48) - 1;
      look<63>(rsv, go, pa, m);
#define CW(i, W_) asm volatile("s_waitcnt vmcnt(%1)" : "+v"(pa[i]) : "i"(W_ ? 6 - (i) : 63) : "memory");
#define CSUM(W_) { CW(0, W_) s0 = MFMA(sel, pa[0], z); PIN(s0) CW(1, W_) s1 = MFMA(sel, pa[1], z); PIN(s1) \
      CW(2, W_) s0 = MFMA(sel, pa[2], s0); PIN(s0) CW(3, W_) s1 = MFMA(sel, pa[3], s1); PIN(s1) CW(4, W_) s0 = MFMA(sel, pa[4], s0); PIN(s0) \
      CW(5, W_) s1 = MFMA(sel, pa[5], s1); PIN(s1) CW(6, W_) s0 = MFMA(sel, pa[6], s0); PIN(s0) s0 += s1; }
      CSUM(1)
      if (__builtin_amdgcn_ballot_w64(__builtin_isunordered(s0[0], s0[1]))) {
        FAIL(3);
        SPINLOOP for (unsigned spins = 0; spins < SPIN_CAP; ++spins) {
          u64 bad = 0;
#pragma unroll
          for (int i = 0; i < 7; ++i) { m[i] = __builtin_amdgcn_ballot_w64(missing(pa[i])); bad |= m[i]; }
          if (!(bad | old)) break;
          old = bad;
          look<7 * (PDC - 1)>(rsv, go, pa, m);
          CSUM(0)
          if (!__builtin_amdgcn_ballot_w64(__builtin_isunordered(s0[0], s0[1]))) break;
        }
      }
    }
    STAMPT(16, 0);
    const float y = bfr(bfr(PAIRSUM(s0)) + b2v);
    if (OUTM & 1) __builtin_amdgcn_raw_buffer_store_b16((unsigned short)cvt2(x1v + bfr(gbv * y), 0.f), ro, oo * 2, 0, FAUX);
    else outp[oo] = __float2bfloat16(x1v + bfr(gbv * y));
    STAMPT(17, 0);
    asm volatile("" :: "v"(pa[0]), "v"(pa[1]), "v"(pa[2]), "v"(pa[3]), "v"(pa[4]), "v"(pa[5]), "v"(pa[6]), "v"(hv));

    if (!EPE && ia == 0 && t == 0)
      __hip_atomic_store(reinterpret_cast<u32*>(a.ws + WS_EPOCH), tg, __ATOMIC_RELAXED, __HIP_MEMORY_SCOPE_AGENT);
  }
  STAMP_FLUSH;
}

extern "C" void kernel_launch(void* const* d_in, const int* in_sizes, int n_in, void* d_out, int out_size, void* d_ws,
                              size_t ws_size, hipStream_t stream) {
  Args a;
  a.x = Desc{(const bf16*)d_in[0], NB * DM * 2, 0x00020000u};
  a.kc = (const bf16*)d_in[1];
  a.vc = (const bf16*)d_in[2];
  a.wq = Desc{(const bf16*)d_in[3], DM * QD * 2, 0x00020000u};
  a.wo = (const bf16*)d_in[4];
  a.w1 = (const bf16*)d_in[6];
  a.b1 = (const bf16*)d_in[7];
  a.w2 = (const bf16*)d_in[8];
  a.b2 = (const bf16*)d_in[9];
  a.c = Desc{(const bf16*)d_in[10], NB * DM * 2, 0x00020000u};
  a.wmod = Desc{(const bf16*)d_in[11], DM * MODN * 2, 0x00020000u};
  a.out = (bf16*)d_out;
  a.ws = (unsigned char*)d_ws;
  mega_kernel<<<NWG, NT, 0, stream>>>(a);
}
```

```cpp
#define SPINLOOP _Pragma("nounroll")
#define P_LAG 14
#define P_NPRE 32
#define P_NVE 6
#define P_NVA 6
#define QODD 4
#define QEVEN 4
#define MFPOS 0
#define EA 0
#define EB 0
#define EC 4
#define APOLL 0
#define N1E N1A
#define TAU_A 236
#define TAU_B 239
#define TAU_X TAU_A
#define TAU_E 244
#define PK 0
#define PL 1
#define QPRE 1
#define QHOLD 0
#define CT0 1
#define TOFF 55
#define BLIGHT 1
#define BPRE 0
#define BW1 1
#define BMASK 1
#define CSLEEP 1
#define CSL 2
#define CPUB 1
#define CXD 20
#define OPOS 1
#define NREPL 2
#define OUTPRE 1
#define ASL 1
#define AEARLY 1
#define PDC 4
#define PDA 3
#define PDB 3
#define PSB 1
#define HC 1
#define HCD 132
#define HAG 40
#define AW 4
#define BFW -1
#define FULLF 1
#define WARM 1
#define HT 0
#define HX 14
#define HTD 55
#define HSL 1
#define NANT 1
#define HAW 65
#define BM 1
#define HPD 1
#define BT 0
#define BSL 2
#define PB 0
#define GOPOS 2
#define JMAP 1
#define N1A 13
#define N1B 5
#define PT 0
#define PTAUX 16
#define LAY 1
#define OUTM 1
#define EPE 0
#define DPOS 2
#define DAUX 16
#define FAUX 16
#define KOUT 1
#define NK0 12
#define GT 540
#define PLO 5
#define PLS 17
#define ANCH 1
#define AOFF 55
#define AGG 1
#define PHW 0
#define CPF 1
#define CLEN 0x8080
#define AGT 150
#define GPAD 1
#define MFE 1
#define E3POS 10
#define E4POS 22
#define L1POS 28
#define L4POS 4
#define T1POS 18
#define HEARLY 0
#define BAR2L 0
#define ROT1 5
#define W2F 0
#define HGT 300
#define BGT 286
#define BPREW 0
#define BAN 2
#define HBD 88
#define CGT 0
#define MLP 30
#define BHOLD 0
#define TAU_L 238
#define DL0 50
#define EVT 340
#define FXA 1
#define RPRI 0
#define EVW 40
#define KP 0
#define KTO 270
#define KTE 0
#define SLS 0
#define SLF 30
#define SLM 50
#define BRT 1
#define BXD 0
#define PDB2 1
#define RP0 100
#define DLS 60
#define BFIRST 1
#define EVL 1
#define DLA 85
#define DLB 30
#define CST 1
#define POFF 0
#define NCL 4
#define CLS (NCL == 8 ? u & 7 : NCL == 4 ? (u >> 1) & 3 : (u >> 3) & (NCL - 1))
#define P_GATE 8
#include <hip/hip_runtime.h>
#include <hip/hip_bf16.h>
#include <stdint.h>

using bf16 = __hip_bfloat16;
using u32 = unsigned;
using u64 = unsigned long long;
using u32x2 = __attribute__((ext_vector_type(2))) unsigned;
using u32x4 = __attribute__((ext_vector_type(4))) unsigned;
using f32x4 = __attribute__((ext_vector_type(4))) float;
using bf16x2 = __attribute__((ext_vector_type(2))) __bf16;
typedef __attribute__((ext_vector_type(2))) float f32x2;
typedef short s16x8 __attribute__((ext_vector_type(8)));
typedef short s16x4 __attribute__((ext_vector_type(4)));
typedef __attribute__((address_space(3))) s16x4* lds_s4p;
typedef __attribute__((ext_vector_type(8))) __bf16 bf16x8v;

constexpr int NB = 4, DM = 640, NH = 32, DH = 128, SK = 8192, WIN = 2048, FF = 1728, QD = 4096, MODN = 3840;
constexpr int NT = 512, NWG = 256;

constexpr u32 WS_EPOCH = 0;
constexpr u32 G_T = 128;
constexpr u32 G_Q = 256;
constexpr u32 G_MOD = G_Q + 8 * 1024 * 16;
constexpr u32 G_MF = G_MOD + 8 * 1024 * 16;
constexpr u32 G_HB = G_MF + 16 * 160 * 16;
constexpr u32 G_HA = G_HB + 64 * 128;
constexpr u32 G_HT = G_HA + 80 * 128;
constexpr u32 G_ML = G_HT + 256 * 128;
constexpr u32 EMPTY = 0xffffffffu;
constexpr int NREP = NREPL;
constexpr u32 G_WO = G_ML + 256 * 64;
constexpr u32 WO_REP = 40 * 1024;
constexpr u32 G_ATT = G_WO + NREP * WO_REP;
constexpr u32 G_TK = G_ATT + 256 * 256;
constexpr u32 G_OUT = G_WO + 37 * 4096;
constexpr u32 WS_END = G_OUT + 40 * 54 * 128;
constexpr u32 OOB = 0xF0000000u;
#define GIDX(n, ks, b, cg) ((((ks) * ((n) / 4) + ((cg) >> 2)) * 16 + (b) * 4 + ((cg) & 3)) * 16)

#define WSTAMP(k) do {} while (0)
#define STAMPW(i) do {} while (0)
#define STAMPT(i, tt) do {} while (0)
#define STAMP_DECL
#define STAMP_FLUSH do {} while (0)
#define FAIL(i) do {} while (0)
#define PAD(name, next) asm volatile(".Lpad_" #name ":\n\ts_cbranch_execz .Lpad_" #next)
#define PADX(name, next, x) asm volatile(".Lpad_" #name ":\n\ts_cbranch_execz .Lpad_" #next : "+v"(x))
#define PADA(name, next) asm volatile(".Lpad_" #name ":\n\ts_cbranch_execz .Lpad_" #next : "+v"(acc[0]), "+v"(acc[1]), "+v"(acc[2]), "+v"(acc[3]), "+v"(acc[4]), "+v"(acc[5]), "+v"(acc[6]), "+v"(acc[7]))
#define STR_(x) #x
#define STR(x) STR_(x)
#define BAR() asm volatile("s_waitcnt lgkmcnt(0)\n\ts_barrier" ::: "memory")
#define VM0() asm volatile("s_waitcnt vmcnt(0)" ::: "memory")

__device__ __forceinline__ float bfr(float x) { return __bfloat162float(__float2bfloat16(x)); }
__device__ __forceinline__ float bf2f(bf16 x) { return __bfloat162float(x); }
__device__ __forceinline__ float lo16(unsigned u) { return __builtin_bit_cast(float, u << 16); }
__device__ __forceinline__ float hi16(unsigned u) { return __builtin_bit_cast(float, u & 0xffff0000u); }
__device__ __forceinline__ u32 f2u(float f) { return __builtin_bit_cast(u32, f); }
__device__ __forceinline__ float u2f(u32 f) { return __builtin_bit_cast(float, f); }
__device__ __forceinline__ u32 cvt2(float a, float b) {
  const f32x2 v = {a, b};
  return __builtin_bit_cast(u32, __builtin_convertvector(v, bf16x2));
}
__device__ __forceinline__ float frcp(float x) { return __builtin_amdgcn_rcpf(x); }
__device__ __forceinline__ float fsqrt(float x) { return __builtin_amdgcn_sqrtf(x); }
__device__ __forceinline__ float fexp(float x) { return __builtin_amdgcn_exp2f(x * 1.4426950408889634f); }
__device__ __forceinline__ float dot2(unsigned k, unsigned q, float acc) {
  return __builtin_amdgcn_fdot2_f32_bf16(__builtin_bit_cast(bf16x2, k), __builtin_bit_cast(bf16x2, q), acc, false);
}
__device__ __forceinline__ float addlo(unsigned w, float acc) { u32 k = 0x00003f80u; asm("" : "+s"(k)); return dot2(w, k, acc); }
__device__ __forceinline__ float addhi(unsigned w, float acc) { return dot2(w, 0x3f800000u, acc); }
template <int CTRL> __device__ __forceinline__ float dpp(float x) {
  return __builtin_bit_cast(float, __builtin_amdgcn_mov_dpp(__builtin_bit_cast(int, x), CTRL, 0xf, 0xf, true));
}
template <int CTRL> __device__ __forceinline__ u32 dppu(u32 x) { return (u32)__builtin_amdgcn_mov_dpp((int)x, CTRL, 0xf, 0xf, true); }
constexpr int XOR1 = 0xB1, XOR2 = 0x4E, XOR7 = 0x141, XOR8 = 0x128, ROR4 = 0x124, ROR2 = 0x122, ROR1 = 0x121, SHL4 = 0x104;
__device__ __forceinline__ float swap32_add(float a, float b) {
  auto s = __builtin_amdgcn_permlane32_swap(f2u(a), f2u(b), false, false);
  return u2f(s[0]) + u2f(s[1]);
}
__device__ __forceinline__ float swap16_add(float a, float b) {
  auto s = __builtin_amdgcn_permlane16_swap(f2u(a), f2u(b), false, false);
  return u2f(s[0]) + u2f(s[1]);
}
__device__ __forceinline__ float cross_rows_sum(float x) { x = swap16_add(x, x); return swap32_add(x, x); }
__device__ __forceinline__ float cross_rows_max(float x) {
  auto s = __builtin_amdgcn_permlane16_swap(f2u(x), f2u(x), false, false);
  x = fmaxf(u2f(s[0]), u2f(s[1]));
  auto s2 = __builtin_amdgcn_permlane32_swap(f2u(x), f2u(x), false, false);
  return fmaxf(u2f(s2[0]), u2f(s2[1]));
}
__device__ __forceinline__ float wave_sum(float x) {
  x += dpp<XOR8>(x); x += dpp<ROR4>(x); x += dpp<ROR2>(x); x += dpp<ROR1>(x);
  return cross_rows_sum(x);
}
__device__ __forceinline__ float wave_max(float x) {
  x = fmaxf(x, dpp<XOR8>(x)); x = fmaxf(x, dpp<ROR4>(x)); x = fmaxf(x, dpp<ROR2>(x)); x = fmaxf(x, dpp<ROR1>(x));
  return cross_rows_max(x);
}

__device__ __forceinline__ void st_gran(__amdgpu_buffer_rsrc_t rs, u32 off, u32 a, u32 b, u32 tg) {
  u32x4 v = {a, tg, b, tg};
  __builtin_amdgcn_raw_buffer_store_b128(v, rs, off, 0, 16);
}
__device__ __forceinline__ u32x4 ld_gran(__amdgpu_buffer_rsrc_t rs, u32 off) {
  return __builtin_amdgcn_raw_buffer_load_b128(rs, off, 0, 16);
}
constexpr unsigned SPIN_CAP = 1u << 18;
template <int N>
__device__ __forceinline__ void gather(__amdgpu_buffer_rsrc_t rs, const u32 (&offs)[N], u32x4 (&g)[N], u32 tg) {
  SPINLOOP for (unsigned spins = 0; spins < SPIN_CAP; ++spins) {
    bool ok = true;
#pragma unroll
    for (int i = 0; i < N; ++i) g[i] = ld_gran(rs, offs[i]);
#pragma unroll
    for (int i = 0; i < N; ++i) ok &= (offs[i] == OOB) | ((g[i][1] == tg) & (g[i][3] == tg));
    if (__all(ok)) break;
    __builtin_amdgcn_s_sleep(1);
  }
}
template <int N>
__device__ __forceinline__ void gather_m(__amdgpu_buffer_rsrc_t rs, u32 (&offs)[N], u32 (&va)[N], u32 (&vb)[N], u32 tg) {
#pragma unroll
  for (int i = 0; i < N; ++i) { va[i] = 0; vb[i] = 0; }
  SPINLOOP for (unsigned spins = 0; spins < SPIN_CAP; ++spins) {
    u32x4 g[N];
#pragma unroll
    for (int i = 0; i < N; ++i) g[i] = ld_gran(rs, offs[i]);
    bool ok = true;
#pragma unroll
    for (int i = 0; i < N; ++i) {
      const bool hit = (offs[i] != OOB) & (g[i][1] == tg) & (g[i][3] == tg);
      va[i] = hit ? g[i][0] : va[i]; vb[i] = hit ? g[i][2] : vb[i];
      offs[i] = hit ? OOB : offs[i];
      ok &= (offs[i] == OOB);
    }
    if (__all(ok)) break;
    __builtin_amdgcn_s_sleep(1);
  }
}

__device__ __forceinline__ bool missing(u32x4 v) { return max(max(v[0], v[1]), max(v[2], v[3])) == EMPTY; }
#define LK(v, o, so, imm, m) "s_mov_b64 exec, %" #m "\n\tbuffer_load_dwordx4 %" #v ", %" #o ", %[rs], " so " offen offset:" #imm " sc1\n\t"
#define LKEND "s_mov_b64 exec, -1\n\ts_waitcnt vmcnt(%[w])"
template <int W> __device__ __forceinline__ void look(u32x4 rs, u32 off, u32x4 (&v)[1], const u64 (&m)[1]) {
  asm volatile(LK(0, 1, "0", 0, 2) LKEND : "+v"(v[0]) : "v"(off), "s"(m[0]), [rs] "s"(rs), [w] "i"(W) : "memory");
}
template <int W> __device__ __forceinline__ void look(u32x4 rs, u32 off, u32x4 (&v)[5], const u64 (&m)[5]) {
  asm volatile(LK(0, 5, "0", 0, 6) LK(1, 5, "0", 1024, 7) LK(2, 5, "0", 2048, 8) LK(3, 5, "0", 3072, 9) LK(4, 5, "%11", 0, 10) LKEND
               : "+v"(v[0]), "+v"(v[1]), "+v"(v[2]), "+v"(v[3]), "+v"(v[4])
               : "v"(off), "s"(m[0]), "s"(m[1]), "s"(m[2]), "s"(m[3]), "s"(m[4]), "s"(4096u), [rs] "s"(rs), [w] "i"(W) : "memory");
}
template <int W> __device__ __forceinline__ void look(u32x4 rs, u32 off, u32x4 (&v)[7], const u64 (&m)[7]) {
  asm volatile(LK(0, 7, "0", 0, 8) LK(1, 7, "0", 1024, 9) LK(2, 7, "0", 2048, 10) LK(3, 7, "0", 3072, 11)
               LK(4, 7, "%15", 0, 12) LK(5, 7, "%15", 1024, 13) LK(6, 7, "%15", 2048, 14) LKEND
               : "+v"(v[0]), "+v"(v[1]), "+v"(v[2]), "+v"(v[3]), "+v"(v[4]), "+v"(v[5]), "+v"(v[6])
               : "v"(off), "s"(m[0]), "s"(m[1]), "s"(m[2]), "s"(m[3]), "s"(m[4]), "s"(m[5]), "s"(m[6]), "s"(4096u), [rs] "s"(rs), [w] "i"(W) : "memory");
}
template <int N, int SLEEP, int D>
__device__ __forceinline__ void gather_e(u32x4 rs, u32 off, u32x4 (&v)[N], u64 last) {
  u64 m[N];
#pragma unroll
  for (int i = 0; i < N; ++i) { m[i] = i < N - 1 ? ~0ull : last; v[i] = u32x4{0u, 0u, 0u, 0u}; }
  look<0>(rs, off, v, m);
  SPINLOOP for (unsigned spins = 0; spins < SPIN_CAP; ++spins) {
    u64 bad = 0;
#pragma unroll
    for (int i = 0; i < N; ++i) { m[i] = __builtin_amdgcn_ballot_w64(missing(v[i])); bad |= m[i]; }
    if (!bad) break;
    if (SLEEP) __builtin_amdgcn_s_sleep(SLEEP);
    look<N * (D - 1)>(rs, off, v, m);
  }
}

#define HLOOK(W_) asm volatile("s_mov_b64 exec, %2\n\tbuffer_load_dwordx2 %0, %1, %3, 0 offen sc1\n\ts_mov_b64 exec, -1\n\ts_waitcnt vmcnt(%4)" \
                               : "+v"(v) : "v"(off), "s"(m), "s"(rs), "i"(W_) : "memory")
__device__ __forceinline__ void heads_up_start(u32x4 rs, u32 off, u64 m, u32 tg, u32x2& v) { v = u32x2{0u, ~tg}; HLOOK(63); }
template <int SLEEP, int W0 = 0>
__device__ __forceinline__ void heads_up(u32x4 rs, u32 off, u64 m, u32 tg, u32x2& v) {
  asm volatile("s_waitcnt vmcnt(%1)" : "+v"(v) : "i"(W0) : "memory");
  SPINLOOP for (unsigned spins = 0; spins < SPIN_CAP; ++spins) {
    m &= __builtin_amdgcn_ballot_w64(v[1] != tg);
    if (!m) break;
    if (SLEEP) __builtin_amdgcn_s_sleep(SLEEP);
    HLOOK(0);
  }
}
__device__ __forceinline__ void heads_up_time(u64 all, u32x2 v) {
  SPINLOOP for (unsigned spins = 0; spins < 64; ++spins)
    if (!(all & __builtin_amdgcn_ballot_w64((int)((u32)__builtin_amdgcn_s_memrealtime() - v[0]) < 0))) break;
}
__device__ __forceinline__ void announce(__amdgpu_buffer_rsrc_t rs, u32 off, u32 a_max, u32 a_cnt, u32 n, u32 tb, u32 d, u32 tg) {
  u32 tk = 0, mx;
  const u32 f = (u32)__builtin_amdgcn_s_memrealtime() - tb + d;
  asm volatile("s_mov_b64 exec, 1\n\tds_max_u32 %2, %3\n\tds_add_rtn_u32 %0, %4, %5\n\tds_read_b32 %1, %2\n\ts_mov_b64 exec, -1\n\ts_waitcnt lgkmcnt(0)"
               : "+v"(tk), "=&v"(mx) : "v"(a_max), "v"(f), "v"(a_cnt), "v"(1u) : "memory");
  const u32 lane = threadIdx.x & 63;
  __builtin_amdgcn_raw_buffer_store_b64(u32x2{__builtin_amdgcn_readfirstlane(mx) + tb, tg}, rs,
                                        (lane < 16 && __builtin_amdgcn_readfirstlane(tk) == n - 1) ? off + lane * 8 : OOB, 0, 16);
}

__device__ __forceinline__ void st_g22(__amdgpu_buffer_rsrc_t rs, u32 off, f32x4 v, u32 tg) {
  const u32 a0 = f2u(v[0]) + 0x200u, a1 = f2u(v[1]) + 0x200u, a2 = f2u(v[2]) + 0x200u, a3 = f2u(v[3]) + 0x200u;
  const u32x4 w = {(a0 >> 10) | ((a1 << 12) & 0xffc00000u), (a1 >> 20) | (tg << 12), (a2 >> 10) | ((a3 << 12) & 0xffc00000u), (a3 >> 20) | (tg << 12)};
  __builtin_amdgcn_raw_buffer_store_b128(w, rs, off, 0, 16);
}
__device__ __forceinline__ f32x4 un_g22(u32x4 w) {
  return f32x4{u2f(w[0] << 10), u2f(__builtin_amdgcn_alignbit(w[1], w[0], 22) << 10), u2f(w[2] << 10), u2f(__builtin_amdgcn_alignbit(w[3], w[2], 22) << 10)};
}
template <int N, int SLEEP>
__device__ __forceinline__ void gather22(__amdgpu_buffer_rsrc_t rs, u32 (&offs)[N], u32x4 (&v)[N], u32 tg, u32 a_inc = 0) {
#pragma unroll
  for (int i = 0; i < N; ++i) v[i] = u32x4{0u, 0u, 0u, 0u};
  SPINLOOP for (unsigned spins = 0; spins < SPIN_CAP; ++spins) {
    u32x4 g[N];
#pragma unroll
    for (int i = 0; i < N; ++i) g[i] = ld_gran(rs, offs[i]);
    if (a_inc) asm volatile("s_mov_b64 exec, 1\n\tds_add_u32 %0, %1\n\ts_mov_b64 exec, -1" :: "v"(a_inc), "v"(1u) : "memory");
    bool ok = true;
#pragma unroll
    for (int i = 0; i < N; ++i) {
      const bool hit = (offs[i] != OOB) & (((g[i][1] ^ (tg << 12)) | (g[i][3] ^ (tg << 12))) < 4096u);
#pragma unroll
      for (int k = 0; k < 4; ++k) v[i][k] = hit ? g[i][k] : v[i][k];
      offs[i] = hit ? OOB : offs[i];
      ok &= (offs[i] == OOB);
    }
    if (__all(ok)) break;
    __builtin_amdgcn_s_sleep(SLEEP);
  }
}

__device__ __forceinline__ int sel_lt(int a, int b, int x, int y) {
  int r;
  asm("s_cmp_lt_i32 %1, %2\n\ts_cselect_b32 %0, %3, %4" : "=s"(r) : "s"(a), "s"(b), "s"(x), "s"(y) : "scc");
  return r;
}
__device__ __forceinline__ u32 next_tag(u32 ep) { const u32 t = ep + 1u; return (t & 0xfffffu) ? t : t + 1u; }

__device__ __forceinline__ void lds_arrive(u32 addr, int lane) {
  asm volatile("s_waitcnt lgkmcnt(0)" ::: "memory");
  if (lane == 0) asm volatile("ds_add_u32 %0, %1" :: "v"(addr), "v"(1u) : "memory");
}
__device__ __forceinline__ u32 lds_ticket(u32 addr, int lane) {
  u32 r = 0;
  asm volatile("s_waitcnt lgkmcnt(0)" ::: "memory");
  if (lane == 0) asm volatile("ds_add_rtn_u32 %0, %1, %2\n\ts_waitcnt lgkmcnt(0)" : "=v"(r) : "v"(addr), "v"(1u) : "memory");
  return __builtin_amdgcn_readfirstlane(r);
}
template <int SLEEP>
__device__ __forceinline__ void lds_wait(u32 addr, u32 target) {
  SPINLOOP for (unsigned spins = 0; spins < (1u << 22); ++spins) {
    u32 v;
    asm volatile("ds_read_b32 %0, %1\n\ts_waitcnt lgkmcnt(0)" : "=v"(v) : "v"(addr) : "memory");
    if (v >= target) break;
    __builtin_amdgcn_s_sleep(SLEEP);
  }
}
__device__ __forceinline__ void dma16(const void* gsrc, u32 lds_dst) {
  u32 keep;
  asm volatile("s_mov_b32 %0, m0\n\ts_mov_b32 m0, %2\n\ts_nop 0\n\tglobal_load_lds_dwordx4 %1, off\n\ts_mov_b32 m0, %0"
               : "=&s"(keep) : "v"(gsrc), "s"(lds_dst) : "memory");
}
__device__ __forceinline__ void dma16nt(const void* gsrc, u32 lds_dst) {
  u32 keep;
  asm volatile("s_mov_b32 %0, m0\n\ts_mov_b32 m0, %2\n\ts_nop 0\n\tglobal_load_lds_dwordx4 %1, off nt\n\ts_mov_b32 m0, %0"
               : "=&s"(keep) : "v"(gsrc), "s"(lds_dst) : "memory");
}
__device__ __forceinline__ u32x4 ldasm(const void* p) {
  u32x4 v;
  asm volatile("global_load_dwordx4 %0, %1, off" : "=v"(v) : "v"(p) : "memory");
  return v;
}
__device__ __forceinline__ u32 ldasm_sc1(const void* p) {
  u32 v;
  asm volatile("global_load_dword %0, %1, off sc1" : "=v"(v) : "v"(p) : "memory");
  return v;
}
__device__ __forceinline__ u32x4 ldw(const bf16* p) { return *reinterpret_cast<const u32x4*>(p); }
template <int D2>
__device__ __forceinline__ s16x8 wfrag(u32 addr) {
  const s16x4 lo = __builtin_amdgcn_ds_read_tr16_b64_v4i16((lds_s4p)(uintptr_t)addr);
  const s16x4 hi = __builtin_amdgcn_ds_read_tr16_b64_v4i16((lds_s4p)(uintptr_t)(addr + D2));
  return s16x8{lo[0], lo[1], lo[2], lo[3], hi[0], hi[1], hi[2], hi[3]};
}
#define MFMA(w_, x_, c_) __builtin_amdgcn_mfma_f32_16x16x32_bf16(__builtin_bit_cast(bf16x8v, w_), __builtin_bit_cast(bf16x8v, x_), c_, 0, 0, 0)

struct Desc { const bf16* p; u32 bytes, flags; };
struct Args {
  Desc wq, x, wmod, c;
  unsigned char* ws;
  const bf16 *kc, *vc, *wo, *w1, *b1, *w2, *b2;
  bf16* out;
};

static_assert(offsetof(Args, ws) == 64 && offsetof(Args, out) == 128, "argument offsets");

constexpr int L_W = 0;
constexpr int L_WM2 = 40960;
constexpr int L_XA = 81920;
constexpr int XS = 1312, XH = 704;
constexpr int L_RED = L_XA + 16384;
constexpr int L_MOD = L_RED + 8192;
constexpr int L_XR = L_MOD + 5120;
constexpr int L_PT = L_XR + 5120;
constexpr int L_ACC = L_PT + 2048;
constexpr int L_QS = L_ACC + 4096;
constexpr int L_FI = L_QS + 256;
constexpr int L_ST = L_FI + 256;
constexpr int L_CNT = L_ST + 256;
constexpr int L_END = L_CNT + 128;
static_assert(L_END <= 160 * 1024, "LDS");
constexpr u32 C_Q = L_CNT, C_SC = L_CNT + 4, C_ISS = L_CNT + 8, C_P1 = L_CNT + 20, C_P2 = L_CNT + 24, C_GO = L_CNT + 28, C_W = L_CNT + 12, C_FIN = L_CNT + 32,
              C_KI = L_CNT + 36, C_PP = L_CNT + 40, C_PUB = L_CNT + 56, C_HT = L_CNT + 64, C_MF = L_CNT + 68, C_RL = L_CNT + 72, C_DL = L_CNT + 76;

constexpr int NL = 32;
constexpr int PVLAG = P_LAG;
constexpr int NPRE = P_NPRE;
constexpr int NVE = P_NVE;
static_assert(PVLAG <= 16 && (PVLAG & 1) == 0 && NVE <= PVLAG && (NVE & 1) == 0, "row groups");

__global__ void __launch_bounds__(NT, 2) mega_kernel(Args a) {
  __shared__ __attribute__((aligned(1024))) unsigned char lds[L_END];
  float* red = reinterpret_cast<float*>(lds + L_RED);
  u32* xraw = reinterpret_cast<u32*>(lds + L_XR);
  u32* pT = reinterpret_cast<u32*>(lds + L_PT);
  float* accs = reinterpret_cast<float*>(lds + L_ACC);
  u32* qs = reinterpret_cast<u32*>(lds + L_QS);
  float* st = reinterpret_cast<float*>(lds + L_ST);
  u32* cnt = reinterpret_cast<u32*>(lds + L_CNT);

  u32x4 rw, rx;
  unsigned char* wsq;
  const void* kcq;
  asm volatile("s_load_dwordx4 %0, %4, %5\n\ts_load_dwordx4 %1, %4, %5 offset:16\n\ts_load_dwordx2 %2, %4, 64\n\ts_load_dwordx2 %3, %4, 72"
               : "=&s"(rw), "=&s"(rx), "=&s"(wsq), "=&s"(kcq) : "s"(__builtin_amdgcn_kernarg_segment_ptr()), "s"((blockIdx.x & 1) * 32));
  __builtin_amdgcn_sched_barrier(0);
  const int t = threadIdx.x, lane = t & 63;
  const int wave = __builtin_amdgcn_readfirstlane(t >> 6);
  const int u = blockIdx.x;
  const int bh = u >> 1, half = u & 1, b_att = bh >> 5, h_att = bh & 31;
  const int r = lane >> 4, c = lane & 15;
  STAMP_DECL
  STAMPT(0, 0);
  u32 tg;
  const int g = u >> 1, ksp = g & 1, cbp = g >> 1;
#define fav (half || (g >= 120 && g < 126))
  const int s128e = (((((lane & 7) >> 1) ^ ((lane >> 4) & 1)) << 1) | (lane & 1)) * 8;
  const u32 tr128 = (8 * r + (c >> 2)) * 128 + ((((c >> 3) & 1) | ((r & 1) << 1)) * 32) + (c & 3) * 8;
  if (wave < 4) {
    __builtin_amdgcn_s_setprio(3);
    const u32 rb = 8192 - 512 * half;
    const u32 hm = 0u - (u32)half;
    const u32 vo = ((u32)(lane >> 3) * rb + (((lane & 7) ^ ((lane >> 3) & 2)) ^ ((wave & 1) * 4)) * 16) | ((u32)(119 - g) & hm & OOB);
    u32 so = (u32)(ksp * 320 + 8 * wave) * rb + cbp * 128;
    const u32 step = 32 * rb, dstep = 4096, dst = L_W + wave * 1024;
    const u32 oe = wave * 1280 + lane * 16, oo = (r * 1280 + ksp * 640 + (c + 16 * wave) * 16) | ((u32)(39 - c - 16 * wave) & OOB);
    const u32 oa = (oo & hm) | (oe & ~hm), ob = (wave * 1280 + 1024 + c * 16) | (hm & OOB), oc = oe | (hm & OOB), zero = 0;
    u32 ep, keep;
    u32x4 xv[4];
    static_assert(G_TK + 1024 <= G_OUT, "37 x 4 KB");
    const u32 pre = ((u32)u << 31) & (u32)(g - 37) & (u32)(2 - wave), po = G_WO + g * 4096 + lane * 16;
    const u32x4 emp = {EMPTY, EMPTY, EMPTY, EMPTY};
#define DMA1 "buffer_load_dwordx4 %7, %8, %1 offen nt lds\n\t"
#define DMAN "s_add_u32 m0, m0, %11\n\ts_add_u32 %1, %1, %10\n\t" DMA1
    asm volatile("s_mov_b32 %0, m0\n\ts_mov_b32 m0, %9\n\t"
                 "s_cmp_gt_i32 %18, 1\n\ts_waitcnt lgkmcnt(0)\n\ts_cbranch_scc1 1f\n\t"
                 "global_load_dword %2, %12, %13 sc1\n\t"
                 "buffer_load_dwordx4 %3, %14, %17, 0 offen\n\tbuffer_load_dwordx4 %4, %15, %17, 0 offen\n\t"
                 "buffer_load_dwordx4 %5, %16, %17, 0 offen offset:2560\n\tbuffer_load_dwordx4 %6, %15, %17, 0 offen offset:2560\n"
                 "1:\n\t" DMA1 DMAN DMAN DMAN DMAN DMAN DMAN DMAN DMAN DMAN "s_mov_b32 m0, %0\n\t"
                 "s_cmp_eq_u32 %19, 0\n\ts_cbranch_scc1 2f\n\tglobal_store_dwordx4 %20, %21, %13 sc1\n\t"
                 "global_store_dwordx4 %20, %21, %13 offset:1024 sc1\n\tglobal_store_dwordx4 %20, %21, %13 offset:2048 sc1\n\t"
                 "global_store_dwordx4 %20, %21, %13 offset:3072 sc1\n2:\n\t"
#define B0W "s_barrier\n\ts_memrealtime vcc\n\ts_waitcnt vmcnt(10) lgkmcnt(0)\n\ts_sub_u32 %0, vcc_lo, " STR(AOFF)
                 B0W
                 : "=&s"(keep), "+s"(so), "=&v"(ep), "=&v"(xv[0]), "=&v"(xv[1]), "=&v"(xv[2]), "=&v"(xv[3])
                 : "v"(vo), "s"(rw), "s"(dst), "s"(step), "s"(dstep), "v"(zero), "s"(wsq), "v"(oa), "v"(ob), "v"(oc), "s"(rx), "s"(wave - half),
                   "s"(pre), "v"(po), "v"(emp) : "memory", "scc", "vcc");
#undef DMAN
#undef DMA1
    asm volatile("" : "+s"(wsq));
    const __amdgpu_buffer_rsrc_t rs = __builtin_amdgcn_make_buffer_rsrc(wsq, 0, WS_END, 0x00020000);
    if (half) {
      if (wave < 3) {
        u32x4 sv;
#pragma unroll
        for (int k = 0; k < 4; ++k) {
          const float c0 = lo16(xv[0][k]), c1 = hi16(xv[0][k]);
          sv[k] = cvt2(c0 * frcp(1.f + fexp(-c0)), c1 * frcp(1.f + fexp(-c1)));
        }
        if (c + 16 * wave < 40) *reinterpret_cast<u32x4*>(lds + L_XA + r * XH + (c + 16 * wave) * 16) = sv;
        if (t == 0) { cnt[4] = next_tag(ep); cnt[15] = keep; }
        lds_arrive(C_P1, lane);
        STAMPT(2, 0);
      }
    } else {
      if (wave < 2) {
#pragma unroll
        for (int i = 0; i < 2; ++i) {
          const int b = wave + 2 * i;
          float xf[16];
#pragma unroll
          for (int k = 0; k < 4; ++k) { xf[2 * k] = lo16(xv[2 * i][k]); xf[2 * k + 1] = hi16(xv[2 * i][k]); xf[8 + 2 * k] = lo16(xv[2 * i + 1][k]); xf[8 + 2 * k + 1] = hi16(xv[2 * i + 1][k]); }
          float s = 0.f, s2 = 0.f;
#pragma unroll
          for (int k = 0; k < 8; ++k) { s += xf[k]; s2 += xf[8 + k]; }
          if (lane < 16) s += s2;
          const float mean = wave_sum(s) * (1.f / DM);
          float v = 0.f, v2 = 0.f;
#pragma unroll
          for (int k = 0; k < 8; ++k) { const float d = xf[k] - mean; v += d * d; const float d2 = xf[8 + k] - mean; v2 += d2 * d2; }
          if (lane < 16) v += v2;
          v = wave_sum(v);
          if (lane == 0) { st[24 + 2 * b] = bfr(mean); st[24 + 2 * b + 1] = frcp(bfr(fsqrt(bfr(bfr(v * (1.f / DM)) + 1e-5f)))); }
          *reinterpret_cast<u32x4*>(xraw + b * 320 + lane * 4) = xv[2 * i];
          if (lane < 16) *reinterpret_cast<u32x4*>(xraw + b * 320 + 256 + lane * 4) = xv[2 * i + 1];
        }
        if (t == 0) { cnt[4] = next_tag(ep); cnt[15] = keep; }
        lds_arrive(C_P1, lane);
      }
      lds_wait<1>(C_P1, 2);
      STAMPT(20, 0);
      tg = __builtin_amdgcn_readfirstlane(cnt[4]);
      if (CT0 && u < 8 && t == 192) { const u32 t0 = cnt[15]; st_gran(rs, G_T + u * 16, t0, t0, tg); }
      if (wave < 3) {
        const int b = LAY == 2 ? t & 3 : (t >> 2) & 3, j = LAY == 2 ? t >> 2 : (t >> 4) * 4 + (t & 3);
        u32 offs[8];
        u32x4 gm[8];
#pragma unroll
        for (int i = 0; i < 8; ++i) offs[i] = t < 160 ? G_MOD + GIDX(1024, i & 1, b, ((i >> 1) & 1) * 160 + (i >> 2) * 40 + ksp * 80 + j) : OOB;
        const float mean = st[24 + 2 * b], sd = st[24 + 2 * b + 1];
        float nv[8];
#pragma unroll
        for (int e = 0; e < 2; ++e) {
          const u32x2 xw = *reinterpret_cast<const u32x2*>(xraw + b * 320 + ksp * 160 + e * 80 + 2 * j);
#pragma unroll
          for (int k = 0; k < 4; ++k) {
            const float xf = (k & 1) ? hi16(xw[k >> 1]) : lo16(xw[k >> 1]);
            nv[4 * e + k] = bfr(bfr(xf - mean) * sd);
            asm volatile("" : "+v"(nv[4 * e + k]));
          }
        }
        {
          const u32 tt = __builtin_amdgcn_readfirstlane(cnt[15]) + GT;
          SPINLOOP while ((int)((u32)__builtin_amdgcn_s_memrealtime() - tt) < 0) __builtin_amdgcn_s_sleep(1);
        }
        STAMPT(14, 0);
        gather22<8, 4>(rs, offs, gm, tg, GOPOS == 2 ? C_GO : 0);
        STAMPT(8, 0);
#pragma unroll
        for (int e = 0; e < 2; ++e) {
          const f32x4 sa = un_g22(gm[4 * e]) + un_g22(gm[4 * e + 1]);
          const f32x4 ca = un_g22(gm[4 * e + 2]) + un_g22(gm[4 * e + 3]);
          const u32 s01 = cvt2(sa[0], sa[1]), s23 = cvt2(sa[2], sa[3]), c01 = cvt2(ca[0], ca[1]), c23 = cvt2(ca[2], ca[3]);
          const u32 d01 = cvt2(1.f + lo16(c01), 1.f + hi16(c01)), d23 = cvt2(1.f + lo16(c23), 1.f + hi16(c23));
          const u32 p01 = cvt2(nv[4 * e] * lo16(d01), nv[4 * e + 1] * hi16(d01)), p23 = cvt2(nv[4 * e + 2] * lo16(d23), nv[4 * e + 3] * hi16(d23));
          const float hv[4] = {lo16(p01) + lo16(s01), hi16(p01) + hi16(s01), lo16(p23) + lo16(s23), hi16(p23) + hi16(s23)};
          if (t < 160) *reinterpret_cast<u32x2*>(lds + L_XA + b * XH + (e * 40 + j) * 8) = u32x2{cvt2(hv[0], hv[1]), cvt2(hv[2], hv[3])};
        }
      }
    }
    VM0();
    STAMPT(5, 128);
    lds_arrive(C_P2, lane);
    lds_wait<1>(C_P2, 4);
    STAMPT(7, 0);
    tg = __builtin_amdgcn_readfirstlane(cnt[4]);
    {
      f32x4 acc = {0.f, 0.f, 0.f, 0.f}, acc2 = acc;
#pragma unroll
      for (int s = 0; s < 10; s += 2) {
        acc = MFMA(wfrag<512>(L_W + (tr128 ^ (wave * 32)) + s * 4096), *reinterpret_cast<const u32x4*>(lds + L_XA + (c & 3) * XH + s * 64 + r * 16), acc);
        acc2 = MFMA(wfrag<512>(L_W + (tr128 ^ (wave * 32)) + s * 4096 + 4096), *reinterpret_cast<const u32x4*>(lds + L_XA + (c & 3) * XH + s * 64 + 64 + r * 16), acc2);
      }
      if (c < 4 && u < 240 + 16 - 16 * half) st_g22(rs, G_Q + half * (G_MOD - G_Q) + GIDX(1024, ksp, c, cbp * 16 + wave * 4 + r), acc + acc2, tg);
    }
    if (CT0 && half && u < 8 && t == 192) { const u32 t0 = cnt[15]; st_gran(rs, G_T + u * 16, t0, t0, tg); }
    STAMPT(9, 0);
  } else {
    __builtin_amdgcn_s_setprio(2);
    {
      const u32 co = min((u32)(u >> 3) * 1024u + (u32)((t >> 5) & 7) * 128u + 768u, (u32)CLEN);
      u64 pc;
      asm volatile("s_getpc_b64 %0\n\tglobal_load_dword v128, %1, %0" : "=&s"(pc) : "v"(co) : "memory", "v128");
    }
    if (t < 256 + 32) cnt[lane] = 0;
    const int rot = __builtin_amdgcn_readfirstlane((u >> 3) + ((t >> 6) & 3) * PHW);
    const u32 ko = (u32)(bh * SK + (SK - WIN)) * 256u + (u32)(half * 8 + wave) * (NL * 1024u) + lane * 16;
    STAMPT(30, 448);
#define LK4(a, b, c, d, o) "global_load_dwordx4 " a ", %" #o ", %[k] nt\n\tglobal_load_dwordx4 " b ", %" #o ", %[k] offset:1024 nt\n\t" \
                           "global_load_dwordx4 " c ", %" #o ", %[k] offset:2048 nt\n\tglobal_load_dwordx4 " d ", %" #o ", %[k] offset:3072 nt\n\t"
    asm volatile("s_waitcnt lgkmcnt(0)\n\ts_barrier\n\t"
                 LK4("v[128:131]", "v[132:135]", "v[136:139]", "v[140:143]", 0) LK4("v[144:147]", "v[148:151]", "v[152:155]", "v[156:159]", 1)
                 LK4("v[160:163]", "v[164:167]", "v[168:171]", "v[172:175]", 2)
                 :: "v"(ko + (rot & 7) * 4096), "v"(ko + ((rot + 1) & 7) * 4096),
                   "v"(ko + ((rot + 2) & 7) * 4096),
                   [k] "s"(kcq) : "memory", "v128", "v129", "v130", "v131", "v132", "v133", "v134", "v135", "v136", "v137", "v138", "v139", "v140", "v141", "v142", "v143", "v144", "v145", "v146", "v147", "v148", "v149", "v150", "v151", "v152", "v153", "v154", "v155", "v156", "v157", "v158", "v159", "v160", "v161", "v162", "v163", "v164", "v165", "v166", "v167", "v168", "v169", "v170", "v171", "v172", "v173", "v174", "v175", "v176", "v177", "v178", "v179", "v180", "v181", "v182", "v183", "v184", "v185", "v186", "v187", "v188", "v189", "v190", "v191");
#undef LK4
    STAMPT(31, 448);
  }
  u32 kvoff = (u32)(bh * SK + (SK - WIN)) * 256u + (u32)(half * 8 + wave) * (NL * 1024u) + lane * 16, ph[8];
  const int rot2 = __builtin_amdgcn_readfirstlane((u >> 3) + ((t >> 6) & 3) * PHW);
#pragma unroll
  for (int i = 0; i < 8; ++i) ph[i] = ((i + rot2) & 7) * 4096;
  asm volatile("" : "+v"(kvoff));
  asm volatile("s_waitcnt lgkmcnt(0)" : "+s"(wsq), "+s"(ph[0]), "+s"(ph[1]), "+s"(ph[2]), "+s"(ph[3]), "+s"(ph[4]), "+s"(ph[5]), "+s"(ph[6]), "+s"(ph[7])
               : "s"(rw), "s"(rx), "s"(kcq));
  const __amdgpu_buffer_rsrc_t rs = __builtin_amdgcn_make_buffer_rsrc(wsq, 0, WS_END, 0x00020000);
  u32 rw2;
#define BLKA(ia_) (((c & 3) * 32 + (((ia_) & 3) + (((ia_) >> 2) & 1) * 16 + (wave & 3) * 4)) * 2 + ((c >> 2) & 1))
#define RD_ROLE() { u32 v_; asm volatile("ds_read_b32 %0, %1\n\ts_waitcnt lgkmcnt(0)" : "=v"(v_) : "v"((u32)C_RL) : "memory"); rw2 = __builtin_amdgcn_readfirstlane(v_); }
#define WAIT_ROLE() SPINLOOP for (unsigned spins = 0; spins < (1u << 22); ++spins) { RD_ROLE() if ((int)rw2 < 0) break; __builtin_amdgcn_s_sleep(2); }
#define HEADA(hh) ((ksA & 3) + (ksA >> 2) * 16 + (hh) * 4)
  u32x4 gmf[5], ml;
  u32 tkf, a_pub;
  f32x4 psc, pw4[4];
  u32x2 xw;
  {
    u32x4 kv[NL], kh[16];
    const __amdgpu_buffer_rsrc_t rk = __builtin_amdgcn_make_buffer_rsrc((void*)a.kc, 0, 1u << 28, 0x00020000);
    const __amdgpu_buffer_rsrc_t rv = __builtin_amdgcn_make_buffer_rsrc((void*)a.vc, 0, 1u << 28, 0x00020000);
#define LDKV(rsrc, off, i) __builtin_amdgcn_raw_buffer_load_b128(rsrc, (off) + ((i) & 3) * 1024, ph[(i) >> 2], 2)
#define LDS_INC(addr) do { asm volatile("" ::: "memory"); if (lane == 0) asm volatile("ds_add_u32 %0, %1" :: "v"(addr), "v"(1u) : "memory"); } while (0)
#define TR_STEP(x, HALF, CTRL, BIT) _Pragma("unroll") for (int hh = 0; hh < (HALF); ++hh) { \
    const float lo_ = x[hh], hi_ = x[hh + (HALF)]; const float t1_ = lo_ + dpp<CTRL>(lo_), t2_ = hi_ + dpp<CTRL>(hi_); \
    x[hh] = (c & (BIT)) ? t2_ : t1_; }
#define SCORE16(x, src, o) float x[16]; \
    _Pragma("unroll") for (int i = 0; i < 16; ++i) { float d = 0.f; _Pragma("unroll") for (int k = 0; k < 4; ++k) d = dot2(src[(o) + i][k], qpk[k], d); x[i] = d; } \
    TR_STEP(x, 8, XOR8, 8) TR_STEP(x, 4, XOR7, 4) TR_STEP(x, 2, XOR2, 2)
#define SCORE16P(x, src, o, pa_, pb_) float x[16]; \
    _Pragma("unroll") for (int i = 0; i < 16; ++i) { float d = 0.f; _Pragma("unroll") for (int k = 0; k < 4; ++k) d = dot2(src[(o) + i][k], qpk[k], d); x[i] = d; } \
    TR_STEP(x, 8, XOR8, 8) PADX(pa_, pb_, x[0]); TR_STEP(x, 4, XOR7, 4) TR_STEP(x, 2, XOR2, 2)
#define SCALED(v) bfr(bfr(v) * 0.08838834764831845f)
#define PV2(i) _Pragma("unroll") for (int k = 0; k < 4; ++k) { \
      acc[2 * k] = dot2(__builtin_amdgcn_perm(kv[(i) + 1][k], kv[i][k], 0x05040100u), pw[(i) / 2], acc[2 * k]); \
      acc[2 * k + 1] = dot2(__builtin_amdgcn_perm(kv[(i) + 1][k], kv[i][k], 0x07060302u), pw[(i) / 2], acc[2 * k + 1]); }
#define GET_Q() lds_wait<2>(C_Q, 1); u32x4 qpk; \
    asm volatile("ds_read_b128 %0, %2\n\tds_read_b32 %1, %3\n\ts_waitcnt lgkmcnt(0)" : "=&v"(qpk), "=&v"(tg) : "v"((u32)(L_QS + 16 * c)), "v"((u32)(L_CNT + 16)) : "memory"); \
    tg = __builtin_amdgcn_readfirstlane(tg);
#define GET_P() _Pragma("unroll") for (int i4 = 0; i4 < 4; ++i4) { \
      const u32x4 pv = *reinterpret_cast<const u32x4*>(pT + (wave * 4 + r) * 16 + i4 * 4); \
      _Pragma("unroll") for (int j = 0; j < 4; ++j) pw[i4 * 4 + j] = pv[j]; }
    auto gather_q = [&]() __attribute__((always_inline)) {
        u32 offs[1] = {G_Q + GIDX(1024, lane >> 5, b_att, h_att * 32 + (lane & 31))};
        u32x4 gq[1];
        {
          gq[0] = ld_gran(rs, offs[0]);
          const u32x4 gt = ld_gran(rs, lane < 8 ? G_T + lane * 16 : OOB);
          LDS_INC(C_KI);
          {
            const u32 own = cnt[15];
            const bool ok = lane < 8 && gt[1] == tg;
            float d = ok ? __builtin_amdgcn_fmed3f((float)(int)(gt[0] - own + TOFF), -400.f, 400.f) : -500.f;
            d = fmaxf(d, dpp<XOR1>(d)); d = fmaxf(d, dpp<XOR2>(d)); d = fmaxf(d, dpp<XOR7>(d));
            if (lane == 0 && d > -450.f) cnt[15] = own + (u32)(int)d;
          }
          const bool hit = (((gq[0][1] ^ (tg << 12)) | (gq[0][3] ^ (tg << 12))) < 4096u);
          if (!__all(hit)) gather22<1, 4>(rs, offs, gq, tg);
        }
        STAMPW(10);
        f32x4 q4 = un_g22(gq[0]);
#pragma unroll
        for (int k = 0; k < 4; ++k) q4[k] = swap32_add(q4[k], q4[k]);
        if (lane < 32) *reinterpret_cast<u32x2*>(qs + 2 * lane) = u32x2{cvt2(q4[0], q4[1]), cvt2(q4[2], q4[3])};
        lds_arrive(C_Q, lane);
    };
    const int qw = half ? QODD : QEVEN;
    const u32 tau = fav ? TAU_L : TAU_E;
#define PACE(n) if ((n) > PB) { const u32 tt_ = t0 + (((n) - PB) * tau >> 4); \
      SPINLOOP while ((int)((u32)__builtin_amdgcn_s_memrealtime() - tt_) < 0) __builtin_amdgcn_s_sleep(1); asm volatile("" ::: "memory"); }
#define PACE2(n) { u32 v_, d_; \
      asm volatile("1:\n\tds_read_b32 %0, %2\n\ts_memrealtime vcc\n\ts_waitcnt lgkmcnt(0)\n\tv_readfirstlane_b32 %1, %0\n\t" \
                   "s_mulk_i32 %1, %5\n\ts_ashr_i32 %1, %1, 5\n\ts_add_u32 %1, %1, vcc_lo\n\t" \
                   "s_sub_u32 %1, %1, %3\n\ts_cmp_ge_i32 %1, %4\n\ts_cbranch_scc1 2f\n\ts_sleep 1\n\ts_branch 1b\n2:" \
                   : "=&v"(v_), "=&s"(d_) : "v"(a_dl), "s"(t0), "i"(((n) * TAU_E >> 4) - POFF), "i"(((n) - RP0) * 32 / (126 - RP0)) : "memory", "scc", "vcc"); }
    float acc[8];
#pragma unroll
    for (int k = 0; k < 8; ++k) acc[k] = 0.f;
    u32 pw[16];
    if (__builtin_expect(wave >= 4, 1)) {
      asm volatile("" : "={v[128:131]}"(kv[0]), "={v[132:135]}"(kv[1]), "={v[136:139]}"(kv[2]), "={v[140:143]}"(kv[3]),
                        "={v[144:147]}"(kv[4]), "={v[148:151]}"(kv[5]), "={v[152:155]}"(kv[6]), "={v[156:159]}"(kv[7])
                        , "={v[160:163]}"(kv[8]), "={v[164:167]}"(kv[9]), "={v[168:171]}"(kv[10]), "={v[172:175]}"(kv[11])
                   );
      u32 t0 = 0;
      const u32 tk0 = KP ? (u32)__builtin_amdgcn_s_memrealtime() : 0u, ktau = half ? KTO : KTE;
      STAMPT(24, 448);
      const u32 kvoff2 = kvoff ^ 131072u;
#pragma unroll
      for (int i = NK0; i < 48; ++i) {
        if (i == NPRE) { STAMPT(12, 448); STAMPT(13, 256); }
        if (i == NPRE && half == 0) lds_wait<2>(C_GO, GOPOS ? 3 : 4);
        if (KP && i >= KP && (i & 3) == 0) { const u32 tt_ = tk0 + (i * ktau >> 4);
          SPINLOOP while ((int)((u32)__builtin_amdgcn_s_memrealtime() - tt_) < 0) __builtin_amdgcn_s_sleep(1); asm volatile("" ::: "memory"); }
        if (i < NL) kv[i < NL ? i : 0] = LDKV(rk, kvoff, i & 31); else kh[i < NL ? 0 : i - NL] = LDKV(rk, kvoff2, i & 31);
      }
      if (!QPRE || wave != qw) LDS_INC(C_KI);
#pragma unroll
      for (int i = 0; i < NK0; i += 4) asm volatile("s_waitcnt vmcnt(%4)" : "+v"(kv[i]), "+v"(kv[i + 1]), "+v"(kv[i + 2]), "+v"(kv[i + 3]) : "i"(48 - NK0) : "memory");
      STAMPT(25, 448);
      if ((QODD >= 4 || QEVEN >= 4) && wave == qw) {
        __builtin_amdgcn_s_setprio(3);
        lds_wait<2>(C_P1, 2 + half);
        tg = __builtin_amdgcn_readfirstlane(cnt[4]);
        gather_q();
        __builtin_amdgcn_s_setprio(2);
      }
      GET_Q()
      SCORE16(xa, kv, 0)
      asm volatile("" : "+v"(xa[0]), "+v"(xa[1]), "+v"(kvoff) :: "memory");
      PAD(e1, l2);
#pragma unroll
      for (int i = 0; i < P_NVA; ++i) kv[i] = LDKV(rv, kvoff, i);
      asm volatile("" ::: "memory");
      SCORE16(xb, kv, 16)
      xa[2] = xb[0]; xa[3] = xb[1];
      TR_STEP(xa, 2, XOR1, 1)
      float s0 = SCALED(xa[0]), s1 = SCALED(xa[1]);
      asm volatile("" : "+v"(s0), "+v"(s1), "+v"(kvoff) :: "memory");
#pragma unroll
      for (int i = P_NVA; i < NVE; ++i) kv[i] = LDKV(rv, kvoff, i);
      asm volatile("" ::: "memory");
      SCORE16P(xh, kh, 0, e2, l3)
      const float s2 = SCALED(xh[0] + dpp<XOR1>(xh[0])), s3 = SCALED(xh[1] + dpp<XOR1>(xh[1]));
      const float m = wave_max(fmaxf(fmaxf(s0, s1), fmaxf(s2, s3)));
      const float p0 = __expf(s0 - m), p1 = __expf(s1 - m), p2 = __expf(s2 - m), p3 = __expf(s3 - m);
      const float l = wave_sum(fmaf(p2 + p3, 0.5f, p0 + p1));
      if (lane == 0) { st[wave] = m; st[8 + wave] = l; }
      pT[(wave * 4 + r) * 16 + (c & 1) * 8 + (c >> 1)] = cvt2(p0, p1);
      pT[((wave - 4) * 4 + r) * 16 + (c >> 1)] = cvt2(p2, p3);
      lds_arrive(C_PP + 4 * (wave - 4), lane);
      __builtin_amdgcn_s_setprio(0);
      STAMPT(27, 448); WSTAMP(1);
      t0 = __builtin_amdgcn_readfirstlane(cnt[15]) - (fav ? DL0 : 0);
      GET_P()
      u32 a_dl = C_DL;
      asm volatile("" : "+v"(a_dl));
#pragma unroll
      for (int i = NVE; i < NL; i += 2) {
        if (i == E3POS) PAD(e3, l4);
        if (i == E4POS) PAD(e4, t1);
        u32 om0;
        if (i == MLP) { RD_ROLE() om0 = (rw2 & 0x80000003u) == 0x80000000u && lane < 8 ? G_ML + BLKA(rw2 >> 8) * 64 : OOB; asm volatile("" : "+v"(om0)); }
        PACE2(96 + i)
        if (i == MLP) ml = ld_gran(rs, om0);
        kv[i] = LDKV(rv, kvoff, i);
        kv[i + 1] = LDKV(rv, kvoff, i + 1);
        asm volatile("" ::: "memory");
        if (i == NL - 2) { LDS_INC(C_ISS); STAMPT(26, 448); }
        if (i >= PVLAG) {
          PV2(i >= PVLAG ? i - PVLAG : 0)
#pragma unroll
          for (int k = 0; k < 8; ++k) asm volatile("" : "+v"(acc[k]));
        }
      }
    } else {
      const int ex = (g >= 120 && g < 126) ? EC : 0;
      u32 kva = (!half && ex > 2) ? OOB : kvoff, kvb = (!half && ex > 0) ? OOB : kvoff;
      const u32 kxa = (half && ex > 2) ? kvoff - 8 * NL * 1024u : OOB, kxb = (half && ex > 0) ? kvoff - 8 * NL * 1024u : OOB;
      u32x4 kx[4];
      const u32 t0 = __builtin_amdgcn_readfirstlane(cnt[15]);
#pragma unroll
      for (int i = 0; i < 16; ++i) {
        if (PK && (i & 3) == 0) PACE(48 + i)
        kh[i] = LDKV(rk, i < 12 ? kvoff : i < 14 ? kva : kvb, 16 + i);
      }
#pragma unroll
      for (int i = 0; i < 4; ++i) kx[i] = LDKV(rk, i < 2 ? kxa : kxb, 28 + i);
      LDS_INC(C_KI);
      if (wave == 0) {
        if (half == 0 && g >= 80 && g < 120) {
          const int fb = g - 80;
          u32 of2[2];
          u32x4 gm[2];
#pragma unroll
          for (int ks = 0; ks < 2; ++ks) of2[ks] = G_MOD + GIDX(1024, ks, r, 320 + fb * 16 + c);
          gather22<2, 4>(rs, of2, gm, tg);
          const f32x4 mv = un_g22(gm[0]) + un_g22(gm[1]);
          st_gran(rs, G_MF + (((fb / 10) * 4 + r) * 160 + (fb % 10) * 16 + c) * 16, cvt2(mv[0], mv[1]), cvt2(mv[2], mv[3]), tg);
        }
      }
      __builtin_amdgcn_s_setprio(0);
      STAMPT(2, 0);
#define GO(r_, n, name) "s_cmp_eq_u32 %" #r_ ", " #n "\n\ts_cbranch_scc1 .Lpad_" #name "\n\t"
      asm volatile("s_mov_b64 exec, 0\n\t" GO(0, 0, e1) GO(0, 1, e2) GO(0, 2, e3) GO(0, 3, l1)
                   ".Lpad_role:\n\t" GO(0, 0, c1) GO(0, 1, c0) GO(0, 2, b3) GO(0, 3, b0)
                   ".Lpad_back:\n\ts_mov_b64 exec, -1" :: "s"(wave) : "scc");
#undef GO
      STAMPT(7, 0);
      lds_wait<2>(C_KI, P_GATE);
      if (QHOLD && wave == qw - 4) lds_wait<2>(C_Q, 1);
      asm volatile("" : "+v"(kvoff), "+v"(kva), "+v"(kvb));
#pragma unroll
      for (int i = 0; i < NL; ++i) {
        if (i == L1POS) PAD(l1, e4);
        if (PL && (i & 3) == 0) PACE(64 + PLO + i * PLS / 16)
        kv[i] = LDKV(rv, i < 28 ? kvoff : i < 30 ? kva : kvb, i);
      }
      STAMPT(1, 0);
      GET_Q()
      const bool fixc = half == 0 && (g < 40 || (FXA && fav));
      const u32 tka = t == 0 ? G_TK + CLS * 128 : OOB;
      u32 tkv = __builtin_amdgcn_raw_ptr_buffer_atomic_add_i32(1, rs, half ? tka : OOB, 0, 16);
      asm volatile("" ::: "memory");
      float xx[4];
#pragma unroll
      for (int i = 0; i < 4; ++i) { float d = 0.f;
#pragma unroll
        for (int k = 0; k < 4; ++k) d = dot2(kx[i][k], qpk[k], d);
        xx[i] = d; }
      asm volatile("" : "+v"(xx[0]), "+v"(xx[1]), "+v"(xx[2]), "+v"(xx[3]) :: "memory");
#pragma unroll
      for (int i = 0; i < 4; ++i) kx[i] = LDKV(rv, i < 2 ? kxa : kxb, 28 + i);
      LDS_INC(C_ISS);
      TR_STEP(xx, 2, XOR8, 8)
#pragma unroll
      for (int i = 0; i < 2; ++i) { xx[i] += dpp<XOR7>(xx[i]); xx[i] += dpp<XOR2>(xx[i]); xx[i] += dpp<XOR1>(xx[i]); }
      SCORE16P(xh, kh, 0, l2, t2)
      const float ninf = -__builtin_inff();
      const bool mine = half || c < 16 - ex, extra = half && c >= 16 - 4 * ex;
      xh[0] += dpp<XOR1>(xh[0]); xh[1] += dpp<XOR1>(xh[1]);
      const float s2 = mine ? SCALED(xh[0]) : ninf, s3 = mine ? SCALED(xh[1]) : ninf;
      const float s4 = extra ? SCALED(xx[0]) : ninf, s5 = extra ? SCALED(xx[1]) : ninf;
      float m = wave_max(fmaxf(fmaxf(s2, s3), fmaxf(s4, s5)));
      lds_wait<2>(C_PP + 4 * wave, 1);
      const float me = st[wave + 4];
      m = fmaxf(m, me);
      const float p2 = __expf(s2 - m), p3 = __expf(s3 - m), p4 = __expf(s4 - m), p5 = __expf(s5 - m);
      const float l = wave_sum(fmaf(p4 + p5, 0.25f, p2 + p3)) * 0.5f;
      const u32 pxo = cvt2(p4, p5), pxt = dppu<XOR8>(pxo);
      const u32 px[2] = {c < 8 ? pxo : pxt, c < 8 ? pxt : pxo};
      if (lane == 0) { st[wave] = m; st[8 + wave] = l; }
      pT[(wave * 4 + r) * 16 + 8 + (c >> 1)] = cvt2(p2, p3);
      STAMPT(4, 0); WSTAMP(1);
      PAD(l3, t3);
      if (lds_ticket(C_SC, lane) == 3) {
        const float mw = st[lane & 7], lw = st[8 + (lane & 7)];
        float M = fmaxf(mw, dpp<XOR1>(mw)); M = fmaxf(M, dpp<XOR2>(M)); M = fmaxf(M, dpp<XOR7>(M));
        const float e = __expf(mw - M);
        float L = lw * e;
        L += dpp<XOR1>(L); L += dpp<XOR2>(L); L += dpp<XOR7>(L);
        if (lane == 0) st_gran(rs, G_ML + u * 64, f2u(M), f2u(L), tg);
        if (lane < 8) st[16 + lane] = e * frcp(L);
      }
      bool sub = false;
      if (wave == 0 && !fav) {
        const u32 tt = __builtin_amdgcn_readfirstlane(cnt[15]) + (126 * TAU_E >> 4) - EVT;
        sub = (int)((u32)__builtin_amdgcn_s_memrealtime() - tt) < EVW;
        SPINLOOP while ((int)((u32)__builtin_amdgcn_s_memrealtime() - tt) < 0) __builtin_amdgcn_s_sleep(1);
      }
      asm volatile("" ::: "memory");
      const u32 tkw = __builtin_amdgcn_raw_ptr_buffer_atomic_add_i32(fixc ? 0 : 0x10001, rs, sub && (SLS || !fixc) ? tka : OOB, 0, 16);
      asm volatile("" ::: "memory");
      asm("" : "=v"(ml));
      GET_P()
#pragma unroll
      for (int i = 0; i < 16; i += 2) { if (i == L4POS) PADA(l4, a0); PV2(i) }
      const float f = __expf(me - m);
#pragma unroll
      for (int k = 0; k < 8; ++k) acc[k] *= f;
#pragma unroll
      for (int i = 16; i < NL - PVLAG; i += 2) { PV2(i) }
#pragma unroll
      for (int i = 0; i < 4; i += 2)
#pragma unroll
        for (int k = 0; k < 4; ++k) {
          acc[2 * k] = dot2(__builtin_amdgcn_perm(kx[i + 1][k], kx[i][k], 0x05040100u), px[i / 2], acc[2 * k]);
          acc[2 * k + 1] = dot2(__builtin_amdgcn_perm(kx[i + 1][k], kx[i][k], 0x07060302u), px[i / 2], acc[2 * k + 1]);
        }
      asm volatile("" : "+v"(acc[0]), "+v"(acc[1]), "+v"(acc[2]), "+v"(acc[3]), "+v"(acc[4]), "+v"(acc[5]), "+v"(acc[6]), "+v"(acc[7]) :: "memory");
      rw2 = 0;
      if (wave == 0) {
        constexpr int NT2 = FXA ? 128 : 134;
        const u32 tks = __builtin_amdgcn_readfirstlane(tkv + tkw + 1u);
        const int n0 = (tks & 0xffff) * NCL + CLS;
        const int slow = sub ? -min(max(SLF - (int)((tks & 0xffff) - (tks >> 16)), 0) * SLS, SLM) : 0;
        const int n = fixc ? (g < 40 ? 134 + g : g + 8) : (fav || sub) ? sel_lt(n0, NT2, n0, 174) : 174;
        constexpr int N0 = 54;
        const int rl = sel_lt(n, N0, 1, sel_lt(n, 134, 0, sel_lt(n, 174, 2, 3)));
        int un = n - sel_lt(n, N0, 0, sel_lt(n, 134, N0, 134));
        if (NCL == 8 && rl == 1 && un < 48) un = (un & 48) | (un & 7) << 1 | (un >> 3 & 1);
        if (NCL == 4 && rl == 1 && un < 48) un = (un & 56) | (un & 3) << 1 | (un >> 2 & 1);
        if (lane == 0) *reinterpret_cast<u32x2*>(lds + C_RL) = u32x2{0x80000000u | (u32)(un & 255) << 8 | (u32)rl, (u32)(sel_lt(n, N0, DLB, sel_lt(n, 134, fav ? DLA : DLS, slow)) - (fav ? DL0 : 0))};
        STAMPT(28, 0);
      }
    }
#pragma unroll
    for (int i = NL - PVLAG; i <= NL - HX; i += 2) { if (i == T1POS) PADA(t1, role); PV2(i) }
    u32 a_aw = L_ACC + (wave * 128 + c * 8 + 2 * r) * 4, a_as = L_ST + 64 + (lane >> 5) * 16, a_ar = L_ACC + (lane >> 5) * 2048 + (lane & 31) * 8, one = 1;
    tkf = C_FIN;
    a_pub = G_ATT + u * 256 + lane * 4;
    asm volatile("" : "+v"(acc[0]), "+v"(acc[1]), "+v"(acc[2]), "+v"(acc[3]), "+v"(acc[4]), "+v"(acc[5]), "+v"(acc[6]), "+v"(acc[7]),
                      "+v"(a_aw), "+v"(a_as), "+v"(a_ar), "+v"(one), "+v"(tkf), "+v"(a_pub));
    if (!HT) { PV2(NL - HX + 2) } else {
      u32 tk = 0;
      u64 tn;
      asm volatile("s_mov_b64 exec, 1\n\tds_add_rtn_u32 %0, %10, %11\n\ts_mov_b64 exec, -1\n\ts_memrealtime %1"
                   : "+v"(tk), "=s"(tn), "+v"(acc[0]), "+v"(acc[1]), "+v"(acc[2]), "+v"(acc[3]), "+v"(acc[4]), "+v"(acc[5]), "+v"(acc[6]), "+v"(acc[7])
                   : "v"((u32)C_HT), "v"(1u) : "memory");
      PV2(NL - HX + 2)
      asm volatile("s_waitcnt lgkmcnt(0)" : "+v"(tk), "+s"(tn), "+v"(acc[0]), "+v"(acc[1]), "+v"(acc[2]), "+v"(acc[3]), "+v"(acc[4]), "+v"(acc[5]), "+v"(acc[6]), "+v"(acc[7]));
      __builtin_amdgcn_raw_buffer_store_b64(u32x2{(u32)tn + HTD, tg}, rs, tk == 7 ? G_HT + u * 128 : OOB, 0, 16);
      asm volatile("" ::: "memory");
    }
#pragma unroll
    for (int i = NL - HX + 4; i < NL - 2; i += 2) { PV2(i) }
    PV2(NL - 2)
#undef PV2
#undef TR_STEP
#undef SCORE16
#undef SCORE16P
#undef SCALED
#undef GET_Q
#undef GET_P
#undef LDKV
#pragma unroll
    for (int i = 0; i < 4; ++i) acc[i] = swap32_add(acc[i], acc[i + 4]);
#pragma unroll
    for (int i = 0; i < 2; ++i) acc[i] = swap16_add(acc[i], acc[i + 2]);
    asm volatile("ds_write_b64 %6, %7\n\ts_mov_b64 exec, 1\n\tds_add_rtn_u32 %0, %0, %8\n\ts_mov_b64 exec, -1\n\tds_read_b128 %1, %9\n\t"
                 "ds_read2_b64 %2, %10 offset1:32\n\tds_read2_b64 %3, %10 offset0:64 offset1:96\n\t"
                 "ds_read2_b64 %4, %10 offset0:128 offset1:160\n\tds_read2_b64 %5, %10 offset0:192 offset1:224\n\ts_waitcnt lgkmcnt(0)"
                 : "+v"(tkf), "=&v"(psc), "=&v"(pw4[0]), "=&v"(pw4[1]), "=&v"(pw4[2]), "=&v"(pw4[3])
                 : "v"(a_aw), "v"(f32x2{acc[0], acc[1]}), "v"(one), "v"(a_as), "v"(a_ar) : "memory");
    STAMPT(6, 0); WSTAMP(2);
  }
  const u32x4 rsv = {(u32)(uintptr_t)wsq, (u32)((uintptr_t)wsq >> 32) & 0xffffu, WS_END, 0x00020000u};
#define MLOOK(W_) asm volatile(LK(0, 1, "0", 0, 2) LKEND : "+v"(ml) : "v"(om), "s"(m2), [rs] "s"(rsv), [w] "i"(W_) : "memory");
  if (__builtin_amdgcn_readfirstlane(tkf) == 7) {
    PAD(t2, role);
    const f32x4 o = (psc[0] * pw4[0] + psc[1] * pw4[1]) + (psc[2] * pw4[2] + psc[3] * pw4[3]);
    __builtin_amdgcn_raw_buffer_store_b32(min(cvt2(swap32_add(o[0], o[2]), swap32_add(o[1], o[3])), EMPTY - 1), rs, a_pub, 0, 16);
    LDS_INC(C_PUB);
    STAMPW(11);
  }
  if ((int)rw2 >= 0) WAIT_ROLE()
  const int role = rw2 & 3, ia = (rw2 >> 8) & 255, jB = ia;
  const int ksA = ia & 7, cbA = ia >> 3;
  const u32 blkA = BLKA(ia), om = G_ML + blkA * 64;
  float b1v;
  u32x4 gaq;
  if (role == 0 && wave < 4) {
    if (RPRI) __builtin_amdgcn_s_setprio(RPRI);
    {
      const u32x4 rxv = {(u32)(uintptr_t)a.x.p, (u32)((uintptr_t)a.x.p >> 32) & 0xffffu, NB * DM * 2, 0x00020000u};
      asm volatile("buffer_load_dwordx4 %0, %2, %4, 0 offen sc1\n\tbuffer_load_dwordx2 %1, %3, %5, 0 offen" : "=&v"(gaq), "=&v"(xw)
                   : "v"(G_MF + (r * 160 + cbA * 16 + c) * 16), "v"(ksA == 0 ? (r * DM + cbA * 64 + c * 4) * 2 : OOB), "s"(rsv), "s"(rxv) : "memory");
    }
    if (half) STAMPT(8, 0);
    lds_wait<2>(C_ISS, 8);
    {
      const bf16* wp = a.wo + (size_t)(HEADA(wave) * 128 + (lane >> 3)) * DM + cbA * 64;
#pragma unroll
      for (int i = 0; i < 16; ++i) {
        if (i == 1) PAD(a0, role);
        if (i == N1A) {
          lds_wait<2>(C_PUB, 1);
          asm volatile("" ::: "memory");
        }
        dma16(wp + (size_t)(8 * i) * DM + (s128e ^ ((i & 1) * 32)), L_W + wave * 16384 + i * 1024);
      }
      LDS_INC(C_ISS);
    }
    STAMPT(16, 0);
  }
#define SELECTOR(sel) u32x4 sel; \
    _Pragma("unroll") for (int d = 0; d < 4; ++d) sel[d] = ((c & 2) == 0 && d == (c >> 2)) ? ((c & 1) ? 0x3f800000u : 0x00003f80u) : 0u;
#define PAIRSUM(sm) (((c & 8) ? sm[1] : sm[0]) + dpp<XOR8>((c & 8) ? sm[0] : sm[1]))
  const u32 tb = __builtin_amdgcn_readfirstlane(cnt[15]);
  if (role == 0) {
    PAD(a1, back);
    if (wave < 4) {
      u32 a_sum = L_RED + lane * 16;
      u32 a_out = wave < NREP ? G_WO + wave * WO_REP + ((cbA * 4 + (c >> 2)) * 8 + ksA) * 128 + r * 32 + (c & 3) * 8 : OOB;
      asm volatile("s_waitcnt vmcnt(4)" : "+v"(a_sum), "+v"(a_out), "+v"(gaq), "+v"(xw) :: "memory");
      LDS_INC(C_PP + 4 * wave);
      if (!__all(gaq[1] == tg && gaq[3] == tg)) {
        u32 offs[1] = {G_MF + (r * 160 + cbA * 16 + c) * 16};
        u32x4 gm[1];
        gather<1>(rs, offs, gm, tg);
        gaq = gm[0];
      }
      float ga4[4] = {lo16(gaq[0]), hi16(gaq[0]), lo16(gaq[2]), hi16(gaq[2])}, x4[4] = {lo16(xw[0]), hi16(xw[0]), lo16(xw[1]), hi16(xw[1])};
      asm volatile("" : "+v"(ga4[0]), "+v"(ga4[1]), "+v"(ga4[2]), "+v"(ga4[3]), "+v"(x4[0]), "+v"(x4[1]), "+v"(x4[2]), "+v"(x4[3]));
      STAMPT(19, 0);
      u32 a_fl = lane < 16 ? G_HA + ia * 128 + lane * 8 : OOB;
      PAD(a2, back);
      asm volatile("s_waitcnt vmcnt(0)" : "+v"(a_fl) :: "memory");
      LDS_INC(C_PP + 4 * wave);
      STAMPT(23, 0);
      if (wave == 0) {
        lds_wait<0>(C_W, 4);
        STAMPT(12, 0);
        __builtin_amdgcn_raw_buffer_store_b64(u32x2{(u32)__builtin_amdgcn_s_memrealtime() + HAG, tg}, rs, a_fl, 0, 16);
      }
      BAR();
      STAMPT(21, 0);
      f32x4 pp[4];
#pragma unroll
      for (int w = 0; w < 4; ++w) pp[w] = *reinterpret_cast<const f32x4*>(lds + a_sum + w * 1024);
      asm volatile("" : "+v"(pp[0]), "+v"(pp[1]), "+v"(pp[2]), "+v"(pp[3]));
      const f32x4 s = (pp[0] + pp[1]) + (pp[2] + pp[3]);
      const u32x2 o = {min(cvt2(fmaf(s[0], ga4[0], x4[0]), fmaf(s[1], ga4[1], x4[1])), EMPTY - 1), min(cvt2(fmaf(s[2], ga4[2], x4[2]), fmaf(s[3], ga4[3], x4[3])), EMPTY - 1)};
      __builtin_amdgcn_raw_buffer_store_b64(o, rs, a_out, 0, 16);
    } else {
      u32 oa = G_ATT + blkA * 256 + (c >> 3) * 64 + r * 16;
      u32 a_red = L_RED + (((wave & 3) * 4 + c) * 64 + r * 4) * 4;
      u32 a_w[4];
#pragma unroll
      for (int nt = 0; nt < 4; ++nt) a_w[nt] = L_W + (wave & 3) * 16384 + (tr128 ^ (nt * 32));
      u32x4 av[2];
      u64 m0 = ~0ull, m1 = ~0ull, m2 = 0xffull;
      av[0] = av[1] = u32x4{0u, 0u, 0u, 0u};
      asm volatile("" : "+v"(oa), "+v"(ml), "+v"(av[0]), "+v"(av[1]), "+v"(a_red), "+v"(a_w[0]), "+v"(a_w[1]), "+v"(a_w[2]), "+v"(a_w[3]) :: "memory");
      SPINLOOP for (unsigned spins = 0; spins < SPIN_CAP; ++spins) {
        m2 &= __builtin_amdgcn_ballot_w64(ml[1] != tg) | __builtin_amdgcn_ballot_w64(ml[3] != tg);
        if (!m2) break;
        if (!spins) FAIL(4);
        __builtin_amdgcn_s_sleep(ASL);
        MLOOK(0)
      }
      float wsc;
      {
        const float M = u2f(ml[0]), L = u2f(ml[2]), Mo = dpp<XOR7>(dpp<0x1B>(M)), Lo = dpp<XOR7>(dpp<0x1B>(L));
        const float Mx = fmaxf(M, Mo), le = L * __expf(M - Mx);
        wsc = le * frcp(fmaf(Lo, __expf(Mo - Mx), le));
        wsc = u2f((u32)__builtin_amdgcn_ds_bpermute((c & 7) * 4, (int)f2u(wsc)));
        asm volatile("" : "+v"(wsc));
      }
#define ALOOK(W_) \
        asm volatile(LK(0, 2, "0", 0, 3) LK(1, 2, "0", 128, 4) LKEND \
                     : "+v"(av[0]), "+v"(av[1]) : "v"(oa), "s"(m0), "s"(m1), [rs] "s"(rsv), [w] "i"(W_) : "memory");
#define ACHK() \
        m0 = __builtin_amdgcn_ballot_w64(missing(av[0])); \
        m1 = __builtin_amdgcn_ballot_w64(missing(av[1]));
      lds_wait<1>(C_PP + 4 * (wave - 4), 2);
      if (AGT) SPINLOOP for (int k = 0; k < 64; ++k) if ((int)((u32)__builtin_amdgcn_s_memrealtime() - tb) >= (126 * TAU_E >> 4) + AGT) break;
      STAMPT(29, 448);
      ALOOK(63)
      s16x8 wf[4][4];
#pragma unroll
      for (int s = 0; s < 3; ++s)
#pragma unroll
        for (int nt = 0; nt < 4; ++nt) wf[s][nt] = wfrag<512>(a_w[nt] + s * 4096);
      lds_wait<1>(C_PP + 4 * (wave - 4), 3);
#pragma unroll
      for (int nt = 0; nt < 4; ++nt) wf[3][nt] = wfrag<512>(a_w[nt] + 3 * 4096);
      u32 a_cw = C_W, one = 1;
      u64 mw = 0x000f000f000f000full;
      asm volatile("" : "+v"(wf[3][0]), "+v"(wf[3][1]), "+v"(wf[3][2]), "+v"(wf[3][3]), "+v"(a_cw), "+v"(one), "+s"(mw));
      PAD(a3, b4);
      asm volatile("s_waitcnt vmcnt(0)" : "+v"(av[0]), "+v"(av[1]) :: "memory");
      ACHK()
      STAMPT(18, 448);
      if (m0 | m1) {
        FAIL(1);
        SPINLOOP for (unsigned spins = 0; spins < SPIN_CAP; ++spins) {
          ALOOK(2 * (PDA - 1))
          ACHK()
          if (!(m0 | m1)) break;
          __builtin_amdgcn_s_sleep(ASL);
        }
      }
#undef ALOOK
#undef ACHK
      asm volatile("" : "+v"(av[0]), "+v"(av[1]));
      f32x4 acc[4], acb[4];
      const f32x4 z = {0.f, 0.f, 0.f, 0.f};
#define SB() __builtin_amdgcn_sched_barrier(0)
#define PIN(x) asm volatile("" : "+v"(x)); SB();
#define M1(n) { acc[n] = MFMA(wf[0][n], av[0], z); PIN(acc[n]) acb[n] = MFMA(wf[1][n], av[0], z); PIN(acb[n]) }
#define M2A(n) { acb[n] = MFMA(wf[3][n], av[1], acb[n]); PIN(acb[n]) }
#define M2B(n) { acc[n] = MFMA(wf[2][n], av[1], acc[n]); PIN(acc[n]) }
#define D8(n, k) { float v = acc[n][k] + dpp<XOR8>(acb[n][k]); PIN(v) acc[n][k] = v; v = acc[n][k + 1] + dpp<XOR8>(acb[n][k + 1]); PIN(v) acc[n][k + 1] = v; }
#define MW(n) { acc[n] *= wsc; PIN(acc[n]) }
#define D4(n) { _Pragma("unroll") for (int k = 0; k < 4; ++k) { float v = acc[n][k] + dpp<SHL4>(acc[n][k]); PIN(v) acc[n][k] = v; } }
#define WR(n) asm volatile("s_mov_b64 exec, %3\n\tds_write_b128 %0, %1 offset:" STR(n * 64) "\n\tds_write_b128 %0, %2 offset:" STR(n * 64 + 64) "\n\ts_mov_b64 exec, -1" \
                           :: "v"(a_red), "v"(acc[n]), "v"(acc[n + 1]), "s"(mw) : "memory")
      SB();
      M1(0)
      asm volatile("s_mov_b64 exec, 1\n\tds_add_u32 %0, %1\n\ts_mov_b64 exec, -1" :: "v"(a_cw), "v"(one) : "memory");
      SB();
      M1(1) M2A(0) M2B(0) M1(2) M2A(1) M2B(1) M1(3)
      M2A(2) D8(0, 0) M2B(2) D8(0, 2) M2A(3) D8(1, 0) M2B(3) D8(1, 2)
      MW(0) MW(1) D4(0) D4(1) D8(2, 0) D8(2, 2)
      WR(0);
      SB();
      D8(3, 0) D8(3, 2) MW(2) MW(3) D4(2) D4(3)
      WR(2);
#undef M1
#undef M2A
#undef M2B
#undef D8
#undef MW
#undef D4
#undef WR
      STAMPT(22, 448);
      BAR();
      asm volatile("" :: "v"(av[0]), "v"(av[1]));
    }
    STAMPT(13, 0);

  } else if (role == 1) {
    PAD(b0, bd);
    u32x2 bw2;
    asm("" : "=v"(bw2));
    if (wave < 4) {
      if (RPRI) __builtin_amdgcn_s_setprio(RPRI);
#pragma unroll
      for (int i = 0; i < 5; ++i) gmf[i] = __builtin_amdgcn_raw_buffer_load_b128(rs, G_MF + (640 + t) * 16, i * 4096, 16);
      const __amdgpu_buffer_rsrc_t rb = __builtin_amdgcn_make_buffer_rsrc((void*)a.b1, 0, FF * 2, 0x00020000);
      bw2 = __builtin_amdgcn_raw_buffer_load_b64(rb, (jB * 32 + (t & 28)) * 2, 0, 0);
      asm volatile("" ::: "memory");
    }
    const int lb = (c >> 1) & 3, lc = (c & 1) * 8 + 2 * r;
    const int fc = lane & 31, hw = lane >> 5;
    SELECTOR(sel)
    constexpr int L_R2 = L_MOD, L_S2 = L_R2 + 4096;
    const u32 gw = G_WO + wave * 5120;
    u32 go = gw + (c >> 3) * 1024 + r * 128 + (c & 7) * 16, go4 = gw + 4096 + lane * 16;
    u32 a_u = L_XA + wave * 640 + lb * 160 + ((c >> 3) * 16 + lc) * 2, a_u4 = L_XA + wave * 640 + lb * 160 + (64 + lc + (c >> 3)) * 2;
    u32 a_av = L_XA + wave * 640 + (c & 3) * 160 + r * 16, a_ah = a_av + 128 - r * 8;
    u32 a_red = L_R2 + ((wave * 4 + c) * 32 + r * 4) * 4;
    u32 a_st = L_S2 + ((lb * 2 + (c >> 3)) * 8 + wave) * 4;
    u32 a_fin = L_R2 + ((hw * 16 + (wave & 3)) * 32 + fc) * 4;
    u32 a_fst = L_S2 + (wave & 3) * 64;
    u32 a_out = c < 4 ? G_OUT + (wave * 5 * 54 + jB) * 128 + c * 32 + r * 8 : OOB;
    constexpr int L_F2 = L_XR;
    u32 a_fi = BM ? L_F2 + c * 64 + r * 16 : L_FI + (c & 3) * 64 + r * 16, a_fo = (BM ? L_F2 + 768 : L_FI) + ((wave & 3) * 32 + fc) * 2;
    u32 a_ou4 = G_OUT + ((wave * 5 + (c >> 2)) * 54 + jB) * 128 + (c & 3) * 32 + r * 8;
    asm volatile("" : "+v"(a_ou4));
    u32 a_pre = lane < 40 ? G_OUT + ((wave * 5 + (lane >> 3)) * 54 + jB) * 128 + (lane & 7) * 16 : OOB;
    u32x4 emp = {EMPTY, EMPTY, EMPTY, EMPTY};
    asm volatile("" : "+v"(a_pre), "+v"(emp));
#define OPRE() asm volatile("buffer_store_dwordx4 %0, %1, %2, 0 offen sc1\n\ts_nop 1" :: "v"(emp), "v"(a_pre), "s"(rsv) : "memory")
    if (OPOS == 0) OPRE();
    u32 a_w1 = L_W + wave * 5120 + (8 * r + (c >> 2)) * 64 + (r & 1) * 32 + (c & 3) * 8;
    u32 a_wh = L_W + wave * 5120 + 4096 + (4 * r + (c >> 2)) * 64 + (r >> 1) * 32 + (c & 3) * 8;
    u32 a_w2[5];
#pragma unroll
    for (int k = 0; k < 5; ++k) { const int n2 = wave * 5 + k; a_w2[k] = L_WM2 + (n2 >> 2) * 4096 + (tr128 ^ ((n2 & 3) * 32)); }
    asm volatile("" : "+v"(sel), "+v"(go), "+v"(go4), "+v"(a_u), "+v"(a_u4), "+v"(a_av), "+v"(a_ah), "+v"(a_red), "+v"(a_st), "+v"(a_fin), "+v"(a_fst), "+v"(a_out));
    asm volatile("" : "+v"(a_fi), "+v"(a_fo), "+v"(a_w1), "+v"(a_wh), "+v"(a_w2[0]), "+v"(a_w2[1]), "+v"(a_w2[2]), "+v"(a_w2[3]), "+v"(a_w2[4]) :: "memory");
    if (BM) *reinterpret_cast<u32*>(lds + L_F2 + t * 4) = 0;
    PAD(b4, back);
    asm volatile("" : "+v"(bw2));
    if (wave < 4) {
      STAMPT(7, 0);
      bool ok = true;
#pragma unroll
      for (int i = 0; i < 5; ++i) ok &= (gmf[i][1] == tg) & (gmf[i][3] == tg);
      if (!__all(ok)) {
        u32 offs[5];
#pragma unroll
        for (int i = 0; i < 5; ++i) offs[i] = G_MF + (640 + t + 256 * i) * 16;
        gather<5>(rs, offs, gmf, tg);
      }
#pragma unroll
      for (int i = 0; i < 5; ++i) {
        if (i == 2) PAD(t3, role);
        const int it = t + 256 * i, row = it / 160, gi = it % 160;
        u32 w0 = gmf[i][0], w1 = gmf[i][2];
        if (row >= 4) { w0 = cvt2(1.f + lo16(w0), 1.f + hi16(w0)); w1 = cvt2(1.f + lo16(w1), 1.f + hi16(w1)); }
        *reinterpret_cast<u32x2*>(lds + L_XA + (row < 4 ? row + 8 : row) * XS + gi * 8) = u32x2{w0, w1};
      }
      const u32 bw = (t & 2) ? bw2[1] : bw2[0];
      b1v = (t & 1) ? hi16(bw) : lo16(bw);
      asm volatile("" : "+v"(b1v));
      lds_arrive(C_MF, lane);
    }
    lds_wait<1>(C_MF, 4);
    float c1f[5];
    {
      const unsigned char* cp = lds + L_XA + (4 + lb) * XS + wave * 160;
#pragma unroll
      for (int i = 0; i < 2; ++i) { const u32 w = *reinterpret_cast<const u32*>(cp + (32 * i + (c >> 3) * 16 + lc) * 2); c1f[2 * i] = lo16(w); c1f[2 * i + 1] = hi16(w); }
      c1f[4] = u2f((u32)*reinterpret_cast<const unsigned short*>(cp + (64 + lc + (c >> 3)) * 2) << 16);
    }
    const unsigned char* pm = lds + L_XA + (4 + (c & 7)) * XS + wave * 160;
    u32x4 mv0 = *reinterpret_cast<const u32x4*>(pm + r * 16), mv1 = *reinterpret_cast<const u32x4*>(pm + 64 + r * 16);
    s16x4 mv2 = *reinterpret_cast<const s16x4*>(pm + 128 + r * 8);
    u32 a_rd = L_RED + ((wave * 8 + c) * 32 + r * 4) * 4;
    asm volatile("" : "+v"(c1f[0]), "+v"(c1f[1]), "+v"(c1f[2]), "+v"(c1f[3]), "+v"(c1f[4]), "+v"(mv0), "+v"(mv1), "+v"(mv2), "+v"(a_rd) :: "memory");
    if (wave < 4) {
      STAMPT(8, 0);
      lds_wait<2>(C_ISS, 8);
      if (BHOLD) SPINLOOP for (int k = 0; k < 256; ++k) { if ((int)((u32)__builtin_amdgcn_s_memrealtime() - tb) >= (126 * TAU_E >> 4) + BHOLD) break; __builtin_amdgcn_s_sleep(2); }
      const int h5 = ROT1 * (jB & 1);
      const bf16* wp = a.w1 + (size_t)(wave * 160 + (lane >> 2)) * FF + jB * 32 + ((((((lane >> 1) & 1) ^ (lane >> 5)) << 1) | (lane & 1)) * 8);
      const bf16* wp2 = a.w2 + (size_t)(jB * 32 + wave * 8 + (lane >> 3)) * DM + (s128e ^ ((wave & 1) * 32));
#pragma unroll
      for (int n = 0; n < 20; ++n) {
        if (n == 0) PAD(bd, back);
        if (n == N1B) lds_wait<2>(C_PUB, 1);
        if (n < 5 || (n >= 5 + W2F && n < 10 + W2F)) {
          const int i2 = n < 5 ? n + h5 : n - W2F - h5;
          dma16(wp + (size_t)(16 * i2) * FF, L_W + (wave * 10 + i2) * 1024);
        } else {
          const int k = n < 5 + W2F ? n - 5 : n - 10;
          dma16(wp2 + k * 64, L_WM2 + (k * 4 + wave) * 1024);
        }
      }
      STAMPT(16, 0); STAMPT(5, 128);
    }
    asm volatile("s_waitcnt vmcnt(%0)" :: "i"(10 - W2F) : "memory");
    STAMPT(23, 0);
    PAD(b1, a1);
    BAR();
    STAMPT(3, 0);
    if (OPOS == 1) OPRE();
    s16x8 wf[2][2], wg[5];
    s16x4 wh[2];
#pragma unroll
    for (int nt = 0; nt < 2; ++nt) {
#pragma unroll
      for (int s = 0; s < 2; ++s) wf[s][nt] = wfrag<256>((a_w1 ^ (nt * 32)) + s * 2048);
      wh[nt] = __builtin_amdgcn_ds_read_tr16_b64_v4i16((lds_s4p)(uintptr_t)(a_wh ^ (nt * 32)));
    }
#define W1MMA(acc) \
      f32x4 acc[2]; \
      _Pragma("unroll") for (int nt = 0; nt < 2; ++nt) { const f32x4 z_ = {0.f, 0.f, 0.f, 0.f}; acc[nt] = MFMA(wf[0][nt], av0, z_); } \
      _Pragma("unroll") for (int nt = 0; nt < 2; ++nt) acc[nt] = MFMA(wf[1][nt], av1, acc[nt]); \
      _Pragma("unroll") for (int nt = 0; nt < 2; ++nt) acc[nt] = __builtin_amdgcn_mfma_f32_16x16x16bf16_1k(wh[nt], av2, acc[nt], 0, 0, 0);
    {
      const u32x4 av0 = mv0, av1 = mv1;
      const s16x4 av2 = mv2;
      W1MMA(acc)
#pragma unroll
      for (int nt = 0; nt < 2; ++nt) if (c < 8) *reinterpret_cast<f32x4*>(lds + a_rd + nt * 64) = acc[nt];
    }
    float Tc = 0.f, Ts = 0.f;
#define TSUM() _Pragma("unroll") for (int k = 0; k < 4; ++k) { Tc += red[((hw * 4 + k) * 8 + (wave & 3)) * 32 + fc]; Ts += red[((hw * 4 + k) * 8 + 4 + (wave & 3)) * 32 + fc]; }
    u32x4 pa[5];
    f32x4 s4, s0, s1;
    u32 a_fl = lane < 16 ? G_HB + jB * 128 + lane * 8 : OOB, tk = C_W;
    u32x2 fv = {1u, tg};
    {
      u64 m[5], old = 0;
#pragma unroll
      for (int i = 0; i < 5; ++i) { m[i] = ~0ull; pa[i] = u32x4{0u, 0u, 0u, 0u}; }
#pragma unroll
      for (int i = 0; i < 5; ++i) asm volatile("" : "+v"(c1f[i]), "+v"(pa[i]));
#define BLOOK(W_) \
        asm volatile(LK(0, 5, "0", 0, 7) LK(1, 5, "0", 512, 8) LK(2, 5, "0", 2048, 9) LK(3, 5, "0", 2560, 10) LK(4, 6, "0", 0, 11) LKEND \
                     : "+v"(pa[0]), "+v"(pa[1]), "+v"(pa[2]), "+v"(pa[3]), "+v"(pa[4]) \
                     : "v"(go), "v"(go4), "s"(m[0]), "s"(m[1]), "s"(m[2]), "s"(m[3]), "s"(m[4]), [rs] "s"(rsv), [w] "i"(W_) : "memory");
      STAMPT(9, 0); STAMPT(30, 448);
      if (BPREW) {
        VM0();
        STAMPT(20, 0); STAMPT(31, 448);
        BAR();
        STAMPT(29, 0);
      }
      SPINLOOP for (int k = 0; k < 256; ++k) if ((int)((u32)__builtin_amdgcn_s_memrealtime() - tb) >= (126 * TAU_E >> 4) + BGT) break;
      STAMPT(21, 0);
      BLOOK(5)
      if (!BPREW) { STAMPT(20, 0); STAMPT(31, 448); }
      if (!BAN) announce(rs, G_HB + jB * 128, C_GO, C_W, 8, tb, HCD, tg);
      if (!BPREW) { BAR(); STAMPT(29, 0); }
#pragma unroll
      for (int k = 0; k < 5; ++k) wg[k] = wfrag<512>(a_w2[k]);
      TSUM()
#pragma unroll
      for (int i = 0; i < 5; ++i) asm volatile("" : "+v"(wg[i]));
      asm volatile("" : "+v"(Tc), "+v"(Ts));
      PAD(b2, a2);
      STAMPT(18, 0);
      asm volatile("s_waitcnt vmcnt(%8)" : "+v"(pa[0]), "+v"(pa[1]), "+v"(pa[2]), "+v"(pa[3]), "+v"(pa[4]), "+v"(a_fl), "+v"(fv), "+v"(tk) : "i"(BAN ? 0 : 1) : "memory");
      STAMPT(22, 0);
#define BSUM() { const f32x4 z = {0.f, 0.f, 0.f, 0.f}; \
      s0 = MFMA(sel, pa[0], z); PIN(s0) s1 = MFMA(sel, pa[2], z); PIN(s1) s0 = MFMA(sel, pa[1], s0); PIN(s0) s1 = MFMA(sel, pa[3], s1); PIN(s1) s4 = MFMA(sel, pa[4], z); PIN(s4) \
      if (BAN) asm volatile("s_memrealtime s[98:99]" ::: "s98", "s99"); SB(); }
#define BNAN() (__builtin_amdgcn_ballot_w64(__builtin_isunordered(s0[0], s0[1])) | __builtin_amdgcn_ballot_w64(__builtin_isunordered(s1[0], s1[1])) | \
                __builtin_amdgcn_ballot_w64(__builtin_isunordered(s4[0], s4[1])))
      BSUM()
      if (BNAN()) {
        FAIL(2);
        bool again = true;
        if (BRT) {
          u32x2 hv;
          const u32 fo = G_HA + ((wave * 5 >> 2) * 8 + lane) * 128;
#pragma unroll
          for (int i = 0; i < 5; ++i) m[i] = __builtin_amdgcn_ballot_w64(missing(pa[i]));
          BLOOK(63)
          heads_up_start(rsv, fo, 0xffffull, tg, hv);
          asm volatile("s_waitcnt vmcnt(1)" : "+v"(pa[0]), "+v"(pa[1]), "+v"(pa[2]), "+v"(pa[3]), "+v"(pa[4]) :: "memory");
          BSUM()
          again = BNAN();
          if (again) {
            FAIL(5);
            heads_up<BSL>(rsv, fo, 0xffffull, tg, hv);
            hv[0] += BXD;
            go += WO_REP; go4 += WO_REP;
            heads_up_time(0xffffull, hv);
          }
          asm volatile("s_waitcnt vmcnt(0)" : "+v"(hv) :: "memory");
        }
        if (again) SPINLOOP for (unsigned spins = 0; spins < SPIN_CAP; ++spins) {
          u64 bad = 0;
#pragma unroll
          for (int i = 0; i < 5; ++i) { m[i] = __builtin_amdgcn_ballot_w64(missing(pa[i])); bad |= m[i]; }
          if (!(bad | old)) break;
          old = bad;
          BLOOK(5 * (PDB2 - 1))
          BSUM()
          if (!BNAN()) break;
        }
      }
    }
#define BFLAG(W_) u32 tl; \
        asm volatile("s_waitcnt lgkmcnt(%3)\n\ts_add_u32 %0, s98, %2" : "=s"(tl), "+v"(tk) : "i"(HBD), "i"(W_) : "scc"); \
        fv[0] = tl; \
        asm volatile("v_readfirstlane_b32 %0, %1\n\ts_cmp_eq_u32 %0, 7\n\ts_cbranch_scc0 1f\n\tbuffer_store_dwordx2 %2, %3, %4, 0 offen sc1\n1:" \
                     : "=&s"(tl) : "v"(tk), "v"(fv), "v"(a_fl), "s"(rsv) : "scc"); \
        SB();
    if (BAN >= 2) asm volatile("s_mov_b64 exec, 1\n\tds_add_rtn_u32 %0, %0, %1\n\ts_mov_b64 exec, -1" : "+v"(tk) : "v"(fv[0]));
    PAD(b3, a3);
    SB();
    STAMPT(14, 0); WSTAMP(3);
    float xf[5];
    {
      const u32 w0 = cvt2(s0[0], s0[1]), w1 = cvt2(s1[0], s1[1]);
      xf[0] = lo16(w0); xf[1] = hi16(w0); xf[2] = lo16(w1); xf[3] = hi16(w1);
      *reinterpret_cast<u32*>(lds + a_u) = cvt2(xf[0] * c1f[0], xf[1] * c1f[1]);
      *reinterpret_cast<u32*>(lds + a_u + 64) = cvt2(xf[2] * c1f[2], xf[3] * c1f[3]);
    }
    STAMPT(19, 0);
    {
      u32x4 av0 = *reinterpret_cast<const u32x4*>(lds + a_av), av1 = *reinterpret_cast<const u32x4*>(lds + a_av + 64);
      SB();
      if (BAN == 2) { BFLAG(3) }
      float e0 = s4[0], e1 = s4[1];
      asm volatile("" : "+v"(e0), "+v"(e1));
      xf[4] = bfr(((c & 8) ? e1 : e0) + dpp<XOR8>((c & 8) ? e0 : e1));
      *reinterpret_cast<unsigned short*>(lds + a_u4) = (unsigned short)cvt2(xf[4] * c1f[4], 0.f);
      s16x4 av2 = *reinterpret_cast<const s16x4*>(lds + a_ah);
      SB();
      if (BAN == 1) asm volatile("s_mov_b64 exec, 1\n\tds_add_rtn_u32 %0, %0, %1\n\ts_mov_b64 exec, -1" : "+v"(tk) : "v"(fv[0]));
      SB();
      if (BAN == 3) { BFLAG(5) }
      const float ps = ((xf[0] + xf[1]) + (xf[2] + xf[3])) + xf[4];
      const float pq = fmaf(xf[0], xf[0], fmaf(xf[1], xf[1], xf[4] * xf[4])) + fmaf(xf[2], xf[2], xf[3] * xf[3]);
      const float t1 = ps + dpp<XOR8>(ps), t2 = pq + dpp<XOR8>(pq);
      float z = (c & 8) ? t2 : t1;
      z += dpp<XOR1>(z);
      asm volatile("" : "+v"(z));
      __builtin_amdgcn_sched_barrier(0);
      W1MMA(acc)
      if (BAN == 1) {
        asm volatile("" : "+v"(acc[0]), "+v"(acc[1]));
        BFLAG(0)
      }
      z = cross_rows_sum(z);
      if ((lane & 49) == 0) *reinterpret_cast<float*>(lds + a_st) = z;
      if (c < 4) {
#pragma unroll
        for (int nt = 0; nt < 2; ++nt) *reinterpret_cast<f32x4*>(lds + a_red + nt * 64) = acc[nt];
      }
    }
#undef W1MMA
    BAR();
    STAMPT(24, 0);
    if (wave < 4) {
      f32x4 sv[4];
      float sp[4];
#pragma unroll
      for (int k = 0; k < 4; ++k) sv[k] = *reinterpret_cast<const f32x4*>(lds + a_fst + k * 16);
#pragma unroll
      for (int k = 0; k < 4; ++k) sp[k] = *reinterpret_cast<const float*>(lds + a_fin + k * 512);
      __builtin_amdgcn_sched_barrier(0);
      const f32x4 s1 = sv[0] + sv[1], s2 = sv[2] + sv[3];
      const float mean = ((s1[0] + s1[1]) + (s1[2] + s1[3])) * (1.f / DM);
      const float var = fmaxf(((s2[0] + s2[1]) + (s2[2] + s2[3])) * (1.f / DM) - mean * mean, 0.f);
      const float rstd = __builtin_amdgcn_rsqf(var + 1e-5f);
      const float zp = fmaf(rstd, fmaf(-mean, Tc, (sp[0] + sp[1]) + (sp[2] + sp[3])), Ts);
      const float hh = bfr(bfr(swap32_add(zp, zp)) + b1v);
      const float e2 = __builtin_amdgcn_exp2f(hh * fmaf(hh * hh, 0.1029432397f, 2.302208198f));
      const float f = fmaf(-hh, frcp(e2 + 1.f), hh);
      *reinterpret_cast<unsigned short*>(lds + a_fo) = (unsigned short)cvt2(f, 0.f);
    }
    BAR();
    if (BM) {
      u32x4 av[4];
#pragma unroll
      for (int k = 0; k < 4; ++k) av[k] = *reinterpret_cast<const u32x4*>(lds + a_fi + (3 - k) * 256);
      SB();
      const f32x4 z = {0.f, 0.f, 0.f, 0.f};
      f32x4 a4 = MFMA(wg[4], av[0], z);
      PIN(a4)
      f32x4 a0 = MFMA(wg[0], av[0], z);
      PIN(a0)
#pragma unroll
      for (int k = 1; k < 4; ++k) { a0 = MFMA(wg[k], av[k], a0); PIN(a0) }
      __builtin_amdgcn_sched_barrier(0);
      const u32x2 o4 = {min(cvt2(a4[0], a4[1]), EMPTY - 1), min(cvt2(a4[2], a4[3]), EMPTY - 1)};
      __builtin_amdgcn_raw_buffer_store_b64(o4, rs, a_out, 4 * (54 * 128), 16);
      const u32x2 o0 = {min(cvt2(a0[0], a0[1]), EMPTY - 1), min(cvt2(a0[2], a0[3]), EMPTY - 1)};
      __builtin_amdgcn_raw_buffer_store_b64(o0, rs, a_ou4, 0, 16);
    } else {
      const u32x4 av = *reinterpret_cast<const u32x4*>(lds + a_fi);
      f32x4 acc[5];
#pragma unroll
      for (int k = 0; k < 5; ++k) { const f32x4 z = {0.f, 0.f, 0.f, 0.f}; acc[k] = MFMA(wg[k], av, z); }
      __builtin_amdgcn_sched_barrier(0);
#pragma unroll
      for (int k = 0; k < 5; ++k) {
        const u32x2 o = {min(cvt2(acc[k][0], acc[k][1]), EMPTY - 1), min(cvt2(acc[k][2], acc[k][3]), EMPTY - 1)};
        __builtin_amdgcn_raw_buffer_store_b64(o, rs, a_out, k * (54 * 128), 16);
      }
    }
    STAMPT(15, 0);
    asm volatile("" :: "v"(pa[0]), "v"(pa[1]), "v"(pa[2]), "v"(pa[3]), "v"(pa[4]));

  } else if (role == 2 && wave == 0) {
    PAD(c0, b1);
    const int lb = (c >> 1) & 3, col = ia * 16 + (c & 1) * 8 + 2 * r + (c >> 3);
    bf16* outp;
    asm volatile("s_load_dwordx2 %0, %1, 128\n\ts_waitcnt lgkmcnt(0)" : "=s"(outp) : "s"(__builtin_amdgcn_kernarg_segment_ptr()));
    const __amdgpu_buffer_rsrc_t ro = __builtin_amdgcn_make_buffer_rsrc(outp, 0, NB * DM * 2, 0x00020000);
#define DUMMY() __builtin_amdgcn_raw_buffer_store_b16((unsigned short)0, ro, (lb * DM + col) * 2, 0, DAUX)
    if (DPOS == 0) DUMMY();
    float b2v = bf2f(a.b2[col]);
    const f32x4 z = {0.f, 0.f, 0.f, 0.f};
    SELECTOR(sel)
    float x1v, gbv;
    {
      u32 offs[1] = {G_MF + ((12 + lb) * 160 + (col >> 2)) * 16};
      u32x4 gm[1];
      gather<1>(rs, offs, gm, tg);
      const u32 gbw = (r & 1) ? gm[0][2] : gm[0][0];
      gbv = (c & 8) ? hi16(gbw) : lo16(gbw);
    }
    u32 oo = lb * DM + col;
    asm volatile("" : "+v"(gbv), "+v"(oo), "+v"(sel), "+v"(b2v));
    u32x4 pa[7];
    u32 go = G_OUT + ia * (54 * 128) + lane * 16, gx = G_WO + ia * 1024 + lane * 16;
#pragma unroll
    for (int i = 0; i < 7; ++i) { pa[i] = u32x4{0u, 0u, 0u, 0u}; asm volatile("" : "+v"(pa[i])); }
    asm volatile("" : "+v"(go), "+v"(gx));
    u32x2 hv;
    if (CPUB) lds_wait<2>(C_PUB, 1);
    if (DPOS == 1) DUMMY();
    heads_up_start(rsv, G_HA + ((ia >> 2) * 8 + lane) * 128, 0xffull, tg, hv);
    heads_up<CSL>(rsv, G_HA + ((ia >> 2) * 8 + lane) * 128, 0xffull, tg, hv);
    if (DPOS == 2) DUMMY();
    if (EPE == 1) __builtin_amdgcn_raw_buffer_store_b32(tg, rs, ia == 0 && lane == 0 ? WS_EPOCH : OOB, 0, 16);
    hv[0] += CXD;
    heads_up_time(0xffull, hv);
    {
      u32x4 gm[1];
      gather_e<1, 2, 1>(rsv, gx, gm, ~0ull);
      const f32x4 sm = MFMA(sel, gm[0], z);
      x1v = bfr(PAIRSUM(sm));
      asm volatile("" : "+v"(x1v));
    }
    STAMPT(23, 0);
    if (DPOS == 3) DUMMY();
    if (EPE == 2) __builtin_amdgcn_raw_buffer_store_b32(tg, rs, ia == 0 && lane == 0 ? WS_EPOCH : OOB, 0, 16);
    PAD(c1, b2);
    if (CGT) SPINLOOP for (int k = 0; k < 256; ++k) if ((int)((u32)__builtin_amdgcn_s_memrealtime() - tb) >= (126 * TAU_E >> 4) + CGT) break;
    heads_up_start(rsv, G_HB + lane * 128, (1ull << 54) - 1, tg, hv);
    heads_up<0>(rsv, G_HB + lane * 128, (1ull << 54) - 1, tg, hv);
    heads_up_time((1ull << 54) - 1, hv);
    STAMPT(14, 0);
    f32x4 s0, s1;
    {
      u64 m[7], old = 0;
#pragma unroll
      for (int i = 0; i < 7; ++i) m[i] = i < 6 ? ~0ull : (1ull << 48) - 1;
      look<63>(rsv, go, pa, m);
#define CW(i, W_) asm volatile("s_waitcnt vmcnt(%1)" : "+v"(pa[i]) : "i"(W_ ? 6 - (i) : 63) : "memory");
#define CSUM(W_) { CW(0, W_) s0 = MFMA(sel, pa[0], z); PIN(s0) CW(1, W_) s1 = MFMA(sel, pa[1], z); PIN(s1) \
      CW(2, W_) s0 = MFMA(sel, pa[2], s0); PIN(s0) CW(3, W_) s1 = MFMA(sel, pa[3], s1); PIN(s1) CW(4, W_) s0 = MFMA(sel, pa[4], s0); PIN(s0) \
      CW(5, W_) s1 = MFMA(sel, pa[5], s1); PIN(s1) CW(6, W_) s0 = MFMA(sel, pa[6], s0); PIN(s0) s0 += s1; }
      CSUM(1)
      if (__builtin_amdgcn_ballot_w64(__builtin_isunordered(s0[0], s0[1]))) {
        FAIL(3);
        SPINLOOP for (unsigned spins = 0; spins < SPIN_CAP; ++spins) {
          u64 bad = 0;
#pragma unroll
          for (int i = 0; i < 7; ++i) { m[i] = __builtin_amdgcn_ballot_w64(missing(pa[i])); bad |= m[i]; }
          if (!(bad | old)) break;
          old = bad;
          look<7 * (PDC - 1)>(rsv, go, pa, m);
          CSUM(0)
          if (!__builtin_amdgcn_ballot_w64(__builtin_isunordered(s0[0], s0[1]))) break;
        }
      }
    }
    STAMPT(16, 0);
    const float y = bfr(bfr(PAIRSUM(s0)) + b2v);
    if (OUTM & 1) __builtin_amdgcn_raw_buffer_store_b16((unsigned short)cvt2(x1v + bfr(gbv * y), 0.f), ro, oo * 2, 0, FAUX);
    else outp[oo] = __float2bfloat16(x1v + bfr(gbv * y));
    STAMPT(17, 0);
    asm volatile("" :: "v"(pa[0]), "v"(pa[1]), "v"(pa[2]), "v"(pa[3]), "v"(pa[4]), "v"(pa[5]), "v"(pa[6]), "v"(hv));

    if (!EPE && ia == 0 && t == 0)
      __hip_atomic_store(reinterpret_cast<u32*>(a.ws + WS_EPOCH), tg, __ATOMIC_RELAXED, __HIP_MEMORY_SCOPE_AGENT);
  }
  STAMP_FLUSH;
}

extern "C" void kernel_launch(void* const* d_in, const int* in_sizes, int n_in, void* d_out, int out_size, void* d_ws,
                              size_t ws_size, hipStream_t stream) {
  Args a;
  a.x = Desc{(const bf16*)d_in[0], NB * DM * 2, 0x00020000u};
  a.kc = (const bf16*)d_in[1];
  a.vc = (const bf16*)d_in[2];
  a.wq = Desc{(const bf16*)d_in[3], DM * QD * 2, 0x00020000u};
  a.wo = (const bf16*)d_in[4];
  a.w1 = (const bf16*)d_in[6];
  a.b1 = (const bf16*)d_in[7];
  a.w2 = (const bf16*)d_in[8];
  a.b2 = (const bf16*)d_in[9];
  a.c = Desc{(const bf16*)d_in[10], NB * DM * 2, 0x00020000u};
  a.wmod = Desc{(const bf16*)d_in[11], DM * MODN * 2, 0x00020000u};
  a.out = (bf16*)d_out;
  a.ws = (unsigned char*)d_ws;
  mega_kernel<<<NWG, NT, 0, stream>>>(a);
}
```

```cpp
#define SPINLOOP _Pragma("nounroll")
#define P_LAG 14
#define P_NPRE 32
#define P_NVE 6
#define P_NVA 6
#define QODD 4
#define QEVEN 4
#define MFPOS 0
#define EA 0
#define EB 0
#define EC 4
#define APOLL 0
#define N1E N1A
#define TAU_A 236
#define TAU_B 239
#define TAU_X TAU_A
#define TAU_E 244
#define PK 0
#define PL 1
#define QPRE 1
#define QHOLD 0
#define CT0 1
#define TOFF 55
#define BLIGHT 1
#define BPRE 0
#define BW1 1
#define BMASK 1
#define CSLEEP 1
#define CSL 2
#define CPUB 1
#define CXD 20
#define OPOS 1
#define NREPL 2
#define OUTPRE 1
#define ASL 1
#define AEARLY 1
#define PDC 4
#define PDA 3
#define PDB 3
#define PSB 1
#define HC 1
#define HCD 132
#define HAG 40
#define AW 4
#define BFW -1
#define FULLF 1
#define WARM 1
#define HT 0
#define HX 14
#define HTD 55
#define HSL 1
#define NANT 1
#define HAW 65
#define BM 1
#define HPD 1
#define BT 0
#define BSL 2
#define PB 0
#define GOPOS 2
#define JMAP 1
#define N1A 13
#define N1B 5
#define PT 0
#define PTAUX 16
#define LAY 1
#define OUTM 1
#define EPE 0
#define DPOS 2
#define DAUX 16
#define FAUX 16
#define KOUT 1
#define NK0 12
#define GT 540
#define PLO 5
#define PLS 17
#define ANCH 1
#define AOFF 55
#define SDZ 80
#define SSH 30
#define GX 0
#define AGG 1
#define PHW 0
#define CPF 1
#define CLEN 0x8080
#define AGT 150
#define GPAD 1
#define MFE 1
#define E3POS 10
#define E4POS 22
#define L1POS 28
#define L4POS 4
#define T1POS 18
#define HEARLY 0
#define BAR2L 0
#define ROT1 5
#define W2F 0
#define HGT 300
#define BGT 286
#define BPREW 0
#define BAN 2
#define HBD 88
#define CGT 0
#define MLP 30
#define BHOLD 0
#define TAU_L 238
#define DL0 50
#define EVT 340
#define FXA 1
#define RPRI 0
#define EVW 40
#define KP 0
#define KTO 270
#define KTE 0
#define SLS 0
#define SLF 30
#define SLM 50
#define BRT 1
#define BXD 0
#define PDB2 1
#define RP0 100
#define DLS 60
#define BFIRST 1
#define EVL 1
#define DLA 85
#define DLB 30
#define CST 1
#define POFF 0
#define NCL 4
#define CLS (NCL == 8 ? u & 7 : NCL == 4 ? (u >> 1) & 3 : (u >> 3) & (NCL - 1))
#define P_GATE 8
#include <hip/hip_runtime.h>
#include <hip/hip_bf16.h>
#include <stdint.h>

using bf16 = __hip_bfloat16;
using u32 = unsigned;
using u64 = unsigned long long;
using u32x2 = __attribute__((ext_vector_type(2))) unsigned;
using u32x4 = __attribute__((ext_vector_type(4))) unsigned;
using f32x4 = __attribute__((ext_vector_type(4))) float;
using bf16x2 = __attribute__((ext_vector_type(2))) __bf16;
typedef __attribute__((ext_vector_type(2))) float f32x2;
typedef short s16x8 __attribute__((ext_vector_type(8)));
typedef short s16x4 __attribute__((ext_vector_type(4)));
typedef __attribute__((address_space(3))) s16x4* lds_s4p;
typedef __attribute__((ext_vector_type(8))) __bf16 bf16x8v;

constexpr int NB = 4, DM = 640, NH = 32, DH = 128, SK = 8192, WIN = 2048, FF = 1728, QD = 4096, MODN = 3840;
constexpr int NT = 512, NWG = 256;

constexpr u32 WS_EPOCH = 0;
constexpr u32 G_T = 128;
constexpr u32 G_Q = 256;
constexpr u32 G_MOD = G_Q + 8 * 1024 * 16;
constexpr u32 G_MF = G_MOD + 8 * 1024 * 16;
constexpr u32 G_HB = G_MF + 16 * 160 * 16;
constexpr u32 G_HA = G_HB + 64 * 128;
constexpr u32 G_HT = G_HA + 80 * 128;
constexpr u32 G_ML = G_HT + 256 * 128;
constexpr u32 EMPTY = 0xffffffffu;
constexpr int NREP = NREPL;
constexpr u32 G_WO = G_ML + 256 * 64;
constexpr u32 WO_REP = 40 * 1024;
constexpr u32 G_ATT = G_WO + NREP * WO_REP;
constexpr u32 G_TK = G_ATT + 256 * 256;
constexpr u32 G_OUT = G_WO + 37 * 4096;
constexpr u32 WS_END = G_OUT + 40 * 54 * 128;
constexpr u32 OOB = 0xF0000000u;
#define GIDX(n, ks, b, cg) ((((ks) * ((n) / 4) + ((cg) >> 2)) * 16 + (b) * 4 + ((cg) & 3)) * 16)

#define WSTAMP(k) do {} while (0)
#define STAMPW(i) do {} while (0)
#define STAMPT(i, tt) do {} while (0)
#define STAMP_DECL
#define STAMP_FLUSH do {} while (0)
#define FAIL(i) do {} while (0)
#define PAD(name, next) asm volatile(".Lpad_" #name ":\n\ts_cbranch_execz .Lpad_" #next)
#define PADX(name, next, x) asm volatile(".Lpad_" #name ":\n\ts_cbranch_execz .Lpad_" #next : "+v"(x))
#define PADA(name, next) asm volatile(".Lpad_" #name ":\n\ts_cbranch_execz .Lpad_" #next : "+v"(acc[0]), "+v"(acc[1]), "+v"(acc[2]), "+v"(acc[3]), "+v"(acc[4]), "+v"(acc[5]), "+v"(acc[6]), "+v"(acc[7]))
#define STR_(x) #x
#define STR(x) STR_(x)
#define BAR() asm volatile("s_waitcnt lgkmcnt(0)\n\ts_barrier" ::: "memory")
#define VM0() asm volatile("s_waitcnt vmcnt(0)" ::: "memory")

__device__ __forceinline__ float bfr(float x) { return __bfloat162float(__float2bfloat16(x)); }
__device__ __forceinline__ float bf2f(bf16 x) { return __bfloat162float(x); }
__device__ __forceinline__ float lo16(unsigned u) { return __builtin_bit_cast(float, u << 16); }
__device__ __forceinline__ float hi16(unsigned u) { return __builtin_bit_cast(float, u & 0xffff0000u); }
__device__ __forceinline__ u32 f2u(float f) { return __builtin_bit_cast(u32, f); }
__device__ __forceinline__ float u2f(u32 f) { return __builtin_bit_cast(float, f); }
__device__ __forceinline__ u32 cvt2(float a, float b) {
  const f32x2 v = {a, b};
  return __builtin_bit_cast(u32, __builtin_convertvector(v, bf16x2));
}
__device__ __forceinline__ float frcp(float x) { return __builtin_amdgcn_rcpf(x); }
__device__ __forceinline__ float fsqrt(float x) { return __builtin_amdgcn_sqrtf(x); }
__device__ __forceinline__ float fexp(float x) { return __builtin_amdgcn_exp2f(x * 1.4426950408889634f); }
__device__ __forceinline__ float dot2(unsigned k, unsigned q, float acc) {
  return __builtin_amdgcn_fdot2_f32_bf16(__builtin_bit_cast(bf16x2, k), __builtin_bit_cast(bf16x2, q), acc, false);
}
__device__ __forceinline__ float addlo(unsigned w, float acc) { u32 k = 0x00003f80u; asm("" : "+s"(k)); return dot2(w, k, acc); }
__device__ __forceinline__ float addhi(unsigned w, float acc) { return dot2(w, 0x3f800000u, acc); }
template <int CTRL> __device__ __forceinline__ float dpp(float x) {
  return __builtin_bit_cast(float, __builtin_amdgcn_mov_dpp(__builtin_bit_cast(int, x), CTRL, 0xf, 0xf, true));
}
template <int CTRL> __device__ __forceinline__ u32 dppu(u32 x) { return (u32)__builtin_amdgcn_mov_dpp((int)x, CTRL, 0xf, 0xf, true); }
constexpr int XOR1 = 0xB1, XOR2 = 0x4E, XOR7 = 0x141, XOR8 = 0x128, ROR4 = 0x124, ROR2 = 0x122, ROR1 = 0x121, SHL4 = 0x104;
__device__ __forceinline__ float swap32_add(float a, float b) {
  auto s = __builtin_amdgcn_permlane32_swap(f2u(a), f2u(b), false, false);
  return u2f(s[0]) + u2f(s[1]);
}
__device__ __forceinline__ float swap16_add(float a, float b) {
  auto s = __builtin_amdgcn_permlane16_swap(f2u(a), f2u(b), false, false);
  return u2f(s[0]) + u2f(s[1]);
}
__device__ __forceinline__ float cross_rows_sum(float x) { x = swap16_add(x, x); return swap32_add(x, x); }
__device__ __forceinline__ float cross_rows_max(float x) {
  auto s = __builtin_amdgcn_permlane16_swap(f2u(x), f2u(x), false, false);
  x = fmaxf(u2f(s[0]), u2f(s[1]));
  auto s2 = __builtin_amdgcn_permlane32_swap(f2u(x), f2u(x), false, false);
  return fmaxf(u2f(s2[0]), u2f(s2[1]));
}
__device__ __forceinline__ float wave_sum(float x) {
  x += dpp<XOR8>(x); x += dpp<ROR4>(x); x += dpp<ROR2>(x); x += dpp<ROR1>(x);
  return cross_rows_sum(x);
}
__device__ __forceinline__ float wave_max(float x) {
  x = fmaxf(x, dpp<XOR8>(x)); x = fmaxf(x, dpp<ROR4>(x)); x = fmaxf(x, dpp<ROR2>(x)); x = fmaxf(x, dpp<ROR1>(x));
  return cross_rows_max(x);
}

__device__ __forceinline__ void st_gran(__amdgpu_buffer_rsrc_t rs, u32 off, u32 a, u32 b, u32 tg) {
  u32x4 v = {a, tg, b, tg};
  __builtin_amdgcn_raw_buffer_store_b128(v, rs, off, 0, 16);
}
__device__ __forceinline__ u32x4 ld_gran(__amdgpu_buffer_rsrc_t rs, u32 off) {
  return __builtin_amdgcn_raw_buffer_load_b128(rs, off, 0, 16);
}
constexpr unsigned SPIN_CAP = 1u << 18;
template <int N>
__device__ __forceinline__ void gather(__amdgpu_buffer_rsrc_t rs, const u32 (&offs)[N], u32x4 (&g)[N], u32 tg) {
  SPINLOOP for (unsigned spins = 0; spins < SPIN_CAP; ++spins) {
    bool ok = true;
#pragma unroll
    for (int i = 0; i < N; ++i) g[i] = ld_gran(rs, offs[i]);
#pragma unroll
    for (int i = 0; i < N; ++i) ok &= (offs[i] == OOB) | ((g[i][1] == tg) & (g[i][3] == tg));
    if (__all(ok)) break;
    __builtin_amdgcn_s_sleep(1);
  }
}
template <int N>
__device__ __forceinline__ void gather_m(__amdgpu_buffer_rsrc_t rs, u32 (&offs)[N], u32 (&va)[N], u32 (&vb)[N], u32 tg) {
#pragma unroll
  for (int i = 0; i < N; ++i) { va[i] = 0; vb[i] = 0; }
  SPINLOOP for (unsigned spins = 0; spins < SPIN_CAP; ++spins) {
    u32x4 g[N];
#pragma unroll
    for (int i = 0; i < N; ++i) g[i] = ld_gran(rs, offs[i]);
    bool ok = true;
#pragma unroll
    for (int i = 0; i < N; ++i) {
      const bool hit = (offs[i] != OOB) & (g[i][1] == tg) & (g[i][3] == tg);
      va[i] = hit ? g[i][0] : va[i]; vb[i] = hit ? g[i][2] : vb[i];
      offs[i] = hit ? OOB : offs[i];
      ok &= (offs[i] == OOB);
    }
    if (__all(ok)) break;
    __builtin_amdgcn_s_sleep(1);
  }
}

__device__ __forceinline__ bool missing(u32x4 v) { return max(max(v[0], v[1]), max(v[2], v[3])) == EMPTY; }
#define LK(v, o, so, imm, m) "s_mov_b64 exec, %" #m "\n\tbuffer_load_dwordx4 %" #v ", %" #o ", %[rs], " so " offen offset:" #imm " sc1\n\t"
#define LKEND "s_mov_b64 exec, -1\n\ts_waitcnt vmcnt(%[w])"
template <int W> __device__ __forceinline__ void look(u32x4 rs, u32 off, u32x4 (&v)[1], const u64 (&m)[1]) {
  asm volatile(LK(0, 1, "0", 0, 2) LKEND : "+v"(v[0]) : "v"(off), "s"(m[0]), [rs] "s"(rs), [w] "i"(W) : "memory");
}
template <int W> __device__ __forceinline__ void look(u32x4 rs, u32 off, u32x4 (&v)[5], const u64 (&m)[5]) {
  asm volatile(LK(0, 5, "0", 0, 6) LK(1, 5, "0", 1024, 7) LK(2, 5, "0", 2048, 8) LK(3, 5, "0", 3072, 9) LK(4, 5, "%11", 0, 10) LKEND
               : "+v"(v[0]), "+v"(v[1]), "+v"(v[2]), "+v"(v[3]), "+v"(v[4])
               : "v"(off), "s"(m[0]), "s"(m[1]), "s"(m[2]), "s"(m[3]), "s"(m[4]), "s"(4096u), [rs] "s"(rs), [w] "i"(W) : "memory");
}
template <int W> __device__ __forceinline__ void look(u32x4 rs, u32 off, u32x4 (&v)[7], const u64 (&m)[7]) {
  asm volatile(LK(0, 7, "0", 0, 8) LK(1, 7, "0", 1024, 9) LK(2, 7, "0", 2048, 10) LK(3, 7, "0", 3072, 11)
               LK(4, 7, "%15", 0, 12) LK(5, 7, "%15", 1024, 13) LK(6, 7, "%15", 2048, 14) LKEND
               : "+v"(v[0]), "+v"(v[1]), "+v"(v[2]), "+v"(v[3]), "+v"(v[4]), "+v"(v[5]), "+v"(v[6])
               : "v"(off), "s"(m[0]), "s"(m[1]), "s"(m[2]), "s"(m[3]), "s"(m[4]), "s"(m[5]), "s"(m[6]), "s"(4096u), [rs] "s"(rs), [w] "i"(W) : "memory");
}
template <int N, int SLEEP, int D>
__device__ __forceinline__ void gather_e(u32x4 rs, u32 off, u32x4 (&v)[N], u64 last) {
  u64 m[N];
#pragma unroll
  for (int i = 0; i < N; ++i) { m[i] = i < N - 1 ? ~0ull : last; v[i] = u32x4{0u, 0u, 0u, 0u}; }
  look<0>(rs, off, v, m);
  SPINLOOP for (unsigned spins = 0; spins < SPIN_CAP; ++spins) {
    u64 bad = 0;
#pragma unroll
    for (int i = 0; i < N; ++i) { m[i] = __builtin_amdgcn_ballot_w64(missing(v[i])); bad |= m[i]; }
    if (!bad) break;
    if (SLEEP) __builtin_amdgcn_s_sleep(SLEEP);
    look<N * (D - 1)>(rs, off, v, m);
  }
}

#define HLOOK(W_) asm volatile("s_mov_b64 exec, %2\n\tbuffer_load_dwordx2 %0, %1, %3, 0 offen sc1\n\ts_mov_b64 exec, -1\n\ts_waitcnt vmcnt(%4)" \
                               : "+v"(v) : "v"(off), "s"(m), "s"(rs), "i"(W_) : "memory")
__device__ __forceinline__ void heads_up_start(u32x4 rs, u32 off, u64 m, u32 tg, u32x2& v) { v = u32x2{0u, ~tg}; HLOOK(63); }
template <int SLEEP, int W0 = 0>
__device__ __forceinline__ void heads_up(u32x4 rs, u32 off, u64 m, u32 tg, u32x2& v) {
  asm volatile("s_waitcnt vmcnt(%1)" : "+v"(v) : "i"(W0) : "memory");
  SPINLOOP for (unsigned spins = 0; spins < SPIN_CAP; ++spins) {
    m &= __builtin_amdgcn_ballot_w64(v[1] != tg);
    if (!m) break;
    if (SLEEP) __builtin_amdgcn_s_sleep(SLEEP);
    HLOOK(0);
  }
}
__device__ __forceinline__ void heads_up_time(u64 all, u32x2 v) {
  SPINLOOP for (unsigned spins = 0; spins < 64; ++spins)
    if (!(all & __builtin_amdgcn_ballot_w64((int)((u32)__builtin_amdgcn_s_memrealtime() - v[0]) < 0))) break;
}
__device__ __forceinline__ void announce(__amdgpu_buffer_rsrc_t rs, u32 off, u32 a_max, u32 a_cnt, u32 n, u32 tb, u32 d, u32 tg) {
  u32 tk = 0, mx;
  const u32 f = (u32)__builtin_amdgcn_s_memrealtime() - tb + d;
  asm volatile("s_mov_b64 exec, 1\n\tds_max_u32 %2, %3\n\tds_add_rtn_u32 %0, %4, %5\n\tds_read_b32 %1, %2\n\ts_mov_b64 exec, -1\n\ts_waitcnt lgkmcnt(0)"
               : "+v"(tk), "=&v"(mx) : "v"(a_max), "v"(f), "v"(a_cnt), "v"(1u) : "memory");
  const u32 lane = threadIdx.x & 63;
  __builtin_amdgcn_raw_buffer_store_b64(u32x2{__builtin_amdgcn_readfirstlane(mx) + tb, tg}, rs,
                                        (lane < 16 && __builtin_amdgcn_readfirstlane(tk) == n - 1) ? off + lane * 8 : OOB, 0, 16);
}

__device__ __forceinline__ void st_g22(__amdgpu_buffer_rsrc_t rs, u32 off, f32x4 v, u32 tg) {
  const u32 a0 = f2u(v[0]) + 0x200u, a1 = f2u(v[1]) + 0x200u, a2 = f2u(v[2]) + 0x200u, a3 = f2u(v[3]) + 0x200u;
  const u32x4 w = {(a0 >> 10) | ((a1 << 12) & 0xffc00000u), (a1 >> 20) | (tg << 12), (a2 >> 10) | ((a3 << 12) & 0xffc00000u), (a3 >> 20) | (tg << 12)};
  __builtin_amdgcn_raw_buffer_store_b128(w, rs, off, 0, 16);
}
__device__ __forceinline__ f32x4 un_g22(u32x4 w) {
  return f32x4{u2f(w[0] << 10), u2f(__builtin_amdgcn_alignbit(w[1], w[0], 22) << 10), u2f(w[2] << 10), u2f(__builtin_amdgcn_alignbit(w[3], w[2], 22) << 10)};
}
template <int N, int SLEEP>
__device__ __forceinline__ void gather22(__amdgpu_buffer_rsrc_t rs, u32 (&offs)[N], u32x4 (&v)[N], u32 tg, u32 a_inc = 0) {
#pragma unroll
  for (int i = 0; i < N; ++i) v[i] = u32x4{0u, 0u, 0u, 0u};
  SPINLOOP for (unsigned spins = 0; spins < SPIN_CAP; ++spins) {
    u32x4 g[N];
#pragma unroll
    for (int i = 0; i < N; ++i) g[i] = ld_gran(rs, offs[i]);
    if (a_inc) asm volatile("s_mov_b64 exec, 1\n\tds_add_u32 %0, %1\n\ts_mov_b64 exec, -1" :: "v"(a_inc), "v"(1u) : "memory");
    bool ok = true;
#pragma unroll
    for (int i = 0; i < N; ++i) {
      const bool hit = (offs[i] != OOB) & (((g[i][1] ^ (tg << 12)) | (g[i][3] ^ (tg << 12))) < 4096u);
#pragma unroll
      for (int k = 0; k < 4; ++k) v[i][k] = hit ? g[i][k] : v[i][k];
      offs[i] = hit ? OOB : offs[i];
      ok &= (offs[i] == OOB);
    }
    if (__all(ok)) break;
    __builtin_amdgcn_s_sleep(SLEEP);
  }
}

__device__ __forceinline__ int sel_lt(int a, int b, int x, int y) {
  int r;
  asm("s_cmp_lt_i32 %1, %2\n\ts_cselect_b32 %0, %3, %4" : "=s"(r) : "s"(a), "s"(b), "s"(x), "s"(y) : "scc");
  return r;
}
__device__ __forceinline__ u32 next_tag(u32 ep) { const u32 t = ep + 1u; return (t & 0xfffffu) ? t : t + 1u; }

__device__ __forceinline__ void lds_arrive(u32 addr, int lane) {
  asm volatile("s_waitcnt lgkmcnt(0)" ::: "memory");
  if (lane == 0) asm volatile("ds_add_u32 %0, %1" :: "v"(addr), "v"(1u) : "memory");
}
__device__ __forceinline__ u32 lds_ticket(u32 addr, int lane) {
  u32 r = 0;
  asm volatile("s_waitcnt lgkmcnt(0)" ::: "memory");
  if (lane == 0) asm volatile("ds_add_rtn_u32 %0, %1, %2\n\ts_waitcnt lgkmcnt(0)" : "=v"(r) : "v"(addr), "v"(1u) : "memory");
  return __builtin_amdgcn_readfirstlane(r);
}
template <int SLEEP>
__device__ __forceinline__ void lds_wait(u32 addr, u32 target) {
  SPINLOOP for (unsigned spins = 0; spins < (1u << 22); ++spins) {
    u32 v;
    asm volatile("ds_read_b32 %0, %1\n\ts_waitcnt lgkmcnt(0)" : "=v"(v) : "v"(addr) : "memory");
    if (v >= target) break;
    __builtin_amdgcn_s_sleep(SLEEP);
  }
}
__device__ __forceinline__ void dma16(const void* gsrc, u32 lds_dst) {
  u32 keep;
  asm volatile("s_mov_b32 %0, m0\n\ts_mov_b32 m0, %2\n\ts_nop 0\n\tglobal_load_lds_dwordx4 %1, off\n\ts_mov_b32 m0, %0"
               : "=&s"(keep) : "v"(gsrc), "s"(lds_dst) : "memory");
}
__device__ __forceinline__ void dma16nt(const void* gsrc, u32 lds_dst) {
  u32 keep;
  asm volatile("s_mov_b32 %0, m0\n\ts_mov_b32 m0, %2\n\ts_nop 0\n\tglobal_load_lds_dwordx4 %1, off nt\n\ts_mov_b32 m0, %0"
               : "=&s"(keep) : "v"(gsrc), "s"(lds_dst) : "memory");
}
__device__ __forceinline__ u32x4 ldasm(const void* p) {
  u32x4 v;
  asm volatile("global_load_dwordx4 %0, %1, off" : "=v"(v) : "v"(p) : "memory");
  return v;
}
__device__ __forceinline__ u32 ldasm_sc1(const void* p) {
  u32 v;
  asm volatile("global_load_dword %0, %1, off sc1" : "=v"(v) : "v"(p) : "memory");
  return v;
}
__device__ __forceinline__ u32x4 ldw(const bf16* p) { return *reinterpret_cast<const u32x4*>(p); }
template <int D2>
__device__ __forceinline__ s16x8 wfrag(u32 addr) {
  const s16x4 lo = __builtin_amdgcn_ds_read_tr16_b64_v4i16((lds_s4p)(uintptr_t)addr);
  const s16x4 hi = __builtin_amdgcn_ds_read_tr16_b64_v4i16((lds_s4p)(uintptr_t)(addr + D2));
  return s16x8{lo[0], lo[1], lo[2], lo[3], hi[0], hi[1], hi[2], hi[3]};
}
#define MFMA(w_, x_, c_) __builtin_amdgcn_mfma_f32_16x16x32_bf16(__builtin_bit_cast(bf16x8v, w_), __builtin_bit_cast(bf16x8v, x_), c_, 0, 0, 0)

struct Desc { const bf16* p; u32 bytes, flags; };
struct Args {
  Desc wq, x, wmod, c;
  unsigned char* ws;
  const bf16 *kc, *vc, *wo, *w1, *b1, *w2, *b2;
  bf16* out;
};

static_assert(offsetof(Args, ws) == 64 && offsetof(Args, out) == 128, "argument offsets");

constexpr int L_W = 0;
constexpr int L_WM2 = 40960;
constexpr int L_XA = 81920;
constexpr int XS = 1312, XH = 704;
constexpr int L_RED = L_XA + 16384;
constexpr int L_MOD = L_RED + 8192;
constexpr int L_XR = L_MOD + 5120;
constexpr int L_PT = L_XR + 5120;
constexpr int L_ACC = L_PT + 2048;
constexpr int L_QS = L_ACC + 4096;
constexpr int L_FI = L_QS + 256;
constexpr int L_ST = L_FI + 256;
constexpr int L_CNT = L_ST + 256;
constexpr int L_END = L_CNT + 128;
static_assert(L_END <= 160 * 1024, "LDS");
constexpr u32 C_Q = L_CNT, C_SC = L_CNT + 4, C_ISS = L_CNT + 8, C_P1 = L_CNT + 20, C_P2 = L_CNT + 24, C_GO = L_CNT + 28, C_W = L_CNT + 12, C_FIN = L_CNT + 32,
              C_KI = L_CNT + 36, C_PP = L_CNT + 40, C_PUB = L_CNT + 56, C_HT = L_CNT + 64, C_MF = L_CNT + 68, C_RL = L_CNT + 72, C_DL = L_CNT + 76;

constexpr int NL = 32;
constexpr int PVLAG = P_LAG;
constexpr int NPRE = P_NPRE;
constexpr int NVE = P_NVE;
static_assert(PVLAG <= 16 && (PVLAG & 1) == 0 && NVE <= PVLAG && (NVE & 1) == 0, "row groups");

__global__ void __launch_bounds__(NT, 2) mega_kernel(Args a) {
  __shared__ __attribute__((aligned(1024))) unsigned char lds[L_END];
  float* red = reinterpret_cast<float*>(lds + L_RED);
  u32* xraw = reinterpret_cast<u32*>(lds + L_XR);
  u32* pT = reinterpret_cast<u32*>(lds + L_PT);
  float* accs = reinterpret_cast<float*>(lds + L_ACC);
  u32* qs = reinterpret_cast<u32*>(lds + L_QS);
  float* st = reinterpret_cast<float*>(lds + L_ST);
  u32* cnt = reinterpret_cast<u32*>(lds + L_CNT);

  u32x4 rw, rx;
  unsigned char* wsq;
  const void* kcq;
  u64 ts64;
  asm volatile("s_load_dwordx4 %0, %5, %6\n\ts_load_dwordx4 %1, %5, %6 offset:16\n\ts_load_dwordx2 %2, %5, 64\n\ts_load_dwordx2 %3, %5, 72\n\ts_memrealtime %4"
               : "=&s"(rw), "=&s"(rx), "=&s"(wsq), "=&s"(kcq), "=&s"(ts64) : "s"(__builtin_amdgcn_kernarg_segment_ptr()), "s"((blockIdx.x & 1) * 32));
  __builtin_amdgcn_sched_barrier(0);
  const int t = threadIdx.x, lane = t & 63;
  const int wave = __builtin_amdgcn_readfirstlane(t >> 6);
  const int u = blockIdx.x;
  const int bh = u >> 1, half = u & 1, b_att = bh >> 5, h_att = bh & 31;
  const int r = lane >> 4, c = lane & 15;
  STAMP_DECL
  STAMPT(0, 0);
  u32 tg;
  const int g = u >> 1, ksp = g & 1, cbp = g >> 1;
#define fav (half || (g >= 120 && g < 126))
  const int s128e = (((((lane & 7) >> 1) ^ ((lane >> 4) & 1)) << 1) | (lane & 1)) * 8;
  const u32 tr128 = (8 * r + (c >> 2)) * 128 + ((((c >> 3) & 1) | ((r & 1) << 1)) * 32) + (c & 3) * 8;
  if (wave < 4) {
    __builtin_amdgcn_s_setprio(3);
    const u32 rb = 8192 - 512 * half;
    const u32 hm = 0u - (u32)half;
    const u32 vo = ((u32)(lane >> 3) * rb + (((lane & 7) ^ ((lane >> 3) & 2)) ^ ((wave & 1) * 4)) * 16) | ((u32)(119 - g) & hm & OOB);
    u32 so = (u32)(ksp * 320 + 8 * wave) * rb + cbp * 128;
    const u32 step = 32 * rb, dstep = 4096, dst = L_W + wave * 1024;
    const u32 oe = wave * 1280 + lane * 16, oo = (r * 1280 + ksp * 640 + (c + 16 * wave) * 16) | ((u32)(39 - c - 16 * wave) & OOB);
    const u32 oa = (oo & hm) | (oe & ~hm), ob = (wave * 1280 + 1024 + c * 16) | (hm & OOB), oc = oe | (hm & OOB), zero = 0;
    u32 ep, keep;
    u32x4 xv[4];
    static_assert(G_TK + 1024 <= G_OUT, "37 x 4 KB");
    const u32 pre = ((u32)u << 31) & (u32)(g - 37) & (u32)(2 - wave), po = G_WO + g * 4096 + lane * 16;
    const u32x4 emp = {EMPTY, EMPTY, EMPTY, EMPTY};
#define DMA1 "buffer_load_dwordx4 %7, %8, %1 offen nt lds\n\t"
#define DMAN "s_add_u32 m0, m0, %11\n\ts_add_u32 %1, %1, %10\n\t" DMA1
    asm volatile("s_mov_b32 %0, m0\n\ts_mov_b32 m0, %9\n\t"
                 "s_cmp_gt_i32 %18, 1\n\ts_waitcnt lgkmcnt(0)\n\ts_cbranch_scc1 1f\n\t"
                 "global_load_dword %2, %12, %13 sc1\n\t"
                 "buffer_load_dwordx4 %3, %14, %17, 0 offen\n\tbuffer_load_dwordx4 %4, %15, %17, 0 offen\n\t"
                 "buffer_load_dwordx4 %5, %16, %17, 0 offen offset:2560\n\tbuffer_load_dwordx4 %6, %15, %17, 0 offen offset:2560\n"
                 "1:\n\t" DMA1 DMAN DMAN DMAN DMAN DMAN DMAN DMAN DMAN DMAN "s_mov_b32 m0, %0\n\t"
                 "s_cmp_eq_u32 %19, 0\n\ts_cbranch_scc1 2f\n\tglobal_store_dwordx4 %20, %21, %13 sc1\n\t"
                 "global_store_dwordx4 %20, %21, %13 offset:1024 sc1\n\tglobal_store_dwordx4 %20, %21, %13 offset:2048 sc1\n\t"
                 "global_store_dwordx4 %20, %21, %13 offset:3072 sc1\n2:\n\t"
#define B0W "s_barrier\n\ts_memrealtime vcc\n\ts_waitcnt vmcnt(10) lgkmcnt(0)\n\ts_sub_u32 %0, vcc_lo, " STR(AOFF)
                 B0W
                 : "=&s"(keep), "+s"(so), "=&v"(ep), "=&v"(xv[0]), "=&v"(xv[1]), "=&v"(xv[2]), "=&v"(xv[3])
                 : "v"(vo), "s"(rw), "s"(dst), "s"(step), "s"(dstep), "v"(zero), "s"(wsq), "v"(oa), "v"(ob), "v"(oc), "s"(rx), "s"(wave - half),
                   "s"(pre), "v"(po), "v"(emp) : "memory", "scc", "vcc");
#undef DMAN
#undef DMA1
    asm volatile("" : "+s"(wsq), "+s"(ts64));
    const u32 ssh = (u32)min(max((int)(keep - (u32)ts64) + AOFF - SDZ, 0), SSH);
    const __amdgpu_buffer_rsrc_t rs = __builtin_amdgcn_make_buffer_rsrc(wsq, 0, WS_END, 0x00020000);
    if (half) {
      if (wave < 3) {
        u32x4 sv;
#pragma unroll
        for (int k = 0; k < 4; ++k) {
          const float c0 = lo16(xv[0][k]), c1 = hi16(xv[0][k]);
          sv[k] = cvt2(c0 * frcp(1.f + fexp(-c0)), c1 * frcp(1.f + fexp(-c1)));
        }
        if (c + 16 * wave < 40) *reinterpret_cast<u32x4*>(lds + L_XA + r * XH + (c + 16 * wave) * 16) = sv;
        if (t == 0) { cnt[4] = next_tag(ep); cnt[15] = keep + ssh; if (GX) cnt[20] = ssh; }
        lds_arrive(C_P1, lane);
        STAMPT(2, 0);
      }
    } else {
      if (wave < 2) {
#pragma unroll
        for (int i = 0; i < 2; ++i) {
          const int b = wave + 2 * i;
          float xf[16];
#pragma unroll
          for (int k = 0; k < 4; ++k) { xf[2 * k] = lo16(xv[2 * i][k]); xf[2 * k + 1] = hi16(xv[2 * i][k]); xf[8 + 2 * k] = lo16(xv[2 * i + 1][k]); xf[8 + 2 * k + 1] = hi16(xv[2 * i + 1][k]); }
          float s = 0.f, s2 = 0.f;
#pragma unroll
          for (int k = 0; k < 8; ++k) { s += xf[k]; s2 += xf[8 + k]; }
          if (lane < 16) s += s2;
          const float mean = wave_sum(s) * (1.f / DM);
          float v = 0.f, v2 = 0.f;
#pragma unroll
          for (int k = 0; k < 8; ++k) { const float d = xf[k] - mean; v += d * d; const float d2 = xf[8 + k] - mean; v2 += d2 * d2; }
          if (lane < 16) v += v2;
          v = wave_sum(v);
          if (lane == 0) { st[24 + 2 * b] = bfr(mean); st[24 + 2 * b + 1] = frcp(bfr(fsqrt(bfr(bfr(v * (1.f / DM)) + 1e-5f)))); }
          *reinterpret_cast<u32x4*>(xraw + b * 320 + lane * 4) = xv[2 * i];
          if (lane < 16) *reinterpret_cast<u32x4*>(xraw + b * 320 + 256 + lane * 4) = xv[2 * i + 1];
        }
        if (t == 0) { cnt[4] = next_tag(ep); cnt[15] = keep + ssh; if (GX) cnt[20] = ssh; }
        lds_arrive(C_P1, lane);
      }
      lds_wait<1>(C_P1, 2);
      STAMPT(20, 0);
      tg = __builtin_amdgcn_readfirstlane(cnt[4]);
      if (CT0 && u < 8 && t == 192) { const u32 t0 = cnt[15]; st_gran(rs, G_T + u * 16, t0, GX ? cnt[20] : t0, tg); }
      if (wave < 3) {
        const int b = LAY == 2 ? t & 3 : (t >> 2) & 3, j = LAY == 2 ? t >> 2 : (t >> 4) * 4 + (t & 3);
        u32 offs[8];
        u32x4 gm[8];
#pragma unroll
        for (int i = 0; i < 8; ++i) offs[i] = t < 160 ? G_MOD + GIDX(1024, i & 1, b, ((i >> 1) & 1) * 160 + (i >> 2) * 40 + ksp * 80 + j) : OOB;
        const float mean = st[24 + 2 * b], sd = st[24 + 2 * b + 1];
        float nv[8];
#pragma unroll
        for (int e = 0; e < 2; ++e) {
          const u32x2 xw = *reinterpret_cast<const u32x2*>(xraw + b * 320 + ksp * 160 + e * 80 + 2 * j);
#pragma unroll
          for (int k = 0; k < 4; ++k) {
            const float xf = (k & 1) ? hi16(xw[k >> 1]) : lo16(xw[k >> 1]);
            nv[4 * e + k] = bfr(bfr(xf - mean) * sd);
            asm volatile("" : "+v"(nv[4 * e + k]));
          }
        }
        {
          const u32 tt = __builtin_amdgcn_readfirstlane(cnt[15]) + GT;
          SPINLOOP while ((int)((u32)__builtin_amdgcn_s_memrealtime() - tt) < 0) __builtin_amdgcn_s_sleep(1);
        }
        STAMPT(14, 0);
        gather22<8, 4>(rs, offs, gm, tg, GOPOS == 2 ? C_GO : 0);
        STAMPT(8, 0);
#pragma unroll
        for (int e = 0; e < 2; ++e) {
          const f32x4 sa = un_g22(gm[4 * e]) + un_g22(gm[4 * e + 1]);
          const f32x4 ca = un_g22(gm[4 * e + 2]) + un_g22(gm[4 * e + 3]);
          const u32 s01 = cvt2(sa[0], sa[1]), s23 = cvt2(sa[2], sa[3]), c01 = cvt2(ca[0], ca[1]), c23 = cvt2(ca[2], ca[3]);
          const u32 d01 = cvt2(1.f + lo16(c01), 1.f + hi16(c01)), d23 = cvt2(1.f + lo16(c23), 1.f + hi16(c23));
          const u32 p01 = cvt2(nv[4 * e] * lo16(d01), nv[4 * e + 1] * hi16(d01)), p23 = cvt2(nv[4 * e + 2] * lo16(d23), nv[4 * e + 3] * hi16(d23));
          const float hv[4] = {lo16(p01) + lo16(s01), hi16(p01) + hi16(s01), lo16(p23) + lo16(s23), hi16(p23) + hi16(s23)};
          if (t < 160) *reinterpret_cast<u32x2*>(lds + L_XA + b * XH + (e * 40 + j) * 8) = u32x2{cvt2(hv[0], hv[1]), cvt2(hv[2], hv[3])};
        }
      }
    }
    VM0();
    STAMPT(5, 128);
    lds_arrive(C_P2, lane);
    lds_wait<1>(C_P2, 4);
    STAMPT(7, 0);
    tg = __builtin_amdgcn_readfirstlane(cnt[4]);
    {
      f32x4 acc = {0.f, 0.f, 0.f, 0.f}, acc2 = acc;
#pragma unroll
      for (int s = 0; s < 10; s += 2) {
        acc = MFMA(wfrag<512>(L_W + (tr128 ^ (wave * 32)) + s * 4096), *reinterpret_cast<const u32x4*>(lds + L_XA + (c & 3) * XH + s * 64 + r * 16), acc);
        acc2 = MFMA(wfrag<512>(L_W + (tr128 ^ (wave * 32)) + s * 4096 + 4096), *reinterpret_cast<const u32x4*>(lds + L_XA + (c & 3) * XH + s * 64 + 64 + r * 16), acc2);
      }
      if (c < 4 && u < 240 + 16 - 16 * half) st_g22(rs, G_Q + half * (G_MOD - G_Q) + GIDX(1024, ksp, c, cbp * 16 + wave * 4 + r), acc + acc2, tg);
    }
    if (CT0 && half && u < 8 && t == 192) { const u32 t0 = cnt[15]; st_gran(rs, G_T + u * 16, t0, GX ? cnt[20] : t0, tg); }
    STAMPT(9, 0);
  } else {
    __builtin_amdgcn_s_setprio(2);
    {
      const u32 co = min((u32)(u >> 3) * 1024u + (u32)((t >> 5) & 7) * 128u + 768u, (u32)CLEN);
      u64 pc;
      asm volatile("s_getpc_b64 %0\n\tglobal_load_dword v128, %1, %0" : "=&s"(pc) : "v"(co) : "memory", "v128");
    }
    if (t < 256 + 32) cnt[lane] = 0;
    const int rot = __builtin_amdgcn_readfirstlane((u >> 3) + ((t >> 6) & 3) * PHW);
    const u32 ko = (u32)(bh * SK + (SK - WIN)) * 256u + (u32)(half * 8 + wave) * (NL * 1024u) + lane * 16;
    STAMPT(30, 448);
#define LK4(a, b, c, d, o) "global_load_dwordx4 " a ", %" #o ", %[k] nt\n\tglobal_load_dwordx4 " b ", %" #o ", %[k] offset:1024 nt\n\t" \
                           "global_load_dwordx4 " c ", %" #o ", %[k] offset:2048 nt\n\tglobal_load_dwordx4 " d ", %" #o ", %[k] offset:3072 nt\n\t"
    asm volatile("s_waitcnt lgkmcnt(0)\n\ts_barrier\n\t"
                 LK4("v[128:131]", "v[132:135]", "v[136:139]", "v[140:143]", 0) LK4("v[144:147]", "v[148:151]", "v[152:155]", "v[156:159]", 1)
                 LK4("v[160:163]", "v[164:167]", "v[168:171]", "v[172:175]", 2)
                 :: "v"(ko + (rot & 7) * 4096), "v"(ko + ((rot + 1) & 7) * 4096),
                   "v"(ko + ((rot + 2) & 7) * 4096),
                   [k] "s"(kcq) : "memory", "v128", "v129", "v130", "v131", "v132", "v133", "v134", "v135", "v136", "v137", "v138", "v139", "v140", "v141", "v142", "v143", "v144", "v145", "v146", "v147", "v148", "v149", "v150", "v151", "v152", "v153", "v154", "v155", "v156", "v157", "v158", "v159", "v160", "v161", "v162", "v163", "v164", "v165", "v166", "v167", "v168", "v169", "v170", "v171", "v172", "v173", "v174", "v175", "v176", "v177", "v178", "v179", "v180", "v181", "v182", "v183", "v184", "v185", "v186", "v187", "v188", "v189", "v190", "v191");
#undef LK4
    STAMPT(31, 448);
  }
  u32 kvoff = (u32)(bh * SK + (SK - WIN)) * 256u + (u32)(half * 8 + wave) * (NL * 1024u) + lane * 16, ph[8];
  const int rot2 = __builtin_amdgcn_readfirstlane((u >> 3) + ((t >> 6) & 3) * PHW);
#pragma unroll
  for (int i = 0; i < 8; ++i) ph[i] = ((i + rot2) & 7) * 4096;
  asm volatile("" : "+v"(kvoff));
  asm volatile("s_waitcnt lgkmcnt(0)" : "+s"(wsq), "+s"(ph[0]), "+s"(ph[1]), "+s"(ph[2]), "+s"(ph[3]), "+s"(ph[4]), "+s"(ph[5]), "+s"(ph[6]), "+s"(ph[7])
               : "s"(rw), "s"(rx), "s"(kcq));
  const __amdgpu_buffer_rsrc_t rs = __builtin_amdgcn_make_buffer_rsrc(wsq, 0, WS_END, 0x00020000);
  u32 rw2;
#define BLKA(ia_) (((c & 3) * 32 + (((ia_) & 3) + (((ia_) >> 2) & 1) * 16 + (wave & 3) * 4)) * 2 + ((c >> 2) & 1))
#define RD_ROLE() { u32 v_; asm volatile("ds_read_b32 %0, %1\n\ts_waitcnt lgkmcnt(0)" : "=v"(v_) : "v"((u32)C_RL) : "memory"); rw2 = __builtin_amdgcn_readfirstlane(v_); }
#define WAIT_ROLE() SPINLOOP for (unsigned spins = 0; spins < (1u << 22); ++spins) { RD_ROLE() if ((int)rw2 < 0) break; __builtin_amdgcn_s_sleep(2); }
#define HEADA(hh) ((ksA & 3) + (ksA >> 2) * 16 + (hh) * 4)
  u32x4 gmf[5], ml;
  u32 tkf, a_pub;
  f32x4 psc, pw4[4];
  u32x2 xw;
  {
    u32x4 kv[NL], kh[16];
    const __amdgpu_buffer_rsrc_t rk = __builtin_amdgcn_make_buffer_rsrc((void*)a.kc, 0, 1u << 28, 0x00020000);
    const __amdgpu_buffer_rsrc_t rv = __builtin_amdgcn_make_buffer_rsrc((void*)a.vc, 0, 1u << 28, 0x00020000);
#define LDKV(rsrc, off, i) __builtin_amdgcn_raw_buffer_load_b128(rsrc, (off) + ((i) & 3) * 1024, ph[(i) >> 2], 2)
#define LDS_INC(addr) do { asm volatile("" ::: "memory"); if (lane == 0) asm volatile("ds_add_u32 %0, %1" :: "v"(addr), "v"(1u) : "memory"); } while (0)
#define TR_STEP(x, HALF, CTRL, BIT) _Pragma("unroll") for (int hh = 0; hh < (HALF); ++hh) { \
    const float lo_ = x[hh], hi_ = x[hh + (HALF)]; const float t1_ = lo_ + dpp<CTRL>(lo_), t2_ = hi_ + dpp<CTRL>(hi_); \
    x[hh] = (c & (BIT)) ? t2_ : t1_; }
#define SCORE16(x, src, o) float x[16]; \
    _Pragma("unroll") for (int i = 0; i < 16; ++i) { float d = 0.f; _Pragma("unroll") for (int k = 0; k < 4; ++k) d = dot2(src[(o) + i][k], qpk[k], d); x[i] = d; } \
    TR_STEP(x, 8, XOR8, 8) TR_STEP(x, 4, XOR7, 4) TR_STEP(x, 2, XOR2, 2)
#define SCORE16P(x, src, o, pa_, pb_) float x[16]; \
    _Pragma("unroll") for (int i = 0; i < 16; ++i) { float d = 0.f; _Pragma("unroll") for (int k = 0; k < 4; ++k) d = dot2(src[(o) + i][k], qpk[k], d); x[i] = d; } \
    TR_STEP(x, 8, XOR8, 8) PADX(pa_, pb_, x[0]); TR_STEP(x, 4, XOR7, 4) TR_STEP(x, 2, XOR2, 2)
#define SCALED(v) bfr(bfr(v) * 0.08838834764831845f)
#define PV2(i) _Pragma("unroll") for (int k = 0; k < 4; ++k) { \
      acc[2 * k] = dot2(__builtin_amdgcn_perm(kv[(i) + 1][k], kv[i][k], 0x05040100u), pw[(i) / 2], acc[2 * k]); \
      acc[2 * k + 1] = dot2(__builtin_amdgcn_perm(kv[(i) + 1][k], kv[i][k], 0x07060302u), pw[(i) / 2], acc[2 * k + 1]); }
#define GET_Q() lds_wait<2>(C_Q, 1); u32x4 qpk; \
    asm volatile("ds_read_b128 %0, %2\n\tds_read_b32 %1, %3\n\ts_waitcnt lgkmcnt(0)" : "=&v"(qpk), "=&v"(tg) : "v"((u32)(L_QS + 16 * c)), "v"((u32)(L_CNT + 16)) : "memory"); \
    tg = __builtin_amdgcn_readfirstlane(tg);
#define GET_P() _Pragma("unroll") for (int i4 = 0; i4 < 4; ++i4) { \
      const u32x4 pv = *reinterpret_cast<const u32x4*>(pT + (wave * 4 + r) * 16 + i4 * 4); \
      _Pragma("unroll") for (int j = 0; j < 4; ++j) pw[i4 * 4 + j] = pv[j]; }
    auto gather_q = [&]() __attribute__((always_inline)) {
        u32 offs[1] = {G_Q + GIDX(1024, lane >> 5, b_att, h_att * 32 + (lane & 31))};
        u32x4 gq[1];
        {
          gq[0] = ld_gran(rs, offs[0]);
          const u32x4 gt = ld_gran(rs, lane < 8 ? G_T + lane * 16 : OOB);
          LDS_INC(C_KI);
          {
            const u32 own = cnt[15];
            const bool ok = lane < 8 && gt[1] == tg;
            float d = ok ? __builtin_amdgcn_fmed3f((float)(int)(gt[0] - own + TOFF), -400.f, 400.f) : -500.f;
            d = fmaxf(d, dpp<XOR1>(d)); d = fmaxf(d, dpp<XOR2>(d)); d = fmaxf(d, dpp<XOR7>(d));
            if (lane == 0 && d > -450.f) cnt[15] = own + (u32)(int)d;
            if (GX) {
              float e = ok ? (float)(gt[2] & 63) : 0.f;
              e = fmaxf(e, dpp<XOR1>(e)); e = fmaxf(e, dpp<XOR2>(e)); e = fmaxf(e, dpp<XOR7>(e));
              if (lane == 0) cnt[20] = (u32)e;
            }
          }
          const bool hit = (((gq[0][1] ^ (tg << 12)) | (gq[0][3] ^ (tg << 12))) < 4096u);
          if (!__all(hit)) gather22<1, 4>(rs, offs, gq, tg);
        }
        STAMPW(10);
        f32x4 q4 = un_g22(gq[0]);
#pragma unroll
        for (int k = 0; k < 4; ++k) q4[k] = swap32_add(q4[k], q4[k]);
        if (lane < 32) *reinterpret_cast<u32x2*>(qs + 2 * lane) = u32x2{cvt2(q4[0], q4[1]), cvt2(q4[2], q4[3])};
        lds_arrive(C_Q, lane);
    };
    const int qw = half ? QODD : QEVEN;
    const u32 tau = fav ? TAU_L : TAU_E;
#define PACE(n) if ((n) > PB) { const u32 tt_ = t0 + (((n) - PB) * tau >> 4); \
      SPINLOOP while ((int)((u32)__builtin_amdgcn_s_memrealtime() - tt_) < 0) __builtin_amdgcn_s_sleep(1); asm volatile("" ::: "memory"); }
#define PACE2(n) { u32 v_, d_; \
      asm volatile("1:\n\tds_read_b32 %0, %2\n\ts_memrealtime vcc\n\ts_waitcnt lgkmcnt(0)\n\tv_readfirstlane_b32 %1, %0\n\t" \
                   "s_mulk_i32 %1, %5\n\ts_ashr_i32 %1, %1, 5\n\ts_add_u32 %1, %1, vcc_lo\n\t" \
                   "s_sub_u32 %1, %1, %3\n\ts_cmp_ge_i32 %1, %4\n\ts_cbranch_scc1 2f\n\ts_sleep 1\n\ts_branch 1b\n2:" \
                   : "=&v"(v_), "=&s"(d_) : "v"(a_dl), "s"(t0), "i"(((n) * TAU_E >> 4) - POFF), "i"(((n) - RP0) * 32 / (126 - RP0)) : "memory", "scc", "vcc"); }
    float acc[8];
#pragma unroll
    for (int k = 0; k < 8; ++k) acc[k] = 0.f;
    u32 pw[16];
    if (__builtin_expect(wave >= 4, 1)) {
      asm volatile("" : "={v[128:131]}"(kv[0]), "={v[132:135]}"(kv[1]), "={v[136:139]}"(kv[2]), "={v[140:143]}"(kv[3]),
                        "={v[144:147]}"(kv[4]), "={v[148:151]}"(kv[5]), "={v[152:155]}"(kv[6]), "={v[156:159]}"(kv[7])
                        , "={v[160:163]}"(kv[8]), "={v[164:167]}"(kv[9]), "={v[168:171]}"(kv[10]), "={v[172:175]}"(kv[11])
                   );
      u32 t0 = 0;
      const u32 tk0 = KP ? (u32)__builtin_amdgcn_s_memrealtime() : 0u, ktau = half ? KTO : KTE;
      STAMPT(24, 448);
      const u32 kvoff2 = kvoff ^ 131072u;
#pragma unroll
      for (int i = NK0; i < 48; ++i) {
        if (i == NPRE) { STAMPT(12, 448); STAMPT(13, 256); }
        if (i == NPRE && half == 0) lds_wait<2>(C_GO, GOPOS ? 3 : 4);
        if (KP && i >= KP && (i & 3) == 0) { const u32 tt_ = tk0 + (i * ktau >> 4);
          SPINLOOP while ((int)((u32)__builtin_amdgcn_s_memrealtime() - tt_) < 0) __builtin_amdgcn_s_sleep(1); asm volatile("" ::: "memory"); }
        if (i < NL) kv[i < NL ? i : 0] = LDKV(rk, kvoff, i & 31); else kh[i < NL ? 0 : i - NL] = LDKV(rk, kvoff2, i & 31);
      }
      if (!QPRE || wave != qw) LDS_INC(C_KI);
#pragma unroll
      for (int i = 0; i < NK0; i += 4) asm volatile("s_waitcnt vmcnt(%4)" : "+v"(kv[i]), "+v"(kv[i + 1]), "+v"(kv[i + 2]), "+v"(kv[i + 3]) : "i"(48 - NK0) : "memory");
      STAMPT(25, 448);
      if ((QODD >= 4 || QEVEN >= 4) && wave == qw) {
        __builtin_amdgcn_s_setprio(3);
        lds_wait<2>(C_P1, 2 + half);
        tg = __builtin_amdgcn_readfirstlane(cnt[4]);
        gather_q();
        __builtin_amdgcn_s_setprio(2);
      }
      GET_Q()
      SCORE16(xa, kv, 0)
      asm volatile("" : "+v"(xa[0]), "+v"(xa[1]), "+v"(kvoff) :: "memory");
      PAD(e1, l2);
#pragma unroll
      for (int i = 0; i < P_NVA; ++i) kv[i] = LDKV(rv, kvoff, i);
      asm volatile("" ::: "memory");
      SCORE16(xb, kv, 16)
      xa[2] = xb[0]; xa[3] = xb[1];
      TR_STEP(xa, 2, XOR1, 1)
      float s0 = SCALED(xa[0]), s1 = SCALED(xa[1]);
      asm volatile("" : "+v"(s0), "+v"(s1), "+v"(kvoff) :: "memory");
#pragma unroll
      for (int i = P_NVA; i < NVE; ++i) kv[i] = LDKV(rv, kvoff, i);
      asm volatile("" ::: "memory");
      SCORE16P(xh, kh, 0, e2, l3)
      const float s2 = SCALED(xh[0] + dpp<XOR1>(xh[0])), s3 = SCALED(xh[1] + dpp<XOR1>(xh[1]));
      const float m = wave_max(fmaxf(fmaxf(s0, s1), fmaxf(s2, s3)));
      const float p0 = __expf(s0 - m), p1 = __expf(s1 - m), p2 = __expf(s2 - m), p3 = __expf(s3 - m);
      const float l = wave_sum(fmaf(p2 + p3, 0.5f, p0 + p1));
      if (lane == 0) { st[wave] = m; st[8 + wave] = l; }
      pT[(wave * 4 + r) * 16 + (c & 1) * 8 + (c >> 1)] = cvt2(p0, p1);
      pT[((wave - 4) * 4 + r) * 16 + (c >> 1)] = cvt2(p2, p3);
      lds_arrive(C_PP + 4 * (wave - 4), lane);
      __builtin_amdgcn_s_setprio(0);
      STAMPT(27, 448); WSTAMP(1);
      t0 = __builtin_amdgcn_readfirstlane(cnt[15]) - (fav ? DL0 : 0);
      GET_P()
      u32 a_dl = C_DL;
      asm volatile("" : "+v"(a_dl));
#pragma unroll
      for (int i = NVE; i < NL; i += 2) {
        if (i == E3POS) PAD(e3, l4);
        if (i == E4POS) PAD(e4, t1);
        u32 om0;
        if (i == MLP) { RD_ROLE() om0 = (rw2 & 0x80000003u) == 0x80000000u && lane < 8 ? G_ML + BLKA(rw2 >> 8) * 64 : OOB; asm volatile("" : "+v"(om0)); }
        PACE2(96 + i)
        if (i == MLP) ml = ld_gran(rs, om0);
        kv[i] = LDKV(rv, kvoff, i);
        kv[i + 1] = LDKV(rv, kvoff, i + 1);
        asm volatile("" ::: "memory");
        if (i == NL - 2) { LDS_INC(C_ISS); STAMPT(26, 448); }
        if (i >= PVLAG) {
          PV2(i >= PVLAG ? i - PVLAG : 0)
#pragma unroll
          for (int k = 0; k < 8; ++k) asm volatile("" : "+v"(acc[k]));
        }
      }
    } else {
      const int ex = (g >= 120 && g < 126) ? EC : 0;
      u32 kva = (!half && ex > 2) ? OOB : kvoff, kvb = (!half && ex > 0) ? OOB : kvoff;
      const u32 kxa = (half && ex > 2) ? kvoff - 8 * NL * 1024u : OOB, kxb = (half && ex > 0) ? kvoff - 8 * NL * 1024u : OOB;
      u32x4 kx[4];
      const u32 t0 = __builtin_amdgcn_readfirstlane(cnt[15]);
#pragma unroll
      for (int i = 0; i < 16; ++i) {
        if (PK && (i & 3) == 0) PACE(48 + i)
        kh[i] = LDKV(rk, i < 12 ? kvoff : i < 14 ? kva : kvb, 16 + i);
      }
#pragma unroll
      for (int i = 0; i < 4; ++i) kx[i] = LDKV(rk, i < 2 ? kxa : kxb, 28 + i);
      LDS_INC(C_KI);
      if (wave == 0) {
        if (half == 0 && g >= 80 && g < 120) {
          const int fb = g - 80;
          u32 of2[2];
          u32x4 gm[2];
#pragma unroll
          for (int ks = 0; ks < 2; ++ks) of2[ks] = G_MOD + GIDX(1024, ks, r, 320 + fb * 16 + c);
          gather22<2, 4>(rs, of2, gm, tg);
          const f32x4 mv = un_g22(gm[0]) + un_g22(gm[1]);
          st_gran(rs, G_MF + (((fb / 10) * 4 + r) * 160 + (fb % 10) * 16 + c) * 16, cvt2(mv[0], mv[1]), cvt2(mv[2], mv[3]), tg);
        }
      }
      __builtin_amdgcn_s_setprio(0);
      STAMPT(2, 0);
#define GO(r_, n, name) "s_cmp_eq_u32 %" #r_ ", " #n "\n\ts_cbranch_scc1 .Lpad_" #name "\n\t"
      asm volatile("s_mov_b64 exec, 0\n\t" GO(0, 0, e1) GO(0, 1, e2) GO(0, 2, e3) GO(0, 3, l1)
                   ".Lpad_role:\n\t" GO(0, 0, c1) GO(0, 1, c0) GO(0, 2, b3) GO(0, 3, b0)
                   ".Lpad_back:\n\ts_mov_b64 exec, -1" :: "s"(wave) : "scc");
#undef GO
      STAMPT(7, 0);
      lds_wait<2>(C_KI, P_GATE);
      if (QHOLD && wave == qw - 4) lds_wait<2>(C_Q, 1);
      asm volatile("" : "+v"(kvoff), "+v"(kva), "+v"(kvb));
#pragma unroll
      for (int i = 0; i < NL; ++i) {
        if (i == L1POS) PAD(l1, e4);
        if (PL && (i & 3) == 0) PACE(64 + PLO + i * PLS / 16)
        kv[i] = LDKV(rv, i < 28 ? kvoff : i < 30 ? kva : kvb, i);
      }
      STAMPT(1, 0);
      GET_Q()
      const bool fixc = half == 0 && (g < 40 || (FXA && fav));
      const u32 tka = t == 0 ? G_TK + CLS * 128 : OOB;
      u32 tkv = __builtin_amdgcn_raw_ptr_buffer_atomic_add_i32(1, rs, half ? tka : OOB, 0, 16);
      asm volatile("" ::: "memory");
      float xx[4];
#pragma unroll
      for (int i = 0; i < 4; ++i) { float d = 0.f;
#pragma unroll
        for (int k = 0; k < 4; ++k) d = dot2(kx[i][k], qpk[k], d);
        xx[i] = d; }
      asm volatile("" : "+v"(xx[0]), "+v"(xx[1]), "+v"(xx[2]), "+v"(xx[3]) :: "memory");
#pragma unroll
      for (int i = 0; i < 4; ++i) kx[i] = LDKV(rv, i < 2 ? kxa : kxb, 28 + i);
      LDS_INC(C_ISS);
      TR_STEP(xx, 2, XOR8, 8)
#pragma unroll
      for (int i = 0; i < 2; ++i) { xx[i] += dpp<XOR7>(xx[i]); xx[i] += dpp<XOR2>(xx[i]); xx[i] += dpp<XOR1>(xx[i]); }
      SCORE16P(xh, kh, 0, l2, t2)
      const float ninf = -__builtin_inff();
      const bool mine = half || c < 16 - ex, extra = half && c >= 16 - 4 * ex;
      xh[0] += dpp<XOR1>(xh[0]); xh[1] += dpp<XOR1>(xh[1]);
      const float s2 = mine ? SCALED(xh[0]) : ninf, s3 = mine ? SCALED(xh[1]) : ninf;
      const float s4 = extra ? SCALED(xx[0]) : ninf, s5 = extra ? SCALED(xx[1]) : ninf;
      float m = wave_max(fmaxf(fmaxf(s2, s3), fmaxf(s4, s5)));
      lds_wait<2>(C_PP + 4 * wave, 1);
      const float me = st[wave + 4];
      m = fmaxf(m, me);
      const float p2 = __expf(s2 - m), p3 = __expf(s3 - m), p4 = __expf(s4 - m), p5 = __expf(s5 - m);
      const float l = wave_sum(fmaf(p4 + p5, 0.25f, p2 + p3)) * 0.5f;
      const u32 pxo = cvt2(p4, p5), pxt = dppu<XOR8>(pxo);
      const u32 px[2] = {c < 8 ? pxo : pxt, c < 8 ? pxt : pxo};
      if (lane == 0) { st[wave] = m; st[8 + wave] = l; }
      pT[(wave * 4 + r) * 16 + 8 + (c >> 1)] = cvt2(p2, p3);
      STAMPT(4, 0); WSTAMP(1);
      PAD(l3, t3);
      if (lds_ticket(C_SC, lane) == 3) {
        const float mw = st[lane & 7], lw = st[8 + (lane & 7)];
        float M = fmaxf(mw, dpp<XOR1>(mw)); M = fmaxf(M, dpp<XOR2>(M)); M = fmaxf(M, dpp<XOR7>(M));
        const float e = __expf(mw - M);
        float L = lw * e;
        L += dpp<XOR1>(L); L += dpp<XOR2>(L); L += dpp<XOR7>(L);
        if (lane == 0) st_gran(rs, G_ML + u * 64, f2u(M), f2u(L), tg);
        if (lane < 8) st[16 + lane] = e * frcp(L);
      }
      bool sub = false;
      if (wave == 0 && !fav) {
        const u32 tt = __builtin_amdgcn_readfirstlane(cnt[15]) + (126 * TAU_E >> 4) - EVT;
        sub = (int)((u32)__builtin_amdgcn_s_memrealtime() - tt) < EVW;
        SPINLOOP while ((int)((u32)__builtin_amdgcn_s_memrealtime() - tt) < 0) __builtin_amdgcn_s_sleep(1);
      }
      asm volatile("" ::: "memory");
      const u32 tkw = __builtin_amdgcn_raw_ptr_buffer_atomic_add_i32(fixc ? 0 : 0x10001, rs, sub && (SLS || !fixc) ? tka : OOB, 0, 16);
      asm volatile("" ::: "memory");
      asm("" : "=v"(ml));
      GET_P()
#pragma unroll
      for (int i = 0; i < 16; i += 2) { if (i == L4POS) PADA(l4, a0); PV2(i) }
      const float f = __expf(me - m);
#pragma unroll
      for (int k = 0; k < 8; ++k) acc[k] *= f;
#pragma unroll
      for (int i = 16; i < NL - PVLAG; i += 2) { PV2(i) }
#pragma unroll
      for (int i = 0; i < 4; i += 2)
#pragma unroll
        for (int k = 0; k < 4; ++k) {
          acc[2 * k] = dot2(__builtin_amdgcn_perm(kx[i + 1][k], kx[i][k], 0x05040100u), px[i / 2], acc[2 * k]);
          acc[2 * k + 1] = dot2(__builtin_amdgcn_perm(kx[i + 1][k], kx[i][k], 0x07060302u), px[i / 2], acc[2 * k + 1]);
        }
      asm volatile("" : "+v"(acc[0]), "+v"(acc[1]), "+v"(acc[2]), "+v"(acc[3]), "+v"(acc[4]), "+v"(acc[5]), "+v"(acc[6]), "+v"(acc[7]) :: "memory");
      rw2 = 0;
      if (wave == 0) {
        constexpr int NT2 = FXA ? 128 : 134;
        const u32 tks = __builtin_amdgcn_readfirstlane(tkv + tkw + 1u);
        const int n0 = (tks & 0xffff) * NCL + CLS;
        const int slow = sub ? -min(max(SLF - (int)((tks & 0xffff) - (tks >> 16)), 0) * SLS, SLM) : 0;
        const int n = fixc ? (g < 40 ? 134 + g : g + 8) : (fav || sub) ? sel_lt(n0, NT2, n0, 174) : 174;
        constexpr int N0 = 54;
        const int rl = sel_lt(n, N0, 1, sel_lt(n, 134, 0, sel_lt(n, 174, 2, 3)));
        int un = n - sel_lt(n, N0, 0, sel_lt(n, 134, N0, 134));
        if (NCL == 8 && rl == 1 && un < 48) un = (un & 48) | (un & 7) << 1 | (un >> 3 & 1);
        if (NCL == 4 && rl == 1 && un < 48) un = (un & 56) | (un & 3) << 1 | (un >> 2 & 1);
        if (lane == 0) *reinterpret_cast<u32x2*>(lds + C_RL) = u32x2{0x80000000u | (u32)(un & 255) << 8 | (u32)rl, (u32)(sel_lt(n, N0, DLB, sel_lt(n, 134, fav ? DLA : DLS, slow)) - (fav ? DL0 : 0))};
        STAMPT(28, 0);
      }
    }
#pragma unroll
    for (int i = NL - PVLAG; i <= NL - HX; i += 2) { if (i == T1POS) PADA(t1, role); PV2(i) }
    u32 a_aw = L_ACC + (wave * 128 + c * 8 + 2 * r) * 4, a_as = L_ST + 64 + (lane >> 5) * 16, a_ar = L_ACC + (lane >> 5) * 2048 + (lane & 31) * 8, one = 1;
    tkf = C_FIN;
    a_pub = G_ATT + u * 256 + lane * 4;
    asm volatile("" : "+v"(acc[0]), "+v"(acc[1]), "+v"(acc[2]), "+v"(acc[3]), "+v"(acc[4]), "+v"(acc[5]), "+v"(acc[6]), "+v"(acc[7]),
                      "+v"(a_aw), "+v"(a_as), "+v"(a_ar), "+v"(one), "+v"(tkf), "+v"(a_pub));
    if (!HT) { PV2(NL - HX + 2) } else {
      u32 tk = 0;
      u64 tn;
      asm volatile("s_mov_b64 exec, 1\n\tds_add_rtn_u32 %0, %10, %11\n\ts_mov_b64 exec, -1\n\ts_memrealtime %1"
                   : "+v"(tk), "=s"(tn), "+v"(acc[0]), "+v"(acc[1]), "+v"(acc[2]), "+v"(acc[3]), "+v"(acc[4]), "+v"(acc[5]), "+v"(acc[6]), "+v"(acc[7])
                   : "v"((u32)C_HT), "v"(1u) : "memory");
      PV2(NL - HX + 2)
      asm volatile("s_waitcnt lgkmcnt(0)" : "+v"(tk), "+s"(tn), "+v"(acc[0]), "+v"(acc[1]), "+v"(acc[2]), "+v"(acc[3]), "+v"(acc[4]), "+v"(acc[5]), "+v"(acc[6]), "+v"(acc[7]));
      __builtin_amdgcn_raw_buffer_store_b64(u32x2{(u32)tn + HTD, tg}, rs, tk == 7 ? G_HT + u * 128 : OOB, 0, 16);
      asm volatile("" ::: "memory");
    }
#pragma unroll
    for (int i = NL - HX + 4; i < NL - 2; i += 2) { PV2(i) }
    PV2(NL - 2)
#undef PV2
#undef TR_STEP
#undef SCORE16
#undef SCORE16P
#undef SCALED
#undef GET_Q
#undef GET_P
#undef LDKV
#pragma unroll
    for (int i = 0; i < 4; ++i) acc[i] = swap32_add(acc[i], acc[i + 4]);
#pragma unroll
    for (int i = 0; i < 2; ++i) acc[i] = swap16_add(acc[i], acc[i + 2]);
    asm volatile("ds_write_b64 %6, %7\n\ts_mov_b64 exec, 1\n\tds_add_rtn_u32 %0, %0, %8\n\ts_mov_b64 exec, -1\n\tds_read_b128 %1, %9\n\t"
                 "ds_read2_b64 %2, %10 offset1:32\n\tds_read2_b64 %3, %10 offset0:64 offset1:96\n\t"
                 "ds_read2_b64 %4, %10 offset0:128 offset1:160\n\tds_read2_b64 %5, %10 offset0:192 offset1:224\n\ts_waitcnt lgkmcnt(0)"
                 : "+v"(tkf), "=&v"(psc), "=&v"(pw4[0]), "=&v"(pw4[1]), "=&v"(pw4[2]), "=&v"(pw4[3])
                 : "v"(a_aw), "v"(f32x2{acc[0], acc[1]}), "v"(one), "v"(a_as), "v"(a_ar) : "memory");
    STAMPT(6, 0); WSTAMP(2);
  }
  const u32x4 rsv = {(u32)(uintptr_t)wsq, (u32)((uintptr_t)wsq >> 32) & 0xffffu, WS_END, 0x00020000u};
#define MLOOK(W_) asm volatile(LK(0, 1, "0", 0, 2) LKEND : "+v"(ml) : "v"(om), "s"(m2), [rs] "s"(rsv), [w] "i"(W_) : "memory");
  if (__builtin_amdgcn_readfirstlane(tkf) == 7) {
    PAD(t2, role);
    const f32x4 o = (psc[0] * pw4[0] + psc[1] * pw4[1]) + (psc[2] * pw4[2] + psc[3] * pw4[3]);
    __builtin_amdgcn_raw_buffer_store_b32(min(cvt2(swap32_add(o[0], o[2]), swap32_add(o[1], o[3])), EMPTY - 1), rs, a_pub, 0, 16);
    LDS_INC(C_PUB);
    STAMPW(11);
  }
  if ((int)rw2 >= 0) WAIT_ROLE()
  const int role = rw2 & 3, ia = (rw2 >> 8) & 255, jB = ia;
  const int ksA = ia & 7, cbA = ia >> 3;
  const u32 blkA = BLKA(ia), om = G_ML + blkA * 64;
  float b1v;
  u32x4 gaq;
  if (role == 0 && wave < 4) {
    if (RPRI) __builtin_amdgcn_s_setprio(RPRI);
    {
      const u32x4 rxv = {(u32)(uintptr_t)a.x.p, (u32)((uintptr_t)a.x.p >> 32) & 0xffffu, NB * DM * 2, 0x00020000u};
      asm volatile("buffer_load_dwordx4 %0, %2, %4, 0 offen sc1\n\tbuffer_load_dwordx2 %1, %3, %5, 0 offen" : "=&v"(gaq), "=&v"(xw)
                   : "v"(G_MF + (r * 160 + cbA * 16 + c) * 16), "v"(ksA == 0 ? (r * DM + cbA * 64 + c * 4) * 2 : OOB), "s"(rsv), "s"(rxv) : "memory");
    }
    if (half) STAMPT(8, 0);
    lds_wait<2>(C_ISS, 8);
    {
      const bf16* wp = a.wo + (size_t)(HEADA(wave) * 128 + (lane >> 3)) * DM + cbA * 64;
#pragma unroll
      for (int i = 0; i < 16; ++i) {
        if (i == 1) PAD(a0, role);
        if (i == N1A) {
          lds_wait<2>(C_PUB, 1);
          asm volatile("" ::: "memory");
        }
        dma16(wp + (size_t)(8 * i) * DM + (s128e ^ ((i & 1) * 32)), L_W + wave * 16384 + i * 1024);
      }
      LDS_INC(C_ISS);
    }
    STAMPT(16, 0);
  }
#define SELECTOR(sel) u32x4 sel; \
    _Pragma("unroll") for (int d = 0; d < 4; ++d) sel[d] = ((c & 2) == 0 && d == (c >> 2)) ? ((c & 1) ? 0x3f800000u : 0x00003f80u) : 0u;
#define PAIRSUM(sm) (((c & 8) ? sm[1] : sm[0]) + dpp<XOR8>((c & 8) ? sm[0] : sm[1]))
  const u32 tb = __builtin_amdgcn_readfirstlane(cnt[15] + (GX ? cnt[20] * GX >> 5 : 0));
  if (role == 0) {
    PAD(a1, back);
    if (wave < 4) {
      u32 a_sum = L_RED + lane * 16;
      u32 a_out = wave < NREP ? G_WO + wave * WO_REP + ((cbA * 4 + (c >> 2)) * 8 + ksA) * 128 + r * 32 + (c & 3) * 8 : OOB;
      asm volatile("s_waitcnt vmcnt(4)" : "+v"(a_sum), "+v"(a_out), "+v"(gaq), "+v"(xw) :: "memory");
      LDS_INC(C_PP + 4 * wave);
      if (!__all(gaq[1] == tg && gaq[3] == tg)) {
        u32 offs[1] = {G_MF + (r * 160 + cbA * 16 + c) * 16};
        u32x4 gm[1];
        gather<1>(rs, offs, gm, tg);
        gaq = gm[0];
      }
      float ga4[4] = {lo16(gaq[0]), hi16(gaq[0]), lo16(gaq[2]), hi16(gaq[2])}, x4[4] = {lo16(xw[0]), hi16(xw[0]), lo16(xw[1]), hi16(xw[1])};
      asm volatile("" : "+v"(ga4[0]), "+v"(ga4[1]), "+v"(ga4[2]), "+v"(ga4[3]), "+v"(x4[0]), "+v"(x4[1]), "+v"(x4[2]), "+v"(x4[3]));
      STAMPT(19, 0);
      u32 a_fl = lane < 16 ? G_HA + ia * 128 + lane * 8 : OOB;
      PAD(a2, back);
      asm volatile("s_waitcnt vmcnt(0)" : "+v"(a_fl) :: "memory");
      LDS_INC(C_PP + 4 * wave);
      STAMPT(23, 0);
      if (wave == 0) {
        lds_wait<0>(C_W, 4);
        STAMPT(12, 0);
        __builtin_amdgcn_raw_buffer_store_b64(u32x2{(u32)__builtin_amdgcn_s_memrealtime() + HAG, tg}, rs, a_fl, 0, 16);
      }
      BAR();
      STAMPT(21, 0);
      f32x4 pp[4];
#pragma unroll
      for (int w = 0; w < 4; ++w) pp[w] = *reinterpret_cast<const f32x4*>(lds + a_sum + w * 1024);
      asm volatile("" : "+v"(pp[0]), "+v"(pp[1]), "+v"(pp[2]), "+v"(pp[3]));
      const f32x4 s = (pp[0] + pp[1]) + (pp[2] + pp[3]);
      const u32x2 o = {min(cvt2(fmaf(s[0], ga4[0], x4[0]), fmaf(s[1], ga4[1], x4[1])), EMPTY - 1), min(cvt2(fmaf(s[2], ga4[2], x4[2]), fmaf(s[3], ga4[3], x4[3])), EMPTY - 1)};
      __builtin_amdgcn_raw_buffer_store_b64(o, rs, a_out, 0, 16);
    } else {
      u32 oa = G_ATT + blkA * 256 + (c >> 3) * 64 + r * 16;
      u32 a_red = L_RED + (((wave & 3) * 4 + c) * 64 + r * 4) * 4;
      u32 a_w[4];
#pragma unroll
      for (int nt = 0; nt < 4; ++nt) a_w[nt] = L_W + (wave & 3) * 16384 + (tr128 ^ (nt * 32));
      u32x4 av[2];
      u64 m0 = ~0ull, m1 = ~0ull, m2 = 0xffull;
      av[0] = av[1] = u32x4{0u, 0u, 0u, 0u};
      asm volatile("" : "+v"(oa), "+v"(ml), "+v"(av[0]), "+v"(av[1]), "+v"(a_red), "+v"(a_w[0]), "+v"(a_w[1]), "+v"(a_w[2]), "+v"(a_w[3]) :: "memory");
      SPINLOOP for (unsigned spins = 0; spins < SPIN_CAP; ++spins) {
        m2 &= __builtin_amdgcn_ballot_w64(ml[1] != tg) | __builtin_amdgcn_ballot_w64(ml[3] != tg);
        if (!m2) break;
        if (!spins) FAIL(4);
        __builtin_amdgcn_s_sleep(ASL);
        MLOOK(0)
      }
      float wsc;
      {
        const float M = u2f(ml[0]), L = u2f(ml[2]), Mo = dpp<XOR7>(dpp<0x1B>(M)), Lo = dpp<XOR7>(dpp<0x1B>(L));
        const float Mx = fmaxf(M, Mo), le = L * __expf(M - Mx);
        wsc = le * frcp(fmaf(Lo, __expf(Mo - Mx), le));
        wsc = u2f((u32)__builtin_amdgcn_ds_bpermute((c & 7) * 4, (int)f2u(wsc)));
        asm volatile("" : "+v"(wsc));
      }
#define ALOOK(W_) \
        asm volatile(LK(0, 2, "0", 0, 3) LK(1, 2, "0", 128, 4) LKEND \
                     : "+v"(av[0]), "+v"(av[1]) : "v"(oa), "s"(m0), "s"(m1), [rs] "s"(rsv), [w] "i"(W_) : "memory");
#define ACHK() \
        m0 = __builtin_amdgcn_ballot_w64(missing(av[0])); \
        m1 = __builtin_amdgcn_ballot_w64(missing(av[1]));
      lds_wait<1>(C_PP + 4 * (wave - 4), 2);
      if (AGT) SPINLOOP for (int k = 0; k < 64; ++k) if ((int)((u32)__builtin_amdgcn_s_memrealtime() - tb) >= (126 * TAU_E >> 4) + AGT) break;
      STAMPT(29, 448);
      ALOOK(63)
      s16x8 wf[4][4];
#pragma unroll
      for (int s = 0; s < 3; ++s)
#pragma unroll
        for (int nt = 0; nt < 4; ++nt) wf[s][nt] = wfrag<512>(a_w[nt] + s * 4096);
      lds_wait<1>(C_PP + 4 * (wave - 4), 3);
#pragma unroll
      for (int nt = 0; nt < 4; ++nt) wf[3][nt] = wfrag<512>(a_w[nt] + 3 * 4096);
      u32 a_cw = C_W, one = 1;
      u64 mw = 0x000f000f000f000full;
      asm volatile("" : "+v"(wf[3][0]), "+v"(wf[3][1]), "+v"(wf[3][2]), "+v"(wf[3][3]), "+v"(a_cw), "+v"(one), "+s"(mw));
      PAD(a3, b4);
      asm volatile("s_waitcnt vmcnt(0)" : "+v"(av[0]), "+v"(av[1]) :: "memory");
      ACHK()
      STAMPT(18, 448);
      if (m0 | m1) {
        FAIL(1);
        SPINLOOP for (unsigned spins = 0; spins < SPIN_CAP; ++spins) {
          ALOOK(2 * (PDA - 1))
          ACHK()
          if (!(m0 | m1)) break;
          __builtin_amdgcn_s_sleep(ASL);
        }
      }
#undef ALOOK
#undef ACHK
      asm volatile("" : "+v"(av[0]), "+v"(av[1]));
      f32x4 acc[4], acb[4];
      const f32x4 z = {0.f, 0.f, 0.f, 0.f};
#define SB() __builtin_amdgcn_sched_barrier(0)
#define PIN(x) asm volatile("" : "+v"(x)); SB();
#define M1(n) { acc[n] = MFMA(wf[0][n], av[0], z); PIN(acc[n]) acb[n] = MFMA(wf[1][n], av[0], z); PIN(acb[n]) }
#define M2A(n) { acb[n] = MFMA(wf[3][n], av[1], acb[n]); PIN(acb[n]) }
#define M2B(n) { acc[n] = MFMA(wf[2][n], av[1], acc[n]); PIN(acc[n]) }
#define D8(n, k) { float v = acc[n][k] + dpp<XOR8>(acb[n][k]); PIN(v) acc[n][k] = v; v = acc[n][k + 1] + dpp<XOR8>(acb[n][k + 1]); PIN(v) acc[n][k + 1] = v; }
#define MW(n) { acc[n] *= wsc; PIN(acc[n]) }
#define D4(n) { _Pragma("unroll") for (int k = 0; k < 4; ++k) { float v = acc[n][k] + dpp<SHL4>(acc[n][k]); PIN(v) acc[n][k] = v; } }
#define WR(n) asm volatile("s_mov_b64 exec, %3\n\tds_write_b128 %0, %1 offset:" STR(n * 64) "\n\tds_write_b128 %0, %2 offset:" STR(n * 64 + 64) "\n\ts_mov_b64 exec, -1" \
                           :: "v"(a_red), "v"(acc[n]), "v"(acc[n + 1]), "s"(mw) : "memory")
      SB();
      M1(0)
      asm volatile("s_mov_b64 exec, 1\n\tds_add_u32 %0, %1\n\ts_mov_b64 exec, -1" :: "v"(a_cw), "v"(one) : "memory");
      SB();
      M1(1) M2A(0) M2B(0) M1(2) M2A(1) M2B(1) M1(3)
      M2A(2) D8(0, 0) M2B(2) D8(0, 2) M2A(3) D8(1, 0) M2B(3) D8(1, 2)
      MW(0) MW(1) D4(0) D4(1) D8(2, 0) D8(2, 2)
      WR(0);
      SB();
      D8(3, 0) D8(3, 2) MW(2) MW(3) D4(2) D4(3)
      WR(2);
#undef M1
#undef M2A
#undef M2B
#undef D8
#undef MW
#undef D4
#undef WR
      STAMPT(22, 448);
      BAR();
      asm volatile("" :: "v"(av[0]), "v"(av[1]));
    }
    STAMPT(13, 0);

  } else if (role == 1) {
    PAD(b0, bd);
    u32x2 bw2;
    asm("" : "=v"(bw2));
    if (wave < 4) {
      if (RPRI) __builtin_amdgcn_s_setprio(RPRI);
#pragma unroll
      for (int i = 0; i < 5; ++i) gmf[i] = __builtin_amdgcn_raw_buffer_load_b128(rs, G_MF + (640 + t) * 16, i * 4096, 16);
      const __amdgpu_buffer_rsrc_t rb = __builtin_amdgcn_make_buffer_rsrc((void*)a.b1, 0, FF * 2, 0x00020000);
      bw2 = __builtin_amdgcn_raw_buffer_load_b64(rb, (jB * 32 + (t & 28)) * 2, 0, 0);
      asm volatile("" ::: "memory");
    }
    const int lb = (c >> 1) & 3, lc = (c & 1) * 8 + 2 * r;
    const int fc = lane & 31, hw = lane >> 5;
    SELECTOR(sel)
    constexpr int L_R2 = L_MOD, L_S2 = L_R2 + 4096;
    const u32 gw = G_WO + wave * 5120;
    u32 go = gw + (c >> 3) * 1024 + r * 128 + (c & 7) * 16, go4 = gw + 4096 + lane * 16;
    u32 a_u = L_XA + wave * 640 + lb * 160 + ((c >> 3) * 16 + lc) * 2, a_u4 = L_XA + wave * 640 + lb * 160 + (64 + lc + (c >> 3)) * 2;
    u32 a_av = L_XA + wave * 640 + (c & 3) * 160 + r * 16, a_ah = a_av + 128 - r * 8;
    u32 a_red = L_R2 + ((wave * 4 + c) * 32 + r * 4) * 4;
    u32 a_st = L_S2 + ((lb * 2 + (c >> 3)) * 8 + wave) * 4;
    u32 a_fin = L_R2 + ((hw * 16 + (wave & 3)) * 32 + fc) * 4;
    u32 a_fst = L_S2 + (wave & 3) * 64;
    u32 a_out = c < 4 ? G_OUT + (wave * 5 * 54 + jB) * 128 + c * 32 + r * 8 : OOB;
    constexpr int L_F2 = L_XR;
    u32 a_fi = BM ? L_F2 + c * 64 + r * 16 : L_FI + (c & 3) * 64 + r * 16, a_fo = (BM ? L_F2 + 768 : L_FI) + ((wave & 3) * 32 + fc) * 2;
    u32 a_ou4 = G_OUT + ((wave * 5 + (c >> 2)) * 54 + jB) * 128 + (c & 3) * 32 + r * 8;
    asm volatile("" : "+v"(a_ou4));
    u32 a_pre = lane < 40 ? G_OUT + ((wave * 5 + (lane >> 3)) * 54 + jB) * 128 + (lane & 7) * 16 : OOB;
    u32x4 emp = {EMPTY, EMPTY, EMPTY, EMPTY};
    asm volatile("" : "+v"(a_pre), "+v"(emp));
#define OPRE() asm volatile("buffer_store_dwordx4 %0, %1, %2, 0 offen sc1\n\ts_nop 1" :: "v"(emp), "v"(a_pre), "s"(rsv) : "memory")
    if (OPOS == 0) OPRE();
    u32 a_w1 = L_W + wave * 5120 + (8 * r + (c >> 2)) * 64 + (r & 1) * 32 + (c & 3) * 8;
    u32 a_wh = L_W + wave * 5120 + 4096 + (4 * r + (c >> 2)) * 64 + (r >> 1) * 32 + (c & 3) * 8;
    u32 a_w2[5];
#pragma unroll
    for (int k = 0; k < 5; ++k) { const int n2 = wave * 5 + k; a_w2[k] = L_WM2 + (n2 >> 2) * 4096 + (tr128 ^ ((n2 & 3) * 32)); }
    asm volatile("" : "+v"(sel), "+v"(go), "+v"(go4), "+v"(a_u), "+v"(a_u4), "+v"(a_av), "+v"(a_ah), "+v"(a_red), "+v"(a_st), "+v"(a_fin), "+v"(a_fst), "+v"(a_out));
    asm volatile("" : "+v"(a_fi), "+v"(a_fo), "+v"(a_w1), "+v"(a_wh), "+v"(a_w2[0]), "+v"(a_w2[1]), "+v"(a_w2[2]), "+v"(a_w2[3]), "+v"(a_w2[4]) :: "memory");
    if (BM) *reinterpret_cast<u32*>(lds + L_F2 + t * 4) = 0;
    PAD(b4, back);
    asm volatile("" : "+v"(bw2));
    if (wave < 4) {
      STAMPT(7, 0);
      bool ok = true;
#pragma unroll
      for (int i = 0; i < 5; ++i) ok &= (gmf[i][1] == tg) & (gmf[i][3] == tg);
      if (!__all(ok)) {
        u32 offs[5];
#pragma unroll
        for (int i = 0; i < 5; ++i) offs[i] = G_MF + (640 + t + 256 * i) * 16;
        gather<5>(rs, offs, gmf, tg);
      }
#pragma unroll
      for (int i = 0; i < 5; ++i) {
        if (i == 2) PAD(t3, role);
        const int it = t + 256 * i, row = it / 160, gi = it % 160;
        u32 w0 = gmf[i][0], w1 = gmf[i][2];
        if (row >= 4) { w0 = cvt2(1.f + lo16(w0), 1.f + hi16(w0)); w1 = cvt2(1.f + lo16(w1), 1.f + hi16(w1)); }
        *reinterpret_cast<u32x2*>(lds + L_XA + (row < 4 ? row + 8 : row) * XS + gi * 8) = u32x2{w0, w1};
      }
      const u32 bw = (t & 2) ? bw2[1] : bw2[0];
      b1v = (t & 1) ? hi16(bw) : lo16(bw);
      asm volatile("" : "+v"(b1v));
      lds_arrive(C_MF, lane);
    }
    lds_wait<1>(C_MF, 4);
    float c1f[5];
    {
      const unsigned char* cp = lds + L_XA + (4 + lb) * XS + wave * 160;
#pragma unroll
      for (int i = 0; i < 2; ++i) { const u32 w = *reinterpret_cast<const u32*>(cp + (32 * i + (c >> 3) * 16 + lc) * 2); c1f[2 * i] = lo16(w); c1f[2 * i + 1] = hi16(w); }
      c1f[4] = u2f((u32)*reinterpret_cast<const unsigned short*>(cp + (64 + lc + (c >> 3)) * 2) << 16);
    }
    const unsigned char* pm = lds + L_XA + (4 + (c & 7)) * XS + wave * 160;
    u32x4 mv0 = *reinterpret_cast<const u32x4*>(pm + r * 16), mv1 = *reinterpret_cast<const u32x4*>(pm + 64 + r * 16);
    s16x4 mv2 = *reinterpret_cast<const s16x4*>(pm + 128 + r * 8);
    u32 a_rd = L_RED + ((wave * 8 + c) * 32 + r * 4) * 4;
    asm volatile("" : "+v"(c1f[0]), "+v"(c1f[1]), "+v"(c1f[2]), "+v"(c1f[3]), "+v"(c1f[4]), "+v"(mv0), "+v"(mv1), "+v"(mv2), "+v"(a_rd) :: "memory");
    if (wave < 4) {
      STAMPT(8, 0);
      lds_wait<2>(C_ISS, 8);
      if (BHOLD) SPINLOOP for (int k = 0; k < 256; ++k) { if ((int)((u32)__builtin_amdgcn_s_memrealtime() - tb) >= (126 * TAU_E >> 4) + BHOLD) break; __builtin_amdgcn_s_sleep(2); }
      const int h5 = ROT1 * (jB & 1);
      const bf16* wp = a.w1 + (size_t)(wave * 160 + (lane >> 2)) * FF + jB * 32 + ((((((lane >> 1) & 1) ^ (lane >> 5)) << 1) | (lane & 1)) * 8);
      const bf16* wp2 = a.w2 + (size_t)(jB * 32 + wave * 8 + (lane >> 3)) * DM + (s128e ^ ((wave & 1) * 32));
#pragma unroll
      for (int n = 0; n < 20; ++n) {
        if (n == 0) PAD(bd, back);
        if (n == N1B) lds_wait<2>(C_PUB, 1);
        if (n < 5 || (n >= 5 + W2F && n < 10 + W2F)) {
          const int i2 = n < 5 ? n + h5 : n - W2F - h5;
          dma16(wp + (size_t)(16 * i2) * FF, L_W + (wave * 10 + i2) * 1024);
        } else {
          const int k = n < 5 + W2F ? n - 5 : n - 10;
          dma16(wp2 + k * 64, L_WM2 + (k * 4 + wave) * 1024);
        }
      }
      STAMPT(16, 0); STAMPT(5, 128);
    }
    asm volatile("s_waitcnt vmcnt(%0)" :: "i"(10 - W2F) : "memory");
    STAMPT(23, 0);
    PAD(b1, a1);
    BAR();
    STAMPT(3, 0);
    if (OPOS == 1) OPRE();
    s16x8 wf[2][2], wg[5];
    s16x4 wh[2];
#pragma unroll
    for (int nt = 0; nt < 2; ++nt) {
#pragma unroll
      for (int s = 0; s < 2; ++s) wf[s][nt] = wfrag<256>((a_w1 ^ (nt * 32)) + s * 2048);
      wh[nt] = __builtin_amdgcn_ds_read_tr16_b64_v4i16((lds_s4p)(uintptr_t)(a_wh ^ (nt * 32)));
    }
#define W1MMA(acc) \
      f32x4 acc[2]; \
      _Pragma("unroll") for (int nt = 0; nt < 2; ++nt) { const f32x4 z_ = {0.f, 0.f, 0.f, 0.f}; acc[nt] = MFMA(wf[0][nt], av0, z_); } \
      _Pragma("unroll") for (int nt = 0; nt < 2; ++nt) acc[nt] = MFMA(wf[1][nt], av1, acc[nt]); \
      _Pragma("unroll") for (int nt = 0; nt < 2; ++nt) acc[nt] = __builtin_amdgcn_mfma_f32_16x16x16bf16_1k(wh[nt], av2, acc[nt], 0, 0, 0);
    {
      const u32x4 av0 = mv0, av1 = mv1;
      const s16x4 av2 = mv2;
      W1MMA(acc)
#pragma unroll
      for (int nt = 0; nt < 2; ++nt) if (c < 8) *reinterpret_cast<f32x4*>(lds + a_rd + nt * 64) = acc[nt];
    }
    float Tc = 0.f, Ts = 0.f;
#define TSUM() _Pragma("unroll") for (int k = 0; k < 4; ++k) { Tc += red[((hw * 4 + k) * 8 + (wave & 3)) * 32 + fc]; Ts += red[((hw * 4 + k) * 8 + 4 + (wave & 3)) * 32 + fc]; }
    u32x4 pa[5];
    f32x4 s4, s0, s1;
    u32 a_fl = lane < 16 ? G_HB + jB * 128 + lane * 8 : OOB, tk = C_W;
    u32x2 fv = {1u, tg};
    {
      u64 m[5], old = 0;
#pragma unroll
      for (int i = 0; i < 5; ++i) { m[i] = ~0ull; pa[i] = u32x4{0u, 0u, 0u, 0u}; }
#pragma unroll
      for (int i = 0; i < 5; ++i) asm volatile("" : "+v"(c1f[i]), "+v"(pa[i]));
#define BLOOK(W_) \
        asm volatile(LK(0, 5, "0", 0, 7) LK(1, 5, "0", 512, 8) LK(2, 5, "0", 2048, 9) LK(3, 5, "0", 2560, 10) LK(4, 6, "0", 0, 11) LKEND \
                     : "+v"(pa[0]), "+v"(pa[1]), "+v"(pa[2]), "+v"(pa[3]), "+v"(pa[4]) \
                     : "v"(go), "v"(go4), "s"(m[0]), "s"(m[1]), "s"(m[2]), "s"(m[3]), "s"(m[4]), [rs] "s"(rsv), [w] "i"(W_) : "memory");
      STAMPT(9, 0); STAMPT(30, 448);
      if (BPREW) {
        VM0();
        STAMPT(20, 0); STAMPT(31, 448);
        BAR();
        STAMPT(29, 0);
      }
      SPINLOOP for (int k = 0; k < 256; ++k) if ((int)((u32)__builtin_amdgcn_s_memrealtime() - tb) >= (126 * TAU_E >> 4) + BGT) break;
      STAMPT(21, 0);
      BLOOK(5)
      if (!BPREW) { STAMPT(20, 0); STAMPT(31, 448); }
      if (!BAN) announce(rs, G_HB + jB * 128, C_GO, C_W, 8, tb, HCD, tg);
      if (!BPREW) { BAR(); STAMPT(29, 0); }
#pragma unroll
      for (int k = 0; k < 5; ++k) wg[k] = wfrag<512>(a_w2[k]);
      TSUM()
#pragma unroll
      for (int i = 0; i < 5; ++i) asm volatile("" : "+v"(wg[i]));
      asm volatile("" : "+v"(Tc), "+v"(Ts));
      PAD(b2, a2);
      STAMPT(18, 0);
      asm volatile("s_waitcnt vmcnt(%8)" : "+v"(pa[0]), "+v"(pa[1]), "+v"(pa[2]), "+v"(pa[3]), "+v"(pa[4]), "+v"(a_fl), "+v"(fv), "+v"(tk) : "i"(BAN ? 0 : 1) : "memory");
      STAMPT(22, 0);
#define BSUM() { const f32x4 z = {0.f, 0.f, 0.f, 0.f}; \
      s0 = MFMA(sel, pa[0], z); PIN(s0) s1 = MFMA(sel, pa[2], z); PIN(s1) s0 = MFMA(sel, pa[1], s0); PIN(s0) s1 = MFMA(sel, pa[3], s1); PIN(s1) s4 = MFMA(sel, pa[4], z); PIN(s4) \
      if (BAN) asm volatile("s_memrealtime s[98:99]" ::: "s98", "s99"); SB(); }
#define BNAN() (__builtin_amdgcn_ballot_w64(__builtin_isunordered(s0[0], s0[1])) | __builtin_amdgcn_ballot_w64(__builtin_isunordered(s1[0], s1[1])) | \
                __builtin_amdgcn_ballot_w64(__builtin_isunordered(s4[0], s4[1])))
      BSUM()
      if (BNAN()) {
        FAIL(2);
        bool again = true;
        if (BRT) {
          u32x2 hv;
          const u32 fo = G_HA + ((wave * 5 >> 2) * 8 + lane) * 128;
#pragma unroll
          for (int i = 0; i < 5; ++i) m[i] = __builtin_amdgcn_ballot_w64(missing(pa[i]));
          BLOOK(63)
          heads_up_start(rsv, fo, 0xffffull, tg, hv);
          asm volatile("s_waitcnt vmcnt(1)" : "+v"(pa[0]), "+v"(pa[1]), "+v"(pa[2]), "+v"(pa[3]), "+v"(pa[4]) :: "memory");
          BSUM()
          again = BNAN();
          if (again) {
            FAIL(5);
            heads_up<BSL>(rsv, fo, 0xffffull, tg, hv);
            hv[0] += BXD;
            go += WO_REP; go4 += WO_REP;
            heads_up_time(0xffffull, hv);
          }
          asm volatile("s_waitcnt vmcnt(0)" : "+v"(hv) :: "memory");
        }
        if (again) SPINLOOP for (unsigned spins = 0; spins < SPIN_CAP; ++spins) {
          u64 bad = 0;
#pragma unroll
          for (int i = 0; i < 5; ++i) { m[i] = __builtin_amdgcn_ballot_w64(missing(pa[i])); bad |= m[i]; }
          if (!(bad | old)) break;
          old = bad;
          BLOOK(5 * (PDB2 - 1))
          BSUM()
          if (!BNAN()) break;
        }
      }
    }
#define BFLAG(W_) u32 tl; \
        asm volatile("s_waitcnt lgkmcnt(%3)\n\ts_add_u32 %0, s98, %2" : "=s"(tl), "+v"(tk) : "i"(HBD), "i"(W_) : "scc"); \
        fv[0] = tl; \
        asm volatile("v_readfirstlane_b32 %0, %1\n\ts_cmp_eq_u32 %0, 7\n\ts_cbranch_scc0 1f\n\tbuffer_store_dwordx2 %2, %3, %4, 0 offen sc1\n1:" \
                     : "=&s"(tl) : "v"(tk), "v"(fv), "v"(a_fl), "s"(rsv) : "scc"); \
        SB();
    if (BAN >= 2) asm volatile("s_mov_b64 exec, 1\n\tds_add_rtn_u32 %0, %0, %1\n\ts_mov_b64 exec, -1" : "+v"(tk) : "v"(fv[0]));
    PAD(b3, a3);
    SB();
    STAMPT(14, 0); WSTAMP(3);
    float xf[5];
    {
      const u32 w0 = cvt2(s0[0], s0[1]), w1 = cvt2(s1[0], s1[1]);
      xf[0] = lo16(w0); xf[1] = hi16(w0); xf[2] = lo16(w1); xf[3] = hi16(w1);
      *reinterpret_cast<u32*>(lds + a_u) = cvt2(xf[0] * c1f[0], xf[1] * c1f[1]);
      *reinterpret_cast<u32*>(lds + a_u + 64) = cvt2(xf[2] * c1f[2], xf[3] * c1f[3]);
    }
    STAMPT(19, 0);
    {
      u32x4 av0 = *reinterpret_cast<const u32x4*>(lds + a_av), av1 = *reinterpret_cast<const u32x4*>(lds + a_av + 64);
      SB();
      if (BAN == 2) { BFLAG(3) }
      float e0 = s4[0], e1 = s4[1];
      asm volatile("" : "+v"(e0), "+v"(e1));
      xf[4] = bfr(((c & 8) ? e1 : e0) + dpp<XOR8>((c & 8) ? e0 : e1));
      *reinterpret_cast<unsigned short*>(lds + a_u4) = (unsigned short)cvt2(xf[4] * c1f[4], 0.f);
      s16x4 av2 = *reinterpret_cast<const s16x4*>(lds + a_ah);
      SB();
      if (BAN == 1) asm volatile("s_mov_b64 exec, 1\n\tds_add_rtn_u32 %0, %0, %1\n\ts_mov_b64 exec, -1" : "+v"(tk) : "v"(fv[0]));
      SB();
      if (BAN == 3) { BFLAG(5) }
      const float ps = ((xf[0] + xf[1]) + (xf[2] + xf[3])) + xf[4];
      const float pq = fmaf(xf[0], xf[0], fmaf(xf[1], xf[1], xf[4] * xf[4])) + fmaf(xf[2], xf[2], xf[3] * xf[3]);
      const float t1 = ps + dpp<XOR8>(ps), t2 = pq + dpp<XOR8>(pq);
      float z = (c & 8) ? t2 : t1;
      z += dpp<XOR1>(z);
      asm volatile("" : "+v"(z));
      __builtin_amdgcn_sched_barrier(0);
      W1MMA(acc)
      if (BAN == 1) {
        asm volatile("" : "+v"(acc[0]), "+v"(acc[1]));
        BFLAG(0)
      }
      z = cross_rows_sum(z);
      if ((lane & 49) == 0) *reinterpret_cast<float*>(lds + a_st) = z;
      if (c < 4) {
#pragma unroll
        for (int nt = 0; nt < 2; ++nt) *reinterpret_cast<f32x4*>(lds + a_red + nt * 64) = acc[nt];
      }
    }
#undef W1MMA
    BAR();
    STAMPT(24, 0);
    if (wave < 4) {
      f32x4 sv[4];
      float sp[4];
#pragma unroll
      for (int k = 0; k < 4; ++k) sv[k] = *reinterpret_cast<const f32x4*>(lds + a_fst + k * 16);
#pragma unroll
      for (int k = 0; k < 4; ++k) sp[k] = *reinterpret_cast<const float*>(lds + a_fin + k * 512);
      __builtin_amdgcn_sched_barrier(0);
      const f32x4 s1 = sv[0] + sv[1], s2 = sv[2] + sv[3];
      const float mean = ((s1[0] + s1[1]) + (s1[2] + s1[3])) * (1.f / DM);
      const float var = fmaxf(((s2[0] + s2[1]) + (s2[2] + s2[3])) * (1.f / DM) - mean * mean, 0.f);
      const float rstd = __builtin_amdgcn_rsqf(var + 1e-5f);
      const float zp = fmaf(rstd, fmaf(-mean, Tc, (sp[0] + sp[1]) + (sp[2] + sp[3])), Ts);
      const float hh = bfr(bfr(swap32_add(zp, zp)) + b1v);
      const float e2 = __builtin_amdgcn_exp2f(hh * fmaf(hh * hh, 0.1029432397f, 2.302208198f));
      const float f = fmaf(-hh, frcp(e2 + 1.f), hh);
      *reinterpret_cast<unsigned short*>(lds + a_fo) = (unsigned short)cvt2(f, 0.f);
    }
    BAR();
    if (BM) {
      u32x4 av[4];
#pragma unroll
      for (int k = 0; k < 4; ++k) av[k] = *reinterpret_cast<const u32x4*>(lds + a_fi + (3 - k) * 256);
      SB();
      const f32x4 z = {0.f, 0.f, 0.f, 0.f};
      f32x4 a4 = MFMA(wg[4], av[0], z);
      PIN(a4)
      f32x4 a0 = MFMA(wg[0], av[0], z);
      PIN(a0)
#pragma unroll
      for (int k = 1; k < 4; ++k) { a0 = MFMA(wg[k], av[k], a0); PIN(a0) }
      __builtin_amdgcn_sched_barrier(0);
      const u32x2 o4 = {min(cvt2(a4[0], a4[1]), EMPTY - 1), min(cvt2(a4[2], a4[3]), EMPTY - 1)};
      __builtin_amdgcn_raw_buffer_store_b64(o4, rs, a_out, 4 * (54 * 128), 16);
      const u32x2 o0 = {min(cvt2(a0[0], a0[1]), EMPTY - 1), min(cvt2(a0[2], a0[3]), EMPTY - 1)};
      __builtin_amdgcn_raw_buffer_store_b64(o0, rs, a_ou4, 0, 16);
    } else {
      const u32x4 av = *reinterpret_cast<const u32x4*>(lds + a_fi);
      f32x4 acc[5];
#pragma unroll
      for (int k = 0; k < 5; ++k) { const f32x4 z = {0.f, 0.f, 0.f, 0.f}; acc[k] = MFMA(wg[k], av, z); }
      __builtin_amdgcn_sched_barrier(0);
#pragma unroll
      for (int k = 0; k < 5; ++k) {
        const u32x2 o = {min(cvt2(acc[k][0], acc[k][1]), EMPTY - 1), min(cvt2(acc[k][2], acc[k][3]), EMPTY - 1)};
        __builtin_amdgcn_raw_buffer_store_b64(o, rs, a_out, k * (54 * 128), 16);
      }
    }
    STAMPT(15, 0);
    asm volatile("" :: "v"(pa[0]), "v"(pa[1]), "v"(pa[2]), "v"(pa[3]), "v"(pa[4]));

  } else if (role == 2 && wave == 0) {
    PAD(c0, b1);
    const int lb = (c >> 1) & 3, col = ia * 16 + (c & 1) * 8 + 2 * r + (c >> 3);
    bf16* outp;
    asm volatile("s_load_dwordx2 %0, %1, 128\n\ts_waitcnt lgkmcnt(0)" : "=s"(outp) : "s"(__builtin_amdgcn_kernarg_segment_ptr()));
    const __amdgpu_buffer_rsrc_t ro = __builtin_amdgcn_make_buffer_rsrc(outp, 0, NB * DM * 2, 0x00020000);
#define DUMMY() __builtin_amdgcn_raw_buffer_store_b16((unsigned short)0, ro, (lb * DM + col) * 2, 0, DAUX)
    if (DPOS == 0) DUMMY();
    float b2v = bf2f(a.b2[col]);
    const f32x4 z = {0.f, 0.f, 0.f, 0.f};
    SELECTOR(sel)
    float x1v, gbv;
    {
      u32 offs[1] = {G_MF + ((12 + lb) * 160 + (col >> 2)) * 16};
      u32x4 gm[1];
      gather<1>(rs, offs, gm, tg);
      const u32 gbw = (r & 1) ? gm[0][2] : gm[0][0];
      gbv = (c & 8) ? hi16(gbw) : lo16(gbw);
    }
    u32 oo = lb * DM + col;
    asm volatile("" : "+v"(gbv), "+v"(oo), "+v"(sel), "+v"(b2v));
    u32x4 pa[7];
    u32 go = G_OUT + ia * (54 * 128) + lane * 16, gx = G_WO + ia * 1024 + lane * 16;
#pragma unroll
    for (int i = 0; i < 7; ++i) { pa[i] = u32x4{0u, 0u, 0u, 0u}; asm volatile("" : "+v"(pa[i])); }
    asm volatile("" : "+v"(go), "+v"(gx));
    u32x2 hv;
    if (CPUB) lds_wait<2>(C_PUB, 1);
    if (DPOS == 1) DUMMY();
    heads_up_start(rsv, G_HA + ((ia >> 2) * 8 + lane) * 128, 0xffull, tg, hv);
    heads_up<CSL>(rsv, G_HA + ((ia >> 2) * 8 + lane) * 128, 0xffull, tg, hv);
    if (DPOS == 2) DUMMY();
    if (EPE == 1) __builtin_amdgcn_raw_buffer_store_b32(tg, rs, ia == 0 && lane == 0 ? WS_EPOCH : OOB, 0, 16);
    hv[0] += CXD;
    heads_up_time(0xffull, hv);
    {
      u32x4 gm[1];
      gather_e<1, 2, 1>(rsv, gx, gm, ~0ull);
      const f32x4 sm = MFMA(sel, gm[0], z);
      x1v = bfr(PAIRSUM(sm));
      asm volatile("" : "+v"(x1v));
    }
    STAMPT(23, 0);
    if (DPOS == 3) DUMMY();
    if (EPE == 2) __builtin_amdgcn_raw_buffer_store_b32(tg, rs, ia == 0 && lane == 0 ? WS_EPOCH : OOB, 0, 16);
    PAD(c1, b2);
    if (CGT) SPINLOOP for (int k = 0; k < 256; ++k) if ((int)((u32)__builtin_amdgcn_s_memrealtime() - tb) >= (126 * TAU_E >> 4) + CGT) break;
    heads_up_start(rsv, G_HB + lane * 128, (1ull << 54) - 1, tg, hv);
    heads_up<0>(rsv, G_HB + lane * 128, (1ull << 54) - 1, tg, hv);
    heads_up_time((1ull << 54) - 1, hv);
    STAMPT(14, 0);
    f32x4 s0, s1;
    {
      u64 m[7], old = 0;
#pragma unroll
      for (int i = 0; i < 7; ++i) m[i] = i < 6 ? ~0ull : (1ull << 48) - 1;
      look<63>(rsv, go, pa, m);
#define CW(i, W_) asm volatile("s_waitcnt vmcnt(%1)" : "+v"(pa[i]) : "i"(W_ ? 6 - (i) : 63) : "memory");
#define CSUM(W_) { CW(0, W_) s0 = MFMA(sel, pa[0], z); PIN(s0) CW(1, W_) s1 = MFMA(sel, pa[1], z); PIN(s1) \
      CW(2, W_) s0 = MFMA(sel, pa[2], s0); PIN(s0) CW(3, W_) s1 = MFMA(sel, pa[3], s1); PIN(s1) CW(4, W_) s0 = MFMA(sel, pa[4], s0); PIN(s0) \
      CW(5, W_) s1 = MFMA(sel, pa[5], s1); PIN(s1) CW(6, W_) s0 = MFMA(sel, pa[6], s0); PIN(s0) s0 += s1; }
      CSUM(1)
      if (__builtin_amdgcn_ballot_w64(__builtin_isunordered(s0[0], s0[1]))) {
        FAIL(3);
        SPINLOOP for (unsigned spins = 0; spins < SPIN_CAP; ++spins) {
          u64 bad = 0;
#pragma unroll
          for (int i = 0; i < 7; ++i) { m[i] = __builtin_amdgcn_ballot_w64(missing(pa[i])); bad |= m[i]; }
          if (!(bad | old)) break;
          old = bad;
          look<7 * (PDC - 1)>(rsv, go, pa, m);
          CSUM(0)
          if (!__builtin_amdgcn_ballot_w64(__builtin_isunordered(s0[0], s0[1]))) break;
        }
      }
    }
    STAMPT(16, 0);
    const float y = bfr(bfr(PAIRSUM(s0)) + b2v);
    if (OUTM & 1) __builtin_amdgcn_raw_buffer_store_b16((unsigned short)cvt2(x1v + bfr(gbv * y), 0.f), ro, oo * 2, 0, FAUX);
    else outp[oo] = __float2bfloat16(x1v + bfr(gbv * y));
    STAMPT(17, 0);
    asm volatile("" :: "v"(pa[0]), "v"(pa[1]), "v"(pa[2]), "v"(pa[3]), "v"(pa[4]), "v"(pa[5]), "v"(pa[6]), "v"(hv));

    if (!EPE && ia == 0 && t == 0)
      __hip_atomic_store(reinterpret_cast<u32*>(a.ws + WS_EPOCH), tg, __ATOMIC_RELAXED, __HIP_MEMORY_SCOPE_AGENT);
  }
  STAMP_FLUSH;
}

extern "C" void kernel_launch(void* const* d_in, const int* in_sizes, int n_in, void* d_out, int out_size, void* d_ws,
                              size_t ws_size, hipStream_t stream) {
  Args a;
  a.x = Desc{(const bf16*)d_in[0], NB * DM * 2, 0x00020000u};
  a.kc = (const bf16*)d_in[1];
  a.vc = (const bf16*)d_in[2];
  a.wq = Desc{(const bf16*)d_in[3], DM * QD * 2, 0x00020000u};
  a.wo = (const bf16*)d_in[4];
  a.w1 = (const bf16*)d_in[6];
  a.b1 = (const bf16*)d_in[7];
  a.w2 = (const bf16*)d_in[8];
  a.b2 = (const bf16*)d_in[9];
  a.c = Desc{(const bf16*)d_in[10], NB * DM * 2, 0x00020000u};
  a.wmod = Desc{(const bf16*)d_in[11], DM * MODN * 2, 0x00020000u};
  a.out = (bf16*)d_out;
  a.ws = (unsigned char*)d_ws;
  mega_kernel<<<NWG, NT, 0, stream>>>(a);
}
```
